```python
import torch

def _softmax(x, axis=-1):
    return torch.softmax(x, dim=axis)

def _gelu(x):
    return torch.nn.functional.gelu(x, approximate='tanh')

def _silu(x):
    return torch.nn.functional.silu(x)

def _sigmoid(x):
    return torch.sigmoid(x)

def _layernorm(x, axis=-1, eps=1e-05):
    m = x.mean(dim=axis, keepdim=True)
    v = x.var(dim=axis, keepdim=True, unbiased=False)
    return (x - m) / torch.sqrt(v + eps)

def _scatter_add(tgt, idx, src, axis=0):
    return tgt.clone().index_add_(axis, idx, src)

def _segment_sum(src, seg, n):
    out = torch.zeros((n,) + src.shape[1:], dtype=src.dtype)
    return out.index_add_(0, seg, src)

def _pad(x, pw):
    flat = tuple((v for pair in reversed(pw) for v in pair))
    return torch.nn.functional.pad(x, flat)
kernel_name = 'adaLN_cond__compressed_kv_cla__dense_activation_silu__prompt'

def setup_inputs():

    _w = lambda a: (lambda t: t.to(torch.bfloat16) if t.is_floating_point() else t)(torch.as_tensor(a))

    def _A_setup():
        B, H, Sq, Sk, D, M = (1, 32, 2048, 2048, 128, 512)
        Dc = 4 * D
        return {'x': _w((lambda *s: torch.randn(*s, dtype=torch.bfloat16) * 0.05)(B, Sq, M)), 'c_kv': _w((lambda *s: torch.randn(*s, dtype=torch.bfloat16) * 0.8)(B, Sk, Dc)), 'w_q': _w((lambda *s: torch.randn(*s, dtype=torch.bfloat16) * 1.6)(M, H, D) * M ** (-0.5)), 'w_uk': _w((lambda *s: torch.randn(*s, dtype=torch.bfloat16) * 1.6)(Dc, H, D) * Dc ** (-0.5)), 'w_uv': _w((lambda *s: torch.randn(*s, dtype=torch.bfloat16) * 0.8)(Dc, H, D) * Dc ** (-0.5)), 'w_o': _w((lambda *s: torch.randn(*s, dtype=torch.bfloat16) * 0.8)(H, D, M) * (H * D) ** (-0.5))}
    _A_in = _A_setup()

    def _B_setup():
        B, S, D, F = (1, 2048, 512, 1792)
        return {'x': _w((lambda *s: torch.randn(*s, dtype=torch.bfloat16) * 0.05)(B, S, D)), 'w1': _w((lambda *s: torch.randn(*s, dtype=torch.bfloat16) * 1.6)(D, F) * D ** (-0.5)), 'w2': _w((lambda *s: torch.randn(*s, dtype=torch.bfloat16) * 0.2)(F, D) * F ** (-0.5))}
    _B_in = _B_setup()
    return {'A_x': _A_in['x'], 'A_c_kv': _A_in['c_kv'], 'A_w_q': _A_in['w_q'], 'A_w_uk': _A_in['w_uk'], 'A_w_uv': _A_in['w_uv'], 'A_w_o': _A_in['w_o'], 'B_x': _B_in['x'], 'B_w1': _B_in['w1'], 'B_w2': _B_in['w2'], 'c': _w((lambda *s: torch.randn(*s, dtype=torch.bfloat16) * 0.1)(1, 512) * 8.0), 'w_mod': _w((lambda *s: torch.randn(*s, dtype=torch.bfloat16) * 0.1)(512, 6 * 512) * 512 ** (-0.5) * 8.0)}

def reference(A_x, A_c_kv, A_w_q, A_w_uk, A_w_uv, A_w_o, B_x, B_w1, B_w2, c, w_mod):

    def _A_ref(x, c_kv, w_q, w_uk, w_uv, w_o):
        B, Sq, M = (x.shape[0], x.shape[1], x.shape[2])
        Sk = c_kv.shape[1]
        H, D = (w_q.shape[1], w_q.shape[2])
        q = torch.einsum('bqm,mhd->bhqd', x, w_q)
        k = torch.einsum('bkc,chd->bhkd', c_kv, w_uk)
        v = torch.einsum('bkc,chd->bhkd', c_kv, w_uv)
        scores = torch.einsum('bhqd,bhkd->bhqk', q, k) * D ** (-0.5)
        off = Sk - Sq
        qi = torch.arange(Sq)[:, None]
        kj = torch.arange(Sk)[None, :]
        cmask = kj <= qi + off
        scores = torch.where(cmask[None, None], scores, -1000000000.0)
        p = _softmax(scores, axis=-1)
        ctx = torch.einsum('bhqk,bhkd->bhqd', p, v)
        return torch.einsum('bhqd,hdm->bqm', ctx, w_o)

    def _B_ref(x, w1, w2):
        h = torch.einsum('bsd,df->bsf', x, w1)
        h = _silu(h)
        out = torch.einsum('bsf,fd->bsd', h, w2)
        return out
    x = A_x
    mod = (_silu(c) @ w_mod).reshape(c.shape[0], 6, 1, -1)
    sa, ca, ga, sb, cb, gb = (mod[:, 0].clone(), mod[:, 1].clone(), mod[:, 2].clone(), mod[:, 3].clone(), mod[:, 4].clone(), mod[:, 5].clone())
    h = _layernorm(x, axis=-1) * (1 + ca) + sa
    x = x + ga * _A_ref(h, A_c_kv, A_w_q, A_w_uk, A_w_uv, A_w_o)
    h = _layernorm(x, axis=-1) * (1 + cb) + sb
    x = x + gb * _B_ref(h, B_w1, B_w2)
    return x
```

```cpp
#include <hip/hip_runtime.h>
#include <hip/hip_bf16.h>
#include <stdint.h>
typedef unsigned short u16;
typedef short bf16x8 __attribute__((ext_vector_type(8)));
typedef short bf16x4 __attribute__((ext_vector_type(4)));
typedef float f32x16 __attribute__((ext_vector_type(16)));
typedef float f32x4 __attribute__((ext_vector_type(4)));
typedef float f32x2 __attribute__((ext_vector_type(2)));
typedef __bf16 bf2 __attribute__((ext_vector_type(2)));
typedef unsigned u32x4 __attribute__((ext_vector_type(4)));
typedef unsigned u32x2 __attribute__((ext_vector_type(2)));
#define DI __device__ __forceinline__
constexpr int S = 2048, M = 512, H = 32, D = 128, HD = 4096, F = 1792;
DI float bf2f(u16 v) { return __uint_as_float((unsigned)v << 16); }
DI unsigned cvtpk(float lo, float hi) { f32x2 v = {lo, hi}; bf2 b = __builtin_convertvector(v, bf2); return __builtin_bit_cast(unsigned, b); }
DI u16 f2bf(float f) { return (u16)(cvtpk(f, 0.f) & 0xffff); }
DI float silu(float x) { return x / (1.f + __expf(-x)); }
DI int crow(int r, int hi) { return (r & 3) + 8 * (r >> 2) + 4 * hi; }

__global__ void k_mod(const u16* c, const u16* wm, float* mod) {
  __shared__ float sc[512]; __shared__ float red[4][64];
  int t = threadIdx.x;
  for (int i = t; i < 512; i += 256) sc[i] = bf2f(f2bf(silu(bf2f(c[i]))));
  __syncthreads();
  int col = blockIdx.x * 64 + (t & 63), g = t >> 6; float a = 0.f;
  for (int k = g * 128; k < g * 128 + 128; ++k) a += sc[k] * bf2f(wm[(size_t)k * 3072 + col]);
  red[g][t & 63] = a; __syncthreads();
  if (t < 64) mod[col] = bf2f(f2bf(red[0][t] + red[1][t] + red[2][t] + red[3][t]));
}
template <class T> DI float ldf(const T* p, int i);
template <> DI float ldf<u16>(const u16* p, int i) { return bf2f(p[i]); }
template <> DI float ldf<float>(const float* p, int i) { return p[i]; }
template <class T> __global__ void k_ln(const T* x, const float* sh, const float* sc, u16* h) {
  int row = blockIdx.x * 4 + (threadIdx.x >> 6), l = threadIdx.x & 63;
  float v[8]; float s = 0.f;
  for (int i = 0; i < 8; ++i) { v[i] = ldf<T>(x, row * 512 + l * 8 + i); s += v[i]; }
  for (int o = 32; o; o >>= 1) s += __shfl_xor(s, o);
  float m = s * (1.f / 512.f), q = 0.f;
  for (int i = 0; i < 8; ++i) { float d = v[i] - m; q += d * d; }
  for (int o = 32; o; o >>= 1) q += __shfl_xor(q, o);
  float r = rsqrtf(q * (1.f / 512.f) + 1e-5f);
  for (int i = 0; i < 8; ++i) { int cidx = l * 8 + i; h[row * 512 + cidx] = f2bf((v[i] - m) * r * (1.f + sc[cidx]) + sh[cidx]); }
}
__global__ void k_tr(const u16* in, u16* out, int R, int C) {
  __shared__ u16 t[64][65];
  int c0 = blockIdx.x * 64, r0 = blockIdx.y * 64;
  for (int i = threadIdx.x; i < 4096; i += 256) t[i >> 6][i & 63] = in[(size_t)(r0 + (i >> 6)) * C + c0 + (i & 63)];
  __syncthreads();
  for (int i = threadIdx.x; i < 4096; i += 256) out[(size_t)(c0 + (i >> 6)) * R + r0 + (i & 63)] = t[i & 63][i >> 6];
}
template <int EPI> __global__ __launch_bounds__(256) void k_gemm(const u16* X, const u16* Y, void* C, int K, int ldc, float scale, const void* res, const float* g) {
  int w = threadIdx.x >> 6, l = threadIdx.x & 63, r = l & 31, hi = l >> 5;
  int i0 = blockIdx.y * 128 + (w >> 1) * 64, j0 = blockIdx.x * 128 + (w & 1) * 64;
  f32x16 acc[2][2] = {};
  const u16* xp = X + (size_t)(i0 + r) * K + hi * 8; const u16* yp = Y + (size_t)(j0 + r) * K + hi * 8;
  for (int k = 0; k < K; k += 16) {
    bf16x8 a0 = *(const bf16x8*)(xp + k), a1 = *(const bf16x8*)(xp + (size_t)32 * K + k);
    bf16x8 b0 = *(const bf16x8*)(yp + k), b1 = *(const bf16x8*)(yp + (size_t)32 * K + k);
    acc[0][0] = __builtin_amdgcn_mfma_f32_32x32x16_bf16(a0, b0, acc[0][0], 0, 0, 0);
    acc[0][1] = __builtin_amdgcn_mfma_f32_32x32x16_bf16(a0, b1, acc[0][1], 0, 0, 0);
    acc[1][0] = __builtin_amdgcn_mfma_f32_32x32x16_bf16(a1, b0, acc[1][0], 0, 0, 0);
    acc[1][1] = __builtin_amdgcn_mfma_f32_32x32x16_bf16(a1, b1, acc[1][1], 0, 0, 0);
  }
  for (int a = 0; a < 2; ++a) for (int b = 0; b < 2; ++b) for (int q = 0; q < 16; ++q) {
    int i = i0 + a * 32 + crow(q, hi), j = j0 + b * 32 + r; float v = acc[a][b][q]; size_t o = (size_t)i * ldc + j;
    if (EPI == 0) ((u16*)C)[o] = f2bf(v * scale);
    if (EPI == 1) ((u16*)C)[o] = f2bf(silu(v));
    if (EPI == 2) ((float*)C)[o] = bf2f(((const u16*)res)[o]) + g[j] * v;
    if (EPI == 3) ((u16*)C)[o] = f2bf(((const float*)res)[o] + g[j] * v);
  }
}
__global__ __launch_bounds__(256) void k_attn(const u16* Q, const u16* Kk, const u16* VT, u16* ctx) {
  int w = threadIdx.x >> 6, l = threadIdx.x & 63, r = l & 31, hi = l >> 5;
  int h = blockIdx.y, q0 = (gridDim.x - 1 - blockIdx.x) * 128 + w * 32;
  bf16x8 qf[8];
  for (int s = 0; s < 8; ++s) qf[s] = *(const bf16x8*)(Q + (size_t)(q0 + r) * HD + h * D + s * 16 + hi * 8);
  f32x16 o[4] = {}; float m = -1e30f, lsum = 0.f;
  int nt = (q0 + 31) / 32 + 1;
  for (int t = 0; t < nt; ++t) {
    int k0 = t * 32; f32x16 sacc = {};
    for (int s = 0; s < 8; ++s) { bf16x8 kf = *(const bf16x8*)(Kk + (size_t)(k0 + r) * HD + h * D + s * 16 + hi * 8);
      sacc = __builtin_amdgcn_mfma_f32_32x32x16_bf16(kf, qf[s], sacc, 0, 0, 0); }
    float mx = -1e30f;
    for (int i = 0; i < 16; ++i) { int key = k0 + crow(i, hi); if (key > q0 + r) sacc[i] = -INFINITY; mx = fmaxf(mx, sacc[i]); }
    mx = fmaxf(mx, __shfl_xor(mx, 32));
    float mn = fmaxf(m, mx), al = exp2f(m - mn); m = mn; float ps = 0.f;
    for (int i = 0; i < 16; ++i) { sacc[i] = exp2f(sacc[i] - mn); ps += sacc[i]; }
    ps += __shfl_xor(ps, 32); lsum = lsum * al + ps;
    for (int d = 0; d < 4; ++d) for (int i = 0; i < 16; ++i) o[d][i] *= al;
    for (int s = 0; s < 2; ++s) {
      u32x4 pw = {cvtpk(sacc[8 * s], sacc[8 * s + 1]), cvtpk(sacc[8 * s + 2], sacc[8 * s + 3]), cvtpk(sacc[8 * s + 4], sacc[8 * s + 5]), cvtpk(sacc[8 * s + 6], sacc[8 * s + 7])};
      bf16x8 pf = __builtin_bit_cast(bf16x8, pw);
      for (int d = 0; d < 4; ++d) { const u16* vp = VT + (size_t)(h * D + d * 32 + r) * S + k0 + 16 * s + 4 * hi;
        u32x2 lo = *(const u32x2*)vp, hi2 = *(const u32x2*)(vp + 8); u32x4 vw = {lo[0], lo[1], hi2[0], hi2[1]};
        o[d] = __builtin_amdgcn_mfma_f32_32x32x16_bf16(__builtin_bit_cast(bf16x8, vw), pf, o[d], 0, 0, 0); }
    }
  }
  float il = 1.f / lsum;
  for (int d = 0; d < 4; ++d) for (int g = 0; g < 4; ++g) {
    u32x2 wv = {cvtpk(o[d][4 * g] * il, o[d][4 * g + 1] * il), cvtpk(o[d][4 * g + 2] * il, o[d][4 * g + 3] * il)};
    *(u32x2*)(ctx + (size_t)(q0 + r) * HD + h * D + d * 32 + 8 * g + 4 * hi) = wv; }
}
#define EXTRA_EMPTY 0
__global__ void k_empty(int* p, int v) { if (p == nullptr) p[0] = v; }
extern "C" void kernel_launch(void* const* d_in, const int* in_sizes, int n_in, void* d_out, int out_size, void* d_ws, size_t ws_size, hipStream_t st) {
  const u16* x = (const u16*)d_in[0]; const u16* ckv = (const u16*)d_in[1]; const u16* wq = (const u16*)d_in[2]; const u16* wuk = (const u16*)d_in[3];
  const u16* wuv = (const u16*)d_in[4]; const u16* wo = (const u16*)d_in[5]; const u16* w1 = (const u16*)d_in[7]; const u16* w2 = (const u16*)d_in[8];
  const u16* c = (const u16*)d_in[9]; const u16* wm = (const u16*)d_in[10];
  char* ws = (char*)d_ws; size_t o = 0; auto al = [&](size_t n) { void* p = ws + o; o += (n + 255) & ~(size_t)255; return p; };
  float* mod = (float*)al(3072 * 4); u16* h = (u16*)al(S * M * 2);
  u16* wqT = (u16*)al(M * HD * 2); u16* wukT = (u16*)al(M * HD * 2); u16* wuvT = (u16*)al(M * HD * 2); u16* woT = (u16*)al(M * HD * 2);
  u16* w1T = (u16*)al(M * F * 2); u16* w2T = (u16*)al(M * F * 2);
  u16* Q = (u16*)al((size_t)S * HD * 2); u16* K = (u16*)al((size_t)S * HD * 2); u16* VT = (u16*)al((size_t)S * HD * 2); u16* ctx = (u16*)al((size_t)S * HD * 2);
  float* x1 = (float*)al(S * M * 4); u16* h2 = (u16*)al(S * M * 2); u16* hid = (u16*)al(S * F * 2);
  k_mod<<<48, 256, 0, st>>>(c, wm, mod);
  k_ln<u16><<<S / 4, 256, 0, st>>>(x, mod, mod + 512, h);
  k_tr<<<dim3(HD / 64, M / 64), 256, 0, st>>>(wq, wqT, M, HD);
  k_tr<<<dim3(HD / 64, M / 64), 256, 0, st>>>(wuk, wukT, M, HD);
  k_tr<<<dim3(HD / 64, M / 64), 256, 0, st>>>(wuv, wuvT, M, HD);
  k_tr<<<dim3(M / 64, HD / 64), 256, 0, st>>>(wo, woT, HD, M);
  k_tr<<<dim3(F / 64, M / 64), 256, 0, st>>>(w1, w1T, M, F);
  k_tr<<<dim3(M / 64, F / 64), 256, 0, st>>>(w2, w2T, F, M);
  const float qs = 0.08838834764831845f * 1.4426950408889634f;
  k_gemm<0><<<dim3(HD / 128, S / 128), 256, 0, st>>>(h, wqT, Q, M, HD, qs, nullptr, nullptr);
  k_gemm<0><<<dim3(HD / 128, S / 128), 256, 0, st>>>(ckv, wukT, K, M, HD, 1.f, nullptr, nullptr);
  k_gemm<0><<<dim3(S / 128, HD / 128), 256, 0, st>>>(wuvT, ckv, VT, M, S, 1.f, nullptr, nullptr);
  k_attn<<<dim3(S / 128, H), 256, 0, st>>>(Q, K, VT, ctx);
  k_gemm<2><<<dim3(M / 128, S / 128), 256, 0, st>>>(ctx, woT, x1, HD, M, 1.f, x, mod + 1024);
  k_ln<float><<<S / 4, 256, 0, st>>>(x1, mod + 1536, mod + 2048, h2);
  k_gemm<1><<<dim3(F / 128, S / 128), 256, 0, st>>>(h2, w1T, hid, M, F, 1.f, nullptr, nullptr);
  k_gemm<3><<<dim3(M / 128, S / 128), 256, 0, st>>>(hid, w2T, d_out, F, M, 1.f, x1, mod + 2560);
  for (int i = 0; i < EXTRA_EMPTY; ++i) k_empty<<<256, 256, 0, st>>>((int*)d_ws, i);
}
```

```cpp
#include <hip/hip_runtime.h>
#include <hip/hip_bf16.h>
#include <stdint.h>
typedef unsigned short u16;
typedef float f32x4 __attribute__((ext_vector_type(4)));
typedef float f32x2 __attribute__((ext_vector_type(2)));
typedef __bf16 bf2 __attribute__((ext_vector_type(2)));
typedef unsigned u32x4 __attribute__((ext_vector_type(4)));
typedef unsigned u32x2 __attribute__((ext_vector_type(2)));
#define DI __device__ __forceinline__
DI float bf2f(u16 v) { return __uint_as_float((unsigned)v << 16); }
DI unsigned cvtpk(float lo, float hi) { f32x2 v = {lo, hi}; bf2 b = __builtin_convertvector(v, bf2); return __builtin_bit_cast(unsigned, b); }
DI u16 f2bf(float f) { return (u16)(cvtpk(f, 0.f) & 0xffff); }
DI float silu(float x) { return x / (1.f + __expf(-x)); }
constexpr size_t WS_FLAGS = 0;
constexpr size_t WS_W0 = 2048;
constexpr size_t WS_MOD = 4096;
constexpr size_t WS_HFF = 16384;
constexpr size_t WS_KVF = 20480;
constexpr size_t WS_CFF = 28672;
constexpr size_t WS_HF = 1048576;
constexpr size_t WS_KF = 4194304;
constexpr size_t WS_VF = 22020096;
constexpr size_t WS_CTX = 39845888;
constexpr size_t WS_PART = 57671680;
constexpr size_t WS_X1 = 75497472;
constexpr size_t WS_H2 = 80740352;
constexpr size_t WS_HID = 83886080;
__global__ __launch_bounds__(256, 1) void k12_attn(const u16* ckv, const u16* wuk, const u16* wuv, const u16* x, const u16* c, const u16* wm, char* ws, const u16* wq, const u16* wo, const u16* w1, const u16* w2) {
  unsigned wave = __builtin_amdgcn_readfirstlane(threadIdx.x >> 6); unsigned blk = blockIdx.x;
  auto ka = __builtin_amdgcn_kernarg_segment_ptr();
  asm volatile(
"s_load_dwordx16 s[8:23], %0, 0x0\n"
"s_mov_b32 s48, %1\n"
"s_mov_b32 s49, %2\n"
"s_lshr_b32 s51, s49, 5\n"
"s_and_b32 s56, s49, 7\n"
"s_lshl_b32 s56, s56, 2\n"
"s_bfe_u32 s57, s49, 0x20003\n"
"s_add_u32 s50, s56, s57\n"
"s_mov_b32 s76, s50\n"
"s_mov_b32 s77, 0\n"
"v_mbcnt_lo_u32_b32 v240, -1, 0\n"
"v_mbcnt_hi_u32_b32 v240, -1, v240\n"
"v_lshrrev_b32 v241, 4, v240\n"
"v_and_b32 v242, 7, v240\n"
"v_xor_b32 v242, v242, v241\n"
"v_lshlrev_b32 v242, 4, v242\n"
"v_lshrrev_b32 v243, 3, v240\n"
"s_lshl_b32 s56, s48, 6\n"
"v_add_u32 v243, s56, v243\n"
"v_lshlrev_b32 v243, 10, v243\n"
"v_add_u32 v233, v242, v243\n"
"v_xor_b32 v242, 64, v233\n"
"v_add_u32 v234, 7168, v242\n"
"v_add_u32 v235, 14336, v233\n"
"v_add_u32 v236, 21504, v242\n"
"s_and_b32 s56, s48, 1\n"
"s_lshl_b32 s56, s56, 2\n"
"v_or_b32 v242, s56, v241\n"
"v_lshlrev_b32 v242, 1, v242\n"
"v_and_b32 v243, 15, v240\n"
"v_xor_b32 v242, v242, v243\n"
"v_lshlrev_b32 v242, 4, v242\n"
"s_lshl_b32 s56, s48, 3\n"
"v_add_u32 v243, s56, v241\n"
"v_lshlrev_b32 v243, 13, v243\n"
"v_add_u32 v237, v242, v243\n"
"v_add_u32 v238, 31744, v237\n"
"s_lshl_b32 s52, s48, 13\n"
"s_add_u32 s52, s52, 0x10000\n"
"s_lshl_b32 s53, s48, 11\n"
"s_waitcnt lgkmcnt(0)\n"
"s_load_dwordx2 s[96:97], s[20:21], 2048\n"
"s_and_b32 s56, s49, 7\n"
"s_mul_i32 s56, s56, 24\n"
"s_lshr_b32 s57, s49, 3\n"
"s_add_u32 s79, s56, s57\n"
"s_mov_b64 s[36:37], s[18:19]\n"
"s_and_b32 s37, s37, 0xffff\n"
"s_mov_b32 s38, 0x300000\n"
"s_mov_b32 s39, 0x20000\n"
"s_mov_b64 s[64:65], s[16:17]\n"
"s_and_b32 s65, s65, 0xffff\n"
"s_mov_b32 s66, 0x400\n"
"s_mov_b32 s67, 0x20000\n"
"s_lshl_b32 s56, s48, 5\n"
"v_lshrrev_b32 v0, 1, v240\n"
"v_add_u32 v0, s56, v0\n"
"v_lshlrev_b32 v2, 1, v0\n"
"v_mul_u32_u24 v0, 6144, v0\n"
"v_and_b32 v1, 1, v240\n"
"v_lshlrev_b32 v1, 4, v1\n"
"v_add_u32 v0, v0, v1\n"
"s_lshl_b32 s58, s79, 5\n"
"buffer_load_ushort v32, v2, s[64:67], 0 offen offset:0\n"
"buffer_load_ushort v33, v2, s[64:67], 0 offen offset:256\n"
"buffer_load_ushort v34, v2, s[64:67], 0 offen offset:512\n"
"buffer_load_ushort v35, v2, s[64:67], 0 offen offset:768\n"
"buffer_load_dwordx4 v[16:19], v0, s[36:39], s58 offen\n"
"s_add_u32 s58, s58, 786432\n"
"buffer_load_dwordx4 v[20:23], v0, s[36:39], s58 offen\n"
"s_add_u32 s58, s58, 786432\n"
"buffer_load_dwordx4 v[24:27], v0, s[36:39], s58 offen\n"
"s_add_u32 s58, s58, 786432\n"
"buffer_load_dwordx4 v[28:31], v0, s[36:39], s58 offen\n"
"s_lshl_b32 s56, s51, 18\n"
"s_mov_b64 s[24:25], s[8:9]\n"
"s_add_u32 s24, s24, s56\n"
"s_addc_u32 s25, s25, 0\n"
"s_and_b32 s25, s25, 0xffff\n"
"s_mov_b32 s26, 0x40000\n"
"s_mov_b32 s27, 0x20000\n"
"s_lshl_b32 s56, s50, 8\n"
"s_mov_b64 s[28:29], s[10:11]\n"
"s_add_u32 s28, s28, s56\n"
"s_addc_u32 s29, s29, 0\n"
"s_and_b32 s29, s29, 0xffff\n"
"s_mov_b32 s30, 0x3fe100\n"
"s_mov_b32 s31, 0x20000\n"
"s_lshl_b32 s56, s76, 8\n"
"s_mov_b64 s[32:33], s[12:13]\n"
"s_add_u32 s32, s32, s56\n"
"s_addc_u32 s33, s33, 0\n"
"s_and_b32 s33, s33, 0xffff\n"
"s_mov_b32 s34, 0x3fe100\n"
"s_mov_b32 s35, 0x20000\n"
"s_add_u32 m0, s52, 0\n"
"s_mov_b32 s54, 0\n"
"s_nop 0\n"
"buffer_load_dwordx4 v233, s[24:27], s54 offen offset:0 lds\n"
"buffer_load_dwordx4 v234, s[24:27], s54 offen offset:1024 lds\n"
"buffer_load_dwordx4 v235, s[24:27], s54 offen offset:2048 lds\n"
"buffer_load_dwordx4 v236, s[24:27], s54 offen offset:3072 lds\n"
"s_add_u32 m0, s52, 4096\n"
"s_mov_b32 s54, 32768\n"
"s_nop 0\n"
"buffer_load_dwordx4 v233, s[24:27], s54 offen offset:0 lds\n"
"buffer_load_dwordx4 v234, s[24:27], s54 offen offset:1024 lds\n"
"buffer_load_dwordx4 v235, s[24:27], s54 offen offset:2048 lds\n"
"buffer_load_dwordx4 v236, s[24:27], s54 offen offset:3072 lds\n"
"s_add_u32 m0, s53, 0\n"
"s_mov_b32 s55, 0\n"
"s_nop 0\n"
"buffer_load_dwordx4 v237, s[28:31], s55 offen offset:0 lds\n"
"buffer_load_dwordx4 v238, s[28:31], s55 offen offset:1024 lds\n"
"s_add_u32 m0, s53, 8192\n"
"s_nop 0\n"
"buffer_load_dwordx4 v237, s[32:35], s55 offen offset:0 lds\n"
"buffer_load_dwordx4 v238, s[32:35], s55 offen offset:1024 lds\n"
"s_getpc_b64 s[68:69]\n"
"L_k1pc_%=:\n"
"s_and_b32 s69, s69, 0xffff\n"
"s_mov_b32 s70, adaln_text_end-L_k1pc_%=\n"
"s_mov_b32 s71, 0x20000\n"
"s_lshr_b32 s56, s49, 3\n"
"s_lshl_b32 s56, s56, 2\n"
"s_add_u32 s56, s56, s48\n"
"s_lshl_b32 s56, s56, 12\n"
"v_lshlrev_b32 v244, 6, v240\n"
"buffer_load_dword a254, v244, s[68:71], s56 offen\n"
"s_add_u32 m0, s53, 16384\n"
"s_mov_b32 s55, 262144\n"
"s_nop 0\n"
"buffer_load_dwordx4 v237, s[28:31], s55 offen offset:0 lds\n"
"buffer_load_dwordx4 v238, s[28:31], s55 offen offset:1024 lds\n"
"s_add_u32 m0, s53, 24576\n"
"s_nop 0\n"
"buffer_load_dwordx4 v237, s[32:35], s55 offen offset:0 lds\n"
"buffer_load_dwordx4 v238, s[32:35], s55 offen offset:1024 lds\n"
"s_mov_b32 s59, 0x3e0293ee\n"
"s_mov_b64 s[40:41], s[14:15]\n"
"s_and_b32 s41, s41, 0xffff\n"
"s_mov_b32 s42, 0x200000\n"
"s_mov_b32 s43, 0x20000\n"
"s_mov_b64 s[44:45], s[20:21]\n"
"s_and_b32 s45, s45, 0xffff\n"
"s_mov_b32 s46, 0x8000000\n"
"s_mov_b32 s47, 0x20000\n"
"s_lshl_b32 s60, s49, 3\n"
"s_lshl_b32 s56, s48, 1\n"
"s_add_u32 s60, s60, s56\n"
"s_lshl_b32 s60, s60, 10\n"
"s_lshr_b32 s56, s60, 10\n"
"s_add_u32 s56, s56, 0\n"
"s_lshr_b32 s57, s56, 5\n"
"s_lshl_b32 s57, s57, 15\n"
"s_and_b32 s56, s56, 31\n"
"s_lshl_b32 s56, s56, 4\n"
"s_add_u32 s56, s56, s57\n"
"s_add_u32 s74, s56, 1048576\n"
"s_lshr_b32 s56, s60, 10\n"
"s_add_u32 s56, s56, 1\n"
"s_lshr_b32 s57, s56, 5\n"
"s_lshl_b32 s57, s57, 15\n"
"s_and_b32 s56, s56, 31\n"
"s_lshl_b32 s56, s56, 4\n"
"s_add_u32 s56, s56, s57\n"
"s_add_u32 s75, s56, 1048576\n"
"v_lshlrev_b32 v239, 5, v240\n"
"v_lshlrev_b32 v255, 4, v240\n"
"v_and_b32 v241, 15, v240\n"
"v_lshrrev_b32 v242, 4, v240\n"
"v_lshrrev_b32 v243, 2, v241\n"
"v_lshrrev_b32 v245, 1, v241\n"
"v_xor_b32 v245, v245, v242\n"
"v_lshlrev_b32 v245, 4, v245\n"
"s_and_b32 s56, s48, 1\n"
"s_lshl_b32 s56, s56, 13\n"
"s_add_u32 s56, s56, 0x10000\n"
"v_lshlrev_b32 v244, 7, v241\n"
"v_add_u32 v244, v244, v245\n"
"v_add_u32 v232, s56, v244\n"
"v_and_b32 v246, 3, v240\n"
"v_and_b32 v244, 1, v242\n"
"v_lshlrev_b32 v244, 2, v244\n"
"v_or_b32 v244, v244, v243\n"
"v_lshlrev_b32 v244, 5, v244\n"
"v_lshrrev_b32 v245, 1, v246\n"
"v_lshlrev_b32 v245, 4, v245\n"
"v_or_b32 v244, v244, v245\n"
"v_and_b32 v245, 1, v246\n"
"v_lshlrev_b32 v245, 3, v245\n"
"v_or_b32 v244, v244, v245\n"
"v_lshlrev_b32 v245, 3, v242\n"
"v_add_u32 v245, v245, v243\n"
"v_lshlrev_b32 v245, 8, v245\n"
"v_add_u32 v244, v244, v245\n"
"s_lshr_b32 s56, s48, 1\n"
"s_lshl_b32 s56, s56, 13\n"
"v_add_u32 v224, s56, v244\n"
"v_xor_b32 v225, 32, v224\n"
"v_xor_b32 v226, 64, v224\n"
"v_xor_b32 v227, 96, v224\n"
"v_xor_b32 v228, 128, v224\n"
"v_xor_b32 v229, 160, v224\n"
"v_xor_b32 v230, 192, v224\n"
"v_xor_b32 v231, 224, v224\n"
"s_waitcnt vmcnt(17)\n"
"v_lshlrev_b32 v32, 16, v32\n"
"v_lshlrev_b32 v33, 16, v33\n"
"v_lshlrev_b32 v34, 16, v34\n"
"v_lshlrev_b32 v35, 16, v35\n"
"v_mul_f32 v36, 0xbfb8aa3b, v32\n"
"v_mul_f32 v37, 0xbfb8aa3b, v33\n"
"v_mul_f32 v38, 0xbfb8aa3b, v34\n"
"v_mul_f32 v39, 0xbfb8aa3b, v35\n"
"v_exp_f32 v36, v36\n"
"v_exp_f32 v37, v37\n"
"v_exp_f32 v38, v38\n"
"v_exp_f32 v39, v39\n"
"s_mov_b32 s56, 0x05040100\n"
"s_mov_b32 s57, 0x07060302\n"
"v_add_f32 v36, 1.0, v36\n"
"v_add_f32 v37, 1.0, v37\n"
"v_add_f32 v38, 1.0, v38\n"
"v_add_f32 v39, 1.0, v39\n"
"v_rcp_f32 v36, v36\n"
"v_rcp_f32 v37, v37\n"
"v_rcp_f32 v38, v38\n"
"v_rcp_f32 v39, v39\n"
"s_nop 0\n"
"v_mul_f32 v36, v32, v36\n"
"v_mul_f32 v37, v33, v37\n"
"v_mul_f32 v38, v34, v38\n"
"v_mul_f32 v39, v35, v39\n"
"v_cvt_pk_bf16_f32 v32, v36, v37\n"
"v_cvt_pk_bf16_f32 v33, v38, v39\n"
"v_perm_b32 v48, v20, v16, s56\n"
"v_perm_b32 v49, v20, v16, s57\n"
"v_perm_b32 v50, v21, v17, s56\n"
"v_perm_b32 v51, v21, v17, s57\n"
"v_perm_b32 v52, v22, v18, s56\n"
"v_perm_b32 v53, v22, v18, s57\n"
"v_perm_b32 v54, v23, v19, s56\n"
"v_perm_b32 v55, v23, v19, s57\n"
"v_dot2_f32_bf16 v40, v48, v32, 0\n"
"v_dot2_f32_bf16 v41, v49, v32, 0\n"
"v_dot2_f32_bf16 v42, v50, v32, 0\n"
"v_dot2_f32_bf16 v43, v51, v32, 0\n"
"v_dot2_f32_bf16 v44, v52, v32, 0\n"
"v_dot2_f32_bf16 v45, v53, v32, 0\n"
"v_dot2_f32_bf16 v46, v54, v32, 0\n"
"v_dot2_f32_bf16 v47, v55, v32, 0\n"
"v_perm_b32 v48, v28, v24, s56\n"
"v_perm_b32 v49, v28, v24, s57\n"
"v_perm_b32 v50, v29, v25, s56\n"
"v_perm_b32 v51, v29, v25, s57\n"
"v_perm_b32 v52, v30, v26, s56\n"
"v_perm_b32 v53, v30, v26, s57\n"
"v_perm_b32 v54, v31, v27, s56\n"
"v_perm_b32 v55, v31, v27, s57\n"
"v_dot2_f32_bf16 v40, v48, v33, v40\n"
"v_dot2_f32_bf16 v41, v49, v33, v41\n"
"v_dot2_f32_bf16 v42, v50, v33, v42\n"
"v_dot2_f32_bf16 v43, v51, v33, v43\n"
"v_dot2_f32_bf16 v44, v52, v33, v44\n"
"v_dot2_f32_bf16 v45, v53, v33, v45\n"
"v_dot2_f32_bf16 v46, v54, v33, v46\n"
"v_dot2_f32_bf16 v47, v55, v33, v47\n"
"s_nop 1\n"
"v_permlane32_swap_b32 v40, v44\n"
"v_permlane32_swap_b32 v41, v45\n"
"v_permlane32_swap_b32 v42, v46\n"
"v_permlane32_swap_b32 v43, v47\n"
"v_add_f32 v40, v40, v44\n"
"v_add_f32 v41, v41, v45\n"
"v_add_f32 v42, v42, v46\n"
"v_add_f32 v43, v43, v47\n"
"s_nop 1\n"
"v_permlane16_swap_b32 v40, v42\n"
"v_permlane16_swap_b32 v41, v43\n"
"v_add_f32 v40, v40, v42\n"
"v_add_f32 v41, v41, v43\n"
"v_and_b32 v2, 1, v240\n"
"v_lshlrev_b32 v2, 5, v2\n"
"v_bfe_u32 v3, v240, 4, 1\n"
"v_lshlrev_b32 v3, 3, v3\n"
"v_add_u32 v2, v2, v3\n"
"v_lshrrev_b32 v3, 5, v240\n"
"v_lshlrev_b32 v3, 4, v3\n"
"v_add_u32 v2, v2, v3\n"
"s_lshl_b32 s56, s48, 6\n"
"s_add_u32 s56, s56, 131072\n"
"v_add_u32 v2, s56, v2\n"
"v_add_f32_dpp v40, v40, v40 row_ror:2 row_mask:0xf bank_mask:0xf\n"
"v_add_f32_dpp v41, v41, v41 row_ror:2 row_mask:0xf bank_mask:0xf\n"
"s_nop 0\n"
"v_add_f32_dpp v40, v40, v40 row_ror:4 row_mask:0xf bank_mask:0xf\n"
"v_add_f32_dpp v41, v41, v41 row_ror:4 row_mask:0xf bank_mask:0xf\n"
"s_nop 0\n"
"v_add_f32_dpp v40, v40, v40 row_ror:8 row_mask:0xf bank_mask:0xf\n"
"v_add_f32_dpp v41, v41, v41 row_ror:8 row_mask:0xf bank_mask:0xf\n"
"s_nop 0\n"
"ds_write2_b32 v2, v40, v41 offset0:0 offset1:1\n"
"s_waitcnt lgkmcnt(0)\n"
"s_add_u32 s96, s96, 1\n"
"s_addc_u32 s97, s97, 0\n"
"s_cmp_ge_u32 s48, 2\n"
"s_cbranch_scc1 L_roleV_%=\n"
"s_lshl_b32 s56, s50, 19\n"
"s_mov_b64 s[36:37], s[20:21]\n"
"s_add_u32 s36, s36, s56\n"
"s_addc_u32 s37, s37, 0\n"
"s_add_u32 s36, s36, 4194304\n"
"s_addc_u32 s37, s37, 0\n"
"s_and_b32 s37, s37, 0xffff\n"
"s_mov_b32 s38, 0x80000\n"
"s_mov_b32 s39, 0x20000\n"
"s_lshl_b32 s57, s51, 3\n"
"s_lshl_b32 s58, s48, 1\n"
"s_add_u32 s57, s57, s58\n"
"s_lshl_b32 s57, s57, 13\n"
"s_add_u32 s64, s57, 0\n"
"s_add_u32 s65, s57, 4096\n"
"s_add_u32 s66, s57, 8192\n"
"s_add_u32 s67, s57, 12288\n"
"s_add_u32 s68, s57, 32768\n"
"s_add_u32 s69, s57, 36864\n"
"s_add_u32 s70, s57, 40960\n"
"s_add_u32 s71, s57, 45056\n"
"s_waitcnt vmcnt(5)\n"
"s_barrier\n"
"ds_read_b128 a[32:35], v232 offset:0\n"
"ds_read_b64_tr_b16 a[0:1], v224 offset:0\n"
"ds_read_b64_tr_b16 a[2:3], v224 offset:1024\n"
"ds_read_b128 a[36:39], v232 offset:2048\n"
"ds_read_b64_tr_b16 a[4:5], v225 offset:0\n"
"ds_read_b64_tr_b16 a[6:7], v225 offset:1024\n"
"ds_read_b128 a[40:43], v232 offset:4096\n"
"ds_read_b64_tr_b16 a[8:9], v226 offset:0\n"
"ds_read_b64_tr_b16 a[10:11], v226 offset:1024\n"
"ds_read_b128 a[44:47], v232 offset:6144\n"
"ds_read_b64_tr_b16 a[12:13], v227 offset:0\n"
"ds_read_b64_tr_b16 a[14:15], v227 offset:1024\n"
"ds_read_b128 a[48:51], v232 offset:16384\n"
"ds_read_b64_tr_b16 a[16:17], v228 offset:0\n"
"ds_read_b64_tr_b16 a[18:19], v228 offset:1024\n"
"ds_read_b128 a[52:55], v232 offset:18432\n"
"ds_read_b64_tr_b16 a[20:21], v229 offset:0\n"
"ds_read_b64_tr_b16 a[22:23], v229 offset:1024\n"
"ds_read_b128 a[56:59], v232 offset:20480\n"
"ds_read_b64_tr_b16 a[24:25], v230 offset:0\n"
"ds_read_b64_tr_b16 a[26:27], v230 offset:1024\n"
"ds_read_b128 a[60:63], v232 offset:22528\n"
"ds_read_b64_tr_b16 a[28:29], v231 offset:0\n"
"ds_read_b64_tr_b16 a[30:31], v231 offset:1024\n"
"s_cmp_lg_u32 s48, 0\n"
"s_cbranch_scc1 L_nomod1_%=\n"
"s_cmp_ge_u32 s49, 192\n"
"s_cbranch_scc1 L_nomod1_%=\n"
"v_lshrrev_b32 v3, 5, v239\n"
"v_lshlrev_b32 v3, 2, v3\n"
"v_add_u32 v2, 131072, v3\n"
"ds_read2_b32 v[4:5], v2 offset0:0 offset1:16\n"
"ds_read2_b32 v[6:7], v2 offset0:32 offset1:48\n"
"s_lshl_b32 s58, s79, 6\n"
"s_add_u32 s58, s58, 4096\n"
"s_waitcnt lgkmcnt(0)\n"
"v_add_f32 v4, v4, v5\n"
"v_add_f32 v6, v6, v7\n"
"v_add_f32 v4, v4, v6\n"
"v_cvt_pk_bf16_f32 v4, v4, v4\n"
"v_lshlrev_b32 v4, 16, v4\n"
"s_mov_b64 exec, 0xffff\n"
"buffer_store_dword v4, v3, s[44:47], s58 offen sc1\n"
"s_mov_b64 exec, -1\n"
"L_nomod1_%=:\n"
"s_add_u32 m0, s52, 32768\n"
"s_mov_b32 s54, 128\n"
"s_nop 0\n"
"buffer_load_dwordx4 v233, s[24:27], s54 offen offset:0 lds\n"
"buffer_load_dwordx4 v234, s[24:27], s54 offen offset:1024 lds\n"
"buffer_load_dwordx4 v235, s[24:27], s54 offen offset:2048 lds\n"
"buffer_load_dwordx4 v236, s[24:27], s54 offen offset:3072 lds\n"
"s_add_u32 m0, s52, 36864\n"
"s_mov_b32 s54, 32896\n"
"s_nop 0\n"
"buffer_load_dwordx4 v233, s[24:27], s54 offen offset:0 lds\n"
"buffer_load_dwordx4 v234, s[24:27], s54 offen offset:1024 lds\n"
"buffer_load_dwordx4 v235, s[24:27], s54 offen offset:2048 lds\n"
"buffer_load_dwordx4 v236, s[24:27], s54 offen offset:3072 lds\n"
"s_add_u32 m0, s53, 32768\n"
"s_mov_b32 s55, 524288\n"
"s_nop 0\n"
"buffer_load_dwordx4 v237, s[28:31], s55 offen offset:0 lds\n"
"buffer_load_dwordx4 v238, s[28:31], s55 offen offset:1024 lds\n"
"s_add_u32 m0, s53, 40960\n"
"s_nop 0\n"
"buffer_load_dwordx4 v237, s[32:35], s55 offen offset:0 lds\n"
"buffer_load_dwordx4 v238, s[32:35], s55 offen offset:1024 lds\n"
"s_waitcnt vmcnt(12) lgkmcnt(0)\n"
"s_barrier\n"
"v_xor_b32 v232, 64, v232\n"
"v_mfma_f32_16x16x32_bf16 v[0:3], a[0:3], a[32:35], 0\n"
"ds_read_b128 a[96:99], v232 offset:0\n"
"s_add_u32 m0, s53, 49152\n"
"s_mov_b32 s55, 786432\n"
"v_mfma_f32_16x16x32_bf16 v[4:7], a[0:3], a[36:39], 0\n"
"buffer_load_dwordx4 v237, s[28:31], s55 offen offset:0 lds\n"
"v_mfma_f32_16x16x32_bf16 v[8:11], a[0:3], a[40:43], 0\n"
"ds_read_b64_tr_b16 a[64:65], v224 offset:16384\n"
"v_mfma_f32_16x16x32_bf16 v[12:15], a[0:3], a[44:47], 0\n"
"v_mfma_f32_16x16x32_bf16 v[16:19], a[0:3], a[48:51], 0\n"
"ds_read_b64_tr_b16 a[66:67], v224 offset:17408\n"
"v_mfma_f32_16x16x32_bf16 v[20:23], a[0:3], a[52:55], 0\n"
"v_mfma_f32_16x16x32_bf16 v[24:27], a[0:3], a[56:59], 0\n"
"ds_read_b128 a[100:103], v232 offset:2048\n"
"v_mfma_f32_16x16x32_bf16 v[28:31], a[0:3], a[60:63], 0\n"
"v_mfma_f32_16x16x32_bf16 v[32:35], a[4:7], a[32:35], 0\n"
"ds_read_b64_tr_b16 a[68:69], v225 offset:16384\n"
"v_mfma_f32_16x16x32_bf16 v[36:39], a[4:7], a[36:39], 0\n"
"buffer_load_dwordx4 v238, s[28:31], s55 offen offset:1024 lds\n"
"v_mfma_f32_16x16x32_bf16 v[40:43], a[4:7], a[40:43], 0\n"
"ds_read_b64_tr_b16 a[70:71], v225 offset:17408\n"
"v_mfma_f32_16x16x32_bf16 v[44:47], a[4:7], a[44:47], 0\n"
"v_mfma_f32_16x16x32_bf16 v[48:51], a[4:7], a[48:51], 0\n"
"ds_read_b128 a[104:107], v232 offset:4096\n"
"v_mfma_f32_16x16x32_bf16 v[52:55], a[4:7], a[52:55], 0\n"
"s_add_u32 m0, s53, 57344\n"
"v_mfma_f32_16x16x32_bf16 v[56:59], a[4:7], a[56:59], 0\n"
"ds_read_b64_tr_b16 a[72:73], v226 offset:16384\n"
"v_mfma_f32_16x16x32_bf16 v[60:63], a[4:7], a[60:63], 0\n"
"buffer_load_dwordx4 v237, s[32:35], s55 offen offset:0 lds\n"
"v_mfma_f32_16x16x32_bf16 v[64:67], a[8:11], a[32:35], 0\n"
"ds_read_b64_tr_b16 a[74:75], v226 offset:17408\n"
"v_mfma_f32_16x16x32_bf16 v[68:71], a[8:11], a[36:39], 0\n"
"v_mfma_f32_16x16x32_bf16 v[72:75], a[8:11], a[40:43], 0\n"
"ds_read_b128 a[108:111], v232 offset:6144\n"
"v_mfma_f32_16x16x32_bf16 v[76:79], a[8:11], a[44:47], 0\n"
"v_mfma_f32_16x16x32_bf16 v[80:83], a[8:11], a[48:51], 0\n"
"ds_read_b64_tr_b16 a[76:77], v227 offset:16384\n"
"v_mfma_f32_16x16x32_bf16 v[84:87], a[8:11], a[52:55], 0\n"
"v_mfma_f32_16x16x32_bf16 v[88:91], a[8:11], a[56:59], 0\n"
"ds_read_b64_tr_b16 a[78:79], v227 offset:17408\n"
"v_mfma_f32_16x16x32_bf16 v[92:95], a[8:11], a[60:63], 0\n"
"buffer_load_dwordx4 v238, s[32:35], s55 offen offset:1024 lds\n"
"v_mfma_f32_16x16x32_bf16 v[96:99], a[12:15], a[32:35], 0\n"
"ds_read_b128 a[112:115], v232 offset:16384\n"
"v_mfma_f32_16x16x32_bf16 v[100:103], a[12:15], a[36:39], 0\n"
"v_mfma_f32_16x16x32_bf16 v[104:107], a[12:15], a[40:43], 0\n"
"ds_read_b64_tr_b16 a[80:81], v228 offset:16384\n"
"v_mfma_f32_16x16x32_bf16 v[108:111], a[12:15], a[44:47], 0\n"
"s_add_u32 m0, s53, 0\n"
"s_mov_b32 s55, 1048576\n"
"v_mfma_f32_16x16x32_bf16 v[112:115], a[12:15], a[48:51], 0\n"
"ds_read_b64_tr_b16 a[82:83], v228 offset:17408\n"
"v_mfma_f32_16x16x32_bf16 v[116:119], a[12:15], a[52:55], 0\n"
"buffer_load_dwordx4 v237, s[28:31], s55 offen offset:0 lds\n"
"v_mfma_f32_16x16x32_bf16 v[120:123], a[12:15], a[56:59], 0\n"
"ds_read_b128 a[116:119], v232 offset:18432\n"
"v_mfma_f32_16x16x32_bf16 v[124:127], a[12:15], a[60:63], 0\n"
"v_mfma_f32_16x16x32_bf16 v[128:131], a[16:19], a[32:35], 0\n"
"ds_read_b64_tr_b16 a[84:85], v229 offset:16384\n"
"v_mfma_f32_16x16x32_bf16 v[132:135], a[16:19], a[36:39], 0\n"
"v_mfma_f32_16x16x32_bf16 v[136:139], a[16:19], a[40:43], 0\n"
"ds_read_b64_tr_b16 a[86:87], v229 offset:17408\n"
"v_mfma_f32_16x16x32_bf16 v[140:143], a[16:19], a[44:47], 0\n"
"v_mfma_f32_16x16x32_bf16 v[144:147], a[16:19], a[48:51], 0\n"
"ds_read_b128 a[120:123], v232 offset:20480\n"
"v_mfma_f32_16x16x32_bf16 v[148:151], a[16:19], a[52:55], 0\n"
"buffer_load_dwordx4 v238, s[28:31], s55 offen offset:1024 lds\n"
"v_mfma_f32_16x16x32_bf16 v[152:155], a[16:19], a[56:59], 0\n"
"ds_read_b64_tr_b16 a[88:89], v230 offset:16384\n"
"v_mfma_f32_16x16x32_bf16 v[156:159], a[16:19], a[60:63], 0\n"
"v_mfma_f32_16x16x32_bf16 v[160:163], a[20:23], a[32:35], 0\n"
"ds_read_b64_tr_b16 a[90:91], v230 offset:17408\n"
"v_mfma_f32_16x16x32_bf16 v[164:167], a[20:23], a[36:39], 0\n"
"s_add_u32 m0, s53, 8192\n"
"v_mfma_f32_16x16x32_bf16 v[168:171], a[20:23], a[40:43], 0\n"
"ds_read_b128 a[124:127], v232 offset:22528\n"
"v_mfma_f32_16x16x32_bf16 v[172:175], a[20:23], a[44:47], 0\n"
"buffer_load_dwordx4 v237, s[32:35], s55 offen offset:0 lds\n"
"v_mfma_f32_16x16x32_bf16 v[176:179], a[20:23], a[48:51], 0\n"
"ds_read_b64_tr_b16 a[92:93], v231 offset:16384\n"
"v_mfma_f32_16x16x32_bf16 v[180:183], a[20:23], a[52:55], 0\n"
"v_mfma_f32_16x16x32_bf16 v[184:187], a[20:23], a[56:59], 0\n"
"ds_read_b64_tr_b16 a[94:95], v231 offset:17408\n"
"v_mfma_f32_16x16x32_bf16 v[188:191], a[20:23], a[60:63], 0\n"
"v_mfma_f32_16x16x32_bf16 v[192:195], a[24:27], a[32:35], 0\n"
"v_mfma_f32_16x16x32_bf16 v[196:199], a[24:27], a[36:39], 0\n"
"v_mfma_f32_16x16x32_bf16 v[200:203], a[24:27], a[40:43], 0\n"
"v_mfma_f32_16x16x32_bf16 v[204:207], a[24:27], a[44:47], 0\n"
"buffer_load_dwordx4 v238, s[32:35], s55 offen offset:1024 lds\n"
"v_mfma_f32_16x16x32_bf16 v[208:211], a[24:27], a[48:51], 0\n"
"v_mfma_f32_16x16x32_bf16 v[212:215], a[24:27], a[52:55], 0\n"
"v_mfma_f32_16x16x32_bf16 v[216:219], a[24:27], a[56:59], 0\n"
"v_mfma_f32_16x16x32_bf16 v[220:223], a[24:27], a[60:63], 0\n"
"v_mfma_f32_16x16x32_bf16 a[128:131], a[28:31], a[32:35], 0\n"
"v_mfma_f32_16x16x32_bf16 a[132:135], a[28:31], a[36:39], 0\n"
"v_mfma_f32_16x16x32_bf16 a[136:139], a[28:31], a[40:43], 0\n"
"s_waitcnt vmcnt(8) lgkmcnt(0)\n"
"v_mfma_f32_16x16x32_bf16 a[140:143], a[28:31], a[44:47], 0\n"
"s_barrier\n"
"v_mfma_f32_16x16x32_bf16 a[144:147], a[28:31], a[48:51], 0\n"
"v_mfma_f32_16x16x32_bf16 a[148:151], a[28:31], a[52:55], 0\n"
"v_mfma_f32_16x16x32_bf16 a[152:155], a[28:31], a[56:59], 0\n"
"v_mfma_f32_16x16x32_bf16 a[156:159], a[28:31], a[60:63], 0\n"
"v_xor_b32 v232, 64, v232\n"
"v_mfma_f32_16x16x32_bf16 v[0:3], a[64:67], a[96:99], v[0:3]\n"
"ds_read_b128 a[32:35], v232 offset:32768\n"
"s_add_u32 m0, s52, 0\n"
"s_mov_b32 s54, 256\n"
"v_mfma_f32_16x16x32_bf16 v[4:7], a[64:67], a[100:103], v[4:7]\n"
"buffer_load_dwordx4 v233, s[24:27], s54 offen offset:0 lds\n"
"v_mfma_f32_16x16x32_bf16 v[8:11], a[64:67], a[104:107], v[8:11]\n"
"ds_read_b64_tr_b16 a[0:1], v224 offset:32768\n"
"s_cmp_lg_u32 s48, 0\n"
"s_cbranch_scc1 L_nofl2_%=\n"
"s_cmp_ge_u32 s49, 192\n"
"s_cbranch_scc1 L_nofl2_%=\n"
"v_mov_b32 v240, s96\n"
"v_mov_b32 v241, s97\n"
"s_lshl_b32 s58, s79, 3\n"
"buffer_store_dwordx2 v[240:241], off, s[44:47], s58 sc1\n"
"L_nofl2_%=:\n"
"v_mfma_f32_16x16x32_bf16 v[12:15], a[64:67], a[108:111], v[12:15]\n"
"v_mfma_f32_16x16x32_bf16 v[16:19], a[64:67], a[112:115], v[16:19]\n"
"ds_read_b64_tr_b16 a[2:3], v224 offset:33792\n"
"v_mfma_f32_16x16x32_bf16 v[20:23], a[64:67], a[116:119], v[20:23]\n"
"buffer_load_dwordx4 v234, s[24:27], s54 offen offset:1024 lds\n"
"v_mfma_f32_16x16x32_bf16 v[24:27], a[64:67], a[120:123], v[24:27]\n"
"ds_read_b128 a[36:39], v232 offset:34816\n"
"v_mfma_f32_16x16x32_bf16 v[28:31], a[64:67], a[124:127], v[28:31]\n"
"v_mfma_f32_16x16x32_bf16 v[32:35], a[68:71], a[96:99], v[32:35]\n"
"ds_read_b64_tr_b16 a[4:5], v225 offset:32768\n"
"v_mfma_f32_16x16x32_bf16 v[36:39], a[68:71], a[100:103], v[36:39]\n"
"buffer_load_dwordx4 v235, s[24:27], s54 offen offset:2048 lds\n"
"v_mfma_f32_16x16x32_bf16 v[40:43], a[68:71], a[104:107], v[40:43]\n"
"ds_read_b64_tr_b16 a[6:7], v225 offset:33792\n"
"v_mfma_f32_16x16x32_bf16 v[44:47], a[68:71], a[108:111], v[44:47]\n"
"v_mfma_f32_16x16x32_bf16 v[48:51], a[68:71], a[112:115], v[48:51]\n"
"ds_read_b128 a[40:43], v232 offset:36864\n"
"v_mfma_f32_16x16x32_bf16 v[52:55], a[68:71], a[116:119], v[52:55]\n"
"buffer_load_dwordx4 v236, s[24:27], s54 offen offset:3072 lds\n"
"v_mfma_f32_16x16x32_bf16 v[56:59], a[68:71], a[120:123], v[56:59]\n"
"ds_read_b64_tr_b16 a[8:9], v226 offset:32768\n"
"v_mfma_f32_16x16x32_bf16 v[60:63], a[68:71], a[124:127], v[60:63]\n"
"s_add_u32 m0, s52, 4096\n"
"s_mov_b32 s54, 33024\n"
"v_mfma_f32_16x16x32_bf16 v[64:67], a[72:75], a[96:99], v[64:67]\n"
"ds_read_b64_tr_b16 a[10:11], v226 offset:33792\n"
"v_mfma_f32_16x16x32_bf16 v[68:71], a[72:75], a[100:103], v[68:71]\n"
"buffer_load_dwordx4 v233, s[24:27], s54 offen offset:0 lds\n"
"v_mfma_f32_16x16x32_bf16 v[72:75], a[72:75], a[104:107], v[72:75]\n"
"ds_read_b128 a[44:47], v232 offset:38912\n"
"v_mfma_f32_16x16x32_bf16 v[76:79], a[72:75], a[108:111], v[76:79]\n"
"v_mfma_f32_16x16x32_bf16 v[80:83], a[72:75], a[112:115], v[80:83]\n"
"ds_read_b64_tr_b16 a[12:13], v227 offset:32768\n"
"v_mfma_f32_16x16x32_bf16 v[84:87], a[72:75], a[116:119], v[84:87]\n"
"buffer_load_dwordx4 v234, s[24:27], s54 offen offset:1024 lds\n"
"v_mfma_f32_16x16x32_bf16 v[88:91], a[72:75], a[120:123], v[88:91]\n"
"ds_read_b64_tr_b16 a[14:15], v227 offset:33792\n"
"v_mfma_f32_16x16x32_bf16 v[92:95], a[72:75], a[124:127], v[92:95]\n"
"v_mfma_f32_16x16x32_bf16 v[96:99], a[76:79], a[96:99], v[96:99]\n"
"ds_read_b128 a[48:51], v232 offset:49152\n"
"v_mfma_f32_16x16x32_bf16 v[100:103], a[76:79], a[100:103], v[100:103]\n"
"buffer_load_dwordx4 v235, s[24:27], s54 offen offset:2048 lds\n"
"v_mfma_f32_16x16x32_bf16 v[104:107], a[76:79], a[104:107], v[104:107]\n"
"ds_read_b64_tr_b16 a[16:17], v228 offset:32768\n"
"v_mfma_f32_16x16x32_bf16 v[108:111], a[76:79], a[108:111], v[108:111]\n"
"v_mfma_f32_16x16x32_bf16 v[112:115], a[76:79], a[112:115], v[112:115]\n"
"ds_read_b64_tr_b16 a[18:19], v228 offset:33792\n"
"v_mfma_f32_16x16x32_bf16 v[116:119], a[76:79], a[116:119], v[116:119]\n"
"buffer_load_dwordx4 v236, s[24:27], s54 offen offset:3072 lds\n"
"s_add_u32 m0, s53, 16384\n"
"s_mov_b32 s55, 1310720\n"
"v_mfma_f32_16x16x32_bf16 v[120:123], a[76:79], a[120:123], v[120:123]\n"
"ds_read_b128 a[52:55], v232 offset:51200\n"
"v_mfma_f32_16x16x32_bf16 v[124:127], a[76:79], a[124:127], v[124:127]\n"
"buffer_load_dwordx4 v237, s[28:31], s55 offen offset:0 lds\n"
"v_mfma_f32_16x16x32_bf16 v[128:131], a[80:83], a[96:99], v[128:131]\n"
"ds_read_b64_tr_b16 a[20:21], v229 offset:32768\n"
"v_mfma_f32_16x16x32_bf16 v[132:135], a[80:83], a[100:103], v[132:135]\n"
"v_mfma_f32_16x16x32_bf16 v[136:139], a[80:83], a[104:107], v[136:139]\n"
"ds_read_b64_tr_b16 a[22:23], v229 offset:33792\n"
"v_mfma_f32_16x16x32_bf16 v[140:143], a[80:83], a[108:111], v[140:143]\n"
"v_mfma_f32_16x16x32_bf16 v[144:147], a[80:83], a[112:115], v[144:147]\n"
"ds_read_b128 a[56:59], v232 offset:53248\n"
"v_mfma_f32_16x16x32_bf16 v[148:151], a[80:83], a[116:119], v[148:151]\n"
"buffer_load_dwordx4 v238, s[28:31], s55 offen offset:1024 lds\n"
"v_mfma_f32_16x16x32_bf16 v[152:155], a[80:83], a[120:123], v[152:155]\n"
"ds_read_b64_tr_b16 a[24:25], v230 offset:32768\n"
"v_mfma_f32_16x16x32_bf16 v[156:159], a[80:83], a[124:127], v[156:159]\n"
"v_mfma_f32_16x16x32_bf16 v[160:163], a[84:87], a[96:99], v[160:163]\n"
"ds_read_b64_tr_b16 a[26:27], v230 offset:33792\n"
"v_mfma_f32_16x16x32_bf16 v[164:167], a[84:87], a[100:103], v[164:167]\n"
"v_mfma_f32_16x16x32_bf16 v[168:171], a[84:87], a[104:107], v[168:171]\n"
"ds_read_b128 a[60:63], v232 offset:55296\n"
"v_mfma_f32_16x16x32_bf16 v[172:175], a[84:87], a[108:111], v[172:175]\n"
"s_add_u32 m0, s53, 24576\n"
"v_mfma_f32_16x16x32_bf16 v[176:179], a[84:87], a[112:115], v[176:179]\n"
"ds_read_b64_tr_b16 a[28:29], v231 offset:32768\n"
"v_mfma_f32_16x16x32_bf16 v[180:183], a[84:87], a[116:119], v[180:183]\n"
"buffer_load_dwordx4 v237, s[32:35], s55 offen offset:0 lds\n"
"v_mfma_f32_16x16x32_bf16 v[184:187], a[84:87], a[120:123], v[184:187]\n"
"ds_read_b64_tr_b16 a[30:31], v231 offset:33792\n"
"v_mfma_f32_16x16x32_bf16 v[188:191], a[84:87], a[124:127], v[188:191]\n"
"v_mfma_f32_16x16x32_bf16 v[192:195], a[88:91], a[96:99], v[192:195]\n"
"v_mfma_f32_16x16x32_bf16 v[196:199], a[88:91], a[100:103], v[196:199]\n"
"v_mfma_f32_16x16x32_bf16 v[200:203], a[88:91], a[104:107], v[200:203]\n"
"v_mfma_f32_16x16x32_bf16 v[204:207], a[88:91], a[108:111], v[204:207]\n"
"buffer_load_dwordx4 v238, s[32:35], s55 offen offset:1024 lds\n"
"v_mfma_f32_16x16x32_bf16 v[208:211], a[88:91], a[112:115], v[208:211]\n"
"v_mfma_f32_16x16x32_bf16 v[212:215], a[88:91], a[116:119], v[212:215]\n"
"v_mfma_f32_16x16x32_bf16 v[216:219], a[88:91], a[120:123], v[216:219]\n"
"v_mfma_f32_16x16x32_bf16 v[220:223], a[88:91], a[124:127], v[220:223]\n"
"v_mfma_f32_16x16x32_bf16 a[128:131], a[92:95], a[96:99], a[128:131]\n"
"v_mfma_f32_16x16x32_bf16 a[132:135], a[92:95], a[100:103], a[132:135]\n"
"v_mfma_f32_16x16x32_bf16 a[136:139], a[92:95], a[104:107], a[136:139]\n"
"s_waitcnt vmcnt(16) lgkmcnt(0)\n"
"v_mfma_f32_16x16x32_bf16 a[140:143], a[92:95], a[108:111], a[140:143]\n"
"s_barrier\n"
"v_mfma_f32_16x16x32_bf16 a[144:147], a[92:95], a[112:115], a[144:147]\n"
"v_mfma_f32_16x16x32_bf16 a[148:151], a[92:95], a[116:119], a[148:151]\n"
"v_mfma_f32_16x16x32_bf16 a[152:155], a[92:95], a[120:123], a[152:155]\n"
"v_mfma_f32_16x16x32_bf16 a[156:159], a[92:95], a[124:127], a[156:159]\n"
"v_xor_b32 v232, 64, v232\n"
"v_mfma_f32_16x16x32_bf16 v[0:3], a[0:3], a[32:35], v[0:3]\n"
"ds_read_b128 a[96:99], v232 offset:32768\n"
"s_add_u32 m0, s53, 32768\n"
"s_mov_b32 s55, 1572864\n"
"v_mfma_f32_16x16x32_bf16 v[4:7], a[0:3], a[36:39], v[4:7]\n"
"buffer_load_dwordx4 v237, s[28:31], s55 offen offset:0 lds\n"
"v_mfma_f32_16x16x32_bf16 v[8:11], a[0:3], a[40:43], v[8:11]\n"
"ds_read_b64_tr_b16 a[64:65], v224 offset:49152\n"
"v_mfma_f32_16x16x32_bf16 v[12:15], a[0:3], a[44:47], v[12:15]\n"
"v_mfma_f32_16x16x32_bf16 v[16:19], a[0:3], a[48:51], v[16:19]\n"
"ds_read_b64_tr_b16 a[66:67], v224 offset:50176\n"
"v_mfma_f32_16x16x32_bf16 v[20:23], a[0:3], a[52:55], v[20:23]\n"
"v_mfma_f32_16x16x32_bf16 v[24:27], a[0:3], a[56:59], v[24:27]\n"
"ds_read_b128 a[100:103], v232 offset:34816\n"
"v_mfma_f32_16x16x32_bf16 v[28:31], a[0:3], a[60:63], v[28:31]\n"
"v_mfma_f32_16x16x32_bf16 v[32:35], a[4:7], a[32:35], v[32:35]\n"
"ds_read_b64_tr_b16 a[68:69], v225 offset:49152\n"
"v_mfma_f32_16x16x32_bf16 v[36:39], a[4:7], a[36:39], v[36:39]\n"
"v_mfma_f32_16x16x32_bf16 v[40:43], a[4:7], a[40:43], v[40:43]\n"
"ds_read_b64_tr_b16 a[70:71], v225 offset:50176\n"
"v_mfma_f32_16x16x32_bf16 v[44:47], a[4:7], a[44:47], v[44:47]\n"
"v_mfma_f32_16x16x32_bf16 v[48:51], a[4:7], a[48:51], v[48:51]\n"
"ds_read_b128 a[104:107], v232 offset:36864\n"
"v_mfma_f32_16x16x32_bf16 v[52:55], a[4:7], a[52:55], v[52:55]\n"
"v_mfma_f32_16x16x32_bf16 v[56:59], a[4:7], a[56:59], v[56:59]\n"
"ds_read_b64_tr_b16 a[72:73], v226 offset:49152\n"
"v_mfma_f32_16x16x32_bf16 v[60:63], a[4:7], a[60:63], v[60:63]\n"
"buffer_load_dwordx4 v238, s[28:31], s55 offen offset:1024 lds\n"
"v_mfma_f32_16x16x32_bf16 v[64:67], a[8:11], a[32:35], v[64:67]\n"
"ds_read_b64_tr_b16 a[74:75], v226 offset:50176\n"
"v_mfma_f32_16x16x32_bf16 v[68:71], a[8:11], a[36:39], v[68:71]\n"
"v_mfma_f32_16x16x32_bf16 v[72:75], a[8:11], a[40:43], v[72:75]\n"
"ds_read_b128 a[108:111], v232 offset:38912\n"
"v_mfma_f32_16x16x32_bf16 v[76:79], a[8:11], a[44:47], v[76:79]\n"
"v_mfma_f32_16x16x32_bf16 v[80:83], a[8:11], a[48:51], v[80:83]\n"
"ds_read_b64_tr_b16 a[76:77], v227 offset:49152\n"
"v_mfma_f32_16x16x32_bf16 v[84:87], a[8:11], a[52:55], v[84:87]\n"
"v_mfma_f32_16x16x32_bf16 v[88:91], a[8:11], a[56:59], v[88:91]\n"
"ds_read_b64_tr_b16 a[78:79], v227 offset:50176\n"
"v_mfma_f32_16x16x32_bf16 v[92:95], a[8:11], a[60:63], v[92:95]\n"
"v_mfma_f32_16x16x32_bf16 v[96:99], a[12:15], a[32:35], v[96:99]\n"
"ds_read_b128 a[112:115], v232 offset:49152\n"
"v_mfma_f32_16x16x32_bf16 v[100:103], a[12:15], a[36:39], v[100:103]\n"
"v_mfma_f32_16x16x32_bf16 v[104:107], a[12:15], a[40:43], v[104:107]\n"
"ds_read_b64_tr_b16 a[80:81], v228 offset:49152\n"
"v_mfma_f32_16x16x32_bf16 v[108:111], a[12:15], a[44:47], v[108:111]\n"
"s_add_u32 m0, s53, 40960\n"
"v_mfma_f32_16x16x32_bf16 v[112:115], a[12:15], a[48:51], v[112:115]\n"
"ds_read_b64_tr_b16 a[82:83], v228 offset:50176\n"
"v_mfma_f32_16x16x32_bf16 v[116:119], a[12:15], a[52:55], v[116:119]\n"
"buffer_load_dwordx4 v237, s[32:35], s55 offen offset:0 lds\n"
"v_mfma_f32_16x16x32_bf16 v[120:123], a[12:15], a[56:59], v[120:123]\n"
"ds_read_b128 a[116:119], v232 offset:51200\n"
"v_mfma_f32_16x16x32_bf16 v[124:127], a[12:15], a[60:63], v[124:127]\n"
"v_mfma_f32_16x16x32_bf16 v[128:131], a[16:19], a[32:35], v[128:131]\n"
"ds_read_b64_tr_b16 a[84:85], v229 offset:49152\n"
"v_mfma_f32_16x16x32_bf16 v[132:135], a[16:19], a[36:39], v[132:135]\n"
"v_mfma_f32_16x16x32_bf16 v[136:139], a[16:19], a[40:43], v[136:139]\n"
"ds_read_b64_tr_b16 a[86:87], v229 offset:50176\n"
"v_mfma_f32_16x16x32_bf16 v[140:143], a[16:19], a[44:47], v[140:143]\n"
"v_mfma_f32_16x16x32_bf16 v[144:147], a[16:19], a[48:51], v[144:147]\n"
"ds_read_b128 a[120:123], v232 offset:53248\n"
"v_mfma_f32_16x16x32_bf16 v[148:151], a[16:19], a[52:55], v[148:151]\n"
"v_mfma_f32_16x16x32_bf16 v[152:155], a[16:19], a[56:59], v[152:155]\n"
"ds_read_b64_tr_b16 a[88:89], v230 offset:49152\n"
"v_mfma_f32_16x16x32_bf16 v[156:159], a[16:19], a[60:63], v[156:159]\n"
"v_mfma_f32_16x16x32_bf16 v[160:163], a[20:23], a[32:35], v[160:163]\n"
"ds_read_b64_tr_b16 a[90:91], v230 offset:50176\n"
"v_mfma_f32_16x16x32_bf16 v[164:167], a[20:23], a[36:39], v[164:167]\n"
"v_mfma_f32_16x16x32_bf16 v[168:171], a[20:23], a[40:43], v[168:171]\n"
"ds_read_b128 a[124:127], v232 offset:55296\n"
"v_mfma_f32_16x16x32_bf16 v[172:175], a[20:23], a[44:47], v[172:175]\n"
"buffer_load_dwordx4 v238, s[32:35], s55 offen offset:1024 lds\n"
"v_mfma_f32_16x16x32_bf16 v[176:179], a[20:23], a[48:51], v[176:179]\n"
"ds_read_b64_tr_b16 a[92:93], v231 offset:49152\n"
"v_mfma_f32_16x16x32_bf16 v[180:183], a[20:23], a[52:55], v[180:183]\n"
"v_mfma_f32_16x16x32_bf16 v[184:187], a[20:23], a[56:59], v[184:187]\n"
"ds_read_b64_tr_b16 a[94:95], v231 offset:50176\n"
"v_mfma_f32_16x16x32_bf16 v[188:191], a[20:23], a[60:63], v[188:191]\n"
"v_mfma_f32_16x16x32_bf16 v[192:195], a[24:27], a[32:35], v[192:195]\n"
"v_mfma_f32_16x16x32_bf16 v[196:199], a[24:27], a[36:39], v[196:199]\n"
"v_mfma_f32_16x16x32_bf16 v[200:203], a[24:27], a[40:43], v[200:203]\n"
"v_mfma_f32_16x16x32_bf16 v[204:207], a[24:27], a[44:47], v[204:207]\n"
"v_mfma_f32_16x16x32_bf16 v[208:211], a[24:27], a[48:51], v[208:211]\n"
"v_mfma_f32_16x16x32_bf16 v[212:215], a[24:27], a[52:55], v[212:215]\n"
"v_mfma_f32_16x16x32_bf16 v[216:219], a[24:27], a[56:59], v[216:219]\n"
"v_mfma_f32_16x16x32_bf16 v[220:223], a[24:27], a[60:63], v[220:223]\n"
"v_mfma_f32_16x16x32_bf16 a[128:131], a[28:31], a[32:35], a[128:131]\n"
"v_mfma_f32_16x16x32_bf16 a[132:135], a[28:31], a[36:39], a[132:135]\n"
"v_mfma_f32_16x16x32_bf16 a[136:139], a[28:31], a[40:43], a[136:139]\n"
"s_waitcnt vmcnt(8) lgkmcnt(0)\n"
"v_mfma_f32_16x16x32_bf16 a[140:143], a[28:31], a[44:47], a[140:143]\n"
"s_barrier\n"
"v_mfma_f32_16x16x32_bf16 a[144:147], a[28:31], a[48:51], a[144:147]\n"
"v_mfma_f32_16x16x32_bf16 a[148:151], a[28:31], a[52:55], a[148:151]\n"
"v_mfma_f32_16x16x32_bf16 a[152:155], a[28:31], a[56:59], a[152:155]\n"
"v_mfma_f32_16x16x32_bf16 a[156:159], a[28:31], a[60:63], a[156:159]\n"
"v_xor_b32 v232, 64, v232\n"
"v_mfma_f32_16x16x32_bf16 v[0:3], a[64:67], a[96:99], v[0:3]\n"
"ds_read_b128 a[32:35], v232 offset:0\n"
"v_lshrrev_b32 v244, 2, v239\n"
"s_add_u32 m0, s52, 32768\n"
"s_mov_b32 s54, 384\n"
"v_mfma_f32_16x16x32_bf16 v[4:7], a[64:67], a[100:103], v[4:7]\n"
"buffer_load_dwordx4 v233, s[24:27], s54 offen offset:0 lds\n"
"v_mfma_f32_16x16x32_bf16 v[8:11], a[64:67], a[104:107], v[8:11]\n"
"ds_read_b64_tr_b16 a[0:1], v224 offset:0\n"
"v_mfma_f32_16x16x32_bf16 v[12:15], a[64:67], a[108:111], v[12:15]\n"
"v_mfma_f32_16x16x32_bf16 v[16:19], a[64:67], a[112:115], v[16:19]\n"
"ds_read_b64_tr_b16 a[2:3], v224 offset:1024\n"
"v_mfma_f32_16x16x32_bf16 v[20:23], a[64:67], a[116:119], v[20:23]\n"
"buffer_load_dwordx4 v234, s[24:27], s54 offen offset:1024 lds\n"
"v_mfma_f32_16x16x32_bf16 v[24:27], a[64:67], a[120:123], v[24:27]\n"
"ds_read_b128 a[36:39], v232 offset:2048\n"
"v_mfma_f32_16x16x32_bf16 v[28:31], a[64:67], a[124:127], v[28:31]\n"
"v_mfma_f32_16x16x32_bf16 v[32:35], a[68:71], a[96:99], v[32:35]\n"
"ds_read_b64_tr_b16 a[4:5], v225 offset:0\n"
"v_mfma_f32_16x16x32_bf16 v[36:39], a[68:71], a[100:103], v[36:39]\n"
"buffer_load_dwordx4 v235, s[24:27], s54 offen offset:2048 lds\n"
"v_mfma_f32_16x16x32_bf16 v[40:43], a[68:71], a[104:107], v[40:43]\n"
"ds_read_b64_tr_b16 a[6:7], v225 offset:1024\n"
"v_mfma_f32_16x16x32_bf16 v[44:47], a[68:71], a[108:111], v[44:47]\n"
"v_mfma_f32_16x16x32_bf16 v[48:51], a[68:71], a[112:115], v[48:51]\n"
"ds_read_b128 a[40:43], v232 offset:4096\n"
"v_mfma_f32_16x16x32_bf16 v[52:55], a[68:71], a[116:119], v[52:55]\n"
"buffer_load_dwordx4 v236, s[24:27], s54 offen offset:3072 lds\n"
"v_mfma_f32_16x16x32_bf16 v[56:59], a[68:71], a[120:123], v[56:59]\n"
"ds_read_b64_tr_b16 a[8:9], v226 offset:0\n"
"v_mfma_f32_16x16x32_bf16 v[60:63], a[68:71], a[124:127], v[60:63]\n"
"s_add_u32 m0, s52, 36864\n"
"s_mov_b32 s54, 33152\n"
"v_mfma_f32_16x16x32_bf16 v[64:67], a[72:75], a[96:99], v[64:67]\n"
"ds_read_b64_tr_b16 a[10:11], v226 offset:1024\n"
"v_mfma_f32_16x16x32_bf16 v[68:71], a[72:75], a[100:103], v[68:71]\n"
"buffer_load_dwordx4 v233, s[24:27], s54 offen offset:0 lds\n"
"v_mfma_f32_16x16x32_bf16 v[72:75], a[72:75], a[104:107], v[72:75]\n"
"ds_read_b128 a[44:47], v232 offset:6144\n"
"v_mfma_f32_16x16x32_bf16 v[76:79], a[72:75], a[108:111], v[76:79]\n"
"v_mfma_f32_16x16x32_bf16 v[80:83], a[72:75], a[112:115], v[80:83]\n"
"ds_read_b64_tr_b16 a[12:13], v227 offset:0\n"
"v_mfma_f32_16x16x32_bf16 v[84:87], a[72:75], a[116:119], v[84:87]\n"
"buffer_load_dwordx4 v234, s[24:27], s54 offen offset:1024 lds\n"
"v_mfma_f32_16x16x32_bf16 v[88:91], a[72:75], a[120:123], v[88:91]\n"
"ds_read_b64_tr_b16 a[14:15], v227 offset:1024\n"
"v_mfma_f32_16x16x32_bf16 v[92:95], a[72:75], a[124:127], v[92:95]\n"
"v_mfma_f32_16x16x32_bf16 v[96:99], a[76:79], a[96:99], v[96:99]\n"
"ds_read_b128 a[48:51], v232 offset:16384\n"
"v_mfma_f32_16x16x32_bf16 v[100:103], a[76:79], a[100:103], v[100:103]\n"
"buffer_load_dwordx4 v235, s[24:27], s54 offen offset:2048 lds\n"
"v_mfma_f32_16x16x32_bf16 v[104:107], a[76:79], a[104:107], v[104:107]\n"
"ds_read_b64_tr_b16 a[16:17], v228 offset:0\n"
"v_mfma_f32_16x16x32_bf16 v[108:111], a[76:79], a[108:111], v[108:111]\n"
"v_mfma_f32_16x16x32_bf16 v[112:115], a[76:79], a[112:115], v[112:115]\n"
"ds_read_b64_tr_b16 a[18:19], v228 offset:1024\n"
"v_mfma_f32_16x16x32_bf16 v[116:119], a[76:79], a[116:119], v[116:119]\n"
"buffer_load_dwordx4 v236, s[24:27], s54 offen offset:3072 lds\n"
"s_movk_i32 s58, 0\n"
"v_mfma_f32_16x16x32_bf16 v[120:123], a[76:79], a[120:123], v[120:123]\n"
"ds_read_b128 a[52:55], v232 offset:18432\n"
"v_mfma_f32_16x16x32_bf16 v[124:127], a[76:79], a[124:127], v[124:127]\n"
"buffer_load_dwordx2 v[242:243], v244, s[44:47], s58 offen sc1\n"
"v_mfma_f32_16x16x32_bf16 v[128:131], a[80:83], a[96:99], v[128:131]\n"
"ds_read_b64_tr_b16 a[20:21], v229 offset:0\n"
"v_mfma_f32_16x16x32_bf16 v[132:135], a[80:83], a[100:103], v[132:135]\n"
"v_mfma_f32_16x16x32_bf16 v[136:139], a[80:83], a[104:107], v[136:139]\n"
"ds_read_b64_tr_b16 a[22:23], v229 offset:1024\n"
"v_mfma_f32_16x16x32_bf16 v[140:143], a[80:83], a[108:111], v[140:143]\n"
"buffer_load_dwordx4 a[192:195], v255, s[40:43], s60 offen offset:0\n"
"v_mfma_f32_16x16x32_bf16 v[144:147], a[80:83], a[112:115], v[144:147]\n"
"ds_read_b128 a[56:59], v232 offset:20480\n"
"v_mfma_f32_16x16x32_bf16 v[148:151], a[80:83], a[116:119], v[148:151]\n"
"v_mfma_f32_16x16x32_bf16 v[152:155], a[80:83], a[120:123], v[152:155]\n"
"ds_read_b64_tr_b16 a[24:25], v230 offset:0\n"
"v_mfma_f32_16x16x32_bf16 v[156:159], a[80:83], a[124:127], v[156:159]\n"
"buffer_load_dwordx4 a[196:199], v255, s[40:43], s60 offen offset:1024\n"
"v_mfma_f32_16x16x32_bf16 v[160:163], a[84:87], a[96:99], v[160:163]\n"
"ds_read_b64_tr_b16 a[26:27], v230 offset:1024\n"
"v_mfma_f32_16x16x32_bf16 v[164:167], a[84:87], a[100:103], v[164:167]\n"
"s_add_u32 m0, s53, 49152\n"
"s_mov_b32 s55, 1835008\n"
"v_mfma_f32_16x16x32_bf16 v[168:171], a[84:87], a[104:107], v[168:171]\n"
"ds_read_b128 a[60:63], v232 offset:22528\n"
"v_mfma_f32_16x16x32_bf16 v[172:175], a[84:87], a[108:111], v[172:175]\n"
"buffer_load_dwordx4 v237, s[28:31], s55 offen offset:0 lds\n"
"v_mfma_f32_16x16x32_bf16 v[176:179], a[84:87], a[112:115], v[176:179]\n"
"ds_read_b64_tr_b16 a[28:29], v231 offset:0\n"
"v_mfma_f32_16x16x32_bf16 v[180:183], a[84:87], a[116:119], v[180:183]\n"
"buffer_load_dwordx4 v238, s[28:31], s55 offen offset:1024 lds\n"
"v_mfma_f32_16x16x32_bf16 v[184:187], a[84:87], a[120:123], v[184:187]\n"
"ds_read_b64_tr_b16 a[30:31], v231 offset:1024\n"
"v_mfma_f32_16x16x32_bf16 v[188:191], a[84:87], a[124:127], v[188:191]\n"
"s_add_u32 m0, s53, 57344\n"
"v_mfma_f32_16x16x32_bf16 v[192:195], a[88:91], a[96:99], v[192:195]\n"
"v_mfma_f32_16x16x32_bf16 v[196:199], a[88:91], a[100:103], v[196:199]\n"
"buffer_load_dwordx4 v237, s[32:35], s55 offen offset:0 lds\n"
"v_mfma_f32_16x16x32_bf16 v[200:203], a[88:91], a[104:107], v[200:203]\n"
"v_mfma_f32_16x16x32_bf16 v[204:207], a[88:91], a[108:111], v[204:207]\n"
"v_mfma_f32_16x16x32_bf16 v[208:211], a[88:91], a[112:115], v[208:211]\n"
"v_mfma_f32_16x16x32_bf16 v[212:215], a[88:91], a[116:119], v[212:215]\n"
"buffer_load_dwordx4 v238, s[32:35], s55 offen offset:1024 lds\n"
"v_mfma_f32_16x16x32_bf16 v[216:219], a[88:91], a[120:123], v[216:219]\n"
"v_mfma_f32_16x16x32_bf16 v[220:223], a[88:91], a[124:127], v[220:223]\n"
"v_mfma_f32_16x16x32_bf16 a[128:131], a[92:95], a[96:99], a[128:131]\n"
"v_mfma_f32_16x16x32_bf16 a[132:135], a[92:95], a[100:103], a[132:135]\n"
"v_mfma_f32_16x16x32_bf16 a[136:139], a[92:95], a[104:107], a[136:139]\n"
"s_waitcnt vmcnt(19) lgkmcnt(0)\n"
"v_mfma_f32_16x16x32_bf16 a[140:143], a[92:95], a[108:111], a[140:143]\n"
"s_barrier\n"
"v_mfma_f32_16x16x32_bf16 a[144:147], a[92:95], a[112:115], a[144:147]\n"
"v_mfma_f32_16x16x32_bf16 a[148:151], a[92:95], a[116:119], a[148:151]\n"
"v_mfma_f32_16x16x32_bf16 a[152:155], a[92:95], a[120:123], a[152:155]\n"
"v_mfma_f32_16x16x32_bf16 a[156:159], a[92:95], a[124:127], a[156:159]\n"
"v_xor_b32 v232, 64, v232\n"
"v_mfma_f32_16x16x32_bf16 v[0:3], a[0:3], a[32:35], v[0:3]\n"
"ds_read_b128 a[96:99], v232 offset:0\n"
"s_add_u32 m0, s53, 0\n"
"s_mov_b32 s55, 2097152\n"
"v_mfma_f32_16x16x32_bf16 v[4:7], a[0:3], a[36:39], v[4:7]\n"
"buffer_load_dwordx4 v237, s[28:31], s55 offen offset:0 lds\n"
"v_mfma_f32_16x16x32_bf16 v[8:11], a[0:3], a[40:43], v[8:11]\n"
"ds_read_b64_tr_b16 a[64:65], v224 offset:16384\n"
"v_mfma_f32_16x16x32_bf16 v[12:15], a[0:3], a[44:47], v[12:15]\n"
"v_mfma_f32_16x16x32_bf16 v[16:19], a[0:3], a[48:51], v[16:19]\n"
"ds_read_b64_tr_b16 a[66:67], v224 offset:17408\n"
"v_mfma_f32_16x16x32_bf16 v[20:23], a[0:3], a[52:55], v[20:23]\n"
"v_mfma_f32_16x16x32_bf16 v[24:27], a[0:3], a[56:59], v[24:27]\n"
"ds_read_b128 a[100:103], v232 offset:2048\n"
"v_mfma_f32_16x16x32_bf16 v[28:31], a[0:3], a[60:63], v[28:31]\n"
"v_mfma_f32_16x16x32_bf16 v[32:35], a[4:7], a[32:35], v[32:35]\n"
"ds_read_b64_tr_b16 a[68:69], v225 offset:16384\n"
"v_mfma_f32_16x16x32_bf16 v[36:39], a[4:7], a[36:39], v[36:39]\n"
"v_mfma_f32_16x16x32_bf16 v[40:43], a[4:7], a[40:43], v[40:43]\n"
"ds_read_b64_tr_b16 a[70:71], v225 offset:17408\n"
"v_mfma_f32_16x16x32_bf16 v[44:47], a[4:7], a[44:47], v[44:47]\n"
"v_mfma_f32_16x16x32_bf16 v[48:51], a[4:7], a[48:51], v[48:51]\n"
"ds_read_b128 a[104:107], v232 offset:4096\n"
"v_mfma_f32_16x16x32_bf16 v[52:55], a[4:7], a[52:55], v[52:55]\n"
"v_mfma_f32_16x16x32_bf16 v[56:59], a[4:7], a[56:59], v[56:59]\n"
"ds_read_b64_tr_b16 a[72:73], v226 offset:16384\n"
"v_mfma_f32_16x16x32_bf16 v[60:63], a[4:7], a[60:63], v[60:63]\n"
"buffer_load_dwordx4 v238, s[28:31], s55 offen offset:1024 lds\n"
"v_mfma_f32_16x16x32_bf16 v[64:67], a[8:11], a[32:35], v[64:67]\n"
"ds_read_b64_tr_b16 a[74:75], v226 offset:17408\n"
"v_mfma_f32_16x16x32_bf16 v[68:71], a[8:11], a[36:39], v[68:71]\n"
"v_mfma_f32_16x16x32_bf16 v[72:75], a[8:11], a[40:43], v[72:75]\n"
"ds_read_b128 a[108:111], v232 offset:6144\n"
"v_mfma_f32_16x16x32_bf16 v[76:79], a[8:11], a[44:47], v[76:79]\n"
"v_mfma_f32_16x16x32_bf16 v[80:83], a[8:11], a[48:51], v[80:83]\n"
"ds_read_b64_tr_b16 a[76:77], v227 offset:16384\n"
"v_mfma_f32_16x16x32_bf16 v[84:87], a[8:11], a[52:55], v[84:87]\n"
"v_mfma_f32_16x16x32_bf16 v[88:91], a[8:11], a[56:59], v[88:91]\n"
"ds_read_b64_tr_b16 a[78:79], v227 offset:17408\n"
"v_mfma_f32_16x16x32_bf16 v[92:95], a[8:11], a[60:63], v[92:95]\n"
"v_mfma_f32_16x16x32_bf16 v[96:99], a[12:15], a[32:35], v[96:99]\n"
"ds_read_b128 a[112:115], v232 offset:16384\n"
"v_mfma_f32_16x16x32_bf16 v[100:103], a[12:15], a[36:39], v[100:103]\n"
"v_mfma_f32_16x16x32_bf16 v[104:107], a[12:15], a[40:43], v[104:107]\n"
"ds_read_b64_tr_b16 a[80:81], v228 offset:16384\n"
"v_mfma_f32_16x16x32_bf16 v[108:111], a[12:15], a[44:47], v[108:111]\n"
"s_add_u32 m0, s53, 8192\n"
"v_mfma_f32_16x16x32_bf16 v[112:115], a[12:15], a[48:51], v[112:115]\n"
"ds_read_b64_tr_b16 a[82:83], v228 offset:17408\n"
"v_mfma_f32_16x16x32_bf16 v[116:119], a[12:15], a[52:55], v[116:119]\n"
"buffer_load_dwordx4 v237, s[32:35], s55 offen offset:0 lds\n"
"v_mfma_f32_16x16x32_bf16 v[120:123], a[12:15], a[56:59], v[120:123]\n"
"ds_read_b128 a[116:119], v232 offset:18432\n"
"v_mfma_f32_16x16x32_bf16 v[124:127], a[12:15], a[60:63], v[124:127]\n"
"v_mfma_f32_16x16x32_bf16 v[128:131], a[16:19], a[32:35], v[128:131]\n"
"ds_read_b64_tr_b16 a[84:85], v229 offset:16384\n"
"v_mfma_f32_16x16x32_bf16 v[132:135], a[16:19], a[36:39], v[132:135]\n"
"v_mfma_f32_16x16x32_bf16 v[136:139], a[16:19], a[40:43], v[136:139]\n"
"ds_read_b64_tr_b16 a[86:87], v229 offset:17408\n"
"v_mfma_f32_16x16x32_bf16 v[140:143], a[16:19], a[44:47], v[140:143]\n"
"v_mfma_f32_16x16x32_bf16 v[144:147], a[16:19], a[48:51], v[144:147]\n"
"ds_read_b128 a[120:123], v232 offset:20480\n"
"v_mfma_f32_16x16x32_bf16 v[148:151], a[16:19], a[52:55], v[148:151]\n"
"v_mfma_f32_16x16x32_bf16 v[152:155], a[16:19], a[56:59], v[152:155]\n"
"ds_read_b64_tr_b16 a[88:89], v230 offset:16384\n"
"v_mfma_f32_16x16x32_bf16 v[156:159], a[16:19], a[60:63], v[156:159]\n"
"v_mfma_f32_16x16x32_bf16 v[160:163], a[20:23], a[32:35], v[160:163]\n"
"ds_read_b64_tr_b16 a[90:91], v230 offset:17408\n"
"v_mfma_f32_16x16x32_bf16 v[164:167], a[20:23], a[36:39], v[164:167]\n"
"v_mfma_f32_16x16x32_bf16 v[168:171], a[20:23], a[40:43], v[168:171]\n"
"ds_read_b128 a[124:127], v232 offset:22528\n"
"v_mfma_f32_16x16x32_bf16 v[172:175], a[20:23], a[44:47], v[172:175]\n"
"buffer_load_dwordx4 v238, s[32:35], s55 offen offset:1024 lds\n"
"v_mfma_f32_16x16x32_bf16 v[176:179], a[20:23], a[48:51], v[176:179]\n"
"ds_read_b64_tr_b16 a[92:93], v231 offset:16384\n"
"v_mfma_f32_16x16x32_bf16 v[180:183], a[20:23], a[52:55], v[180:183]\n"
"v_mfma_f32_16x16x32_bf16 v[184:187], a[20:23], a[56:59], v[184:187]\n"
"ds_read_b64_tr_b16 a[94:95], v231 offset:17408\n"
"v_mfma_f32_16x16x32_bf16 v[188:191], a[20:23], a[60:63], v[188:191]\n"
"v_mfma_f32_16x16x32_bf16 v[192:195], a[24:27], a[32:35], v[192:195]\n"
"v_mfma_f32_16x16x32_bf16 v[196:199], a[24:27], a[36:39], v[196:199]\n"
"v_mfma_f32_16x16x32_bf16 v[200:203], a[24:27], a[40:43], v[200:203]\n"
"v_mfma_f32_16x16x32_bf16 v[204:207], a[24:27], a[44:47], v[204:207]\n"
"v_mfma_f32_16x16x32_bf16 v[208:211], a[24:27], a[48:51], v[208:211]\n"
"v_mfma_f32_16x16x32_bf16 v[212:215], a[24:27], a[52:55], v[212:215]\n"
"v_mfma_f32_16x16x32_bf16 v[216:219], a[24:27], a[56:59], v[216:219]\n"
"v_mfma_f32_16x16x32_bf16 v[220:223], a[24:27], a[60:63], v[220:223]\n"
"v_mfma_f32_16x16x32_bf16 a[128:131], a[28:31], a[32:35], a[128:131]\n"
"v_mfma_f32_16x16x32_bf16 a[132:135], a[28:31], a[36:39], a[132:135]\n"
"v_mfma_f32_16x16x32_bf16 a[136:139], a[28:31], a[40:43], a[136:139]\n"
"s_waitcnt vmcnt(8) lgkmcnt(0)\n"
"v_mfma_f32_16x16x32_bf16 a[140:143], a[28:31], a[44:47], a[140:143]\n"
"s_barrier\n"
"v_mfma_f32_16x16x32_bf16 a[144:147], a[28:31], a[48:51], a[144:147]\n"
"v_mfma_f32_16x16x32_bf16 a[148:151], a[28:31], a[52:55], a[148:151]\n"
"v_mfma_f32_16x16x32_bf16 a[152:155], a[28:31], a[56:59], a[152:155]\n"
"v_mfma_f32_16x16x32_bf16 a[156:159], a[28:31], a[60:63], a[156:159]\n"
"v_xor_b32 v232, 64, v232\n"
"v_mfma_f32_16x16x32_bf16 v[0:3], a[64:67], a[96:99], v[0:3]\n"
"ds_read_b128 a[32:35], v232 offset:32768\n"
"v_cmp_ne_u64 vcc, s[96:97], v[242:243]\n"
"v_mfma_f32_16x16x32_bf16 v[4:7], a[64:67], a[100:103], v[4:7]\n"
"s_cbranch_vccnz L_spin3_%=\n"
"L_spinr4_%=:\n"
"v_mfma_f32_16x16x32_bf16 v[8:11], a[64:67], a[104:107], v[8:11]\n"
"ds_read_b64_tr_b16 a[0:1], v224 offset:32768\n"
"s_lshl_b32 s58, s48, 10\n"
"s_add_u32 m0, s58, 155648\n"
"s_add_u32 s58, s58, 4096\n"
"v_mfma_f32_16x16x32_bf16 v[12:15], a[64:67], a[108:111], v[12:15]\n"
"v_mfma_f32_16x16x32_bf16 v[16:19], a[64:67], a[112:115], v[16:19]\n"
"ds_read_b64_tr_b16 a[2:3], v224 offset:33792\n"
"buffer_load_dwordx4 v255, s[44:47], s58 offen sc1 lds\n"
"v_mfma_f32_16x16x32_bf16 v[20:23], a[64:67], a[116:119], v[20:23]\n"
"v_mfma_f32_16x16x32_bf16 v[24:27], a[64:67], a[120:123], v[24:27]\n"
"ds_read_b128 a[36:39], v232 offset:34816\n"
"v_mfma_f32_16x16x32_bf16 v[28:31], a[64:67], a[124:127], v[28:31]\n"
"s_add_u32 m0, s52, 0\n"
"s_mov_b32 s54, 512\n"
"v_mfma_f32_16x16x32_bf16 v[32:35], a[68:71], a[96:99], v[32:35]\n"
"ds_read_b64_tr_b16 a[4:5], v225 offset:32768\n"
"v_mfma_f32_16x16x32_bf16 v[36:39], a[68:71], a[100:103], v[36:39]\n"
"buffer_load_dwordx4 v233, s[24:27], s54 offen offset:0 lds\n"
"v_mfma_f32_16x16x32_bf16 v[40:43], a[68:71], a[104:107], v[40:43]\n"
"ds_read_b64_tr_b16 a[6:7], v225 offset:33792\n"
"v_mfma_f32_16x16x32_bf16 v[44:47], a[68:71], a[108:111], v[44:47]\n"
"v_mfma_f32_16x16x32_bf16 v[48:51], a[68:71], a[112:115], v[48:51]\n"
"ds_read_b128 a[40:43], v232 offset:36864\n"
"v_mfma_f32_16x16x32_bf16 v[52:55], a[68:71], a[116:119], v[52:55]\n"
"buffer_load_dwordx4 v234, s[24:27], s54 offen offset:1024 lds\n"
"v_mfma_f32_16x16x32_bf16 v[56:59], a[68:71], a[120:123], v[56:59]\n"
"ds_read_b64_tr_b16 a[8:9], v226 offset:32768\n"
"v_mfma_f32_16x16x32_bf16 v[60:63], a[68:71], a[124:127], v[60:63]\n"
"buffer_load_dwordx4 v235, s[24:27], s54 offen offset:2048 lds\n"
"v_mfma_f32_16x16x32_bf16 v[64:67], a[72:75], a[96:99], v[64:67]\n"
"ds_read_b64_tr_b16 a[10:11], v226 offset:33792\n"
"v_mfma_f32_16x16x32_bf16 v[68:71], a[72:75], a[100:103], v[68:71]\n"
"v_mfma_f32_16x16x32_bf16 v[72:75], a[72:75], a[104:107], v[72:75]\n"
"ds_read_b128 a[44:47], v232 offset:38912\n"
"v_mfma_f32_16x16x32_bf16 v[76:79], a[72:75], a[108:111], v[76:79]\n"
"buffer_load_dwordx4 v236, s[24:27], s54 offen offset:3072 lds\n"
"s_add_u32 m0, s52, 4096\n"
"s_mov_b32 s54, 33280\n"
"v_mfma_f32_16x16x32_bf16 v[80:83], a[72:75], a[112:115], v[80:83]\n"
"ds_read_b64_tr_b16 a[12:13], v227 offset:32768\n"
"v_mfma_f32_16x16x32_bf16 v[84:87], a[72:75], a[116:119], v[84:87]\n"
"buffer_load_dwordx4 v233, s[24:27], s54 offen offset:0 lds\n"
"v_mfma_f32_16x16x32_bf16 v[88:91], a[72:75], a[120:123], v[88:91]\n"
"ds_read_b64_tr_b16 a[14:15], v227 offset:33792\n"
"v_mfma_f32_16x16x32_bf16 v[92:95], a[72:75], a[124:127], v[92:95]\n"
"v_mfma_f32_16x16x32_bf16 v[96:99], a[76:79], a[96:99], v[96:99]\n"
"ds_read_b128 a[48:51], v232 offset:49152\n"
"v_mfma_f32_16x16x32_bf16 v[100:103], a[76:79], a[100:103], v[100:103]\n"
"buffer_load_dwordx4 v234, s[24:27], s54 offen offset:1024 lds\n"
"v_mfma_f32_16x16x32_bf16 v[104:107], a[76:79], a[104:107], v[104:107]\n"
"ds_read_b64_tr_b16 a[16:17], v228 offset:32768\n"
"v_mfma_f32_16x16x32_bf16 v[108:111], a[76:79], a[108:111], v[108:111]\n"
"buffer_load_dwordx4 v235, s[24:27], s54 offen offset:2048 lds\n"
"v_mfma_f32_16x16x32_bf16 v[112:115], a[76:79], a[112:115], v[112:115]\n"
"ds_read_b64_tr_b16 a[18:19], v228 offset:33792\n"
"v_mfma_f32_16x16x32_bf16 v[116:119], a[76:79], a[116:119], v[116:119]\n"
"v_mfma_f32_16x16x32_bf16 v[120:123], a[76:79], a[120:123], v[120:123]\n"
"ds_read_b128 a[52:55], v232 offset:51200\n"
"v_mfma_f32_16x16x32_bf16 v[124:127], a[76:79], a[124:127], v[124:127]\n"
"buffer_load_dwordx4 v236, s[24:27], s54 offen offset:3072 lds\n"
"s_add_u32 m0, s53, 16384\n"
"s_mov_b32 s55, 2359296\n"
"v_mfma_f32_16x16x32_bf16 v[128:131], a[80:83], a[96:99], v[128:131]\n"
"ds_read_b64_tr_b16 a[20:21], v229 offset:32768\n"
"v_mfma_f32_16x16x32_bf16 v[132:135], a[80:83], a[100:103], v[132:135]\n"
"buffer_load_dwordx4 v237, s[28:31], s55 offen offset:0 lds\n"
"v_mfma_f32_16x16x32_bf16 v[136:139], a[80:83], a[104:107], v[136:139]\n"
"ds_read_b64_tr_b16 a[22:23], v229 offset:33792\n"
"v_mfma_f32_16x16x32_bf16 v[140:143], a[80:83], a[108:111], v[140:143]\n"
"v_mfma_f32_16x16x32_bf16 v[144:147], a[80:83], a[112:115], v[144:147]\n"
"ds_read_b128 a[56:59], v232 offset:53248\n"
"v_mfma_f32_16x16x32_bf16 v[148:151], a[80:83], a[116:119], v[148:151]\n"
"v_mfma_f32_16x16x32_bf16 v[152:155], a[80:83], a[120:123], v[152:155]\n"
"ds_read_b64_tr_b16 a[24:25], v230 offset:32768\n"
"v_mfma_f32_16x16x32_bf16 v[156:159], a[80:83], a[124:127], v[156:159]\n"
"buffer_load_dwordx4 v238, s[28:31], s55 offen offset:1024 lds\n"
"v_mfma_f32_16x16x32_bf16 v[160:163], a[84:87], a[96:99], v[160:163]\n"
"ds_read_b64_tr_b16 a[26:27], v230 offset:33792\n"
"v_mfma_f32_16x16x32_bf16 v[164:167], a[84:87], a[100:103], v[164:167]\n"
"v_mfma_f32_16x16x32_bf16 v[168:171], a[84:87], a[104:107], v[168:171]\n"
"ds_read_b128 a[60:63], v232 offset:55296\n"
"v_mfma_f32_16x16x32_bf16 v[172:175], a[84:87], a[108:111], v[172:175]\n"
"s_add_u32 m0, s53, 24576\n"
"v_mfma_f32_16x16x32_bf16 v[176:179], a[84:87], a[112:115], v[176:179]\n"
"ds_read_b64_tr_b16 a[28:29], v231 offset:32768\n"
"v_mfma_f32_16x16x32_bf16 v[180:183], a[84:87], a[116:119], v[180:183]\n"
"buffer_load_dwordx4 v237, s[32:35], s55 offen offset:0 lds\n"
"v_mfma_f32_16x16x32_bf16 v[184:187], a[84:87], a[120:123], v[184:187]\n"
"ds_read_b64_tr_b16 a[30:31], v231 offset:33792\n"
"v_mfma_f32_16x16x32_bf16 v[188:191], a[84:87], a[124:127], v[188:191]\n"
"v_mfma_f32_16x16x32_bf16 v[192:195], a[88:91], a[96:99], v[192:195]\n"
"v_mfma_f32_16x16x32_bf16 v[196:199], a[88:91], a[100:103], v[196:199]\n"
"v_mfma_f32_16x16x32_bf16 v[200:203], a[88:91], a[104:107], v[200:203]\n"
"v_mfma_f32_16x16x32_bf16 v[204:207], a[88:91], a[108:111], v[204:207]\n"
"buffer_load_dwordx4 v238, s[32:35], s55 offen offset:1024 lds\n"
"v_mfma_f32_16x16x32_bf16 v[208:211], a[88:91], a[112:115], v[208:211]\n"
"v_mfma_f32_16x16x32_bf16 v[212:215], a[88:91], a[116:119], v[212:215]\n"
"v_mfma_f32_16x16x32_bf16 v[216:219], a[88:91], a[120:123], v[216:219]\n"
"v_mfma_f32_16x16x32_bf16 v[220:223], a[88:91], a[124:127], v[220:223]\n"
"v_mfma_f32_16x16x32_bf16 a[128:131], a[92:95], a[96:99], a[128:131]\n"
"v_mfma_f32_16x16x32_bf16 a[132:135], a[92:95], a[100:103], a[132:135]\n"
"v_mfma_f32_16x16x32_bf16 a[136:139], a[92:95], a[104:107], a[136:139]\n"
"s_waitcnt vmcnt(17) lgkmcnt(0)\n"
"v_mfma_f32_16x16x32_bf16 a[140:143], a[92:95], a[108:111], a[140:143]\n"
"s_barrier\n"
"v_mfma_f32_16x16x32_bf16 a[144:147], a[92:95], a[112:115], a[144:147]\n"
"v_mfma_f32_16x16x32_bf16 a[148:151], a[92:95], a[116:119], a[148:151]\n"
"v_mfma_f32_16x16x32_bf16 a[152:155], a[92:95], a[120:123], a[152:155]\n"
"v_mfma_f32_16x16x32_bf16 a[156:159], a[92:95], a[124:127], a[156:159]\n"
"v_xor_b32 v232, 64, v232\n"
"v_mfma_f32_16x16x32_bf16 v[0:3], a[0:3], a[32:35], v[0:3]\n"
"ds_read_b128 a[96:99], v232 offset:32768\n"
"s_add_u32 m0, s53, 32768\n"
"s_mov_b32 s55, 2621440\n"
"v_mfma_f32_16x16x32_bf16 v[4:7], a[0:3], a[36:39], v[4:7]\n"
"buffer_load_dwordx4 v237, s[28:31], s55 offen offset:0 lds\n"
"v_mfma_f32_16x16x32_bf16 v[8:11], a[0:3], a[40:43], v[8:11]\n"
"ds_read_b64_tr_b16 a[64:65], v224 offset:49152\n"
"v_mfma_f32_16x16x32_bf16 v[12:15], a[0:3], a[44:47], v[12:15]\n"
"s_lshl_b32 s62, s48, 1\n"
"s_add_u32 s63, s62, 1\n"
"s_mov_b32 s61, 147456\n"
"v_mfma_f32_16x16x32_bf16 v[16:19], a[0:3], a[48:51], v[16:19]\n"
"ds_read_b64_tr_b16 a[66:67], v224 offset:50176\n"
"v_mfma_f32_16x16x32_bf16 v[20:23], a[0:3], a[52:55], v[20:23]\n"
"s_lshr_b32 s56, s49, 2\n"
"s_lshl_b32 s56, s56, 15\n"
"s_and_b32 s57, s49, 3\n"
"v_mfma_f32_16x16x32_bf16 v[24:27], a[0:3], a[56:59], v[24:27]\n"
"ds_read_b128 a[100:103], v232 offset:34816\n"
"v_mfma_f32_16x16x32_bf16 v[28:31], a[0:3], a[60:63], v[28:31]\n"
"s_lshl_b32 s57, s57, 7\n"
"s_add_u32 s56, s56, s57\n"
"s_lshl_b32 s57, s48, 13\n"
"v_mfma_f32_16x16x32_bf16 v[32:35], a[4:7], a[32:35], v[32:35]\n"
"ds_read_b64_tr_b16 a[68:69], v225 offset:49152\n"
"v_mfma_f32_16x16x32_bf16 v[36:39], a[4:7], a[36:39], v[36:39]\n"
"s_add_u32 s56, s56, s57\n"
"s_add_u32 s74, s56, 1048576\n"
"s_add_u32 s75, s74, 4096\n"
"v_mfma_f32_16x16x32_bf16 v[40:43], a[4:7], a[40:43], v[40:43]\n"
"ds_read_b64_tr_b16 a[70:71], v225 offset:50176\n"
"v_mfma_f32_16x16x32_bf16 v[44:47], a[4:7], a[44:47], v[44:47]\n"
"v_add_u32 v254, 155648, v239\n"
"v_mfma_f32_16x16x32_bf16 v[48:51], a[4:7], a[48:51], v[48:51]\n"
"ds_read_b128 a[104:107], v232 offset:36864\n"
"v_mfma_f32_16x16x32_bf16 v[52:55], a[4:7], a[52:55], v[52:55]\n"
"v_mfma_f32_16x16x32_bf16 v[56:59], a[4:7], a[56:59], v[56:59]\n"
"ds_read_b64_tr_b16 a[72:73], v226 offset:49152\n"
"v_mfma_f32_16x16x32_bf16 v[60:63], a[4:7], a[60:63], v[60:63]\n"
"buffer_load_dwordx4 v238, s[28:31], s55 offen offset:1024 lds\n"
"v_mfma_f32_16x16x32_bf16 v[64:67], a[8:11], a[32:35], v[64:67]\n"
"ds_read_b64_tr_b16 a[74:75], v226 offset:50176\n"
"v_mfma_f32_16x16x32_bf16 v[68:71], a[8:11], a[36:39], v[68:71]\n"
"v_mfma_f32_16x16x32_bf16 v[72:75], a[8:11], a[40:43], v[72:75]\n"
"ds_read_b128 a[108:111], v232 offset:38912\n"
"v_mfma_f32_16x16x32_bf16 v[76:79], a[8:11], a[44:47], v[76:79]\n"
"v_mfma_f32_16x16x32_bf16 v[80:83], a[8:11], a[48:51], v[80:83]\n"
"ds_read_b64_tr_b16 a[76:77], v227 offset:49152\n"
"v_mfma_f32_16x16x32_bf16 v[84:87], a[8:11], a[52:55], v[84:87]\n"
"v_mfma_f32_16x16x32_bf16 v[88:91], a[8:11], a[56:59], v[88:91]\n"
"ds_read_b64_tr_b16 a[78:79], v227 offset:50176\n"
"v_mfma_f32_16x16x32_bf16 v[92:95], a[8:11], a[60:63], v[92:95]\n"
"v_mfma_f32_16x16x32_bf16 v[96:99], a[12:15], a[32:35], v[96:99]\n"
"ds_read_b128 a[112:115], v232 offset:49152\n"
"v_mfma_f32_16x16x32_bf16 v[100:103], a[12:15], a[36:39], v[100:103]\n"
"v_mfma_f32_16x16x32_bf16 v[104:107], a[12:15], a[40:43], v[104:107]\n"
"ds_read_b64_tr_b16 a[80:81], v228 offset:49152\n"
"v_mfma_f32_16x16x32_bf16 v[108:111], a[12:15], a[44:47], v[108:111]\n"
"s_add_u32 m0, s53, 40960\n"
"v_mfma_f32_16x16x32_bf16 v[112:115], a[12:15], a[48:51], v[112:115]\n"
"ds_read_b64_tr_b16 a[82:83], v228 offset:50176\n"
"v_mfma_f32_16x16x32_bf16 v[116:119], a[12:15], a[52:55], v[116:119]\n"
"buffer_load_dwordx4 v237, s[32:35], s55 offen offset:0 lds\n"
"v_mfma_f32_16x16x32_bf16 v[120:123], a[12:15], a[56:59], v[120:123]\n"
"ds_read_b128 a[116:119], v232 offset:51200\n"
"v_mfma_f32_16x16x32_bf16 v[124:127], a[12:15], a[60:63], v[124:127]\n"
"v_mfma_f32_16x16x32_bf16 v[128:131], a[16:19], a[32:35], v[128:131]\n"
"ds_read_b64_tr_b16 a[84:85], v229 offset:49152\n"
"v_mfma_f32_16x16x32_bf16 v[132:135], a[16:19], a[36:39], v[132:135]\n"
"v_mfma_f32_16x16x32_bf16 v[136:139], a[16:19], a[40:43], v[136:139]\n"
"ds_read_b64_tr_b16 a[86:87], v229 offset:50176\n"
"v_mfma_f32_16x16x32_bf16 v[140:143], a[16:19], a[44:47], v[140:143]\n"
"v_mfma_f32_16x16x32_bf16 v[144:147], a[16:19], a[48:51], v[144:147]\n"
"ds_read_b128 a[120:123], v232 offset:53248\n"
"v_mfma_f32_16x16x32_bf16 v[148:151], a[16:19], a[52:55], v[148:151]\n"
"v_mfma_f32_16x16x32_bf16 v[152:155], a[16:19], a[56:59], v[152:155]\n"
"ds_read_b64_tr_b16 a[88:89], v230 offset:49152\n"
"v_mfma_f32_16x16x32_bf16 v[156:159], a[16:19], a[60:63], v[156:159]\n"
"v_mfma_f32_16x16x32_bf16 v[160:163], a[20:23], a[32:35], v[160:163]\n"
"ds_read_b64_tr_b16 a[90:91], v230 offset:50176\n"
"v_mfma_f32_16x16x32_bf16 v[164:167], a[20:23], a[36:39], v[164:167]\n"
"v_mfma_f32_16x16x32_bf16 v[168:171], a[20:23], a[40:43], v[168:171]\n"
"ds_read_b128 a[124:127], v232 offset:55296\n"
"v_mfma_f32_16x16x32_bf16 v[172:175], a[20:23], a[44:47], v[172:175]\n"
"buffer_load_dwordx4 v238, s[32:35], s55 offen offset:1024 lds\n"
"v_mfma_f32_16x16x32_bf16 v[176:179], a[20:23], a[48:51], v[176:179]\n"
"ds_read_b64_tr_b16 a[92:93], v231 offset:49152\n"
"v_mfma_f32_16x16x32_bf16 v[180:183], a[20:23], a[52:55], v[180:183]\n"
"v_mfma_f32_16x16x32_bf16 v[184:187], a[20:23], a[56:59], v[184:187]\n"
"ds_read_b64_tr_b16 a[94:95], v231 offset:50176\n"
"v_mfma_f32_16x16x32_bf16 v[188:191], a[20:23], a[60:63], v[188:191]\n"
"v_mfma_f32_16x16x32_bf16 v[192:195], a[24:27], a[32:35], v[192:195]\n"
"v_mfma_f32_16x16x32_bf16 v[196:199], a[24:27], a[36:39], v[196:199]\n"
"v_mfma_f32_16x16x32_bf16 v[200:203], a[24:27], a[40:43], v[200:203]\n"
"v_mfma_f32_16x16x32_bf16 v[204:207], a[24:27], a[44:47], v[204:207]\n"
"v_mfma_f32_16x16x32_bf16 v[208:211], a[24:27], a[48:51], v[208:211]\n"
"v_mfma_f32_16x16x32_bf16 v[212:215], a[24:27], a[52:55], v[212:215]\n"
"v_mfma_f32_16x16x32_bf16 v[216:219], a[24:27], a[56:59], v[216:219]\n"
"v_mfma_f32_16x16x32_bf16 v[220:223], a[24:27], a[60:63], v[220:223]\n"
"v_mfma_f32_16x16x32_bf16 a[128:131], a[28:31], a[32:35], a[128:131]\n"
"v_mfma_f32_16x16x32_bf16 a[132:135], a[28:31], a[36:39], a[132:135]\n"
"v_mfma_f32_16x16x32_bf16 a[136:139], a[28:31], a[40:43], a[136:139]\n"
"s_waitcnt vmcnt(8) lgkmcnt(0)\n"
"v_mfma_f32_16x16x32_bf16 a[140:143], a[28:31], a[44:47], a[140:143]\n"
"s_barrier\n"
"v_mfma_f32_16x16x32_bf16 a[144:147], a[28:31], a[48:51], a[144:147]\n"
"v_mfma_f32_16x16x32_bf16 a[148:151], a[28:31], a[52:55], a[148:151]\n"
"v_mfma_f32_16x16x32_bf16 a[152:155], a[28:31], a[56:59], a[152:155]\n"
"v_mfma_f32_16x16x32_bf16 a[156:159], a[28:31], a[60:63], a[156:159]\n"
"v_xor_b32 v232, 64, v232\n"
"v_mfma_f32_16x16x32_bf16 v[0:3], a[64:67], a[96:99], v[0:3]\n"
"ds_read_b128 a[200:203], v254\n"
"ds_read_b128 a[204:207], v254 offset:16\n"
"ds_read_b128 a[208:211], v254 offset:2048\n"
"ds_read_b128 a[212:215], v254 offset:2064\n"
"ds_read_b128 a[32:35], v232 offset:0\n"
"v_accvgpr_read_b32 v250, a192\n"
"v_lshlrev_b32 v240, 16, v250\n"
"s_add_u32 m0, s52, 32768\n"
"s_mov_b32 s54, 640\n"
"v_mfma_f32_16x16x32_bf16 v[4:7], a[64:67], a[100:103], v[4:7]\n"
"buffer_load_dwordx4 v233, s[24:27], s54 offen offset:0 lds\n"
"v_and_b32 v241, 0xffff0000, v250\n"
"v_accvgpr_read_b32 v251, a193\n"
"v_mfma_f32_16x16x32_bf16 v[8:11], a[64:67], a[104:107], v[8:11]\n"
"ds_read_b64_tr_b16 a[0:1], v224 offset:0\n"
"v_lshlrev_b32 v242, 16, v251\n"
"v_and_b32 v243, 0xffff0000, v251\n"
"v_mfma_f32_16x16x32_bf16 v[12:15], a[64:67], a[108:111], v[12:15]\n"
"v_accvgpr_read_b32 v250, a194\n"
"v_lshlrev_b32 v244, 16, v250\n"
"v_mfma_f32_16x16x32_bf16 v[16:19], a[64:67], a[112:115], v[16:19]\n"
"ds_read_b64_tr_b16 a[2:3], v224 offset:1024\n"
"v_and_b32 v245, 0xffff0000, v250\n"
"v_accvgpr_read_b32 v251, a195\n"
"v_mfma_f32_16x16x32_bf16 v[20:23], a[64:67], a[116:119], v[20:23]\n"
"buffer_load_dwordx4 v234, s[24:27], s54 offen offset:1024 lds\n"
"v_lshlrev_b32 v246, 16, v251\n"
"v_and_b32 v247, 0xffff0000, v251\n"
"v_mfma_f32_16x16x32_bf16 v[24:27], a[64:67], a[120:123], v[24:27]\n"
"ds_read_b128 a[36:39], v232 offset:2048\n"
"v_add_f32 v248, v240, v241\n"
"v_add_f32 v248, v248, v242\n"
"v_mfma_f32_16x16x32_bf16 v[28:31], a[64:67], a[124:127], v[28:31]\n"
"v_add_f32 v248, v248, v243\n"
"v_add_f32 v248, v248, v244\n"
"v_mfma_f32_16x16x32_bf16 v[32:35], a[68:71], a[96:99], v[32:35]\n"
"ds_read_b64_tr_b16 a[4:5], v225 offset:0\n"
"v_add_f32 v248, v248, v245\n"
"v_add_f32 v248, v248, v246\n"
"v_mfma_f32_16x16x32_bf16 v[36:39], a[68:71], a[100:103], v[36:39]\n"
"s_waitcnt lgkmcnt(5)\n"
"buffer_load_dwordx4 v235, s[24:27], s54 offen offset:2048 lds\n"
"v_add_f32 v248, v248, v247\n"
"s_nop 1\n"
"v_add_f32_dpp v248, v248, v248 quad_perm:[1,0,3,2] row_mask:0xf bank_mask:0xf\n"
"v_mfma_f32_16x16x32_bf16 v[40:43], a[68:71], a[104:107], v[40:43]\n"
"ds_read_b64_tr_b16 a[6:7], v225 offset:1024\n"
"v_add_f32_dpp v248, v248, v248 quad_perm:[2,3,0,1] row_mask:0xf bank_mask:0xf\n"
"s_nop 1\n"
"v_add_f32_dpp v248, v248, v248 row_half_mirror row_mask:0xf bank_mask:0xf\n"
"v_mfma_f32_16x16x32_bf16 v[44:47], a[68:71], a[108:111], v[44:47]\n"
"s_nop 0\n"
"v_add_f32_dpp v248, v248, v248 row_mirror row_mask:0xf bank_mask:0xf\n"
"v_mov_b32 v249, v248\n"
"v_mfma_f32_16x16x32_bf16 v[48:51], a[68:71], a[112:115], v[48:51]\n"
"ds_read_b128 a[40:43], v232 offset:4096\n"
"v_permlane16_swap_b32 v248, v249\n"
"v_add_f32 v248, v248, v249\n"
"v_mfma_f32_16x16x32_bf16 v[52:55], a[68:71], a[116:119], v[52:55]\n"
"buffer_load_dwordx4 v236, s[24:27], s54 offen offset:3072 lds\n"
"v_mov_b32 v249, v248\n"
"s_nop 1\n"
"v_permlane32_swap_b32 v248, v249\n"
"v_mfma_f32_16x16x32_bf16 v[56:59], a[68:71], a[120:123], v[56:59]\n"
"ds_read_b64_tr_b16 a[8:9], v226 offset:0\n"
"v_add_f32 v248, v248, v249\n"
"v_mfma_f32_16x16x32_bf16 v[60:63], a[68:71], a[124:127], v[60:63]\n"
"v_mul_f32 v248, 0x3b000000, v248\n"
"v_sub_f32 v240, v240, v248\n"
"s_add_u32 m0, s52, 36864\n"
"s_mov_b32 s54, 33408\n"
"v_mfma_f32_16x16x32_bf16 v[64:67], a[72:75], a[96:99], v[64:67]\n"
"ds_read_b64_tr_b16 a[10:11], v226 offset:1024\n"
"v_sub_f32 v241, v241, v248\n"
"v_sub_f32 v242, v242, v248\n"
"v_mfma_f32_16x16x32_bf16 v[68:71], a[72:75], a[100:103], v[68:71]\n"
"buffer_load_dwordx4 v233, s[24:27], s54 offen offset:0 lds\n"
"v_sub_f32 v243, v243, v248\n"
"v_sub_f32 v244, v244, v248\n"
"v_mfma_f32_16x16x32_bf16 v[72:75], a[72:75], a[104:107], v[72:75]\n"
"ds_read_b128 a[44:47], v232 offset:6144\n"
"v_sub_f32 v245, v245, v248\n"
"v_sub_f32 v246, v246, v248\n"
"v_mfma_f32_16x16x32_bf16 v[76:79], a[72:75], a[108:111], v[76:79]\n"
"v_sub_f32 v247, v247, v248\n"
"v_mul_f32 v248, v240, v240\n"
"v_mfma_f32_16x16x32_bf16 v[80:83], a[72:75], a[112:115], v[80:83]\n"
"ds_read_b64_tr_b16 a[12:13], v227 offset:0\n"
"v_fmac_f32 v248, v241, v241\n"
"v_fmac_f32 v248, v242, v242\n"
"v_mfma_f32_16x16x32_bf16 v[84:87], a[72:75], a[116:119], v[84:87]\n"
"buffer_load_dwordx4 v234, s[24:27], s54 offen offset:1024 lds\n"
"v_fmac_f32 v248, v243, v243\n"
"v_fmac_f32 v248, v244, v244\n"
"v_mfma_f32_16x16x32_bf16 v[88:91], a[72:75], a[120:123], v[88:91]\n"
"ds_read_b64_tr_b16 a[14:15], v227 offset:1024\n"
"v_fmac_f32 v248, v245, v245\n"
"v_fmac_f32 v248, v246, v246\n"
"v_mfma_f32_16x16x32_bf16 v[92:95], a[72:75], a[124:127], v[92:95]\n"
"v_fmac_f32 v248, v247, v247\n"
"s_nop 1\n"
"v_add_f32_dpp v248, v248, v248 quad_perm:[1,0,3,2] row_mask:0xf bank_mask:0xf\n"
"v_mfma_f32_16x16x32_bf16 v[96:99], a[76:79], a[96:99], v[96:99]\n"
"ds_read_b128 a[48:51], v232 offset:16384\n"
"v_add_f32_dpp v248, v248, v248 quad_perm:[2,3,0,1] row_mask:0xf bank_mask:0xf\n"
"s_nop 1\n"
"v_add_f32_dpp v248, v248, v248 row_half_mirror row_mask:0xf bank_mask:0xf\n"
"v_mfma_f32_16x16x32_bf16 v[100:103], a[76:79], a[100:103], v[100:103]\n"
"buffer_load_dwordx4 v235, s[24:27], s54 offen offset:2048 lds\n"
"v_add_f32_dpp v248, v248, v248 row_mirror row_mask:0xf bank_mask:0xf\n"
"v_mov_b32 v249, v248\n"
"v_mfma_f32_16x16x32_bf16 v[104:107], a[76:79], a[104:107], v[104:107]\n"
"ds_read_b64_tr_b16 a[16:17], v228 offset:0\n"
"v_permlane16_swap_b32 v248, v249\n"
"v_add_f32 v248, v248, v249\n"
"v_mfma_f32_16x16x32_bf16 v[108:111], a[76:79], a[108:111], v[108:111]\n"
"v_mov_b32 v249, v248\n"
"s_nop 1\n"
"v_permlane32_swap_b32 v248, v249\n"
"v_mfma_f32_16x16x32_bf16 v[112:115], a[76:79], a[112:115], v[112:115]\n"
"ds_read_b64_tr_b16 a[18:19], v228 offset:1024\n"
"v_add_f32 v248, v248, v249\n"
"v_mfma_f32_16x16x32_bf16 v[116:119], a[76:79], a[116:119], v[116:119]\n"
"buffer_load_dwordx4 v236, s[24:27], s54 offen offset:3072 lds\n"
"v_mul_f32 v248, 0x3b000000, v248\n"
"v_add_f32 v248, 0x3727c5ac, v248\n"
"s_add_u32 m0, s53, 49152\n"
"s_mov_b32 s55, 2883584\n"
"v_mfma_f32_16x16x32_bf16 v[120:123], a[76:79], a[120:123], v[120:123]\n"
"ds_read_b128 a[52:55], v232 offset:18432\n"
"v_rsq_f32 v248, v248\n"
"s_nop 0\n"
"v_mul_f32 v248, s59, v248\n"
"v_mfma_f32_16x16x32_bf16 v[124:127], a[76:79], a[124:127], v[124:127]\n"
"buffer_load_dwordx4 v237, s[28:31], s55 offen offset:0 lds\n"
"v_bfe_u32 v249, v239, 5, 3\n"
"v_xor_b32 v249, s62, v249\n"
"v_mfma_f32_16x16x32_bf16 v[128:131], a[80:83], a[96:99], v[128:131]\n"
"ds_read_b64_tr_b16 a[20:21], v229 offset:0\n"
"v_lshlrev_b32 v249, 4, v249\n"
"v_lshlrev_b32 v250, 2, v239\n"
"v_mfma_f32_16x16x32_bf16 v[132:135], a[80:83], a[100:103], v[132:135]\n"
"v_add3_u32 v249, v249, v250, s61\n"
"v_mul_f32 v240, v240, v248\n"
"v_mfma_f32_16x16x32_bf16 v[136:139], a[80:83], a[104:107], v[136:139]\n"
"ds_read_b64_tr_b16 a[22:23], v229 offset:1024\n"
"v_mul_f32 v241, v241, v248\n"
"v_mul_f32 v242, v242, v248\n"
"v_mfma_f32_16x16x32_bf16 v[140:143], a[80:83], a[108:111], v[140:143]\n"
"v_mul_f32 v243, v243, v248\n"
"v_mul_f32 v244, v244, v248\n"
"v_mfma_f32_16x16x32_bf16 v[144:147], a[80:83], a[112:115], v[144:147]\n"
"ds_read_b128 a[56:59], v232 offset:20480\n"
"v_mul_f32 v245, v245, v248\n"
"v_mul_f32 v246, v246, v248\n"
"v_mfma_f32_16x16x32_bf16 v[148:151], a[80:83], a[116:119], v[148:151]\n"
"buffer_load_dwordx4 v238, s[28:31], s55 offen offset:1024 lds\n"
"v_mul_f32 v247, v247, v248\n"
"v_accvgpr_read_b32 v250, a208\n"
"v_mfma_f32_16x16x32_bf16 v[152:155], a[80:83], a[120:123], v[152:155]\n"
"ds_read_b64_tr_b16 a[24:25], v230 offset:0\n"
"v_accvgpr_read_b32 v251, a200\n"
"v_fma_f32 v240, v240, v250, v240\n"
"v_mfma_f32_16x16x32_bf16 v[156:159], a[80:83], a[124:127], v[156:159]\n"
"v_fmac_f32 v240, s59, v251\n"
"v_accvgpr_read_b32 v250, a209\n"
"v_mfma_f32_16x16x32_bf16 v[160:163], a[84:87], a[96:99], v[160:163]\n"
"ds_read_b64_tr_b16 a[26:27], v230 offset:1024\n"
"v_accvgpr_read_b32 v251, a201\n"
"v_fma_f32 v241, v241, v250, v241\n"
"v_mfma_f32_16x16x32_bf16 v[164:167], a[84:87], a[100:103], v[164:167]\n"
"v_fmac_f32 v241, s59, v251\n"
"v_accvgpr_read_b32 v250, a210\n"
"v_mfma_f32_16x16x32_bf16 v[168:171], a[84:87], a[104:107], v[168:171]\n"
"ds_read_b128 a[60:63], v232 offset:22528\n"
"v_accvgpr_read_b32 v251, a202\n"
"v_mfma_f32_16x16x32_bf16 v[172:175], a[84:87], a[108:111], v[172:175]\n"
"v_fma_f32 v242, v242, v250, v242\n"
"v_fmac_f32 v242, s59, v251\n"
"s_add_u32 m0, s53, 57344\n"
"v_mfma_f32_16x16x32_bf16 v[176:179], a[84:87], a[112:115], v[176:179]\n"
"ds_read_b64_tr_b16 a[28:29], v231 offset:0\n"
"v_accvgpr_read_b32 v250, a211\n"
"v_accvgpr_read_b32 v251, a203\n"
"v_mfma_f32_16x16x32_bf16 v[180:183], a[84:87], a[116:119], v[180:183]\n"
"buffer_load_dwordx4 v237, s[32:35], s55 offen offset:0 lds\n"
"v_fma_f32 v243, v243, v250, v243\n"
"v_fmac_f32 v243, s59, v251\n"
"v_mfma_f32_16x16x32_bf16 v[184:187], a[84:87], a[120:123], v[184:187]\n"
"ds_read_b64_tr_b16 a[30:31], v231 offset:1024\n"
"v_accvgpr_read_b32 v250, a212\n"
"v_accvgpr_read_b32 v251, a204\n"
"v_mfma_f32_16x16x32_bf16 v[188:191], a[84:87], a[124:127], v[188:191]\n"
"v_fma_f32 v244, v244, v250, v244\n"
"v_fmac_f32 v244, s59, v251\n"
"v_mfma_f32_16x16x32_bf16 v[192:195], a[88:91], a[96:99], v[192:195]\n"
"v_accvgpr_read_b32 v250, a213\n"
"v_accvgpr_read_b32 v251, a205\n"
"v_mfma_f32_16x16x32_bf16 v[196:199], a[88:91], a[100:103], v[196:199]\n"
"v_fma_f32 v245, v245, v250, v245\n"
"v_fmac_f32 v245, s59, v251\n"
"v_mfma_f32_16x16x32_bf16 v[200:203], a[88:91], a[104:107], v[200:203]\n"
"v_accvgpr_read_b32 v250, a214\n"
"v_accvgpr_read_b32 v251, a206\n"
"v_mfma_f32_16x16x32_bf16 v[204:207], a[88:91], a[108:111], v[204:207]\n"
"buffer_load_dwordx4 v238, s[32:35], s55 offen offset:1024 lds\n"
"v_fma_f32 v246, v246, v250, v246\n"
"v_fmac_f32 v246, s59, v251\n"
"v_mfma_f32_16x16x32_bf16 v[208:211], a[88:91], a[112:115], v[208:211]\n"
"v_accvgpr_read_b32 v250, a215\n"
"v_accvgpr_read_b32 v251, a207\n"
"v_mfma_f32_16x16x32_bf16 v[212:215], a[88:91], a[116:119], v[212:215]\n"
"v_fma_f32 v247, v247, v250, v247\n"
"v_fmac_f32 v247, s59, v251\n"
"v_mfma_f32_16x16x32_bf16 v[216:219], a[88:91], a[120:123], v[216:219]\n"
"v_cvt_pk_bf16_f32 v252, v240, v241\n"
"v_cvt_pk_bf16_f32 v253, v242, v243\n"
"v_mfma_f32_16x16x32_bf16 v[220:223], a[88:91], a[124:127], v[220:223]\n"
"v_cvt_pk_bf16_f32 v254, v244, v245\n"
"v_cvt_pk_bf16_f32 v255, v246, v247\n"
"v_mfma_f32_16x16x32_bf16 a[128:131], a[92:95], a[96:99], a[128:131]\n"
"ds_write_b128 v249, v[252:255]\n"
"v_mfma_f32_16x16x32_bf16 a[132:135], a[92:95], a[100:103], a[132:135]\n"
"v_mfma_f32_16x16x32_bf16 a[136:139], a[92:95], a[104:107], a[136:139]\n"
"s_waitcnt vmcnt(16) lgkmcnt(0)\n"
"v_mfma_f32_16x16x32_bf16 a[140:143], a[92:95], a[108:111], a[140:143]\n"
"s_barrier\n"
"v_mfma_f32_16x16x32_bf16 a[144:147], a[92:95], a[112:115], a[144:147]\n"
"v_mfma_f32_16x16x32_bf16 a[148:151], a[92:95], a[116:119], a[148:151]\n"
"v_mfma_f32_16x16x32_bf16 a[152:155], a[92:95], a[120:123], a[152:155]\n"
"v_mfma_f32_16x16x32_bf16 a[156:159], a[92:95], a[124:127], a[156:159]\n"
"v_xor_b32 v232, 64, v232\n"
"v_mfma_f32_16x16x32_bf16 v[0:3], a[0:3], a[32:35], v[0:3]\n"
"ds_read_b128 a[96:99], v232 offset:0\n"
"v_accvgpr_read_b32 v250, a196\n"
"v_lshlrev_b32 v240, 16, v250\n"
"s_add_u32 m0, s53, 0\n"
"s_mov_b32 s55, 3145728\n"
"v_mfma_f32_16x16x32_bf16 v[4:7], a[0:3], a[36:39], v[4:7]\n"
"buffer_load_dwordx4 v237, s[28:31], s55 offen offset:0 lds\n"
"v_and_b32 v241, 0xffff0000, v250\n"
"v_accvgpr_read_b32 v251, a197\n"
"v_mfma_f32_16x16x32_bf16 v[8:11], a[0:3], a[40:43], v[8:11]\n"
"ds_read_b64_tr_b16 a[64:65], v224 offset:16384\n"
"v_lshlrev_b32 v242, 16, v251\n"
"v_and_b32 v243, 0xffff0000, v251\n"
"v_mfma_f32_16x16x32_bf16 v[12:15], a[0:3], a[44:47], v[12:15]\n"
"v_accvgpr_read_b32 v250, a198\n"
"v_lshlrev_b32 v244, 16, v250\n"
"v_mfma_f32_16x16x32_bf16 v[16:19], a[0:3], a[48:51], v[16:19]\n"
"ds_read_b64_tr_b16 a[66:67], v224 offset:17408\n"
"v_and_b32 v245, 0xffff0000, v250\n"
"v_accvgpr_read_b32 v251, a199\n"
"v_mfma_f32_16x16x32_bf16 v[20:23], a[0:3], a[52:55], v[20:23]\n"
"v_lshlrev_b32 v246, 16, v251\n"
"v_and_b32 v247, 0xffff0000, v251\n"
"v_mfma_f32_16x16x32_bf16 v[24:27], a[0:3], a[56:59], v[24:27]\n"
"ds_read_b128 a[100:103], v232 offset:2048\n"
"v_add_f32 v248, v240, v241\n"
"v_add_f32 v248, v248, v242\n"
"v_mfma_f32_16x16x32_bf16 v[28:31], a[0:3], a[60:63], v[28:31]\n"
"v_add_f32 v248, v248, v243\n"
"v_add_f32 v248, v248, v244\n"
"v_mfma_f32_16x16x32_bf16 v[32:35], a[4:7], a[32:35], v[32:35]\n"
"ds_read_b64_tr_b16 a[68:69], v225 offset:16384\n"
"v_add_f32 v248, v248, v245\n"
"v_add_f32 v248, v248, v246\n"
"v_mfma_f32_16x16x32_bf16 v[36:39], a[4:7], a[36:39], v[36:39]\n"
"v_add_f32 v248, v248, v247\n"
"s_nop 1\n"
"v_add_f32_dpp v248, v248, v248 quad_perm:[1,0,3,2] row_mask:0xf bank_mask:0xf\n"
"v_mfma_f32_16x16x32_bf16 v[40:43], a[4:7], a[40:43], v[40:43]\n"
"ds_read_b64_tr_b16 a[70:71], v225 offset:17408\n"
"v_add_f32_dpp v248, v248, v248 quad_perm:[2,3,0,1] row_mask:0xf bank_mask:0xf\n"
"s_nop 1\n"
"v_add_f32_dpp v248, v248, v248 row_half_mirror row_mask:0xf bank_mask:0xf\n"
"v_mfma_f32_16x16x32_bf16 v[44:47], a[4:7], a[44:47], v[44:47]\n"
"s_nop 0\n"
"v_add_f32_dpp v248, v248, v248 row_mirror row_mask:0xf bank_mask:0xf\n"
"v_mov_b32 v249, v248\n"
"v_mfma_f32_16x16x32_bf16 v[48:51], a[4:7], a[48:51], v[48:51]\n"
"ds_read_b128 a[104:107], v232 offset:4096\n"
"v_permlane16_swap_b32 v248, v249\n"
"v_add_f32 v248, v248, v249\n"
"v_mfma_f32_16x16x32_bf16 v[52:55], a[4:7], a[52:55], v[52:55]\n"
"v_mov_b32 v249, v248\n"
"s_nop 1\n"
"v_permlane32_swap_b32 v248, v249\n"
"v_mfma_f32_16x16x32_bf16 v[56:59], a[4:7], a[56:59], v[56:59]\n"
"ds_read_b64_tr_b16 a[72:73], v226 offset:16384\n"
"v_add_f32 v248, v248, v249\n"
"v_mfma_f32_16x16x32_bf16 v[60:63], a[4:7], a[60:63], v[60:63]\n"
"buffer_load_dwordx4 v238, s[28:31], s55 offen offset:1024 lds\n"
"v_mul_f32 v248, 0x3b000000, v248\n"
"v_sub_f32 v240, v240, v248\n"
"v_mfma_f32_16x16x32_bf16 v[64:67], a[8:11], a[32:35], v[64:67]\n"
"ds_read_b64_tr_b16 a[74:75], v226 offset:17408\n"
"v_sub_f32 v241, v241, v248\n"
"v_sub_f32 v242, v242, v248\n"
"v_mfma_f32_16x16x32_bf16 v[68:71], a[8:11], a[36:39], v[68:71]\n"
"v_sub_f32 v243, v243, v248\n"
"v_sub_f32 v244, v244, v248\n"
"v_mfma_f32_16x16x32_bf16 v[72:75], a[8:11], a[40:43], v[72:75]\n"
"ds_read_b128 a[108:111], v232 offset:6144\n"
"v_sub_f32 v245, v245, v248\n"
"v_sub_f32 v246, v246, v248\n"
"v_mfma_f32_16x16x32_bf16 v[76:79], a[8:11], a[44:47], v[76:79]\n"
"v_sub_f32 v247, v247, v248\n"
"v_mul_f32 v248, v240, v240\n"
"v_mfma_f32_16x16x32_bf16 v[80:83], a[8:11], a[48:51], v[80:83]\n"
"ds_read_b64_tr_b16 a[76:77], v227 offset:16384\n"
"v_fmac_f32 v248, v241, v241\n"
"v_fmac_f32 v248, v242, v242\n"
"v_mfma_f32_16x16x32_bf16 v[84:87], a[8:11], a[52:55], v[84:87]\n"
"v_fmac_f32 v248, v243, v243\n"
"v_fmac_f32 v248, v244, v244\n"
"v_mfma_f32_16x16x32_bf16 v[88:91], a[8:11], a[56:59], v[88:91]\n"
"ds_read_b64_tr_b16 a[78:79], v227 offset:17408\n"
"v_fmac_f32 v248, v245, v245\n"
"v_fmac_f32 v248, v246, v246\n"
"v_mfma_f32_16x16x32_bf16 v[92:95], a[8:11], a[60:63], v[92:95]\n"
"v_fmac_f32 v248, v247, v247\n"
"s_nop 1\n"
"v_add_f32_dpp v248, v248, v248 quad_perm:[1,0,3,2] row_mask:0xf bank_mask:0xf\n"
"v_mfma_f32_16x16x32_bf16 v[96:99], a[12:15], a[32:35], v[96:99]\n"
"ds_read_b128 a[112:115], v232 offset:16384\n"
"v_add_f32_dpp v248, v248, v248 quad_perm:[2,3,0,1] row_mask:0xf bank_mask:0xf\n"
"s_nop 1\n"
"v_add_f32_dpp v248, v248, v248 row_half_mirror row_mask:0xf bank_mask:0xf\n"
"v_mfma_f32_16x16x32_bf16 v[100:103], a[12:15], a[36:39], v[100:103]\n"
"s_nop 0\n"
"v_add_f32_dpp v248, v248, v248 row_mirror row_mask:0xf bank_mask:0xf\n"
"v_mov_b32 v249, v248\n"
"v_mfma_f32_16x16x32_bf16 v[104:107], a[12:15], a[40:43], v[104:107]\n"
"ds_read_b64_tr_b16 a[80:81], v228 offset:16384\n"
"v_permlane16_swap_b32 v248, v249\n"
"v_add_f32 v248, v248, v249\n"
"v_mfma_f32_16x16x32_bf16 v[108:111], a[12:15], a[44:47], v[108:111]\n"
"v_mov_b32 v249, v248\n"
"s_nop 1\n"
"v_permlane32_swap_b32 v248, v249\n"
"s_add_u32 m0, s53, 8192\n"
"v_mfma_f32_16x16x32_bf16 v[112:115], a[12:15], a[48:51], v[112:115]\n"
"ds_read_b64_tr_b16 a[82:83], v228 offset:17408\n"
"v_add_f32 v248, v248, v249\n"
"v_mfma_f32_16x16x32_bf16 v[116:119], a[12:15], a[52:55], v[116:119]\n"
"buffer_load_dwordx4 v237, s[32:35], s55 offen offset:0 lds\n"
"v_mul_f32 v248, 0x3b000000, v248\n"
"v_add_f32 v248, 0x3727c5ac, v248\n"
"v_mfma_f32_16x16x32_bf16 v[120:123], a[12:15], a[56:59], v[120:123]\n"
"ds_read_b128 a[116:119], v232 offset:18432\n"
"v_rsq_f32 v248, v248\n"
"s_nop 0\n"
"v_mul_f32 v248, s59, v248\n"
"v_mfma_f32_16x16x32_bf16 v[124:127], a[12:15], a[60:63], v[124:127]\n"
"v_bfe_u32 v249, v239, 5, 3\n"
"v_xor_b32 v249, s63, v249\n"
"v_mfma_f32_16x16x32_bf16 v[128:131], a[16:19], a[32:35], v[128:131]\n"
"ds_read_b64_tr_b16 a[84:85], v229 offset:16384\n"
"v_lshlrev_b32 v249, 4, v249\n"
"v_lshlrev_b32 v250, 2, v239\n"
"v_mfma_f32_16x16x32_bf16 v[132:135], a[16:19], a[36:39], v[132:135]\n"
"v_add3_u32 v249, v249, v250, s61\n"
"v_mul_f32 v240, v240, v248\n"
"v_mfma_f32_16x16x32_bf16 v[136:139], a[16:19], a[40:43], v[136:139]\n"
"ds_read_b64_tr_b16 a[86:87], v229 offset:17408\n"
"v_mul_f32 v241, v241, v248\n"
"v_mul_f32 v242, v242, v248\n"
"v_mfma_f32_16x16x32_bf16 v[140:143], a[16:19], a[44:47], v[140:143]\n"
"v_mul_f32 v243, v243, v248\n"
"v_mul_f32 v244, v244, v248\n"
"v_mfma_f32_16x16x32_bf16 v[144:147], a[16:19], a[48:51], v[144:147]\n"
"ds_read_b128 a[120:123], v232 offset:20480\n"
"v_mul_f32 v245, v245, v248\n"
"v_mul_f32 v246, v246, v248\n"
"v_mfma_f32_16x16x32_bf16 v[148:151], a[16:19], a[52:55], v[148:151]\n"
"v_mul_f32 v247, v247, v248\n"
"v_accvgpr_read_b32 v250, a208\n"
"v_mfma_f32_16x16x32_bf16 v[152:155], a[16:19], a[56:59], v[152:155]\n"
"ds_read_b64_tr_b16 a[88:89], v230 offset:16384\n"
"v_accvgpr_read_b32 v251, a200\n"
"v_fma_f32 v240, v240, v250, v240\n"
"v_mfma_f32_16x16x32_bf16 v[156:159], a[16:19], a[60:63], v[156:159]\n"
"v_fmac_f32 v240, s59, v251\n"
"v_accvgpr_read_b32 v250, a209\n"
"v_mfma_f32_16x16x32_bf16 v[160:163], a[20:23], a[32:35], v[160:163]\n"
"ds_read_b64_tr_b16 a[90:91], v230 offset:17408\n"
"v_accvgpr_read_b32 v251, a201\n"
"v_fma_f32 v241, v241, v250, v241\n"
"v_mfma_f32_16x16x32_bf16 v[164:167], a[20:23], a[36:39], v[164:167]\n"
"v_fmac_f32 v241, s59, v251\n"
"v_accvgpr_read_b32 v250, a210\n"
"v_mfma_f32_16x16x32_bf16 v[168:171], a[20:23], a[40:43], v[168:171]\n"
"ds_read_b128 a[124:127], v232 offset:22528\n"
"v_accvgpr_read_b32 v251, a202\n"
"v_mfma_f32_16x16x32_bf16 v[172:175], a[20:23], a[44:47], v[172:175]\n"
"buffer_load_dwordx4 v238, s[32:35], s55 offen offset:1024 lds\n"
"v_fma_f32 v242, v242, v250, v242\n"
"v_fmac_f32 v242, s59, v251\n"
"v_mfma_f32_16x16x32_bf16 v[176:179], a[20:23], a[48:51], v[176:179]\n"
"ds_read_b64_tr_b16 a[92:93], v231 offset:16384\n"
"v_accvgpr_read_b32 v250, a211\n"
"v_accvgpr_read_b32 v251, a203\n"
"v_mfma_f32_16x16x32_bf16 v[180:183], a[20:23], a[52:55], v[180:183]\n"
"v_fma_f32 v243, v243, v250, v243\n"
"v_fmac_f32 v243, s59, v251\n"
"v_mfma_f32_16x16x32_bf16 v[184:187], a[20:23], a[56:59], v[184:187]\n"
"ds_read_b64_tr_b16 a[94:95], v231 offset:17408\n"
"v_accvgpr_read_b32 v250, a212\n"
"v_accvgpr_read_b32 v251, a204\n"
"v_mfma_f32_16x16x32_bf16 v[188:191], a[20:23], a[60:63], v[188:191]\n"
"v_fma_f32 v244, v244, v250, v244\n"
"v_fmac_f32 v244, s59, v251\n"
"v_mfma_f32_16x16x32_bf16 v[192:195], a[24:27], a[32:35], v[192:195]\n"
"v_accvgpr_read_b32 v250, a213\n"
"v_accvgpr_read_b32 v251, a205\n"
"v_mfma_f32_16x16x32_bf16 v[196:199], a[24:27], a[36:39], v[196:199]\n"
"v_fma_f32 v245, v245, v250, v245\n"
"v_fmac_f32 v245, s59, v251\n"
"v_mfma_f32_16x16x32_bf16 v[200:203], a[24:27], a[40:43], v[200:203]\n"
"v_accvgpr_read_b32 v250, a214\n"
"v_accvgpr_read_b32 v251, a206\n"
"v_mfma_f32_16x16x32_bf16 v[204:207], a[24:27], a[44:47], v[204:207]\n"
"v_fma_f32 v246, v246, v250, v246\n"
"v_fmac_f32 v246, s59, v251\n"
"v_mfma_f32_16x16x32_bf16 v[208:211], a[24:27], a[48:51], v[208:211]\n"
"v_accvgpr_read_b32 v250, a215\n"
"v_accvgpr_read_b32 v251, a207\n"
"v_mfma_f32_16x16x32_bf16 v[212:215], a[24:27], a[52:55], v[212:215]\n"
"v_fma_f32 v247, v247, v250, v247\n"
"v_fmac_f32 v247, s59, v251\n"
"v_mfma_f32_16x16x32_bf16 v[216:219], a[24:27], a[56:59], v[216:219]\n"
"v_cvt_pk_bf16_f32 v252, v240, v241\n"
"v_cvt_pk_bf16_f32 v253, v242, v243\n"
"v_mfma_f32_16x16x32_bf16 v[220:223], a[24:27], a[60:63], v[220:223]\n"
"v_cvt_pk_bf16_f32 v254, v244, v245\n"
"v_cvt_pk_bf16_f32 v255, v246, v247\n"
"v_mfma_f32_16x16x32_bf16 a[128:131], a[28:31], a[32:35], a[128:131]\n"
"ds_write_b128 v249, v[252:255]\n"
"v_mfma_f32_16x16x32_bf16 a[132:135], a[28:31], a[36:39], a[132:135]\n"
"v_mfma_f32_16x16x32_bf16 a[136:139], a[28:31], a[40:43], a[136:139]\n"
"s_waitcnt vmcnt(8) lgkmcnt(0)\n"
"v_mfma_f32_16x16x32_bf16 a[140:143], a[28:31], a[44:47], a[140:143]\n"
"s_barrier\n"
"v_mfma_f32_16x16x32_bf16 a[144:147], a[28:31], a[48:51], a[144:147]\n"
"v_mfma_f32_16x16x32_bf16 a[148:151], a[28:31], a[52:55], a[148:151]\n"
"v_mfma_f32_16x16x32_bf16 a[152:155], a[28:31], a[56:59], a[152:155]\n"
"v_mfma_f32_16x16x32_bf16 a[156:159], a[28:31], a[60:63], a[156:159]\n"
"v_xor_b32 v232, 64, v232\n"
"s_lshl_b32 s62, s48, 11\n"
"s_add_u32 s62, s62, 147456\n"
"v_lshrrev_b32 v248, 1, v239\n"
"v_add_u32 v248, s62, v248\n"
"v_mfma_f32_16x16x32_bf16 v[0:3], a[64:67], a[96:99], v[0:3]\n"
"ds_read_b128 v[240:243], v248\n"
"ds_read_b128 v[244:247], v248 offset:1024\n"
"ds_read_b128 a[32:35], v232 offset:32768\n"
"s_add_u32 m0, s52, 0\n"
"s_mov_b32 s54, 768\n"
"v_mfma_f32_16x16x32_bf16 v[4:7], a[64:67], a[100:103], v[4:7]\n"
"buffer_load_dwordx4 v233, s[24:27], s54 offen offset:0 lds\n"
"v_lshrrev_b32 v249, 8, v239\n"
"v_mfma_f32_16x16x32_bf16 v[8:11], a[64:67], a[104:107], v[8:11]\n"
"ds_read_b64_tr_b16 a[0:1], v224 offset:32768\n"
"v_bfe_u32 v250, v239, 5, 3\n"
"v_mfma_f32_16x16x32_bf16 v[12:15], a[64:67], a[108:111], v[12:15]\n"
"v_xor_b32 v250, v250, v249\n"
"s_lshl_b32 s58, s49, 2\n"
"s_add_u32 s58, s58, s48\n"
"s_lshl_b32 s58, s58, 12\n"
"v_mfma_f32_16x16x32_bf16 v[16:19], a[64:67], a[112:115], v[16:19]\n"
"ds_read_b64_tr_b16 a[2:3], v224 offset:33792\n"
"v_lshlrev_b32 v249, 9, v249\n"
"v_mfma_f32_16x16x32_bf16 v[20:23], a[64:67], a[116:119], v[20:23]\n"
"buffer_load_dwordx4 v234, s[24:27], s54 offen offset:1024 lds\n"
"v_lshlrev_b32 v250, 4, v250\n"
"s_mov_b64 s[80:81], s[22:23]\n"
"s_add_u32 s80, s80, s58\n"
"s_addc_u32 s81, s81, 0\n"
"v_mfma_f32_16x16x32_bf16 v[24:27], a[64:67], a[120:123], v[24:27]\n"
"ds_read_b128 a[36:39], v232 offset:34816\n"
"v_or_b32 v249, v249, v250\n"
"v_mfma_f32_16x16x32_bf16 v[28:31], a[64:67], a[124:127], v[28:31]\n"
"s_and_b32 s81, s81, 0xffff\n"
"s_or_b32 s81, s81, 0x20000\n"
"s_mov_b32 s82, 0x10000\n"
"v_mfma_f32_16x16x32_bf16 v[32:35], a[68:71], a[96:99], v[32:35]\n"
"ds_read_b64_tr_b16 a[4:5], v225 offset:32768\n"
"v_mfma_f32_16x16x32_bf16 v[36:39], a[68:71], a[100:103], v[36:39]\n"
"s_waitcnt lgkmcnt(5)\n"
"buffer_load_dwordx4 v235, s[24:27], s54 offen offset:2048 lds\n"
"s_mov_b32 s83, 0x20000\n"
"v_mfma_f32_16x16x32_bf16 v[40:43], a[68:71], a[104:107], v[40:43]\n"
"ds_read_b64_tr_b16 a[6:7], v225 offset:33792\n"
"v_mfma_f32_16x16x32_bf16 v[44:47], a[68:71], a[108:111], v[44:47]\n"
"v_mfma_f32_16x16x32_bf16 v[48:51], a[68:71], a[112:115], v[48:51]\n"
"ds_read_b128 a[40:43], v232 offset:36864\n"
"v_mfma_f32_16x16x32_bf16 v[52:55], a[68:71], a[116:119], v[52:55]\n"
"buffer_load_dwordx4 v236, s[24:27], s54 offen offset:3072 lds\n"
"v_mfma_f32_16x16x32_bf16 v[56:59], a[68:71], a[120:123], v[56:59]\n"
"ds_read_b64_tr_b16 a[8:9], v226 offset:32768\n"
"v_mfma_f32_16x16x32_bf16 v[60:63], a[68:71], a[124:127], v[60:63]\n"
"s_add_u32 m0, s52, 4096\n"
"s_mov_b32 s54, 33536\n"
"v_mfma_f32_16x16x32_bf16 v[64:67], a[72:75], a[96:99], v[64:67]\n"
"ds_read_b64_tr_b16 a[10:11], v226 offset:33792\n"
"v_mfma_f32_16x16x32_bf16 v[68:71], a[72:75], a[100:103], v[68:71]\n"
"buffer_load_dwordx4 v233, s[24:27], s54 offen offset:0 lds\n"
"v_mfma_f32_16x16x32_bf16 v[72:75], a[72:75], a[104:107], v[72:75]\n"
"ds_read_b128 a[44:47], v232 offset:38912\n"
"v_mfma_f32_16x16x32_bf16 v[76:79], a[72:75], a[108:111], v[76:79]\n"
"v_mfma_f32_16x16x32_bf16 v[80:83], a[72:75], a[112:115], v[80:83]\n"
"ds_read_b64_tr_b16 a[12:13], v227 offset:32768\n"
"v_mfma_f32_16x16x32_bf16 v[84:87], a[72:75], a[116:119], v[84:87]\n"
"buffer_load_dwordx4 v234, s[24:27], s54 offen offset:1024 lds\n"
"v_mfma_f32_16x16x32_bf16 v[88:91], a[72:75], a[120:123], v[88:91]\n"
"ds_read_b64_tr_b16 a[14:15], v227 offset:33792\n"
"v_mfma_f32_16x16x32_bf16 v[92:95], a[72:75], a[124:127], v[92:95]\n"
"v_mfma_f32_16x16x32_bf16 v[96:99], a[76:79], a[96:99], v[96:99]\n"
"ds_read_b128 a[48:51], v232 offset:49152\n"
"v_mfma_f32_16x16x32_bf16 v[100:103], a[76:79], a[100:103], v[100:103]\n"
"buffer_load_dwordx4 v235, s[24:27], s54 offen offset:2048 lds\n"
"v_mfma_f32_16x16x32_bf16 v[104:107], a[76:79], a[104:107], v[104:107]\n"
"ds_read_b64_tr_b16 a[16:17], v228 offset:32768\n"
"v_mfma_f32_16x16x32_bf16 v[108:111], a[76:79], a[108:111], v[108:111]\n"
"v_mfma_f32_16x16x32_bf16 v[112:115], a[76:79], a[112:115], v[112:115]\n"
"ds_read_b64_tr_b16 a[18:19], v228 offset:33792\n"
"v_mfma_f32_16x16x32_bf16 v[116:119], a[76:79], a[116:119], v[116:119]\n"
"buffer_load_dwordx4 v236, s[24:27], s54 offen offset:3072 lds\n"
"v_mfma_f32_16x16x32_bf16 v[120:123], a[76:79], a[120:123], v[120:123]\n"
"ds_read_b128 a[52:55], v232 offset:51200\n"
"v_mfma_f32_16x16x32_bf16 v[124:127], a[76:79], a[124:127], v[124:127]\n"
"buffer_store_dwordx4 v[240:243], v249, s[44:47], s74 offen sc1\n"
"v_mfma_f32_16x16x32_bf16 v[128:131], a[80:83], a[96:99], v[128:131]\n"
"ds_read_b64_tr_b16 a[20:21], v229 offset:32768\n"
"v_mfma_f32_16x16x32_bf16 v[132:135], a[80:83], a[100:103], v[132:135]\n"
"v_mfma_f32_16x16x32_bf16 v[136:139], a[80:83], a[104:107], v[136:139]\n"
"ds_read_b64_tr_b16 a[22:23], v229 offset:33792\n"
"v_mfma_f32_16x16x32_bf16 v[140:143], a[80:83], a[108:111], v[140:143]\n"
"buffer_store_dwordx4 v[244:247], v249, s[44:47], s75 offen sc1\n"
"v_mfma_f32_16x16x32_bf16 v[144:147], a[80:83], a[112:115], v[144:147]\n"
"ds_read_b128 a[56:59], v232 offset:53248\n"
"v_mfma_f32_16x16x32_bf16 v[148:151], a[80:83], a[116:119], v[148:151]\n"
"s_add_u32 m0, s53, 16384\n"
"s_mov_b32 s55, 3407872\n"
"v_mfma_f32_16x16x32_bf16 v[152:155], a[80:83], a[120:123], v[152:155]\n"
"ds_read_b64_tr_b16 a[24:25], v230 offset:32768\n"
"v_mfma_f32_16x16x32_bf16 v[156:159], a[80:83], a[124:127], v[156:159]\n"
"buffer_load_dwordx4 v237, s[28:31], s55 offen offset:0 lds\n"
"v_mfma_f32_16x16x32_bf16 v[160:163], a[84:87], a[96:99], v[160:163]\n"
"ds_read_b64_tr_b16 a[26:27], v230 offset:33792\n"
"v_mfma_f32_16x16x32_bf16 v[164:167], a[84:87], a[100:103], v[164:167]\n"
"v_mfma_f32_16x16x32_bf16 v[168:171], a[84:87], a[104:107], v[168:171]\n"
"ds_read_b128 a[60:63], v232 offset:55296\n"
"v_mfma_f32_16x16x32_bf16 v[172:175], a[84:87], a[108:111], v[172:175]\n"
"buffer_load_dwordx4 v238, s[28:31], s55 offen offset:1024 lds\n"
"s_add_u32 m0, s53, 24576\n"
"v_mfma_f32_16x16x32_bf16 v[176:179], a[84:87], a[112:115], v[176:179]\n"
"ds_read_b64_tr_b16 a[28:29], v231 offset:32768\n"
"v_mfma_f32_16x16x32_bf16 v[180:183], a[84:87], a[116:119], v[180:183]\n"
"buffer_load_dwordx4 v237, s[32:35], s55 offen offset:0 lds\n"
"v_mfma_f32_16x16x32_bf16 v[184:187], a[84:87], a[120:123], v[184:187]\n"
"ds_read_b64_tr_b16 a[30:31], v231 offset:33792\n"
"v_mfma_f32_16x16x32_bf16 v[188:191], a[84:87], a[124:127], v[188:191]\n"
"v_mfma_f32_16x16x32_bf16 v[192:195], a[88:91], a[96:99], v[192:195]\n"
"v_mfma_f32_16x16x32_bf16 v[196:199], a[88:91], a[100:103], v[196:199]\n"
"buffer_load_dwordx4 v238, s[32:35], s55 offen offset:1024 lds\n"
"v_mfma_f32_16x16x32_bf16 v[200:203], a[88:91], a[104:107], v[200:203]\n"
"v_mfma_f32_16x16x32_bf16 v[204:207], a[88:91], a[108:111], v[204:207]\n"
"v_mfma_f32_16x16x32_bf16 v[208:211], a[88:91], a[112:115], v[208:211]\n"
"v_mfma_f32_16x16x32_bf16 v[212:215], a[88:91], a[116:119], v[212:215]\n"
"buffer_load_dword a255, v239, s[80:83], 0 idxen\n"
"v_mfma_f32_16x16x32_bf16 v[216:219], a[88:91], a[120:123], v[216:219]\n"
"v_mfma_f32_16x16x32_bf16 v[220:223], a[88:91], a[124:127], v[220:223]\n"
"v_mfma_f32_16x16x32_bf16 a[128:131], a[92:95], a[96:99], a[128:131]\n"
"v_mfma_f32_16x16x32_bf16 a[132:135], a[92:95], a[100:103], a[132:135]\n"
"v_mfma_f32_16x16x32_bf16 a[136:139], a[92:95], a[104:107], a[136:139]\n"
"s_waitcnt vmcnt(19) lgkmcnt(0)\n"
"v_mfma_f32_16x16x32_bf16 a[140:143], a[92:95], a[108:111], a[140:143]\n"
"s_barrier\n"
"v_mfma_f32_16x16x32_bf16 a[144:147], a[92:95], a[112:115], a[144:147]\n"
"v_mfma_f32_16x16x32_bf16 a[148:151], a[92:95], a[116:119], a[148:151]\n"
"v_mfma_f32_16x16x32_bf16 a[152:155], a[92:95], a[120:123], a[152:155]\n"
"v_mfma_f32_16x16x32_bf16 a[156:159], a[92:95], a[124:127], a[156:159]\n"
"v_xor_b32 v232, 64, v232\n"
"v_mfma_f32_16x16x32_bf16 v[0:3], a[0:3], a[32:35], v[0:3]\n"
"ds_read_b128 a[96:99], v232 offset:32768\n"
"s_add_u32 m0, s53, 32768\n"
"s_mov_b32 s55, 3670016\n"
"v_mfma_f32_16x16x32_bf16 v[4:7], a[0:3], a[36:39], v[4:7]\n"
"buffer_load_dwordx4 v237, s[28:31], s55 offen offset:0 lds\n"
"v_mfma_f32_16x16x32_bf16 v[8:11], a[0:3], a[40:43], v[8:11]\n"
"ds_read_b64_tr_b16 a[64:65], v224 offset:49152\n"
"v_mfma_f32_16x16x32_bf16 v[12:15], a[0:3], a[44:47], v[12:15]\n"
"v_mfma_f32_16x16x32_bf16 v[16:19], a[0:3], a[48:51], v[16:19]\n"
"ds_read_b64_tr_b16 a[66:67], v224 offset:50176\n"
"v_mfma_f32_16x16x32_bf16 v[20:23], a[0:3], a[52:55], v[20:23]\n"
"v_mfma_f32_16x16x32_bf16 v[24:27], a[0:3], a[56:59], v[24:27]\n"
"ds_read_b128 a[100:103], v232 offset:34816\n"
"v_mfma_f32_16x16x32_bf16 v[28:31], a[0:3], a[60:63], v[28:31]\n"
"v_mfma_f32_16x16x32_bf16 v[32:35], a[4:7], a[32:35], v[32:35]\n"
"ds_read_b64_tr_b16 a[68:69], v225 offset:49152\n"
"v_mfma_f32_16x16x32_bf16 v[36:39], a[4:7], a[36:39], v[36:39]\n"
"v_mfma_f32_16x16x32_bf16 v[40:43], a[4:7], a[40:43], v[40:43]\n"
"ds_read_b64_tr_b16 a[70:71], v225 offset:50176\n"
"v_mfma_f32_16x16x32_bf16 v[44:47], a[4:7], a[44:47], v[44:47]\n"
"v_mfma_f32_16x16x32_bf16 v[48:51], a[4:7], a[48:51], v[48:51]\n"
"ds_read_b128 a[104:107], v232 offset:36864\n"
"v_mfma_f32_16x16x32_bf16 v[52:55], a[4:7], a[52:55], v[52:55]\n"
"v_mfma_f32_16x16x32_bf16 v[56:59], a[4:7], a[56:59], v[56:59]\n"
"ds_read_b64_tr_b16 a[72:73], v226 offset:49152\n"
"v_mfma_f32_16x16x32_bf16 v[60:63], a[4:7], a[60:63], v[60:63]\n"
"buffer_load_dwordx4 v238, s[28:31], s55 offen offset:1024 lds\n"
"v_mfma_f32_16x16x32_bf16 v[64:67], a[8:11], a[32:35], v[64:67]\n"
"ds_read_b64_tr_b16 a[74:75], v226 offset:50176\n"
"v_mfma_f32_16x16x32_bf16 v[68:71], a[8:11], a[36:39], v[68:71]\n"
"v_mfma_f32_16x16x32_bf16 v[72:75], a[8:11], a[40:43], v[72:75]\n"
"ds_read_b128 a[108:111], v232 offset:38912\n"
"v_mfma_f32_16x16x32_bf16 v[76:79], a[8:11], a[44:47], v[76:79]\n"
"v_mfma_f32_16x16x32_bf16 v[80:83], a[8:11], a[48:51], v[80:83]\n"
"ds_read_b64_tr_b16 a[76:77], v227 offset:49152\n"
"v_mfma_f32_16x16x32_bf16 v[84:87], a[8:11], a[52:55], v[84:87]\n"
"v_mfma_f32_16x16x32_bf16 v[88:91], a[8:11], a[56:59], v[88:91]\n"
"ds_read_b64_tr_b16 a[78:79], v227 offset:50176\n"
"v_mfma_f32_16x16x32_bf16 v[92:95], a[8:11], a[60:63], v[92:95]\n"
"v_mfma_f32_16x16x32_bf16 v[96:99], a[12:15], a[32:35], v[96:99]\n"
"ds_read_b128 a[112:115], v232 offset:49152\n"
"v_mfma_f32_16x16x32_bf16 v[100:103], a[12:15], a[36:39], v[100:103]\n"
"v_mfma_f32_16x16x32_bf16 v[104:107], a[12:15], a[40:43], v[104:107]\n"
"ds_read_b64_tr_b16 a[80:81], v228 offset:49152\n"
"v_mfma_f32_16x16x32_bf16 v[108:111], a[12:15], a[44:47], v[108:111]\n"
"s_add_u32 m0, s53, 40960\n"
"v_mfma_f32_16x16x32_bf16 v[112:115], a[12:15], a[48:51], v[112:115]\n"
"ds_read_b64_tr_b16 a[82:83], v228 offset:50176\n"
"v_mfma_f32_16x16x32_bf16 v[116:119], a[12:15], a[52:55], v[116:119]\n"
"buffer_load_dwordx4 v237, s[32:35], s55 offen offset:0 lds\n"
"v_mfma_f32_16x16x32_bf16 v[120:123], a[12:15], a[56:59], v[120:123]\n"
"ds_read_b128 a[116:119], v232 offset:51200\n"
"v_mfma_f32_16x16x32_bf16 v[124:127], a[12:15], a[60:63], v[124:127]\n"
"v_mfma_f32_16x16x32_bf16 v[128:131], a[16:19], a[32:35], v[128:131]\n"
"ds_read_b64_tr_b16 a[84:85], v229 offset:49152\n"
"v_mfma_f32_16x16x32_bf16 v[132:135], a[16:19], a[36:39], v[132:135]\n"
"v_mfma_f32_16x16x32_bf16 v[136:139], a[16:19], a[40:43], v[136:139]\n"
"ds_read_b64_tr_b16 a[86:87], v229 offset:50176\n"
"v_mfma_f32_16x16x32_bf16 v[140:143], a[16:19], a[44:47], v[140:143]\n"
"v_mfma_f32_16x16x32_bf16 v[144:147], a[16:19], a[48:51], v[144:147]\n"
"ds_read_b128 a[120:123], v232 offset:53248\n"
"v_mfma_f32_16x16x32_bf16 v[148:151], a[16:19], a[52:55], v[148:151]\n"
"v_mfma_f32_16x16x32_bf16 v[152:155], a[16:19], a[56:59], v[152:155]\n"
"ds_read_b64_tr_b16 a[88:89], v230 offset:49152\n"
"v_mfma_f32_16x16x32_bf16 v[156:159], a[16:19], a[60:63], v[156:159]\n"
"v_mfma_f32_16x16x32_bf16 v[160:163], a[20:23], a[32:35], v[160:163]\n"
"ds_read_b64_tr_b16 a[90:91], v230 offset:50176\n"
"v_mfma_f32_16x16x32_bf16 v[164:167], a[20:23], a[36:39], v[164:167]\n"
"v_mfma_f32_16x16x32_bf16 v[168:171], a[20:23], a[40:43], v[168:171]\n"
"ds_read_b128 a[124:127], v232 offset:55296\n"
"v_mfma_f32_16x16x32_bf16 v[172:175], a[20:23], a[44:47], v[172:175]\n"
"buffer_load_dwordx4 v238, s[32:35], s55 offen offset:1024 lds\n"
"v_mfma_f32_16x16x32_bf16 v[176:179], a[20:23], a[48:51], v[176:179]\n"
"ds_read_b64_tr_b16 a[92:93], v231 offset:49152\n"
"v_mfma_f32_16x16x32_bf16 v[180:183], a[20:23], a[52:55], v[180:183]\n"
"v_mfma_f32_16x16x32_bf16 v[184:187], a[20:23], a[56:59], v[184:187]\n"
"ds_read_b64_tr_b16 a[94:95], v231 offset:50176\n"
"v_mfma_f32_16x16x32_bf16 v[188:191], a[20:23], a[60:63], v[188:191]\n"
"v_mfma_f32_16x16x32_bf16 v[192:195], a[24:27], a[32:35], v[192:195]\n"
"v_mfma_f32_16x16x32_bf16 v[196:199], a[24:27], a[36:39], v[196:199]\n"
"v_mfma_f32_16x16x32_bf16 v[200:203], a[24:27], a[40:43], v[200:203]\n"
"v_mfma_f32_16x16x32_bf16 v[204:207], a[24:27], a[44:47], v[204:207]\n"
"v_mfma_f32_16x16x32_bf16 v[208:211], a[24:27], a[48:51], v[208:211]\n"
"v_mfma_f32_16x16x32_bf16 v[212:215], a[24:27], a[52:55], v[212:215]\n"
"v_mfma_f32_16x16x32_bf16 v[216:219], a[24:27], a[56:59], v[216:219]\n"
"v_mfma_f32_16x16x32_bf16 v[220:223], a[24:27], a[60:63], v[220:223]\n"
"v_mfma_f32_16x16x32_bf16 a[128:131], a[28:31], a[32:35], a[128:131]\n"
"v_mfma_f32_16x16x32_bf16 a[132:135], a[28:31], a[36:39], a[132:135]\n"
"v_mfma_f32_16x16x32_bf16 a[136:139], a[28:31], a[40:43], a[136:139]\n"
"s_waitcnt vmcnt(9) lgkmcnt(0)\n"
"v_mfma_f32_16x16x32_bf16 a[140:143], a[28:31], a[44:47], a[140:143]\n"
"s_barrier\n"
"v_mfma_f32_16x16x32_bf16 a[144:147], a[28:31], a[48:51], a[144:147]\n"
"v_mfma_f32_16x16x32_bf16 a[148:151], a[28:31], a[52:55], a[148:151]\n"
"v_mfma_f32_16x16x32_bf16 a[152:155], a[28:31], a[56:59], a[152:155]\n"
"v_mfma_f32_16x16x32_bf16 a[156:159], a[28:31], a[60:63], a[156:159]\n"
"v_xor_b32 v232, 64, v232\n"
"v_mfma_f32_16x16x32_bf16 v[0:3], a[64:67], a[96:99], v[0:3]\n"
"ds_read_b128 a[32:35], v232 offset:0\n"
"s_add_u32 m0, s52, 32768\n"
"s_mov_b32 s54, 896\n"
"v_mfma_f32_16x16x32_bf16 v[4:7], a[64:67], a[100:103], v[4:7]\n"
"buffer_load_dwordx4 v233, s[24:27], s54 offen offset:0 lds\n"
"v_mfma_f32_16x16x32_bf16 v[8:11], a[64:67], a[104:107], v[8:11]\n"
"ds_read_b64_tr_b16 a[0:1], v224 offset:0\n"
"s_cmp_lg_u32 s48, 0\n"
"s_cbranch_scc1 L_nohf6_%=\n"
"v_mov_b32 v240, s96\n"
"v_mov_b32 v241, s97\n"
"s_lshl_b32 s58, s49, 3\n"
"s_add_u32 s58, s58, 16384\n"
"buffer_store_dwordx2 v[240:241], off, s[44:47], s58 sc1\n"
"L_nohf6_%=:\n"
"v_mfma_f32_16x16x32_bf16 v[12:15], a[64:67], a[108:111], v[12:15]\n"
"v_mfma_f32_16x16x32_bf16 v[16:19], a[64:67], a[112:115], v[16:19]\n"
"ds_read_b64_tr_b16 a[2:3], v224 offset:1024\n"
"v_mfma_f32_16x16x32_bf16 v[20:23], a[64:67], a[116:119], v[20:23]\n"
"buffer_load_dwordx4 v234, s[24:27], s54 offen offset:1024 lds\n"
"v_mfma_f32_16x16x32_bf16 v[24:27], a[64:67], a[120:123], v[24:27]\n"
"ds_read_b128 a[36:39], v232 offset:2048\n"
"v_mfma_f32_16x16x32_bf16 v[28:31], a[64:67], a[124:127], v[28:31]\n"
"v_mfma_f32_16x16x32_bf16 v[32:35], a[68:71], a[96:99], v[32:35]\n"
"ds_read_b64_tr_b16 a[4:5], v225 offset:0\n"
"v_mfma_f32_16x16x32_bf16 v[36:39], a[68:71], a[100:103], v[36:39]\n"
"buffer_load_dwordx4 v235, s[24:27], s54 offen offset:2048 lds\n"
"v_mfma_f32_16x16x32_bf16 v[40:43], a[68:71], a[104:107], v[40:43]\n"
"ds_read_b64_tr_b16 a[6:7], v225 offset:1024\n"
"v_mfma_f32_16x16x32_bf16 v[44:47], a[68:71], a[108:111], v[44:47]\n"
"v_mfma_f32_16x16x32_bf16 v[48:51], a[68:71], a[112:115], v[48:51]\n"
"ds_read_b128 a[40:43], v232 offset:4096\n"
"v_mfma_f32_16x16x32_bf16 v[52:55], a[68:71], a[116:119], v[52:55]\n"
"buffer_load_dwordx4 v236, s[24:27], s54 offen offset:3072 lds\n"
"v_mfma_f32_16x16x32_bf16 v[56:59], a[68:71], a[120:123], v[56:59]\n"
"ds_read_b64_tr_b16 a[8:9], v226 offset:0\n"
"v_mfma_f32_16x16x32_bf16 v[60:63], a[68:71], a[124:127], v[60:63]\n"
"s_add_u32 m0, s52, 36864\n"
"s_mov_b32 s54, 33664\n"
"v_mfma_f32_16x16x32_bf16 v[64:67], a[72:75], a[96:99], v[64:67]\n"
"ds_read_b64_tr_b16 a[10:11], v226 offset:1024\n"
"v_mfma_f32_16x16x32_bf16 v[68:71], a[72:75], a[100:103], v[68:71]\n"
"buffer_load_dwordx4 v233, s[24:27], s54 offen offset:0 lds\n"
"v_mfma_f32_16x16x32_bf16 v[72:75], a[72:75], a[104:107], v[72:75]\n"
"ds_read_b128 a[44:47], v232 offset:6144\n"
"v_mfma_f32_16x16x32_bf16 v[76:79], a[72:75], a[108:111], v[76:79]\n"
"v_mfma_f32_16x16x32_bf16 v[80:83], a[72:75], a[112:115], v[80:83]\n"
"ds_read_b64_tr_b16 a[12:13], v227 offset:0\n"
"v_mfma_f32_16x16x32_bf16 v[84:87], a[72:75], a[116:119], v[84:87]\n"
"buffer_load_dwordx4 v234, s[24:27], s54 offen offset:1024 lds\n"
"v_mfma_f32_16x16x32_bf16 v[88:91], a[72:75], a[120:123], v[88:91]\n"
"ds_read_b64_tr_b16 a[14:15], v227 offset:1024\n"
"v_mfma_f32_16x16x32_bf16 v[92:95], a[72:75], a[124:127], v[92:95]\n"
"v_mfma_f32_16x16x32_bf16 v[96:99], a[76:79], a[96:99], v[96:99]\n"
"ds_read_b128 a[48:51], v232 offset:16384\n"
"v_mfma_f32_16x16x32_bf16 v[100:103], a[76:79], a[100:103], v[100:103]\n"
"buffer_load_dwordx4 v235, s[24:27], s54 offen offset:2048 lds\n"
"v_mfma_f32_16x16x32_bf16 v[104:107], a[76:79], a[104:107], v[104:107]\n"
"ds_read_b64_tr_b16 a[16:17], v228 offset:0\n"
"v_mfma_f32_16x16x32_bf16 v[108:111], a[76:79], a[108:111], v[108:111]\n"
"v_mfma_f32_16x16x32_bf16 v[112:115], a[76:79], a[112:115], v[112:115]\n"
"ds_read_b64_tr_b16 a[18:19], v228 offset:1024\n"
"v_mfma_f32_16x16x32_bf16 v[116:119], a[76:79], a[116:119], v[116:119]\n"
"buffer_load_dwordx4 v236, s[24:27], s54 offen offset:3072 lds\n"
"s_add_u32 m0, s53, 49152\n"
"s_mov_b32 s55, 3932160\n"
"v_mfma_f32_16x16x32_bf16 v[120:123], a[76:79], a[120:123], v[120:123]\n"
"ds_read_b128 a[52:55], v232 offset:18432\n"
"v_mfma_f32_16x16x32_bf16 v[124:127], a[76:79], a[124:127], v[124:127]\n"
"buffer_load_dwordx4 v237, s[28:31], s55 offen offset:0 lds\n"
"v_mfma_f32_16x16x32_bf16 v[128:131], a[80:83], a[96:99], v[128:131]\n"
"ds_read_b64_tr_b16 a[20:21], v229 offset:0\n"
"v_mfma_f32_16x16x32_bf16 v[132:135], a[80:83], a[100:103], v[132:135]\n"
"v_mfma_f32_16x16x32_bf16 v[136:139], a[80:83], a[104:107], v[136:139]\n"
"ds_read_b64_tr_b16 a[22:23], v229 offset:1024\n"
"v_mfma_f32_16x16x32_bf16 v[140:143], a[80:83], a[108:111], v[140:143]\n"
"v_mfma_f32_16x16x32_bf16 v[144:147], a[80:83], a[112:115], v[144:147]\n"
"ds_read_b128 a[56:59], v232 offset:20480\n"
"v_mfma_f32_16x16x32_bf16 v[148:151], a[80:83], a[116:119], v[148:151]\n"
"buffer_load_dwordx4 v238, s[28:31], s55 offen offset:1024 lds\n"
"v_mfma_f32_16x16x32_bf16 v[152:155], a[80:83], a[120:123], v[152:155]\n"
"ds_read_b64_tr_b16 a[24:25], v230 offset:0\n"
"v_mfma_f32_16x16x32_bf16 v[156:159], a[80:83], a[124:127], v[156:159]\n"
"v_mfma_f32_16x16x32_bf16 v[160:163], a[84:87], a[96:99], v[160:163]\n"
"ds_read_b64_tr_b16 a[26:27], v230 offset:1024\n"
"v_mfma_f32_16x16x32_bf16 v[164:167], a[84:87], a[100:103], v[164:167]\n"
"v_mfma_f32_16x16x32_bf16 v[168:171], a[84:87], a[104:107], v[168:171]\n"
"ds_read_b128 a[60:63], v232 offset:22528\n"
"v_mfma_f32_16x16x32_bf16 v[172:175], a[84:87], a[108:111], v[172:175]\n"
"s_add_u32 m0, s53, 57344\n"
"v_mfma_f32_16x16x32_bf16 v[176:179], a[84:87], a[112:115], v[176:179]\n"
"ds_read_b64_tr_b16 a[28:29], v231 offset:0\n"
"v_mfma_f32_16x16x32_bf16 v[180:183], a[84:87], a[116:119], v[180:183]\n"
"buffer_load_dwordx4 v237, s[32:35], s55 offen offset:0 lds\n"
"v_mfma_f32_16x16x32_bf16 v[184:187], a[84:87], a[120:123], v[184:187]\n"
"ds_read_b64_tr_b16 a[30:31], v231 offset:1024\n"
"v_mfma_f32_16x16x32_bf16 v[188:191], a[84:87], a[124:127], v[188:191]\n"
"v_mfma_f32_16x16x32_bf16 v[192:195], a[88:91], a[96:99], v[192:195]\n"
"v_mfma_f32_16x16x32_bf16 v[196:199], a[88:91], a[100:103], v[196:199]\n"
"v_mfma_f32_16x16x32_bf16 v[200:203], a[88:91], a[104:107], v[200:203]\n"
"v_mfma_f32_16x16x32_bf16 v[204:207], a[88:91], a[108:111], v[204:207]\n"
"buffer_load_dwordx4 v238, s[32:35], s55 offen offset:1024 lds\n"
"v_mfma_f32_16x16x32_bf16 v[208:211], a[88:91], a[112:115], v[208:211]\n"
"v_mfma_f32_16x16x32_bf16 v[212:215], a[88:91], a[116:119], v[212:215]\n"
"v_mfma_f32_16x16x32_bf16 v[216:219], a[88:91], a[120:123], v[216:219]\n"
"v_mfma_f32_16x16x32_bf16 v[220:223], a[88:91], a[124:127], v[220:223]\n"
"v_mfma_f32_16x16x32_bf16 a[128:131], a[92:95], a[96:99], a[128:131]\n"
"v_mfma_f32_16x16x32_bf16 a[132:135], a[92:95], a[100:103], a[132:135]\n"
"v_mfma_f32_16x16x32_bf16 a[136:139], a[92:95], a[104:107], a[136:139]\n"
"s_waitcnt vmcnt(17) lgkmcnt(0)\n"
"v_mfma_f32_16x16x32_bf16 a[140:143], a[92:95], a[108:111], a[140:143]\n"
"s_barrier\n"
"v_mfma_f32_16x16x32_bf16 a[144:147], a[92:95], a[112:115], a[144:147]\n"
"v_mfma_f32_16x16x32_bf16 a[148:151], a[92:95], a[116:119], a[148:151]\n"
"v_mfma_f32_16x16x32_bf16 a[152:155], a[92:95], a[120:123], a[152:155]\n"
"v_mfma_f32_16x16x32_bf16 a[156:159], a[92:95], a[124:127], a[156:159]\n"
"v_xor_b32 v232, 64, v232\n"
"s_lshl_b32 s62, s48, 2\n"
"s_lshl_b32 s63, s51, 4\n"
"s_add_u32 s63, s63, s62\n"
"s_lshl_b32 s57, s51, 5\n"
"s_sub_u32 s57, 240, s57\n"
"v_mfma_f32_16x16x32_bf16 v[0:3], a[0:3], a[32:35], v[0:3]\n"
"ds_read_b128 a[96:99], v232 offset:0\n"
"v_lshrrev_b32 v236, 5, v239\n"
"v_bfe_u32 v251, v236, 2, 1\n"
"v_mul_u32_u24 v251, s57, v251\n"
"v_mfma_f32_16x16x32_bf16 v[4:7], a[0:3], a[36:39], v[4:7]\n"
"v_and_b32 v252, 3, v236\n"
"v_mfma_f32_16x16x32_bf16 v[8:11], a[0:3], a[40:43], v[8:11]\n"
"ds_read_b64_tr_b16 a[64:65], v224 offset:16384\n"
"v_add3_u32 v250, v251, v252, s63\n"
"s_lshl_b32 s56, s50, 8\n"
"v_mfma_f32_16x16x32_bf16 v[12:15], a[0:3], a[44:47], v[12:15]\n"
"v_lshlrev_b32 v250, 3, v250\n"
"s_add_u32 s28, s22, s56\n"
"s_addc_u32 s29, s23, 0\n"
"v_mfma_f32_16x16x32_bf16 v[16:19], a[0:3], a[48:51], v[16:19]\n"
"ds_read_b64_tr_b16 a[66:67], v224 offset:17408\n"
"v_lshlrev_b32 v234, 4, v236\n"
"s_and_b32 s29, s29, 0xffff\n"
"v_mfma_f32_16x16x32_bf16 v[20:23], a[0:3], a[52:55], v[20:23]\n"
"v_lshrrev_b32 v251, 4, v236\n"
"s_mov_b32 s30, 0x3fe100\n"
"v_mfma_f32_16x16x32_bf16 v[24:27], a[0:3], a[56:59], v[24:27]\n"
"ds_read_b128 a[100:103], v232 offset:2048\n"
"v_add_u32 v233, s62, v251\n"
"s_mov_b32 s31, 0x20000\n"
"v_mfma_f32_16x16x32_bf16 v[28:31], a[0:3], a[60:63], v[28:31]\n"
"v_lshlrev_b32 v233, 13, v233\n"
"s_lshl_b32 s18, s48, 10\n"
"v_mfma_f32_16x16x32_bf16 v[32:35], a[4:7], a[32:35], v[32:35]\n"
"ds_read_b64_tr_b16 a[68:69], v225 offset:16384\n"
"v_lshlrev_b32 v251, 2, v251\n"
"s_add_u32 s32, s20, 1048576\n"
"s_addc_u32 s33, s21, 0\n"
"v_mfma_f32_16x16x32_bf16 v[36:39], a[4:7], a[36:39], v[36:39]\n"
"v_or_b32 v251, s48, v251\n"
"s_and_b32 s33, s33, 0xffff\n"
"v_mfma_f32_16x16x32_bf16 v[40:43], a[4:7], a[40:43], v[40:43]\n"
"ds_read_b64_tr_b16 a[70:71], v225 offset:17408\n"
"v_and_b32 v252, 15, v236\n"
"s_mov_b32 s34, 0x200000\n"
"v_mfma_f32_16x16x32_bf16 v[44:47], a[4:7], a[44:47], v[44:47]\n"
"v_xor_b32 v251, v251, v252\n"
"s_mov_b32 s35, 0x20000\n"
"v_mfma_f32_16x16x32_bf16 v[48:51], a[4:7], a[48:51], v[48:51]\n"
"ds_read_b128 a[104:107], v232 offset:4096\n"
"v_lshlrev_b32 v251, 4, v251\n"
"s_lshl_b32 s57, s51, 2\n"
"s_add_u32 s8, s57, s48\n"
"v_mfma_f32_16x16x32_bf16 v[52:55], a[4:7], a[52:55], v[52:55]\n"
"v_add_u32 v233, v233, v251\n"
"s_sub_u32 s9, 60, s57\n"
"s_add_u32 s9, s9, s48\n"
"v_mfma_f32_16x16x32_bf16 v[56:59], a[4:7], a[56:59], v[56:59]\n"
"ds_read_b64_tr_b16 a[72:73], v226 offset:16384\n"
"s_lshl_b32 s16, s8, 15\n"
"s_lshl_b32 s17, s9, 15\n"
"v_mfma_f32_16x16x32_bf16 v[60:63], a[4:7], a[60:63], v[60:63]\n"
"s_load_dwordx4 s[80:83], %0, 0x40\n"
"s_load_dwordx2 s[84:85], %0, 0x50\n"
"v_mfma_f32_16x16x32_bf16 v[64:67], a[8:11], a[32:35], v[64:67]\n"
"ds_read_b64_tr_b16 a[74:75], v226 offset:17408\n"
"v_mfma_f32_16x16x32_bf16 v[68:71], a[8:11], a[36:39], v[68:71]\n"
"s_movk_i32 s58, 16384\n"
"v_mfma_f32_16x16x32_bf16 v[72:75], a[8:11], a[40:43], v[72:75]\n"
"ds_read_b128 a[108:111], v232 offset:6144\n"
"v_mfma_f32_16x16x32_bf16 v[76:79], a[8:11], a[44:47], v[76:79]\n"
"buffer_load_dwordx2 v[248:249], v250, s[44:47], s58 offen sc1\n"
"v_mfma_f32_16x16x32_bf16 v[80:83], a[8:11], a[48:51], v[80:83]\n"
"ds_read_b64_tr_b16 a[76:77], v227 offset:16384\n"
"v_mfma_f32_16x16x32_bf16 v[84:87], a[8:11], a[52:55], v[84:87]\n"
"s_add_u32 m0, s18, 131072\n"
"s_mov_b32 s19, 3145728\n"
"v_mfma_f32_16x16x32_bf16 v[88:91], a[8:11], a[56:59], v[88:91]\n"
"ds_read_b64_tr_b16 a[78:79], v227 offset:17408\n"
"v_mfma_f32_16x16x32_bf16 v[92:95], a[8:11], a[60:63], v[92:95]\n"
"buffer_load_dwordx4 v233, s[28:31], s19 offen lds\n"
"v_mfma_f32_16x16x32_bf16 v[96:99], a[12:15], a[32:35], v[96:99]\n"
"ds_read_b128 a[112:115], v232 offset:16384\n"
"v_mfma_f32_16x16x32_bf16 v[100:103], a[12:15], a[36:39], v[100:103]\n"
"s_add_u32 m0, s18, 135168\n"
"s_mov_b32 s19, 3276800\n"
"v_mfma_f32_16x16x32_bf16 v[104:107], a[12:15], a[40:43], v[104:107]\n"
"ds_read_b64_tr_b16 a[80:81], v228 offset:16384\n"
"v_mfma_f32_16x16x32_bf16 v[108:111], a[12:15], a[44:47], v[108:111]\n"
"buffer_load_dwordx4 v233, s[28:31], s19 offen lds\n"
"v_mfma_f32_16x16x32_bf16 v[112:115], a[12:15], a[48:51], v[112:115]\n"
"ds_read_b64_tr_b16 a[82:83], v228 offset:17408\n"
"v_mfma_f32_16x16x32_bf16 v[116:119], a[12:15], a[52:55], v[116:119]\n"
"s_add_u32 m0, s18, 139264\n"
"s_mov_b32 s19, 3407872\n"
"v_mfma_f32_16x16x32_bf16 v[120:123], a[12:15], a[56:59], v[120:123]\n"
"ds_read_b128 a[116:119], v232 offset:18432\n"
"v_mfma_f32_16x16x32_bf16 v[124:127], a[12:15], a[60:63], v[124:127]\n"
"buffer_load_dwordx4 v233, s[28:31], s19 offen lds\n"
"v_mfma_f32_16x16x32_bf16 v[128:131], a[16:19], a[32:35], v[128:131]\n"
"ds_read_b64_tr_b16 a[84:85], v229 offset:16384\n"
"v_mfma_f32_16x16x32_bf16 v[132:135], a[16:19], a[36:39], v[132:135]\n"
"s_add_u32 m0, s18, 143360\n"
"s_mov_b32 s19, 3538944\n"
"v_mfma_f32_16x16x32_bf16 v[136:139], a[16:19], a[40:43], v[136:139]\n"
"ds_read_b64_tr_b16 a[86:87], v229 offset:17408\n"
"v_mfma_f32_16x16x32_bf16 v[140:143], a[16:19], a[44:47], v[140:143]\n"
"buffer_load_dwordx4 v233, s[28:31], s19 offen lds\n"
"v_mfma_f32_16x16x32_bf16 v[144:147], a[16:19], a[48:51], v[144:147]\n"
"ds_read_b128 a[120:123], v232 offset:20480\n"
"v_mfma_f32_16x16x32_bf16 v[148:151], a[16:19], a[52:55], v[148:151]\n"
"v_mfma_f32_16x16x32_bf16 v[152:155], a[16:19], a[56:59], v[152:155]\n"
"ds_read_b64_tr_b16 a[88:89], v230 offset:16384\n"
"v_mfma_f32_16x16x32_bf16 v[156:159], a[16:19], a[60:63], v[156:159]\n"
"s_add_u32 m0, s18, 147456\n"
"s_mov_b32 s19, 3670016\n"
"v_mfma_f32_16x16x32_bf16 v[160:163], a[20:23], a[32:35], v[160:163]\n"
"ds_read_b64_tr_b16 a[90:91], v230 offset:17408\n"
"v_mfma_f32_16x16x32_bf16 v[164:167], a[20:23], a[36:39], v[164:167]\n"
"buffer_load_dwordx4 v233, s[28:31], s19 offen lds\n"
"v_mfma_f32_16x16x32_bf16 v[168:171], a[20:23], a[40:43], v[168:171]\n"
"ds_read_b128 a[124:127], v232 offset:22528\n"
"v_mfma_f32_16x16x32_bf16 v[172:175], a[20:23], a[44:47], v[172:175]\n"
"s_add_u32 m0, s18, 151552\n"
"s_mov_b32 s19, 3801088\n"
"v_mfma_f32_16x16x32_bf16 v[176:179], a[20:23], a[48:51], v[176:179]\n"
"ds_read_b64_tr_b16 a[92:93], v231 offset:16384\n"
"v_mfma_f32_16x16x32_bf16 v[180:183], a[20:23], a[52:55], v[180:183]\n"
"buffer_load_dwordx4 v233, s[28:31], s19 offen lds\n"
"v_mfma_f32_16x16x32_bf16 v[184:187], a[20:23], a[56:59], v[184:187]\n"
"ds_read_b64_tr_b16 a[94:95], v231 offset:17408\n"
"v_mfma_f32_16x16x32_bf16 v[188:191], a[20:23], a[60:63], v[188:191]\n"
"s_add_u32 m0, s18, 155648\n"
"s_mov_b32 s19, 3932160\n"
"v_mfma_f32_16x16x32_bf16 v[192:195], a[24:27], a[32:35], v[192:195]\n"
"v_mfma_f32_16x16x32_bf16 v[196:199], a[24:27], a[36:39], v[196:199]\n"
"buffer_load_dwordx4 v233, s[28:31], s19 offen lds\n"
"v_mfma_f32_16x16x32_bf16 v[200:203], a[24:27], a[40:43], v[200:203]\n"
"v_mfma_f32_16x16x32_bf16 v[204:207], a[24:27], a[44:47], v[204:207]\n"
"s_add_u32 m0, s18, 159744\n"
"s_mov_b32 s19, 4063232\n"
"v_mfma_f32_16x16x32_bf16 v[208:211], a[24:27], a[48:51], v[208:211]\n"
"v_mfma_f32_16x16x32_bf16 v[212:215], a[24:27], a[52:55], v[212:215]\n"
"buffer_load_dwordx4 v233, s[28:31], s19 offen lds\n"
"v_mfma_f32_16x16x32_bf16 v[216:219], a[24:27], a[56:59], v[216:219]\n"
"v_mfma_f32_16x16x32_bf16 v[220:223], a[24:27], a[60:63], v[220:223]\n"
"v_mfma_f32_16x16x32_bf16 a[128:131], a[28:31], a[32:35], a[128:131]\n"
"v_mfma_f32_16x16x32_bf16 a[132:135], a[28:31], a[36:39], a[132:135]\n"
"v_mfma_f32_16x16x32_bf16 a[136:139], a[28:31], a[40:43], a[136:139]\n"
"s_waitcnt vmcnt(13) lgkmcnt(0)\n"
"v_mfma_f32_16x16x32_bf16 a[140:143], a[28:31], a[44:47], a[140:143]\n"
"s_barrier\n"
"v_mfma_f32_16x16x32_bf16 a[144:147], a[28:31], a[48:51], a[144:147]\n"
"v_mfma_f32_16x16x32_bf16 a[148:151], a[28:31], a[52:55], a[148:151]\n"
"v_mfma_f32_16x16x32_bf16 a[152:155], a[28:31], a[56:59], a[152:155]\n"
"v_mfma_f32_16x16x32_bf16 a[156:159], a[28:31], a[60:63], a[156:159]\n"
"v_xor_b32 v232, 64, v232\n"
"v_mfma_f32_16x16x32_bf16 v[0:3], a[64:67], a[96:99], v[0:3]\n"
"ds_read_b128 a[32:35], v232 offset:32768\n"
"s_add_u32 m0, s18, 16384\n"
"s_mov_b32 s19, 0\n"
"v_mfma_f32_16x16x32_bf16 v[4:7], a[64:67], a[100:103], v[4:7]\n"
"buffer_load_dwordx4 v233, s[28:31], s19 offen lds\n"
"v_lshrrev_b32 v252, 4, v236\n"
"v_mfma_f32_16x16x32_bf16 v[8:11], a[64:67], a[104:107], v[8:11]\n"
"ds_read_b64_tr_b16 a[0:1], v224 offset:32768\n"
"v_and_b32 v253, 15, v236\n"
"v_mfma_f32_16x16x32_bf16 v[12:15], a[64:67], a[108:111], v[12:15]\n"
"v_lshrrev_b32 v254, 1, v252\n"
"v_mfma_f32_16x16x32_bf16 v[16:19], a[64:67], a[112:115], v[16:19]\n"
"ds_read_b64_tr_b16 a[2:3], v224 offset:33792\n"
"v_and_b32 v252, 1, v252\n"
"v_mfma_f32_16x16x32_bf16 v[20:23], a[64:67], a[116:119], v[20:23]\n"
"v_lshlrev_b32 v254, 4, v254\n"
"v_mfma_f32_16x16x32_bf16 v[24:27], a[64:67], a[120:123], v[24:27]\n"
"ds_read_b128 a[36:39], v232 offset:34816\n"
"v_lshlrev_b32 v252, 5, v252\n"
"v_mfma_f32_16x16x32_bf16 v[28:31], a[64:67], a[124:127], v[28:31]\n"
"v_add_u32 v253, v253, v254\n"
"s_add_u32 m0, s18, 20480\n"
"s_mov_b32 s19, 131072\n"
"v_mfma_f32_16x16x32_bf16 v[32:35], a[68:71], a[96:99], v[32:35]\n"
"ds_read_b64_tr_b16 a[4:5], v225 offset:32768\n"
"v_add_u32 v253, v253, v252\n"
"v_mfma_f32_16x16x32_bf16 v[36:39], a[68:71], a[100:103], v[36:39]\n"
"buffer_load_dwordx4 v233, s[28:31], s19 offen lds\n"
"v_lshlrev_b32 v235, 4, v253\n"
"v_mfma_f32_16x16x32_bf16 v[40:43], a[68:71], a[104:107], v[40:43]\n"
"ds_read_b64_tr_b16 a[6:7], v225 offset:33792\n"
"v_lshrrev_b32 v251, 5, v236\n"
"v_mfma_f32_16x16x32_bf16 v[44:47], a[68:71], a[108:111], v[44:47]\n"
"v_bfe_u32 v252, v236, 4, 1\n"
"v_mfma_f32_16x16x32_bf16 v[48:51], a[68:71], a[112:115], v[48:51]\n"
"ds_read_b128 a[40:43], v232 offset:36864\n"
"v_bfe_u32 v253, v236, 2, 2\n"
"v_mfma_f32_16x16x32_bf16 v[52:55], a[68:71], a[116:119], v[52:55]\n"
"v_and_b32 v254, 3, v236\n"
"s_add_u32 m0, s18, 24576\n"
"s_mov_b32 s19, 262144\n"
"v_mfma_f32_16x16x32_bf16 v[56:59], a[68:71], a[120:123], v[56:59]\n"
"ds_read_b64_tr_b16 a[8:9], v226 offset:32768\n"
"v_lshlrev_b32 v255, 3, v251\n"
"v_mfma_f32_16x16x32_bf16 v[60:63], a[68:71], a[124:127], v[60:63]\n"
"buffer_load_dwordx4 v233, s[28:31], s19 offen lds\n"
"v_add_u32 v255, v255, v253\n"
"v_mfma_f32_16x16x32_bf16 v[64:67], a[72:75], a[96:99], v[64:67]\n"
"ds_read_b64_tr_b16 a[10:11], v226 offset:33792\n"
"v_lshlrev_b32 v240, 8, v255\n"
"v_mfma_f32_16x16x32_bf16 v[68:71], a[72:75], a[100:103], v[68:71]\n"
"v_lshlrev_b32 v255, 1, v252\n"
"v_mfma_f32_16x16x32_bf16 v[72:75], a[72:75], a[104:107], v[72:75]\n"
"ds_read_b128 a[44:47], v232 offset:38912\n"
"v_lshrrev_b32 v237, 1, v254\n"
"v_mfma_f32_16x16x32_bf16 v[76:79], a[72:75], a[108:111], v[76:79]\n"
"v_add_u32 v255, v255, v237\n"
"v_mfma_f32_16x16x32_bf16 v[80:83], a[72:75], a[112:115], v[80:83]\n"
"ds_read_b64_tr_b16 a[12:13], v227 offset:32768\n"
"v_lshlrev_b32 v237, 1, v251\n"
"v_mfma_f32_16x16x32_bf16 v[84:87], a[72:75], a[116:119], v[84:87]\n"
"v_xor_b32 v255, v255, v237\n"
"s_add_u32 m0, s18, 28672\n"
"s_mov_b32 s19, 393216\n"
"v_mfma_f32_16x16x32_bf16 v[88:91], a[72:75], a[120:123], v[88:91]\n"
"ds_read_b64_tr_b16 a[14:15], v227 offset:33792\n"
"v_lshlrev_b32 v237, 2, v253\n"
"v_mfma_f32_16x16x32_bf16 v[92:95], a[72:75], a[124:127], v[92:95]\n"
"buffer_load_dwordx4 v233, s[28:31], s19 offen lds\n"
"v_or_b32 v255, v255, v237\n"
"v_mfma_f32_16x16x32_bf16 v[96:99], a[76:79], a[96:99], v[96:99]\n"
"ds_read_b128 a[48:51], v232 offset:49152\n"
"v_lshlrev_b32 v255, 4, v255\n"
"v_mfma_f32_16x16x32_bf16 v[100:103], a[76:79], a[100:103], v[100:103]\n"
"v_add_u32 v240, v240, v255\n"
"v_mfma_f32_16x16x32_bf16 v[104:107], a[76:79], a[104:107], v[104:107]\n"
"ds_read_b64_tr_b16 a[16:17], v228 offset:32768\n"
"v_and_b32 v237, 1, v254\n"
"v_mfma_f32_16x16x32_bf16 v[108:111], a[76:79], a[108:111], v[108:111]\n"
"v_lshlrev_b32 v237, 3, v237\n"
"s_add_u32 m0, s18, 81920\n"
"s_mov_b32 s19, 1572864\n"
"v_mfma_f32_16x16x32_bf16 v[112:115], a[76:79], a[112:115], v[112:115]\n"
"ds_read_b64_tr_b16 a[18:19], v228 offset:33792\n"
"v_add_u32 v240, v240, v237\n"
"v_mfma_f32_16x16x32_bf16 v[116:119], a[76:79], a[116:119], v[116:119]\n"
"buffer_load_dwordx4 v233, s[28:31], s19 offen lds\n"
"v_add_u32 v241, 1024, v240\n"
"v_mfma_f32_16x16x32_bf16 v[120:123], a[76:79], a[120:123], v[120:123]\n"
"ds_read_b128 a[52:55], v232 offset:51200\n"
"v_xor_b32 v241, 16, v241\n"
"v_mfma_f32_16x16x32_bf16 v[124:127], a[76:79], a[124:127], v[124:127]\n"
"v_xor_b32 v242, 64, v240\n"
"v_mfma_f32_16x16x32_bf16 v[128:131], a[80:83], a[96:99], v[128:131]\n"
"ds_read_b64_tr_b16 a[20:21], v229 offset:32768\n"
"v_xor_b32 v243, 64, v241\n"
"v_mfma_f32_16x16x32_bf16 v[132:135], a[80:83], a[100:103], v[132:135]\n"
"v_xor_b32 v244, 128, v240\n"
"v_mfma_f32_16x16x32_bf16 v[136:139], a[80:83], a[104:107], v[136:139]\n"
"ds_read_b64_tr_b16 a[22:23], v229 offset:33792\n"
"v_xor_b32 v245, 128, v241\n"
"v_mfma_f32_16x16x32_bf16 v[140:143], a[80:83], a[108:111], v[140:143]\n"
"v_xor_b32 v246, 192, v240\n"
"s_add_u32 m0, s18, 86016\n"
"s_mov_b32 s19, 1703936\n"
"v_mfma_f32_16x16x32_bf16 v[144:147], a[80:83], a[112:115], v[144:147]\n"
"ds_read_b128 a[56:59], v232 offset:53248\n"
"v_xor_b32 v247, 192, v241\n"
"v_mfma_f32_16x16x32_bf16 v[148:151], a[80:83], a[116:119], v[148:151]\n"
"buffer_load_dwordx4 v233, s[28:31], s19 offen lds\n"
"v_mfma_f32_16x16x32_bf16 v[152:155], a[80:83], a[120:123], v[152:155]\n"
"ds_read_b64_tr_b16 a[24:25], v230 offset:32768\n"
"v_mfma_f32_16x16x32_bf16 v[156:159], a[80:83], a[124:127], v[156:159]\n"
"v_mfma_f32_16x16x32_bf16 v[160:163], a[84:87], a[96:99], v[160:163]\n"
"ds_read_b64_tr_b16 a[26:27], v230 offset:33792\n"
"s_lshl_b32 s56, s49, 2\n"
"s_add_u32 s56, s56, s48\n"
"s_min_u32 s56, s56, 959\n"
"v_mfma_f32_16x16x32_bf16 v[164:167], a[84:87], a[100:103], v[164:167]\n"
"s_add_u32 m0, s18, 90112\n"
"s_mov_b32 s19, 1835008\n"
"v_mfma_f32_16x16x32_bf16 v[168:171], a[84:87], a[104:107], v[168:171]\n"
"ds_read_b128 a[60:63], v232 offset:55296\n"
"s_cmp_ge_u32 s56, 512\n"
"s_cselect_b64 s[22:23], s[82:83], s[80:81]\n"
"s_cselect_b32 s57, 512, 0\n"
"v_mfma_f32_16x16x32_bf16 v[172:175], a[84:87], a[108:111], v[172:175]\n"
"buffer_load_dwordx4 v233, s[28:31], s19 offen lds\n"
"v_mfma_f32_16x16x32_bf16 v[176:179], a[84:87], a[112:115], v[176:179]\n"
"ds_read_b64_tr_b16 a[28:29], v231 offset:32768\n"
"s_cmp_ge_u32 s56, 736\n"
"s_cselect_b64 s[22:23], s[84:85], s[22:23]\n"
"s_cselect_b32 s57, 736, s57\n"
"v_mfma_f32_16x16x32_bf16 v[180:183], a[84:87], a[116:119], v[180:183]\n"
"v_mfma_f32_16x16x32_bf16 v[184:187], a[84:87], a[120:123], v[184:187]\n"
"ds_read_b64_tr_b16 a[30:31], v231 offset:33792\n"
"s_sub_u32 s56, s56, s57\n"
"s_lshl_b32 s56, s56, 13\n"
"v_mfma_f32_16x16x32_bf16 v[188:191], a[84:87], a[124:127], v[188:191]\n"
"v_mfma_f32_16x16x32_bf16 v[192:195], a[88:91], a[96:99], v[192:195]\n"
"s_add_u32 s22, s22, s56\n"
"s_addc_u32 s23, s23, 0\n"
"v_mfma_f32_16x16x32_bf16 v[196:199], a[88:91], a[100:103], v[196:199]\n"
"s_add_u32 m0, s18, 94208\n"
"s_mov_b32 s19, 1966080\n"
"v_mfma_f32_16x16x32_bf16 v[200:203], a[88:91], a[104:107], v[200:203]\n"
"v_mfma_f32_16x16x32_bf16 v[204:207], a[88:91], a[108:111], v[204:207]\n"
"buffer_load_dwordx4 v233, s[28:31], s19 offen lds\n"
"v_mfma_f32_16x16x32_bf16 v[208:211], a[88:91], a[112:115], v[208:211]\n"
"v_mfma_f32_16x16x32_bf16 v[212:215], a[88:91], a[116:119], v[212:215]\n"
"v_mfma_f32_16x16x32_bf16 v[216:219], a[88:91], a[120:123], v[216:219]\n"
"v_mfma_f32_16x16x32_bf16 v[220:223], a[88:91], a[124:127], v[220:223]\n"
"v_mfma_f32_16x16x32_bf16 a[128:131], a[92:95], a[96:99], a[128:131]\n"
"v_mfma_f32_16x16x32_bf16 a[132:135], a[92:95], a[100:103], a[132:135]\n"
"v_mfma_f32_16x16x32_bf16 a[136:139], a[92:95], a[104:107], a[136:139]\n"
"s_waitcnt vmcnt(16) lgkmcnt(0)\n"
"v_mfma_f32_16x16x32_bf16 a[140:143], a[92:95], a[108:111], a[140:143]\n"
"s_barrier\n"
"v_mfma_f32_16x16x32_bf16 a[144:147], a[92:95], a[112:115], a[144:147]\n"
"v_mfma_f32_16x16x32_bf16 a[148:151], a[92:95], a[116:119], a[148:151]\n"
"v_mfma_f32_16x16x32_bf16 a[152:155], a[92:95], a[120:123], a[152:155]\n"
"v_mfma_f32_16x16x32_bf16 a[156:159], a[92:95], a[124:127], a[156:159]\n"
"v_xor_b32 v232, 64, v232\n"
"v_mfma_f32_16x16x32_bf16 v[0:3], a[0:3], a[32:35], v[0:3]\n"
"ds_read_b128 a[96:99], v232 offset:32768\n"
"v_cmp_ne_u64 vcc, s[96:97], v[248:249]\n"
"v_mfma_f32_16x16x32_bf16 v[4:7], a[0:3], a[36:39], v[4:7]\n"
"s_cbranch_vccnz L_hspin7_%=\n"
"L_hspinr8_%=:\n"
"v_mfma_f32_16x16x32_bf16 v[8:11], a[0:3], a[40:43], v[8:11]\n"
"ds_read_b64_tr_b16 a[64:65], v224 offset:49152\n"
"v_mfma_f32_16x16x32_bf16 v[12:15], a[0:3], a[44:47], v[12:15]\n"
"buffer_load_dwordx4 a[160:163], v234, s[32:35], s16 offen offset:0 sc1\n"
"v_mfma_f32_16x16x32_bf16 v[16:19], a[0:3], a[48:51], v[16:19]\n"
"ds_read_b64_tr_b16 a[66:67], v224 offset:50176\n"
"v_mfma_f32_16x16x32_bf16 v[20:23], a[0:3], a[52:55], v[20:23]\n"
"v_mfma_f32_16x16x32_bf16 v[24:27], a[0:3], a[56:59], v[24:27]\n"
"ds_read_b128 a[100:103], v232 offset:34816\n"
"v_mfma_f32_16x16x32_bf16 v[28:31], a[0:3], a[60:63], v[28:31]\n"
"buffer_load_dwordx4 a[164:167], v234, s[32:35], s16 offen offset:1024 sc1\n"
"v_mfma_f32_16x16x32_bf16 v[32:35], a[4:7], a[32:35], v[32:35]\n"
"ds_read_b64_tr_b16 a[68:69], v225 offset:49152\n"
"v_mfma_f32_16x16x32_bf16 v[36:39], a[4:7], a[36:39], v[36:39]\n"
"v_mfma_f32_16x16x32_bf16 v[40:43], a[4:7], a[40:43], v[40:43]\n"
"ds_read_b64_tr_b16 a[70:71], v225 offset:50176\n"
"v_mfma_f32_16x16x32_bf16 v[44:47], a[4:7], a[44:47], v[44:47]\n"
"v_mfma_f32_16x16x32_bf16 v[48:51], a[4:7], a[48:51], v[48:51]\n"
"ds_read_b128 a[104:107], v232 offset:36864\n"
"v_mfma_f32_16x16x32_bf16 v[52:55], a[4:7], a[52:55], v[52:55]\n"
"buffer_load_dwordx4 a[168:171], v234, s[32:35], s16 offen offset:2048 sc1\n"
"v_mfma_f32_16x16x32_bf16 v[56:59], a[4:7], a[56:59], v[56:59]\n"
"ds_read_b64_tr_b16 a[72:73], v226 offset:49152\n"
"v_mfma_f32_16x16x32_bf16 v[60:63], a[4:7], a[60:63], v[60:63]\n"
"v_mfma_f32_16x16x32_bf16 v[64:67], a[8:11], a[32:35], v[64:67]\n"
"ds_read_b64_tr_b16 a[74:75], v226 offset:50176\n"
"v_mfma_f32_16x16x32_bf16 v[68:71], a[8:11], a[36:39], v[68:71]\n"
"buffer_load_dwordx4 a[172:175], v234, s[32:35], s16 offen offset:3072 sc1\n"
"v_mfma_f32_16x16x32_bf16 v[72:75], a[8:11], a[40:43], v[72:75]\n"
"ds_read_b128 a[108:111], v232 offset:38912\n"
"v_mfma_f32_16x16x32_bf16 v[76:79], a[8:11], a[44:47], v[76:79]\n"
"v_mfma_f32_16x16x32_bf16 v[80:83], a[8:11], a[48:51], v[80:83]\n"
"ds_read_b64_tr_b16 a[76:77], v227 offset:49152\n"
"v_mfma_f32_16x16x32_bf16 v[84:87], a[8:11], a[52:55], v[84:87]\n"
"buffer_load_dwordx4 a[176:179], v234, s[32:35], s17 offen offset:0 sc1\n"
"v_mfma_f32_16x16x32_bf16 v[88:91], a[8:11], a[56:59], v[88:91]\n"
"ds_read_b64_tr_b16 a[78:79], v227 offset:50176\n"
"v_mfma_f32_16x16x32_bf16 v[92:95], a[8:11], a[60:63], v[92:95]\n"
"v_mfma_f32_16x16x32_bf16 v[96:99], a[12:15], a[32:35], v[96:99]\n"
"ds_read_b128 a[112:115], v232 offset:49152\n"
"v_mfma_f32_16x16x32_bf16 v[100:103], a[12:15], a[36:39], v[100:103]\n"
"buffer_load_dwordx4 a[180:183], v234, s[32:35], s17 offen offset:1024 sc1\n"
"v_mfma_f32_16x16x32_bf16 v[104:107], a[12:15], a[40:43], v[104:107]\n"
"ds_read_b64_tr_b16 a[80:81], v228 offset:49152\n"
"v_mfma_f32_16x16x32_bf16 v[108:111], a[12:15], a[44:47], v[108:111]\n"
"v_mfma_f32_16x16x32_bf16 v[112:115], a[12:15], a[48:51], v[112:115]\n"
"ds_read_b64_tr_b16 a[82:83], v228 offset:50176\n"
"v_mfma_f32_16x16x32_bf16 v[116:119], a[12:15], a[52:55], v[116:119]\n"
"v_mfma_f32_16x16x32_bf16 v[120:123], a[12:15], a[56:59], v[120:123]\n"
"ds_read_b128 a[116:119], v232 offset:51200\n"
"v_mfma_f32_16x16x32_bf16 v[124:127], a[12:15], a[60:63], v[124:127]\n"
"buffer_load_dwordx4 a[184:187], v234, s[32:35], s17 offen offset:2048 sc1\n"
"v_mfma_f32_16x16x32_bf16 v[128:131], a[16:19], a[32:35], v[128:131]\n"
"ds_read_b64_tr_b16 a[84:85], v229 offset:49152\n"
"v_mfma_f32_16x16x32_bf16 v[132:135], a[16:19], a[36:39], v[132:135]\n"
"v_mfma_f32_16x16x32_bf16 v[136:139], a[16:19], a[40:43], v[136:139]\n"
"ds_read_b64_tr_b16 a[86:87], v229 offset:50176\n"
"v_mfma_f32_16x16x32_bf16 v[140:143], a[16:19], a[44:47], v[140:143]\n"
"buffer_load_dwordx4 a[188:191], v234, s[32:35], s17 offen offset:3072 sc1\n"
"s_add_u32 s16, s16, 4096\n"
"s_add_u32 s17, s17, 4096\n"
"v_mfma_f32_16x16x32_bf16 v[144:147], a[16:19], a[48:51], v[144:147]\n"
"ds_read_b128 a[120:123], v232 offset:53248\n"
"v_mfma_f32_16x16x32_bf16 v[148:151], a[16:19], a[52:55], v[148:151]\n"
"s_add_u32 m0, s18, 32768\n"
"s_mov_b32 s19, 524288\n"
"v_mfma_f32_16x16x32_bf16 v[152:155], a[16:19], a[56:59], v[152:155]\n"
"ds_read_b64_tr_b16 a[88:89], v230 offset:49152\n"
"v_mfma_f32_16x16x32_bf16 v[156:159], a[16:19], a[60:63], v[156:159]\n"
"buffer_load_dwordx4 v233, s[28:31], s19 offen lds\n"
"v_mfma_f32_16x16x32_bf16 v[160:163], a[20:23], a[32:35], v[160:163]\n"
"ds_read_b64_tr_b16 a[90:91], v230 offset:50176\n"
"v_mfma_f32_16x16x32_bf16 v[164:167], a[20:23], a[36:39], v[164:167]\n"
"s_add_u32 m0, s18, 36864\n"
"s_mov_b32 s19, 655360\n"
"v_mfma_f32_16x16x32_bf16 v[168:171], a[20:23], a[40:43], v[168:171]\n"
"ds_read_b128 a[124:127], v232 offset:55296\n"
"v_mfma_f32_16x16x32_bf16 v[172:175], a[20:23], a[44:47], v[172:175]\n"
"buffer_load_dwordx4 v233, s[28:31], s19 offen lds\n"
"v_mfma_f32_16x16x32_bf16 v[176:179], a[20:23], a[48:51], v[176:179]\n"
"ds_read_b64_tr_b16 a[92:93], v231 offset:49152\n"
"v_mfma_f32_16x16x32_bf16 v[180:183], a[20:23], a[52:55], v[180:183]\n"
"v_mfma_f32_16x16x32_bf16 v[184:187], a[20:23], a[56:59], v[184:187]\n"
"ds_read_b64_tr_b16 a[94:95], v231 offset:50176\n"
"v_mfma_f32_16x16x32_bf16 v[188:191], a[20:23], a[60:63], v[188:191]\n"
"s_add_u32 m0, s18, 40960\n"
"s_mov_b32 s19, 786432\n"
"v_mfma_f32_16x16x32_bf16 v[192:195], a[24:27], a[32:35], v[192:195]\n"
"v_mfma_f32_16x16x32_bf16 v[196:199], a[24:27], a[36:39], v[196:199]\n"
"buffer_load_dwordx4 v233, s[28:31], s19 offen lds\n"
"v_mfma_f32_16x16x32_bf16 v[200:203], a[24:27], a[40:43], v[200:203]\n"
"v_mfma_f32_16x16x32_bf16 v[204:207], a[24:27], a[44:47], v[204:207]\n"
"s_add_u32 m0, s18, 45056\n"
"s_mov_b32 s19, 917504\n"
"v_mfma_f32_16x16x32_bf16 v[208:211], a[24:27], a[48:51], v[208:211]\n"
"v_mfma_f32_16x16x32_bf16 v[212:215], a[24:27], a[52:55], v[212:215]\n"
"buffer_load_dwordx4 v233, s[28:31], s19 offen lds\n"
"v_mfma_f32_16x16x32_bf16 v[216:219], a[24:27], a[56:59], v[216:219]\n"
"v_mfma_f32_16x16x32_bf16 v[220:223], a[24:27], a[60:63], v[220:223]\n"
"v_mfma_f32_16x16x32_bf16 a[128:131], a[28:31], a[32:35], a[128:131]\n"
"v_mfma_f32_16x16x32_bf16 a[132:135], a[28:31], a[36:39], a[132:135]\n"
"v_mfma_f32_16x16x32_bf16 a[136:139], a[28:31], a[40:43], a[136:139]\n"
"s_waitcnt lgkmcnt(0)\n"
"v_mfma_f32_16x16x32_bf16 a[140:143], a[28:31], a[44:47], a[140:143]\n"
"s_barrier\n"
"v_mfma_f32_16x16x32_bf16 a[144:147], a[28:31], a[48:51], a[144:147]\n"
"v_mfma_f32_16x16x32_bf16 a[148:151], a[28:31], a[52:55], a[148:151]\n"
"v_mfma_f32_16x16x32_bf16 a[152:155], a[28:31], a[56:59], a[152:155]\n"
"v_mfma_f32_16x16x32_bf16 a[156:159], a[28:31], a[60:63], a[156:159]\n"
"v_mfma_f32_16x16x32_bf16 v[0:3], a[64:67], a[96:99], v[0:3]\n"
"buffer_load_dwordx4 a[192:195], v234, s[32:35], s16 offen offset:0 sc1\n"
"v_mfma_f32_16x16x32_bf16 v[4:7], a[64:67], a[100:103], v[4:7]\n"
"buffer_load_dwordx4 a[196:199], v234, s[32:35], s16 offen offset:1024 sc1\n"
"v_mfma_f32_16x16x32_bf16 v[8:11], a[64:67], a[104:107], v[8:11]\n"
"buffer_load_dwordx4 a[200:203], v234, s[32:35], s16 offen offset:2048 sc1\n"
"v_mfma_f32_16x16x32_bf16 v[12:15], a[64:67], a[108:111], v[12:15]\n"
"buffer_load_dwordx4 a[204:207], v234, s[32:35], s16 offen offset:3072 sc1\n"
"v_mfma_f32_16x16x32_bf16 v[16:19], a[64:67], a[112:115], v[16:19]\n"
"buffer_load_dwordx4 a[208:211], v234, s[32:35], s17 offen offset:0 sc1\n"
"v_mfma_f32_16x16x32_bf16 v[20:23], a[64:67], a[116:119], v[20:23]\n"
"buffer_load_dwordx4 a[212:215], v234, s[32:35], s17 offen offset:1024 sc1\n"
"v_mfma_f32_16x16x32_bf16 v[24:27], a[64:67], a[120:123], v[24:27]\n"
"buffer_load_dwordx4 a[216:219], v234, s[32:35], s17 offen offset:2048 sc1\n"
"v_mfma_f32_16x16x32_bf16 v[28:31], a[64:67], a[124:127], v[28:31]\n"
"buffer_load_dwordx4 a[220:223], v234, s[32:35], s17 offen offset:3072 sc1\n"
"s_add_u32 s16, s16, 4096\n"
"s_add_u32 s17, s17, 4096\n"
"s_add_u32 m0, s18, 49152\n"
"s_mov_b32 s19, 1048576\n"
"v_mfma_f32_16x16x32_bf16 v[32:35], a[68:71], a[96:99], v[32:35]\n"
"buffer_load_dwordx4 v233, s[28:31], s19 offen lds\n"
"s_add_u32 m0, s18, 53248\n"
"s_mov_b32 s19, 1179648\n"
"v_mfma_f32_16x16x32_bf16 v[36:39], a[68:71], a[100:103], v[36:39]\n"
"buffer_load_dwordx4 v233, s[28:31], s19 offen lds\n"
"s_add_u32 m0, s18, 57344\n"
"s_mov_b32 s19, 1310720\n"
"v_mfma_f32_16x16x32_bf16 v[40:43], a[68:71], a[104:107], v[40:43]\n"
"buffer_load_dwordx4 v233, s[28:31], s19 offen lds\n"
"s_add_u32 m0, s18, 61440\n"
"s_mov_b32 s19, 1441792\n"
"v_mfma_f32_16x16x32_bf16 v[44:47], a[68:71], a[108:111], v[44:47]\n"
"buffer_load_dwordx4 v233, s[28:31], s19 offen lds\n"
"v_mfma_f32_16x16x32_bf16 v[48:51], a[68:71], a[112:115], v[48:51]\n"
"v_cvt_pk_bf16_f32 v0, v0, v1\n"
"v_cvt_pk_bf16_f32 v1, v2, v3\n"
"v_cvt_pk_bf16_f32 v2, v4, v5\n"
"v_mfma_f32_16x16x32_bf16 v[52:55], a[68:71], a[116:119], v[52:55]\n"
"v_cvt_pk_bf16_f32 v3, v6, v7\n"
"v_cvt_pk_bf16_f32 v8, v8, v9\n"
"v_cvt_pk_bf16_f32 v9, v10, v11\n"
"v_mfma_f32_16x16x32_bf16 v[56:59], a[68:71], a[120:123], v[56:59]\n"
"v_cvt_pk_bf16_f32 v10, v12, v13\n"
"v_cvt_pk_bf16_f32 v11, v14, v15\n"
"v_permlane32_swap_b32 v0, v2\n"
"v_mfma_f32_16x16x32_bf16 v[60:63], a[68:71], a[124:127], v[60:63]\n"
"v_permlane32_swap_b32 v1, v3\n"
"buffer_store_dwordx4 v[0:3], v235, s[36:39], s64 offen offset:0 sc1\n"
"s_nop 1\n"
"v_cvt_pk_bf16_f32 v0, v16, v17\n"
"v_mfma_f32_16x16x32_bf16 v[64:67], a[72:75], a[96:99], v[64:67]\n"
"v_cvt_pk_bf16_f32 v1, v18, v19\n"
"v_cvt_pk_bf16_f32 v2, v20, v21\n"
"v_cvt_pk_bf16_f32 v3, v22, v23\n"
"v_mfma_f32_16x16x32_bf16 v[68:71], a[72:75], a[100:103], v[68:71]\n"
"v_permlane32_swap_b32 v8, v10\n"
"v_permlane32_swap_b32 v9, v11\n"
"v_accvgpr_write_b32 a32, v8\n"
"v_mfma_f32_16x16x32_bf16 v[72:75], a[72:75], a[104:107], v[72:75]\n"
"v_accvgpr_write_b32 a33, v9\n"
"v_accvgpr_write_b32 a34, v10\n"
"v_accvgpr_write_b32 a35, v11\n"
"v_mfma_f32_16x16x32_bf16 v[76:79], a[72:75], a[108:111], v[76:79]\n"
"v_cvt_pk_bf16_f32 v4, v24, v25\n"
"v_cvt_pk_bf16_f32 v5, v26, v27\n"
"v_cvt_pk_bf16_f32 v6, v28, v29\n"
"v_mfma_f32_16x16x32_bf16 v[80:83], a[72:75], a[112:115], v[80:83]\n"
"v_cvt_pk_bf16_f32 v7, v30, v31\n"
"v_permlane32_swap_b32 v0, v2\n"
"v_permlane32_swap_b32 v1, v3\n"
"v_mfma_f32_16x16x32_bf16 v[84:87], a[72:75], a[116:119], v[84:87]\n"
"v_cvt_pk_bf16_f32 v32, v32, v33\n"
"v_cvt_pk_bf16_f32 v33, v34, v35\n"
"v_cvt_pk_bf16_f32 v34, v36, v37\n"
"v_mfma_f32_16x16x32_bf16 v[88:91], a[72:75], a[120:123], v[88:91]\n"
"v_cvt_pk_bf16_f32 v35, v38, v39\n"
"v_permlane32_swap_b32 v4, v6\n"
"v_permlane32_swap_b32 v5, v7\n"
"v_mfma_f32_16x16x32_bf16 v[92:95], a[72:75], a[124:127], v[92:95]\n"
"v_cvt_pk_bf16_f32 v40, v40, v41\n"
"v_cvt_pk_bf16_f32 v41, v42, v43\n"
"v_cvt_pk_bf16_f32 v42, v44, v45\n"
"v_mfma_f32_16x16x32_bf16 v[96:99], a[76:79], a[96:99], v[96:99]\n"
"v_cvt_pk_bf16_f32 v43, v46, v47\n"
"v_permlane32_swap_b32 v32, v34\n"
"v_permlane32_swap_b32 v33, v35\n"
"v_mfma_f32_16x16x32_bf16 v[100:103], a[76:79], a[100:103], v[100:103]\n"
"buffer_store_dwordx4 v[32:35], v235, s[36:39], s64 offen offset:1024 sc1\n"
"v_cvt_pk_bf16_f32 v8, v48, v49\n"
"v_cvt_pk_bf16_f32 v9, v50, v51\n"
"v_mfma_f32_16x16x32_bf16 v[104:107], a[76:79], a[104:107], v[104:107]\n"
"v_cvt_pk_bf16_f32 v10, v52, v53\n"
"v_cvt_pk_bf16_f32 v11, v54, v55\n"
"v_permlane32_swap_b32 v40, v42\n"
"v_mfma_f32_16x16x32_bf16 v[108:111], a[76:79], a[108:111], v[108:111]\n"
"v_permlane32_swap_b32 v41, v43\n"
"v_accvgpr_write_b32 a36, v40\n"
"v_accvgpr_write_b32 a37, v41\n"
"v_mfma_f32_16x16x32_bf16 v[112:115], a[76:79], a[112:115], v[112:115]\n"
"v_accvgpr_write_b32 a38, v42\n"
"v_accvgpr_write_b32 a39, v43\n"
"v_cvt_pk_bf16_f32 v12, v56, v57\n"
"v_mfma_f32_16x16x32_bf16 v[116:119], a[76:79], a[116:119], v[116:119]\n"
"v_cvt_pk_bf16_f32 v13, v58, v59\n"
"v_cvt_pk_bf16_f32 v14, v60, v61\n"
"v_cvt_pk_bf16_f32 v15, v62, v63\n"
"v_mfma_f32_16x16x32_bf16 v[120:123], a[76:79], a[120:123], v[120:123]\n"
"v_permlane32_swap_b32 v8, v10\n"
"v_permlane32_swap_b32 v9, v11\n"
"v_cvt_pk_bf16_f32 v64, v64, v65\n"
"v_mfma_f32_16x16x32_bf16 v[124:127], a[76:79], a[124:127], v[124:127]\n"
"v_cvt_pk_bf16_f32 v65, v66, v67\n"
"v_cvt_pk_bf16_f32 v66, v68, v69\n"
"v_cvt_pk_bf16_f32 v67, v70, v71\n"
"v_mfma_f32_16x16x32_bf16 v[128:131], a[80:83], a[96:99], v[128:131]\n"
"v_permlane32_swap_b32 v12, v14\n"
"v_permlane32_swap_b32 v13, v15\n"
"v_cvt_pk_bf16_f32 v72, v72, v73\n"
"v_mfma_f32_16x16x32_bf16 v[132:135], a[80:83], a[100:103], v[132:135]\n"
"v_cvt_pk_bf16_f32 v73, v74, v75\n"
"v_cvt_pk_bf16_f32 v74, v76, v77\n"
"v_cvt_pk_bf16_f32 v75, v78, v79\n"
"v_mfma_f32_16x16x32_bf16 v[136:139], a[80:83], a[104:107], v[136:139]\n"
"v_permlane32_swap_b32 v64, v66\n"
"v_permlane32_swap_b32 v65, v67\n"
"buffer_store_dwordx4 v[64:67], v235, s[36:39], s64 offen offset:2048 sc1\n"
"v_mfma_f32_16x16x32_bf16 v[140:143], a[80:83], a[108:111], v[140:143]\n"
"v_cvt_pk_bf16_f32 v16, v80, v81\n"
"v_cvt_pk_bf16_f32 v17, v82, v83\n"
"v_cvt_pk_bf16_f32 v18, v84, v85\n"
"v_mfma_f32_16x16x32_bf16 v[144:147], a[80:83], a[112:115], v[144:147]\n"
"v_cvt_pk_bf16_f32 v19, v86, v87\n"
"v_permlane32_swap_b32 v72, v74\n"
"v_permlane32_swap_b32 v73, v75\n"
"v_mfma_f32_16x16x32_bf16 v[148:151], a[80:83], a[116:119], v[148:151]\n"
"v_accvgpr_write_b32 a40, v72\n"
"v_accvgpr_write_b32 a41, v73\n"
"v_accvgpr_write_b32 a42, v74\n"
"v_mfma_f32_16x16x32_bf16 v[152:155], a[80:83], a[120:123], v[152:155]\n"
"v_accvgpr_write_b32 a43, v75\n"
"v_cvt_pk_bf16_f32 v20, v88, v89\n"
"v_cvt_pk_bf16_f32 v21, v90, v91\n"
"v_mfma_f32_16x16x32_bf16 v[156:159], a[80:83], a[124:127], v[156:159]\n"
"v_cvt_pk_bf16_f32 v22, v92, v93\n"
"v_cvt_pk_bf16_f32 v23, v94, v95\n"
"v_permlane32_swap_b32 v16, v18\n"
"v_mfma_f32_16x16x32_bf16 v[160:163], a[84:87], a[96:99], v[160:163]\n"
"v_permlane32_swap_b32 v17, v19\n"
"v_cvt_pk_bf16_f32 v96, v96, v97\n"
"v_cvt_pk_bf16_f32 v97, v98, v99\n"
"v_mfma_f32_16x16x32_bf16 v[164:167], a[84:87], a[100:103], v[164:167]\n"
"v_cvt_pk_bf16_f32 v98, v100, v101\n"
"v_cvt_pk_bf16_f32 v99, v102, v103\n"
"v_permlane32_swap_b32 v20, v22\n"
"v_mfma_f32_16x16x32_bf16 v[168:171], a[84:87], a[104:107], v[168:171]\n"
"v_permlane32_swap_b32 v21, v23\n"
"v_cvt_pk_bf16_f32 v104, v104, v105\n"
"v_cvt_pk_bf16_f32 v105, v106, v107\n"
"v_mfma_f32_16x16x32_bf16 v[172:175], a[84:87], a[108:111], v[172:175]\n"
"v_cvt_pk_bf16_f32 v106, v108, v109\n"
"v_cvt_pk_bf16_f32 v107, v110, v111\n"
"v_permlane32_swap_b32 v96, v98\n"
"v_mfma_f32_16x16x32_bf16 v[176:179], a[84:87], a[112:115], v[176:179]\n"
"v_permlane32_swap_b32 v97, v99\n"
"buffer_store_dwordx4 v[96:99], v235, s[36:39], s64 offen offset:3072 sc1\n"
"v_cvt_pk_bf16_f32 v24, v112, v113\n"
"v_mfma_f32_16x16x32_bf16 v[180:183], a[84:87], a[116:119], v[180:183]\n"
"v_cvt_pk_bf16_f32 v25, v114, v115\n"
"v_cvt_pk_bf16_f32 v26, v116, v117\n"
"v_cvt_pk_bf16_f32 v27, v118, v119\n"
"v_mfma_f32_16x16x32_bf16 v[184:187], a[84:87], a[120:123], v[184:187]\n"
"v_permlane32_swap_b32 v104, v106\n"
"v_permlane32_swap_b32 v105, v107\n"
"v_accvgpr_write_b32 a44, v104\n"
"v_mfma_f32_16x16x32_bf16 v[188:191], a[84:87], a[124:127], v[188:191]\n"
"v_accvgpr_write_b32 a45, v105\n"
"v_accvgpr_write_b32 a46, v106\n"
"v_accvgpr_write_b32 a47, v107\n"
"v_mfma_f32_16x16x32_bf16 v[192:195], a[88:91], a[96:99], v[192:195]\n"
"v_cvt_pk_bf16_f32 v28, v120, v121\n"
"v_cvt_pk_bf16_f32 v29, v122, v123\n"
"v_cvt_pk_bf16_f32 v30, v124, v125\n"
"v_mfma_f32_16x16x32_bf16 v[196:199], a[88:91], a[100:103], v[196:199]\n"
"v_cvt_pk_bf16_f32 v31, v126, v127\n"
"v_permlane32_swap_b32 v24, v26\n"
"v_permlane32_swap_b32 v25, v27\n"
"v_mfma_f32_16x16x32_bf16 v[200:203], a[88:91], a[104:107], v[200:203]\n"
"v_cvt_pk_bf16_f32 v128, v128, v129\n"
"v_cvt_pk_bf16_f32 v129, v130, v131\n"
"v_cvt_pk_bf16_f32 v130, v132, v133\n"
"v_mfma_f32_16x16x32_bf16 v[204:207], a[88:91], a[108:111], v[204:207]\n"
"v_cvt_pk_bf16_f32 v131, v134, v135\n"
"v_permlane32_swap_b32 v28, v30\n"
"v_permlane32_swap_b32 v29, v31\n"
"v_mfma_f32_16x16x32_bf16 v[208:211], a[88:91], a[112:115], v[208:211]\n"
"v_cvt_pk_bf16_f32 v136, v136, v137\n"
"v_cvt_pk_bf16_f32 v137, v138, v139\n"
"v_cvt_pk_bf16_f32 v138, v140, v141\n"
"v_mfma_f32_16x16x32_bf16 v[212:215], a[88:91], a[116:119], v[212:215]\n"
"v_cvt_pk_bf16_f32 v139, v142, v143\n"
"v_permlane32_swap_b32 v128, v130\n"
"v_permlane32_swap_b32 v129, v131\n"
"v_mfma_f32_16x16x32_bf16 v[216:219], a[88:91], a[120:123], v[216:219]\n"
"buffer_store_dwordx4 v[128:131], v235, s[36:39], s65 offen offset:0 sc1\n"
"v_cvt_pk_bf16_f32 v32, v144, v145\n"
"v_cvt_pk_bf16_f32 v33, v146, v147\n"
"v_mfma_f32_16x16x32_bf16 v[220:223], a[88:91], a[124:127], v[220:223]\n"
"v_cvt_pk_bf16_f32 v34, v148, v149\n"
"v_cvt_pk_bf16_f32 v35, v150, v151\n"
"v_permlane32_swap_b32 v136, v138\n"
"v_mfma_f32_16x16x32_bf16 a[128:131], a[92:95], a[96:99], a[128:131]\n"
"v_permlane32_swap_b32 v137, v139\n"
"v_accvgpr_write_b32 a48, v136\n"
"v_accvgpr_write_b32 a49, v137\n"
"v_mfma_f32_16x16x32_bf16 a[132:135], a[92:95], a[100:103], a[132:135]\n"
"v_accvgpr_write_b32 a50, v138\n"
"v_accvgpr_write_b32 a51, v139\n"
"v_cvt_pk_bf16_f32 v36, v152, v153\n"
"v_mfma_f32_16x16x32_bf16 a[136:139], a[92:95], a[104:107], a[136:139]\n"
"v_cvt_pk_bf16_f32 v37, v154, v155\n"
"v_cvt_pk_bf16_f32 v38, v156, v157\n"
"v_cvt_pk_bf16_f32 v39, v158, v159\n"
"v_mfma_f32_16x16x32_bf16 a[140:143], a[92:95], a[108:111], a[140:143]\n"
"v_permlane32_swap_b32 v32, v34\n"
"v_permlane32_swap_b32 v33, v35\n"
"v_cvt_pk_bf16_f32 v160, v160, v161\n"
"v_mfma_f32_16x16x32_bf16 a[144:147], a[92:95], a[112:115], a[144:147]\n"
"v_cvt_pk_bf16_f32 v161, v162, v163\n"
"v_cvt_pk_bf16_f32 v162, v164, v165\n"
"v_cvt_pk_bf16_f32 v163, v166, v167\n"
"v_mfma_f32_16x16x32_bf16 a[148:151], a[92:95], a[116:119], a[148:151]\n"
"v_permlane32_swap_b32 v36, v38\n"
"v_permlane32_swap_b32 v37, v39\n"
"v_cvt_pk_bf16_f32 v168, v168, v169\n"
"v_mfma_f32_16x16x32_bf16 a[152:155], a[92:95], a[120:123], a[152:155]\n"
"v_cvt_pk_bf16_f32 v169, v170, v171\n"
"v_cvt_pk_bf16_f32 v170, v172, v173\n"
"v_cvt_pk_bf16_f32 v171, v174, v175\n"
"v_mfma_f32_16x16x32_bf16 a[156:159], a[92:95], a[124:127], a[156:159]\n"
"v_permlane32_swap_b32 v160, v162\n"
"v_permlane32_swap_b32 v161, v163\n"
"buffer_store_dwordx4 v[160:163], v235, s[36:39], s65 offen offset:1024 sc1\n"
"s_nop 7\n"
"v_cvt_pk_bf16_f32 v40, v176, v177\n"
"v_cvt_pk_bf16_f32 v41, v178, v179\n"
"v_cvt_pk_bf16_f32 v42, v180, v181\n"
"v_cvt_pk_bf16_f32 v43, v182, v183\n"
"v_permlane32_swap_b32 v168, v170\n"
"v_permlane32_swap_b32 v169, v171\n"
"v_accvgpr_write_b32 a52, v168\n"
"v_accvgpr_write_b32 a53, v169\n"
"v_accvgpr_write_b32 a54, v170\n"
"v_accvgpr_write_b32 a55, v171\n"
"v_cvt_pk_bf16_f32 v44, v184, v185\n"
"v_cvt_pk_bf16_f32 v45, v186, v187\n"
"v_cvt_pk_bf16_f32 v46, v188, v189\n"
"v_cvt_pk_bf16_f32 v47, v190, v191\n"
"v_permlane32_swap_b32 v40, v42\n"
"v_permlane32_swap_b32 v41, v43\n"
"v_cvt_pk_bf16_f32 v192, v192, v193\n"
"v_cvt_pk_bf16_f32 v193, v194, v195\n"
"v_cvt_pk_bf16_f32 v194, v196, v197\n"
"v_cvt_pk_bf16_f32 v195, v198, v199\n"
"v_permlane32_swap_b32 v44, v46\n"
"v_permlane32_swap_b32 v45, v47\n"
"v_cvt_pk_bf16_f32 v200, v200, v201\n"
"v_cvt_pk_bf16_f32 v201, v202, v203\n"
"v_cvt_pk_bf16_f32 v202, v204, v205\n"
"v_cvt_pk_bf16_f32 v203, v206, v207\n"
"v_permlane32_swap_b32 v192, v194\n"
"v_permlane32_swap_b32 v193, v195\n"
"buffer_store_dwordx4 v[192:195], v235, s[36:39], s65 offen offset:2048 sc1\n"
"v_cvt_pk_bf16_f32 v48, v208, v209\n"
"v_cvt_pk_bf16_f32 v49, v210, v211\n"
"v_cvt_pk_bf16_f32 v50, v212, v213\n"
"v_cvt_pk_bf16_f32 v51, v214, v215\n"
"v_permlane32_swap_b32 v200, v202\n"
"v_permlane32_swap_b32 v201, v203\n"
"v_accvgpr_write_b32 a56, v200\n"
"v_accvgpr_write_b32 a57, v201\n"
"v_accvgpr_write_b32 a58, v202\n"
"v_accvgpr_write_b32 a59, v203\n"
"v_cvt_pk_bf16_f32 v52, v216, v217\n"
"v_cvt_pk_bf16_f32 v53, v218, v219\n"
"v_cvt_pk_bf16_f32 v54, v220, v221\n"
"v_cvt_pk_bf16_f32 v55, v222, v223\n"
"v_permlane32_swap_b32 v48, v50\n"
"v_permlane32_swap_b32 v49, v51\n"
"v_accvgpr_read_b32 v64, a128\n"
"v_accvgpr_read_b32 v65, a129\n"
"v_accvgpr_read_b32 v66, a130\n"
"v_accvgpr_read_b32 v67, a131\n"
"v_accvgpr_read_b32 v68, a132\n"
"v_accvgpr_read_b32 v69, a133\n"
"v_accvgpr_read_b32 v70, a134\n"
"v_accvgpr_read_b32 v71, a135\n"
"v_cvt_pk_bf16_f32 v64, v64, v65\n"
"v_cvt_pk_bf16_f32 v65, v66, v67\n"
"v_cvt_pk_bf16_f32 v66, v68, v69\n"
"v_cvt_pk_bf16_f32 v67, v70, v71\n"
"v_permlane32_swap_b32 v52, v54\n"
"v_permlane32_swap_b32 v53, v55\n"
"v_accvgpr_read_b32 v72, a136\n"
"v_accvgpr_read_b32 v73, a137\n"
"v_accvgpr_read_b32 v74, a138\n"
"v_accvgpr_read_b32 v75, a139\n"
"v_accvgpr_read_b32 v76, a140\n"
"v_accvgpr_read_b32 v77, a141\n"
"v_accvgpr_read_b32 v78, a142\n"
"v_accvgpr_read_b32 v79, a143\n"
"v_cvt_pk_bf16_f32 v72, v72, v73\n"
"v_cvt_pk_bf16_f32 v73, v74, v75\n"
"v_cvt_pk_bf16_f32 v74, v76, v77\n"
"v_cvt_pk_bf16_f32 v75, v78, v79\n"
"v_permlane32_swap_b32 v64, v66\n"
"v_permlane32_swap_b32 v65, v67\n"
"buffer_store_dwordx4 v[64:67], v235, s[36:39], s65 offen offset:3072 sc1\n"
"v_accvgpr_read_b32 v80, a144\n"
"v_accvgpr_read_b32 v81, a145\n"
"v_accvgpr_read_b32 v82, a146\n"
"v_accvgpr_read_b32 v83, a147\n"
"v_accvgpr_read_b32 v84, a148\n"
"v_accvgpr_read_b32 v85, a149\n"
"v_accvgpr_read_b32 v86, a150\n"
"v_accvgpr_read_b32 v87, a151\n"
"v_cvt_pk_bf16_f32 v56, v80, v81\n"
"v_cvt_pk_bf16_f32 v57, v82, v83\n"
"v_cvt_pk_bf16_f32 v58, v84, v85\n"
"v_cvt_pk_bf16_f32 v59, v86, v87\n"
"v_permlane32_swap_b32 v72, v74\n"
"v_permlane32_swap_b32 v73, v75\n"
"v_accvgpr_write_b32 a60, v72\n"
"v_accvgpr_write_b32 a61, v73\n"
"v_accvgpr_write_b32 a62, v74\n"
"v_accvgpr_write_b32 a63, v75\n"
"v_accvgpr_read_b32 v88, a152\n"
"v_accvgpr_read_b32 v89, a153\n"
"v_accvgpr_read_b32 v90, a154\n"
"v_accvgpr_read_b32 v91, a155\n"
"v_accvgpr_read_b32 v92, a156\n"
"v_accvgpr_read_b32 v93, a157\n"
"v_accvgpr_read_b32 v94, a158\n"
"v_accvgpr_read_b32 v95, a159\n"
"v_cvt_pk_bf16_f32 v60, v88, v89\n"
"v_cvt_pk_bf16_f32 v61, v90, v91\n"
"v_cvt_pk_bf16_f32 v62, v92, v93\n"
"v_cvt_pk_bf16_f32 v63, v94, v95\n"
"v_permlane32_swap_b32 v56, v58\n"
"v_permlane32_swap_b32 v57, v59\n"
"v_permlane32_swap_b32 v60, v62\n"
"v_permlane32_swap_b32 v61, v63\n"
"s_branch L_join_%=\n"
"L_roleV_%=:\n"
"s_lshl_b32 s56, s76, 19\n"
"s_mov_b64 s[36:37], s[20:21]\n"
"s_add_u32 s36, s36, s56\n"
"s_addc_u32 s37, s37, 0\n"
"s_add_u32 s36, s36, 22020096\n"
"s_addc_u32 s37, s37, 0\n"
"s_and_b32 s37, s37, 0xffff\n"
"s_mov_b32 s38, 0x80000\n"
"s_mov_b32 s39, 0x20000\n"
"s_lshl_b32 s57, s51, 4\n"
"s_and_b32 s58, s48, 1\n"
"s_lshl_b32 s58, s58, 2\n"
"s_add_u32 s57, s57, s58\n"
"s_lshl_b32 s57, s57, 12\n"
"s_add_u32 s64, s57, 0\n"
"s_add_u32 s65, s57, 4096\n"
"s_add_u32 s66, s57, 8192\n"
"s_add_u32 s67, s57, 12288\n"
"s_add_u32 s68, s57, 32768\n"
"s_add_u32 s69, s57, 36864\n"
"s_add_u32 s70, s57, 40960\n"
"s_add_u32 s71, s57, 45056\n"
"s_waitcnt vmcnt(5)\n"
"s_barrier\n"
"ds_read_b128 a[32:35], v232 offset:0\n"
"ds_read_b64_tr_b16 a[0:1], v224 offset:0\n"
"ds_read_b64_tr_b16 a[2:3], v224 offset:1024\n"
"ds_read_b128 a[36:39], v232 offset:2048\n"
"ds_read_b64_tr_b16 a[4:5], v225 offset:0\n"
"ds_read_b64_tr_b16 a[6:7], v225 offset:1024\n"
"ds_read_b128 a[40:43], v232 offset:4096\n"
"ds_read_b64_tr_b16 a[8:9], v226 offset:0\n"
"ds_read_b64_tr_b16 a[10:11], v226 offset:1024\n"
"ds_read_b128 a[44:47], v232 offset:6144\n"
"ds_read_b64_tr_b16 a[12:13], v227 offset:0\n"
"ds_read_b64_tr_b16 a[14:15], v227 offset:1024\n"
"ds_read_b128 a[48:51], v232 offset:16384\n"
"ds_read_b64_tr_b16 a[16:17], v228 offset:0\n"
"ds_read_b64_tr_b16 a[18:19], v228 offset:1024\n"
"ds_read_b128 a[52:55], v232 offset:18432\n"
"ds_read_b64_tr_b16 a[20:21], v229 offset:0\n"
"ds_read_b64_tr_b16 a[22:23], v229 offset:1024\n"
"ds_read_b128 a[56:59], v232 offset:20480\n"
"ds_read_b64_tr_b16 a[24:25], v230 offset:0\n"
"ds_read_b64_tr_b16 a[26:27], v230 offset:1024\n"
"ds_read_b128 a[60:63], v232 offset:22528\n"
"ds_read_b64_tr_b16 a[28:29], v231 offset:0\n"
"ds_read_b64_tr_b16 a[30:31], v231 offset:1024\n"
"s_cmp_lg_u32 s48, 0\n"
"s_cbranch_scc1 L_nomod10_%=\n"
"s_cmp_ge_u32 s49, 192\n"
"s_cbranch_scc1 L_nomod10_%=\n"
"v_lshrrev_b32 v3, 5, v239\n"
"v_lshlrev_b32 v3, 2, v3\n"
"v_add_u32 v2, 131072, v3\n"
"ds_read2_b32 v[4:5], v2 offset0:0 offset1:16\n"
"ds_read2_b32 v[6:7], v2 offset0:32 offset1:48\n"
"s_lshl_b32 s58, s79, 6\n"
"s_add_u32 s58, s58, 4096\n"
"s_waitcnt lgkmcnt(0)\n"
"v_add_f32 v4, v4, v5\n"
"v_add_f32 v6, v6, v7\n"
"v_add_f32 v4, v4, v6\n"
"v_cvt_pk_bf16_f32 v4, v4, v4\n"
"v_lshlrev_b32 v4, 16, v4\n"
"s_mov_b64 exec, 0xffff\n"
"buffer_store_dword v4, v3, s[44:47], s58 offen sc1\n"
"s_mov_b64 exec, -1\n"
"L_nomod10_%=:\n"
"s_add_u32 m0, s52, 32768\n"
"s_mov_b32 s54, 128\n"
"s_nop 0\n"
"buffer_load_dwordx4 v233, s[24:27], s54 offen offset:0 lds\n"
"buffer_load_dwordx4 v234, s[24:27], s54 offen offset:1024 lds\n"
"buffer_load_dwordx4 v235, s[24:27], s54 offen offset:2048 lds\n"
"buffer_load_dwordx4 v236, s[24:27], s54 offen offset:3072 lds\n"
"s_add_u32 m0, s52, 36864\n"
"s_mov_b32 s54, 32896\n"
"s_nop 0\n"
"buffer_load_dwordx4 v233, s[24:27], s54 offen offset:0 lds\n"
"buffer_load_dwordx4 v234, s[24:27], s54 offen offset:1024 lds\n"
"buffer_load_dwordx4 v235, s[24:27], s54 offen offset:2048 lds\n"
"buffer_load_dwordx4 v236, s[24:27], s54 offen offset:3072 lds\n"
"s_add_u32 m0, s53, 32768\n"
"s_mov_b32 s55, 524288\n"
"s_nop 0\n"
"buffer_load_dwordx4 v237, s[28:31], s55 offen offset:0 lds\n"
"buffer_load_dwordx4 v238, s[28:31], s55 offen offset:1024 lds\n"
"s_add_u32 m0, s53, 40960\n"
"s_nop 0\n"
"buffer_load_dwordx4 v237, s[32:35], s55 offen offset:0 lds\n"
"buffer_load_dwordx4 v238, s[32:35], s55 offen offset:1024 lds\n"
"s_waitcnt vmcnt(12) lgkmcnt(0)\n"
"s_barrier\n"
"v_xor_b32 v232, 64, v232\n"
"v_mfma_f32_16x16x32_bf16 v[0:3], a[32:35], a[0:3], 0\n"
"ds_read_b128 a[96:99], v232 offset:0\n"
"s_add_u32 m0, s53, 49152\n"
"s_mov_b32 s55, 786432\n"
"v_mfma_f32_16x16x32_bf16 v[4:7], a[32:35], a[4:7], 0\n"
"buffer_load_dwordx4 v237, s[28:31], s55 offen offset:0 lds\n"
"v_mfma_f32_16x16x32_bf16 v[8:11], a[32:35], a[8:11], 0\n"
"ds_read_b64_tr_b16 a[64:65], v224 offset:16384\n"
"v_mfma_f32_16x16x32_bf16 v[12:15], a[32:35], a[12:15], 0\n"
"v_mfma_f32_16x16x32_bf16 v[16:19], a[32:35], a[16:19], 0\n"
"ds_read_b64_tr_b16 a[66:67], v224 offset:17408\n"
"v_mfma_f32_16x16x32_bf16 v[20:23], a[32:35], a[20:23], 0\n"
"v_mfma_f32_16x16x32_bf16 v[24:27], a[32:35], a[24:27], 0\n"
"ds_read_b128 a[100:103], v232 offset:2048\n"
"v_mfma_f32_16x16x32_bf16 v[28:31], a[32:35], a[28:31], 0\n"
"v_mfma_f32_16x16x32_bf16 v[32:35], a[36:39], a[0:3], 0\n"
"ds_read_b64_tr_b16 a[68:69], v225 offset:16384\n"
"v_mfma_f32_16x16x32_bf16 v[36:39], a[36:39], a[4:7], 0\n"
"buffer_load_dwordx4 v238, s[28:31], s55 offen offset:1024 lds\n"
"v_mfma_f32_16x16x32_bf16 v[40:43], a[36:39], a[8:11], 0\n"
"ds_read_b64_tr_b16 a[70:71], v225 offset:17408\n"
"v_mfma_f32_16x16x32_bf16 v[44:47], a[36:39], a[12:15], 0\n"
"v_mfma_f32_16x16x32_bf16 v[48:51], a[36:39], a[16:19], 0\n"
"ds_read_b128 a[104:107], v232 offset:4096\n"
"v_mfma_f32_16x16x32_bf16 v[52:55], a[36:39], a[20:23], 0\n"
"s_add_u32 m0, s53, 57344\n"
"v_mfma_f32_16x16x32_bf16 v[56:59], a[36:39], a[24:27], 0\n"
"ds_read_b64_tr_b16 a[72:73], v226 offset:16384\n"
"v_mfma_f32_16x16x32_bf16 v[60:63], a[36:39], a[28:31], 0\n"
"buffer_load_dwordx4 v237, s[32:35], s55 offen offset:0 lds\n"
"v_mfma_f32_16x16x32_bf16 v[64:67], a[40:43], a[0:3], 0\n"
"ds_read_b64_tr_b16 a[74:75], v226 offset:17408\n"
"v_mfma_f32_16x16x32_bf16 v[68:71], a[40:43], a[4:7], 0\n"
"v_mfma_f32_16x16x32_bf16 v[72:75], a[40:43], a[8:11], 0\n"
"ds_read_b128 a[108:111], v232 offset:6144\n"
"v_mfma_f32_16x16x32_bf16 v[76:79], a[40:43], a[12:15], 0\n"
"v_mfma_f32_16x16x32_bf16 v[80:83], a[40:43], a[16:19], 0\n"
"ds_read_b64_tr_b16 a[76:77], v227 offset:16384\n"
"v_mfma_f32_16x16x32_bf16 v[84:87], a[40:43], a[20:23], 0\n"
"v_mfma_f32_16x16x32_bf16 v[88:91], a[40:43], a[24:27], 0\n"
"ds_read_b64_tr_b16 a[78:79], v227 offset:17408\n"
"v_mfma_f32_16x16x32_bf16 v[92:95], a[40:43], a[28:31], 0\n"
"buffer_load_dwordx4 v238, s[32:35], s55 offen offset:1024 lds\n"
"v_mfma_f32_16x16x32_bf16 v[96:99], a[44:47], a[0:3], 0\n"
"ds_read_b128 a[112:115], v232 offset:16384\n"
"v_mfma_f32_16x16x32_bf16 v[100:103], a[44:47], a[4:7], 0\n"
"v_mfma_f32_16x16x32_bf16 v[104:107], a[44:47], a[8:11], 0\n"
"ds_read_b64_tr_b16 a[80:81], v228 offset:16384\n"
"v_mfma_f32_16x16x32_bf16 v[108:111], a[44:47], a[12:15], 0\n"
"s_add_u32 m0, s53, 0\n"
"s_mov_b32 s55, 1048576\n"
"v_mfma_f32_16x16x32_bf16 v[112:115], a[44:47], a[16:19], 0\n"
"ds_read_b64_tr_b16 a[82:83], v228 offset:17408\n"
"v_mfma_f32_16x16x32_bf16 v[116:119], a[44:47], a[20:23], 0\n"
"buffer_load_dwordx4 v237, s[28:31], s55 offen offset:0 lds\n"
"v_mfma_f32_16x16x32_bf16 v[120:123], a[44:47], a[24:27], 0\n"
"ds_read_b128 a[116:119], v232 offset:18432\n"
"v_mfma_f32_16x16x32_bf16 v[124:127], a[44:47], a[28:31], 0\n"
"v_mfma_f32_16x16x32_bf16 v[128:131], a[48:51], a[0:3], 0\n"
"ds_read_b64_tr_b16 a[84:85], v229 offset:16384\n"
"v_mfma_f32_16x16x32_bf16 v[132:135], a[48:51], a[4:7], 0\n"
"v_mfma_f32_16x16x32_bf16 v[136:139], a[48:51], a[8:11], 0\n"
"ds_read_b64_tr_b16 a[86:87], v229 offset:17408\n"
"v_mfma_f32_16x16x32_bf16 v[140:143], a[48:51], a[12:15], 0\n"
"v_mfma_f32_16x16x32_bf16 v[144:147], a[48:51], a[16:19], 0\n"
"ds_read_b128 a[120:123], v232 offset:20480\n"
"v_mfma_f32_16x16x32_bf16 v[148:151], a[48:51], a[20:23], 0\n"
"buffer_load_dwordx4 v238, s[28:31], s55 offen offset:1024 lds\n"
"v_mfma_f32_16x16x32_bf16 v[152:155], a[48:51], a[24:27], 0\n"
"ds_read_b64_tr_b16 a[88:89], v230 offset:16384\n"
"v_mfma_f32_16x16x32_bf16 v[156:159], a[48:51], a[28:31], 0\n"
"v_mfma_f32_16x16x32_bf16 v[160:163], a[52:55], a[0:3], 0\n"
"ds_read_b64_tr_b16 a[90:91], v230 offset:17408\n"
"v_mfma_f32_16x16x32_bf16 v[164:167], a[52:55], a[4:7], 0\n"
"s_add_u32 m0, s53, 8192\n"
"v_mfma_f32_16x16x32_bf16 v[168:171], a[52:55], a[8:11], 0\n"
"ds_read_b128 a[124:127], v232 offset:22528\n"
"v_mfma_f32_16x16x32_bf16 v[172:175], a[52:55], a[12:15], 0\n"
"buffer_load_dwordx4 v237, s[32:35], s55 offen offset:0 lds\n"
"v_mfma_f32_16x16x32_bf16 v[176:179], a[52:55], a[16:19], 0\n"
"ds_read_b64_tr_b16 a[92:93], v231 offset:16384\n"
"v_mfma_f32_16x16x32_bf16 v[180:183], a[52:55], a[20:23], 0\n"
"v_mfma_f32_16x16x32_bf16 v[184:187], a[52:55], a[24:27], 0\n"
"ds_read_b64_tr_b16 a[94:95], v231 offset:17408\n"
"v_mfma_f32_16x16x32_bf16 v[188:191], a[52:55], a[28:31], 0\n"
"v_mfma_f32_16x16x32_bf16 v[192:195], a[56:59], a[0:3], 0\n"
"v_mfma_f32_16x16x32_bf16 v[196:199], a[56:59], a[4:7], 0\n"
"v_mfma_f32_16x16x32_bf16 v[200:203], a[56:59], a[8:11], 0\n"
"v_mfma_f32_16x16x32_bf16 v[204:207], a[56:59], a[12:15], 0\n"
"buffer_load_dwordx4 v238, s[32:35], s55 offen offset:1024 lds\n"
"v_mfma_f32_16x16x32_bf16 v[208:211], a[56:59], a[16:19], 0\n"
"v_mfma_f32_16x16x32_bf16 v[212:215], a[56:59], a[20:23], 0\n"
"v_mfma_f32_16x16x32_bf16 v[216:219], a[56:59], a[24:27], 0\n"
"v_mfma_f32_16x16x32_bf16 v[220:223], a[56:59], a[28:31], 0\n"
"v_mfma_f32_16x16x32_bf16 a[128:131], a[60:63], a[0:3], 0\n"
"v_mfma_f32_16x16x32_bf16 a[132:135], a[60:63], a[4:7], 0\n"
"v_mfma_f32_16x16x32_bf16 a[136:139], a[60:63], a[8:11], 0\n"
"s_waitcnt vmcnt(8) lgkmcnt(0)\n"
"v_mfma_f32_16x16x32_bf16 a[140:143], a[60:63], a[12:15], 0\n"
"s_barrier\n"
"v_mfma_f32_16x16x32_bf16 a[144:147], a[60:63], a[16:19], 0\n"
"v_mfma_f32_16x16x32_bf16 a[148:151], a[60:63], a[20:23], 0\n"
"v_mfma_f32_16x16x32_bf16 a[152:155], a[60:63], a[24:27], 0\n"
"v_mfma_f32_16x16x32_bf16 a[156:159], a[60:63], a[28:31], 0\n"
"v_xor_b32 v232, 64, v232\n"
"v_mfma_f32_16x16x32_bf16 v[0:3], a[96:99], a[64:67], v[0:3]\n"
"ds_read_b128 a[32:35], v232 offset:32768\n"
"s_add_u32 m0, s52, 0\n"
"s_mov_b32 s54, 256\n"
"v_mfma_f32_16x16x32_bf16 v[4:7], a[96:99], a[68:71], v[4:7]\n"
"buffer_load_dwordx4 v233, s[24:27], s54 offen offset:0 lds\n"
"v_mfma_f32_16x16x32_bf16 v[8:11], a[96:99], a[72:75], v[8:11]\n"
"ds_read_b64_tr_b16 a[0:1], v224 offset:32768\n"
"v_mfma_f32_16x16x32_bf16 v[12:15], a[96:99], a[76:79], v[12:15]\n"
"v_mfma_f32_16x16x32_bf16 v[16:19], a[96:99], a[80:83], v[16:19]\n"
"ds_read_b64_tr_b16 a[2:3], v224 offset:33792\n"
"v_mfma_f32_16x16x32_bf16 v[20:23], a[96:99], a[84:87], v[20:23]\n"
"buffer_load_dwordx4 v234, s[24:27], s54 offen offset:1024 lds\n"
"v_mfma_f32_16x16x32_bf16 v[24:27], a[96:99], a[88:91], v[24:27]\n"
"ds_read_b128 a[36:39], v232 offset:34816\n"
"v_mfma_f32_16x16x32_bf16 v[28:31], a[96:99], a[92:95], v[28:31]\n"
"v_mfma_f32_16x16x32_bf16 v[32:35], a[100:103], a[64:67], v[32:35]\n"
"ds_read_b64_tr_b16 a[4:5], v225 offset:32768\n"
"v_mfma_f32_16x16x32_bf16 v[36:39], a[100:103], a[68:71], v[36:39]\n"
"buffer_load_dwordx4 v235, s[24:27], s54 offen offset:2048 lds\n"
"v_mfma_f32_16x16x32_bf16 v[40:43], a[100:103], a[72:75], v[40:43]\n"
"ds_read_b64_tr_b16 a[6:7], v225 offset:33792\n"
"v_mfma_f32_16x16x32_bf16 v[44:47], a[100:103], a[76:79], v[44:47]\n"
"v_mfma_f32_16x16x32_bf16 v[48:51], a[100:103], a[80:83], v[48:51]\n"
"ds_read_b128 a[40:43], v232 offset:36864\n"
"v_mfma_f32_16x16x32_bf16 v[52:55], a[100:103], a[84:87], v[52:55]\n"
"buffer_load_dwordx4 v236, s[24:27], s54 offen offset:3072 lds\n"
"v_mfma_f32_16x16x32_bf16 v[56:59], a[100:103], a[88:91], v[56:59]\n"
"ds_read_b64_tr_b16 a[8:9], v226 offset:32768\n"
"v_mfma_f32_16x16x32_bf16 v[60:63], a[100:103], a[92:95], v[60:63]\n"
"s_add_u32 m0, s52, 4096\n"
"s_mov_b32 s54, 33024\n"
"v_mfma_f32_16x16x32_bf16 v[64:67], a[104:107], a[64:67], v[64:67]\n"
"ds_read_b64_tr_b16 a[10:11], v226 offset:33792\n"
"v_mfma_f32_16x16x32_bf16 v[68:71], a[104:107], a[68:71], v[68:71]\n"
"buffer_load_dwordx4 v233, s[24:27], s54 offen offset:0 lds\n"
"v_mfma_f32_16x16x32_bf16 v[72:75], a[104:107], a[72:75], v[72:75]\n"
"ds_read_b128 a[44:47], v232 offset:38912\n"
"v_mfma_f32_16x16x32_bf16 v[76:79], a[104:107], a[76:79], v[76:79]\n"
"v_mfma_f32_16x16x32_bf16 v[80:83], a[104:107], a[80:83], v[80:83]\n"
"ds_read_b64_tr_b16 a[12:13], v227 offset:32768\n"
"v_mfma_f32_16x16x32_bf16 v[84:87], a[104:107], a[84:87], v[84:87]\n"
"buffer_load_dwordx4 v234, s[24:27], s54 offen offset:1024 lds\n"
"v_mfma_f32_16x16x32_bf16 v[88:91], a[104:107], a[88:91], v[88:91]\n"
"ds_read_b64_tr_b16 a[14:15], v227 offset:33792\n"
"v_mfma_f32_16x16x32_bf16 v[92:95], a[104:107], a[92:95], v[92:95]\n"
"v_mfma_f32_16x16x32_bf16 v[96:99], a[108:111], a[64:67], v[96:99]\n"
"ds_read_b128 a[48:51], v232 offset:49152\n"
"v_mfma_f32_16x16x32_bf16 v[100:103], a[108:111], a[68:71], v[100:103]\n"
"buffer_load_dwordx4 v235, s[24:27], s54 offen offset:2048 lds\n"
"v_mfma_f32_16x16x32_bf16 v[104:107], a[108:111], a[72:75], v[104:107]\n"
"ds_read_b64_tr_b16 a[16:17], v228 offset:32768\n"
"v_mfma_f32_16x16x32_bf16 v[108:111], a[108:111], a[76:79], v[108:111]\n"
"v_mfma_f32_16x16x32_bf16 v[112:115], a[108:111], a[80:83], v[112:115]\n"
"ds_read_b64_tr_b16 a[18:19], v228 offset:33792\n"
"v_mfma_f32_16x16x32_bf16 v[116:119], a[108:111], a[84:87], v[116:119]\n"
"buffer_load_dwordx4 v236, s[24:27], s54 offen offset:3072 lds\n"
"s_add_u32 m0, s53, 16384\n"
"s_mov_b32 s55, 1310720\n"
"v_mfma_f32_16x16x32_bf16 v[120:123], a[108:111], a[88:91], v[120:123]\n"
"ds_read_b128 a[52:55], v232 offset:51200\n"
"v_mfma_f32_16x16x32_bf16 v[124:127], a[108:111], a[92:95], v[124:127]\n"
"buffer_load_dwordx4 v237, s[28:31], s55 offen offset:0 lds\n"
"v_mfma_f32_16x16x32_bf16 v[128:131], a[112:115], a[64:67], v[128:131]\n"
"ds_read_b64_tr_b16 a[20:21], v229 offset:32768\n"
"v_mfma_f32_16x16x32_bf16 v[132:135], a[112:115], a[68:71], v[132:135]\n"
"v_mfma_f32_16x16x32_bf16 v[136:139], a[112:115], a[72:75], v[136:139]\n"
"ds_read_b64_tr_b16 a[22:23], v229 offset:33792\n"
"v_mfma_f32_16x16x32_bf16 v[140:143], a[112:115], a[76:79], v[140:143]\n"
"v_mfma_f32_16x16x32_bf16 v[144:147], a[112:115], a[80:83], v[144:147]\n"
"ds_read_b128 a[56:59], v232 offset:53248\n"
"v_mfma_f32_16x16x32_bf16 v[148:151], a[112:115], a[84:87], v[148:151]\n"
"buffer_load_dwordx4 v238, s[28:31], s55 offen offset:1024 lds\n"
"v_mfma_f32_16x16x32_bf16 v[152:155], a[112:115], a[88:91], v[152:155]\n"
"ds_read_b64_tr_b16 a[24:25], v230 offset:32768\n"
"v_mfma_f32_16x16x32_bf16 v[156:159], a[112:115], a[92:95], v[156:159]\n"
"v_mfma_f32_16x16x32_bf16 v[160:163], a[116:119], a[64:67], v[160:163]\n"
"ds_read_b64_tr_b16 a[26:27], v230 offset:33792\n"
"v_mfma_f32_16x16x32_bf16 v[164:167], a[116:119], a[68:71], v[164:167]\n"
"v_mfma_f32_16x16x32_bf16 v[168:171], a[116:119], a[72:75], v[168:171]\n"
"ds_read_b128 a[60:63], v232 offset:55296\n"
"v_mfma_f32_16x16x32_bf16 v[172:175], a[116:119], a[76:79], v[172:175]\n"
"s_add_u32 m0, s53, 24576\n"
"v_mfma_f32_16x16x32_bf16 v[176:179], a[116:119], a[80:83], v[176:179]\n"
"ds_read_b64_tr_b16 a[28:29], v231 offset:32768\n"
"v_mfma_f32_16x16x32_bf16 v[180:183], a[116:119], a[84:87], v[180:183]\n"
"buffer_load_dwordx4 v237, s[32:35], s55 offen offset:0 lds\n"
"v_mfma_f32_16x16x32_bf16 v[184:187], a[116:119], a[88:91], v[184:187]\n"
"ds_read_b64_tr_b16 a[30:31], v231 offset:33792\n"
"v_mfma_f32_16x16x32_bf16 v[188:191], a[116:119], a[92:95], v[188:191]\n"
"v_mfma_f32_16x16x32_bf16 v[192:195], a[120:123], a[64:67], v[192:195]\n"
"v_mfma_f32_16x16x32_bf16 v[196:199], a[120:123], a[68:71], v[196:199]\n"
"v_mfma_f32_16x16x32_bf16 v[200:203], a[120:123], a[72:75], v[200:203]\n"
"v_mfma_f32_16x16x32_bf16 v[204:207], a[120:123], a[76:79], v[204:207]\n"
"buffer_load_dwordx4 v238, s[32:35], s55 offen offset:1024 lds\n"
"v_mfma_f32_16x16x32_bf16 v[208:211], a[120:123], a[80:83], v[208:211]\n"
"v_mfma_f32_16x16x32_bf16 v[212:215], a[120:123], a[84:87], v[212:215]\n"
"v_mfma_f32_16x16x32_bf16 v[216:219], a[120:123], a[88:91], v[216:219]\n"
"v_mfma_f32_16x16x32_bf16 v[220:223], a[120:123], a[92:95], v[220:223]\n"
"v_mfma_f32_16x16x32_bf16 a[128:131], a[124:127], a[64:67], a[128:131]\n"
"v_mfma_f32_16x16x32_bf16 a[132:135], a[124:127], a[68:71], a[132:135]\n"
"v_mfma_f32_16x16x32_bf16 a[136:139], a[124:127], a[72:75], a[136:139]\n"
"s_waitcnt vmcnt(16) lgkmcnt(0)\n"
"v_mfma_f32_16x16x32_bf16 a[140:143], a[124:127], a[76:79], a[140:143]\n"
"s_barrier\n"
"v_mfma_f32_16x16x32_bf16 a[144:147], a[124:127], a[80:83], a[144:147]\n"
"v_mfma_f32_16x16x32_bf16 a[148:151], a[124:127], a[84:87], a[148:151]\n"
"v_mfma_f32_16x16x32_bf16 a[152:155], a[124:127], a[88:91], a[152:155]\n"
"v_mfma_f32_16x16x32_bf16 a[156:159], a[124:127], a[92:95], a[156:159]\n"
"v_xor_b32 v232, 64, v232\n"
"v_mfma_f32_16x16x32_bf16 v[0:3], a[32:35], a[0:3], v[0:3]\n"
"ds_read_b128 a[96:99], v232 offset:32768\n"
"s_add_u32 m0, s53, 32768\n"
"s_mov_b32 s55, 1572864\n"
"v_mfma_f32_16x16x32_bf16 v[4:7], a[32:35], a[4:7], v[4:7]\n"
"buffer_load_dwordx4 v237, s[28:31], s55 offen offset:0 lds\n"
"v_mfma_f32_16x16x32_bf16 v[8:11], a[32:35], a[8:11], v[8:11]\n"
"ds_read_b64_tr_b16 a[64:65], v224 offset:49152\n"
"v_mfma_f32_16x16x32_bf16 v[12:15], a[32:35], a[12:15], v[12:15]\n"
"v_mfma_f32_16x16x32_bf16 v[16:19], a[32:35], a[16:19], v[16:19]\n"
"ds_read_b64_tr_b16 a[66:67], v224 offset:50176\n"
"v_mfma_f32_16x16x32_bf16 v[20:23], a[32:35], a[20:23], v[20:23]\n"
"v_mfma_f32_16x16x32_bf16 v[24:27], a[32:35], a[24:27], v[24:27]\n"
"ds_read_b128 a[100:103], v232 offset:34816\n"
"v_mfma_f32_16x16x32_bf16 v[28:31], a[32:35], a[28:31], v[28:31]\n"
"v_mfma_f32_16x16x32_bf16 v[32:35], a[36:39], a[0:3], v[32:35]\n"
"ds_read_b64_tr_b16 a[68:69], v225 offset:49152\n"
"v_mfma_f32_16x16x32_bf16 v[36:39], a[36:39], a[4:7], v[36:39]\n"
"v_mfma_f32_16x16x32_bf16 v[40:43], a[36:39], a[8:11], v[40:43]\n"
"ds_read_b64_tr_b16 a[70:71], v225 offset:50176\n"
"v_mfma_f32_16x16x32_bf16 v[44:47], a[36:39], a[12:15], v[44:47]\n"
"v_mfma_f32_16x16x32_bf16 v[48:51], a[36:39], a[16:19], v[48:51]\n"
"ds_read_b128 a[104:107], v232 offset:36864\n"
"v_mfma_f32_16x16x32_bf16 v[52:55], a[36:39], a[20:23], v[52:55]\n"
"v_mfma_f32_16x16x32_bf16 v[56:59], a[36:39], a[24:27], v[56:59]\n"
"ds_read_b64_tr_b16 a[72:73], v226 offset:49152\n"
"v_mfma_f32_16x16x32_bf16 v[60:63], a[36:39], a[28:31], v[60:63]\n"
"buffer_load_dwordx4 v238, s[28:31], s55 offen offset:1024 lds\n"
"v_mfma_f32_16x16x32_bf16 v[64:67], a[40:43], a[0:3], v[64:67]\n"
"ds_read_b64_tr_b16 a[74:75], v226 offset:50176\n"
"v_mfma_f32_16x16x32_bf16 v[68:71], a[40:43], a[4:7], v[68:71]\n"
"v_mfma_f32_16x16x32_bf16 v[72:75], a[40:43], a[8:11], v[72:75]\n"
"ds_read_b128 a[108:111], v232 offset:38912\n"
"v_mfma_f32_16x16x32_bf16 v[76:79], a[40:43], a[12:15], v[76:79]\n"
"v_mfma_f32_16x16x32_bf16 v[80:83], a[40:43], a[16:19], v[80:83]\n"
"ds_read_b64_tr_b16 a[76:77], v227 offset:49152\n"
"v_mfma_f32_16x16x32_bf16 v[84:87], a[40:43], a[20:23], v[84:87]\n"
"v_mfma_f32_16x16x32_bf16 v[88:91], a[40:43], a[24:27], v[88:91]\n"
"ds_read_b64_tr_b16 a[78:79], v227 offset:50176\n"
"v_mfma_f32_16x16x32_bf16 v[92:95], a[40:43], a[28:31], v[92:95]\n"
"v_mfma_f32_16x16x32_bf16 v[96:99], a[44:47], a[0:3], v[96:99]\n"
"ds_read_b128 a[112:115], v232 offset:49152\n"
"v_mfma_f32_16x16x32_bf16 v[100:103], a[44:47], a[4:7], v[100:103]\n"
"v_mfma_f32_16x16x32_bf16 v[104:107], a[44:47], a[8:11], v[104:107]\n"
"ds_read_b64_tr_b16 a[80:81], v228 offset:49152\n"
"v_mfma_f32_16x16x32_bf16 v[108:111], a[44:47], a[12:15], v[108:111]\n"
"s_add_u32 m0, s53, 40960\n"
"v_mfma_f32_16x16x32_bf16 v[112:115], a[44:47], a[16:19], v[112:115]\n"
"ds_read_b64_tr_b16 a[82:83], v228 offset:50176\n"
"v_mfma_f32_16x16x32_bf16 v[116:119], a[44:47], a[20:23], v[116:119]\n"
"buffer_load_dwordx4 v237, s[32:35], s55 offen offset:0 lds\n"
"v_mfma_f32_16x16x32_bf16 v[120:123], a[44:47], a[24:27], v[120:123]\n"
"ds_read_b128 a[116:119], v232 offset:51200\n"
"v_mfma_f32_16x16x32_bf16 v[124:127], a[44:47], a[28:31], v[124:127]\n"
"v_mfma_f32_16x16x32_bf16 v[128:131], a[48:51], a[0:3], v[128:131]\n"
"ds_read_b64_tr_b16 a[84:85], v229 offset:49152\n"
"v_mfma_f32_16x16x32_bf16 v[132:135], a[48:51], a[4:7], v[132:135]\n"
"v_mfma_f32_16x16x32_bf16 v[136:139], a[48:51], a[8:11], v[136:139]\n"
"ds_read_b64_tr_b16 a[86:87], v229 offset:50176\n"
"v_mfma_f32_16x16x32_bf16 v[140:143], a[48:51], a[12:15], v[140:143]\n"
"v_mfma_f32_16x16x32_bf16 v[144:147], a[48:51], a[16:19], v[144:147]\n"
"ds_read_b128 a[120:123], v232 offset:53248\n"
"v_mfma_f32_16x16x32_bf16 v[148:151], a[48:51], a[20:23], v[148:151]\n"
"v_mfma_f32_16x16x32_bf16 v[152:155], a[48:51], a[24:27], v[152:155]\n"
"ds_read_b64_tr_b16 a[88:89], v230 offset:49152\n"
"v_mfma_f32_16x16x32_bf16 v[156:159], a[48:51], a[28:31], v[156:159]\n"
"v_mfma_f32_16x16x32_bf16 v[160:163], a[52:55], a[0:3], v[160:163]\n"
"ds_read_b64_tr_b16 a[90:91], v230 offset:50176\n"
"v_mfma_f32_16x16x32_bf16 v[164:167], a[52:55], a[4:7], v[164:167]\n"
"v_mfma_f32_16x16x32_bf16 v[168:171], a[52:55], a[8:11], v[168:171]\n"
"ds_read_b128 a[124:127], v232 offset:55296\n"
"v_mfma_f32_16x16x32_bf16 v[172:175], a[52:55], a[12:15], v[172:175]\n"
"buffer_load_dwordx4 v238, s[32:35], s55 offen offset:1024 lds\n"
"v_mfma_f32_16x16x32_bf16 v[176:179], a[52:55], a[16:19], v[176:179]\n"
"ds_read_b64_tr_b16 a[92:93], v231 offset:49152\n"
"v_mfma_f32_16x16x32_bf16 v[180:183], a[52:55], a[20:23], v[180:183]\n"
"v_mfma_f32_16x16x32_bf16 v[184:187], a[52:55], a[24:27], v[184:187]\n"
"ds_read_b64_tr_b16 a[94:95], v231 offset:50176\n"
"v_mfma_f32_16x16x32_bf16 v[188:191], a[52:55], a[28:31], v[188:191]\n"
"v_mfma_f32_16x16x32_bf16 v[192:195], a[56:59], a[0:3], v[192:195]\n"
"v_mfma_f32_16x16x32_bf16 v[196:199], a[56:59], a[4:7], v[196:199]\n"
"v_mfma_f32_16x16x32_bf16 v[200:203], a[56:59], a[8:11], v[200:203]\n"
"v_mfma_f32_16x16x32_bf16 v[204:207], a[56:59], a[12:15], v[204:207]\n"
"v_mfma_f32_16x16x32_bf16 v[208:211], a[56:59], a[16:19], v[208:211]\n"
"v_mfma_f32_16x16x32_bf16 v[212:215], a[56:59], a[20:23], v[212:215]\n"
"v_mfma_f32_16x16x32_bf16 v[216:219], a[56:59], a[24:27], v[216:219]\n"
"v_mfma_f32_16x16x32_bf16 v[220:223], a[56:59], a[28:31], v[220:223]\n"
"v_mfma_f32_16x16x32_bf16 a[128:131], a[60:63], a[0:3], a[128:131]\n"
"v_mfma_f32_16x16x32_bf16 a[132:135], a[60:63], a[4:7], a[132:135]\n"
"v_mfma_f32_16x16x32_bf16 a[136:139], a[60:63], a[8:11], a[136:139]\n"
"s_waitcnt vmcnt(8) lgkmcnt(0)\n"
"v_mfma_f32_16x16x32_bf16 a[140:143], a[60:63], a[12:15], a[140:143]\n"
"s_barrier\n"
"v_mfma_f32_16x16x32_bf16 a[144:147], a[60:63], a[16:19], a[144:147]\n"
"v_mfma_f32_16x16x32_bf16 a[148:151], a[60:63], a[20:23], a[148:151]\n"
"v_mfma_f32_16x16x32_bf16 a[152:155], a[60:63], a[24:27], a[152:155]\n"
"v_mfma_f32_16x16x32_bf16 a[156:159], a[60:63], a[28:31], a[156:159]\n"
"v_xor_b32 v232, 64, v232\n"
"v_mfma_f32_16x16x32_bf16 v[0:3], a[96:99], a[64:67], v[0:3]\n"
"ds_read_b128 a[32:35], v232 offset:0\n"
"v_lshrrev_b32 v244, 2, v239\n"
"s_add_u32 m0, s52, 32768\n"
"s_mov_b32 s54, 384\n"
"v_mfma_f32_16x16x32_bf16 v[4:7], a[96:99], a[68:71], v[4:7]\n"
"buffer_load_dwordx4 v233, s[24:27], s54 offen offset:0 lds\n"
"v_mfma_f32_16x16x32_bf16 v[8:11], a[96:99], a[72:75], v[8:11]\n"
"ds_read_b64_tr_b16 a[0:1], v224 offset:0\n"
"v_mfma_f32_16x16x32_bf16 v[12:15], a[96:99], a[76:79], v[12:15]\n"
"v_mfma_f32_16x16x32_bf16 v[16:19], a[96:99], a[80:83], v[16:19]\n"
"ds_read_b64_tr_b16 a[2:3], v224 offset:1024\n"
"v_mfma_f32_16x16x32_bf16 v[20:23], a[96:99], a[84:87], v[20:23]\n"
"buffer_load_dwordx4 v234, s[24:27], s54 offen offset:1024 lds\n"
"v_mfma_f32_16x16x32_bf16 v[24:27], a[96:99], a[88:91], v[24:27]\n"
"ds_read_b128 a[36:39], v232 offset:2048\n"
"v_mfma_f32_16x16x32_bf16 v[28:31], a[96:99], a[92:95], v[28:31]\n"
"v_mfma_f32_16x16x32_bf16 v[32:35], a[100:103], a[64:67], v[32:35]\n"
"ds_read_b64_tr_b16 a[4:5], v225 offset:0\n"
"v_mfma_f32_16x16x32_bf16 v[36:39], a[100:103], a[68:71], v[36:39]\n"
"buffer_load_dwordx4 v235, s[24:27], s54 offen offset:2048 lds\n"
"v_mfma_f32_16x16x32_bf16 v[40:43], a[100:103], a[72:75], v[40:43]\n"
"ds_read_b64_tr_b16 a[6:7], v225 offset:1024\n"
"v_mfma_f32_16x16x32_bf16 v[44:47], a[100:103], a[76:79], v[44:47]\n"
"v_mfma_f32_16x16x32_bf16 v[48:51], a[100:103], a[80:83], v[48:51]\n"
"ds_read_b128 a[40:43], v232 offset:4096\n"
"v_mfma_f32_16x16x32_bf16 v[52:55], a[100:103], a[84:87], v[52:55]\n"
"buffer_load_dwordx4 v236, s[24:27], s54 offen offset:3072 lds\n"
"v_mfma_f32_16x16x32_bf16 v[56:59], a[100:103], a[88:91], v[56:59]\n"
"ds_read_b64_tr_b16 a[8:9], v226 offset:0\n"
"v_mfma_f32_16x16x32_bf16 v[60:63], a[100:103], a[92:95], v[60:63]\n"
"s_add_u32 m0, s52, 36864\n"
"s_mov_b32 s54, 33152\n"
"v_mfma_f32_16x16x32_bf16 v[64:67], a[104:107], a[64:67], v[64:67]\n"
"ds_read_b64_tr_b16 a[10:11], v226 offset:1024\n"
"v_mfma_f32_16x16x32_bf16 v[68:71], a[104:107], a[68:71], v[68:71]\n"
"buffer_load_dwordx4 v233, s[24:27], s54 offen offset:0 lds\n"
"v_mfma_f32_16x16x32_bf16 v[72:75], a[104:107], a[72:75], v[72:75]\n"
"ds_read_b128 a[44:47], v232 offset:6144\n"
"v_mfma_f32_16x16x32_bf16 v[76:79], a[104:107], a[76:79], v[76:79]\n"
"v_mfma_f32_16x16x32_bf16 v[80:83], a[104:107], a[80:83], v[80:83]\n"
"ds_read_b64_tr_b16 a[12:13], v227 offset:0\n"
"v_mfma_f32_16x16x32_bf16 v[84:87], a[104:107], a[84:87], v[84:87]\n"
"buffer_load_dwordx4 v234, s[24:27], s54 offen offset:1024 lds\n"
"v_mfma_f32_16x16x32_bf16 v[88:91], a[104:107], a[88:91], v[88:91]\n"
"ds_read_b64_tr_b16 a[14:15], v227 offset:1024\n"
"v_mfma_f32_16x16x32_bf16 v[92:95], a[104:107], a[92:95], v[92:95]\n"
"v_mfma_f32_16x16x32_bf16 v[96:99], a[108:111], a[64:67], v[96:99]\n"
"ds_read_b128 a[48:51], v232 offset:16384\n"
"v_mfma_f32_16x16x32_bf16 v[100:103], a[108:111], a[68:71], v[100:103]\n"
"buffer_load_dwordx4 v235, s[24:27], s54 offen offset:2048 lds\n"
"v_mfma_f32_16x16x32_bf16 v[104:107], a[108:111], a[72:75], v[104:107]\n"
"ds_read_b64_tr_b16 a[16:17], v228 offset:0\n"
"v_mfma_f32_16x16x32_bf16 v[108:111], a[108:111], a[76:79], v[108:111]\n"
"v_mfma_f32_16x16x32_bf16 v[112:115], a[108:111], a[80:83], v[112:115]\n"
"ds_read_b64_tr_b16 a[18:19], v228 offset:1024\n"
"v_mfma_f32_16x16x32_bf16 v[116:119], a[108:111], a[84:87], v[116:119]\n"
"buffer_load_dwordx4 v236, s[24:27], s54 offen offset:3072 lds\n"
"s_movk_i32 s58, 0\n"
"v_mfma_f32_16x16x32_bf16 v[120:123], a[108:111], a[88:91], v[120:123]\n"
"ds_read_b128 a[52:55], v232 offset:18432\n"
"v_mfma_f32_16x16x32_bf16 v[124:127], a[108:111], a[92:95], v[124:127]\n"
"buffer_load_dwordx2 v[242:243], v244, s[44:47], s58 offen sc1\n"
"v_mfma_f32_16x16x32_bf16 v[128:131], a[112:115], a[64:67], v[128:131]\n"
"ds_read_b64_tr_b16 a[20:21], v229 offset:0\n"
"v_mfma_f32_16x16x32_bf16 v[132:135], a[112:115], a[68:71], v[132:135]\n"
"v_mfma_f32_16x16x32_bf16 v[136:139], a[112:115], a[72:75], v[136:139]\n"
"ds_read_b64_tr_b16 a[22:23], v229 offset:1024\n"
"v_mfma_f32_16x16x32_bf16 v[140:143], a[112:115], a[76:79], v[140:143]\n"
"buffer_load_dwordx4 a[192:195], v255, s[40:43], s60 offen offset:0\n"
"v_mfma_f32_16x16x32_bf16 v[144:147], a[112:115], a[80:83], v[144:147]\n"
"ds_read_b128 a[56:59], v232 offset:20480\n"
"v_mfma_f32_16x16x32_bf16 v[148:151], a[112:115], a[84:87], v[148:151]\n"
"v_mfma_f32_16x16x32_bf16 v[152:155], a[112:115], a[88:91], v[152:155]\n"
"ds_read_b64_tr_b16 a[24:25], v230 offset:0\n"
"v_mfma_f32_16x16x32_bf16 v[156:159], a[112:115], a[92:95], v[156:159]\n"
"buffer_load_dwordx4 a[196:199], v255, s[40:43], s60 offen offset:1024\n"
"v_mfma_f32_16x16x32_bf16 v[160:163], a[116:119], a[64:67], v[160:163]\n"
"ds_read_b64_tr_b16 a[26:27], v230 offset:1024\n"
"v_mfma_f32_16x16x32_bf16 v[164:167], a[116:119], a[68:71], v[164:167]\n"
"s_add_u32 m0, s53, 49152\n"
"s_mov_b32 s55, 1835008\n"
"v_mfma_f32_16x16x32_bf16 v[168:171], a[116:119], a[72:75], v[168:171]\n"
"ds_read_b128 a[60:63], v232 offset:22528\n"
"v_mfma_f32_16x16x32_bf16 v[172:175], a[116:119], a[76:79], v[172:175]\n"
"buffer_load_dwordx4 v237, s[28:31], s55 offen offset:0 lds\n"
"v_mfma_f32_16x16x32_bf16 v[176:179], a[116:119], a[80:83], v[176:179]\n"
"ds_read_b64_tr_b16 a[28:29], v231 offset:0\n"
"v_mfma_f32_16x16x32_bf16 v[180:183], a[116:119], a[84:87], v[180:183]\n"
"buffer_load_dwordx4 v238, s[28:31], s55 offen offset:1024 lds\n"
"v_mfma_f32_16x16x32_bf16 v[184:187], a[116:119], a[88:91], v[184:187]\n"
"ds_read_b64_tr_b16 a[30:31], v231 offset:1024\n"
"v_mfma_f32_16x16x32_bf16 v[188:191], a[116:119], a[92:95], v[188:191]\n"
"s_add_u32 m0, s53, 57344\n"
"v_mfma_f32_16x16x32_bf16 v[192:195], a[120:123], a[64:67], v[192:195]\n"
"v_mfma_f32_16x16x32_bf16 v[196:199], a[120:123], a[68:71], v[196:199]\n"
"buffer_load_dwordx4 v237, s[32:35], s55 offen offset:0 lds\n"
"v_mfma_f32_16x16x32_bf16 v[200:203], a[120:123], a[72:75], v[200:203]\n"
"v_mfma_f32_16x16x32_bf16 v[204:207], a[120:123], a[76:79], v[204:207]\n"
"v_mfma_f32_16x16x32_bf16 v[208:211], a[120:123], a[80:83], v[208:211]\n"
"v_mfma_f32_16x16x32_bf16 v[212:215], a[120:123], a[84:87], v[212:215]\n"
"buffer_load_dwordx4 v238, s[32:35], s55 offen offset:1024 lds\n"
"v_mfma_f32_16x16x32_bf16 v[216:219], a[120:123], a[88:91], v[216:219]\n"
"v_mfma_f32_16x16x32_bf16 v[220:223], a[120:123], a[92:95], v[220:223]\n"
"v_mfma_f32_16x16x32_bf16 a[128:131], a[124:127], a[64:67], a[128:131]\n"
"v_mfma_f32_16x16x32_bf16 a[132:135], a[124:127], a[68:71], a[132:135]\n"
"v_mfma_f32_16x16x32_bf16 a[136:139], a[124:127], a[72:75], a[136:139]\n"
"s_waitcnt vmcnt(19) lgkmcnt(0)\n"
"v_mfma_f32_16x16x32_bf16 a[140:143], a[124:127], a[76:79], a[140:143]\n"
"s_barrier\n"
"v_mfma_f32_16x16x32_bf16 a[144:147], a[124:127], a[80:83], a[144:147]\n"
"v_mfma_f32_16x16x32_bf16 a[148:151], a[124:127], a[84:87], a[148:151]\n"
"v_mfma_f32_16x16x32_bf16 a[152:155], a[124:127], a[88:91], a[152:155]\n"
"v_mfma_f32_16x16x32_bf16 a[156:159], a[124:127], a[92:95], a[156:159]\n"
"v_xor_b32 v232, 64, v232\n"
"v_mfma_f32_16x16x32_bf16 v[0:3], a[32:35], a[0:3], v[0:3]\n"
"ds_read_b128 a[96:99], v232 offset:0\n"
"s_add_u32 m0, s53, 0\n"
"s_mov_b32 s55, 2097152\n"
"v_mfma_f32_16x16x32_bf16 v[4:7], a[32:35], a[4:7], v[4:7]\n"
"buffer_load_dwordx4 v237, s[28:31], s55 offen offset:0 lds\n"
"v_mfma_f32_16x16x32_bf16 v[8:11], a[32:35], a[8:11], v[8:11]\n"
"ds_read_b64_tr_b16 a[64:65], v224 offset:16384\n"
"v_mfma_f32_16x16x32_bf16 v[12:15], a[32:35], a[12:15], v[12:15]\n"
"v_mfma_f32_16x16x32_bf16 v[16:19], a[32:35], a[16:19], v[16:19]\n"
"ds_read_b64_tr_b16 a[66:67], v224 offset:17408\n"
"v_mfma_f32_16x16x32_bf16 v[20:23], a[32:35], a[20:23], v[20:23]\n"
"v_mfma_f32_16x16x32_bf16 v[24:27], a[32:35], a[24:27], v[24:27]\n"
"ds_read_b128 a[100:103], v232 offset:2048\n"
"v_mfma_f32_16x16x32_bf16 v[28:31], a[32:35], a[28:31], v[28:31]\n"
"v_mfma_f32_16x16x32_bf16 v[32:35], a[36:39], a[0:3], v[32:35]\n"
"ds_read_b64_tr_b16 a[68:69], v225 offset:16384\n"
"v_mfma_f32_16x16x32_bf16 v[36:39], a[36:39], a[4:7], v[36:39]\n"
"v_mfma_f32_16x16x32_bf16 v[40:43], a[36:39], a[8:11], v[40:43]\n"
"ds_read_b64_tr_b16 a[70:71], v225 offset:17408\n"
"v_mfma_f32_16x16x32_bf16 v[44:47], a[36:39], a[12:15], v[44:47]\n"
"v_mfma_f32_16x16x32_bf16 v[48:51], a[36:39], a[16:19], v[48:51]\n"
"ds_read_b128 a[104:107], v232 offset:4096\n"
"v_mfma_f32_16x16x32_bf16 v[52:55], a[36:39], a[20:23], v[52:55]\n"
"v_mfma_f32_16x16x32_bf16 v[56:59], a[36:39], a[24:27], v[56:59]\n"
"ds_read_b64_tr_b16 a[72:73], v226 offset:16384\n"
"v_mfma_f32_16x16x32_bf16 v[60:63], a[36:39], a[28:31], v[60:63]\n"
"buffer_load_dwordx4 v238, s[28:31], s55 offen offset:1024 lds\n"
"v_mfma_f32_16x16x32_bf16 v[64:67], a[40:43], a[0:3], v[64:67]\n"
"ds_read_b64_tr_b16 a[74:75], v226 offset:17408\n"
"v_mfma_f32_16x16x32_bf16 v[68:71], a[40:43], a[4:7], v[68:71]\n"
"v_mfma_f32_16x16x32_bf16 v[72:75], a[40:43], a[8:11], v[72:75]\n"
"ds_read_b128 a[108:111], v232 offset:6144\n"
"v_mfma_f32_16x16x32_bf16 v[76:79], a[40:43], a[12:15], v[76:79]\n"
"v_mfma_f32_16x16x32_bf16 v[80:83], a[40:43], a[16:19], v[80:83]\n"
"ds_read_b64_tr_b16 a[76:77], v227 offset:16384\n"
"v_mfma_f32_16x16x32_bf16 v[84:87], a[40:43], a[20:23], v[84:87]\n"
"v_mfma_f32_16x16x32_bf16 v[88:91], a[40:43], a[24:27], v[88:91]\n"
"ds_read_b64_tr_b16 a[78:79], v227 offset:17408\n"
"v_mfma_f32_16x16x32_bf16 v[92:95], a[40:43], a[28:31], v[92:95]\n"
"v_mfma_f32_16x16x32_bf16 v[96:99], a[44:47], a[0:3], v[96:99]\n"
"ds_read_b128 a[112:115], v232 offset:16384\n"
"v_mfma_f32_16x16x32_bf16 v[100:103], a[44:47], a[4:7], v[100:103]\n"
"v_mfma_f32_16x16x32_bf16 v[104:107], a[44:47], a[8:11], v[104:107]\n"
"ds_read_b64_tr_b16 a[80:81], v228 offset:16384\n"
"v_mfma_f32_16x16x32_bf16 v[108:111], a[44:47], a[12:15], v[108:111]\n"
"s_add_u32 m0, s53, 8192\n"
"v_mfma_f32_16x16x32_bf16 v[112:115], a[44:47], a[16:19], v[112:115]\n"
"ds_read_b64_tr_b16 a[82:83], v228 offset:17408\n"
"v_mfma_f32_16x16x32_bf16 v[116:119], a[44:47], a[20:23], v[116:119]\n"
"buffer_load_dwordx4 v237, s[32:35], s55 offen offset:0 lds\n"
"v_mfma_f32_16x16x32_bf16 v[120:123], a[44:47], a[24:27], v[120:123]\n"
"ds_read_b128 a[116:119], v232 offset:18432\n"
"v_mfma_f32_16x16x32_bf16 v[124:127], a[44:47], a[28:31], v[124:127]\n"
"v_mfma_f32_16x16x32_bf16 v[128:131], a[48:51], a[0:3], v[128:131]\n"
"ds_read_b64_tr_b16 a[84:85], v229 offset:16384\n"
"v_mfma_f32_16x16x32_bf16 v[132:135], a[48:51], a[4:7], v[132:135]\n"
"v_mfma_f32_16x16x32_bf16 v[136:139], a[48:51], a[8:11], v[136:139]\n"
"ds_read_b64_tr_b16 a[86:87], v229 offset:17408\n"
"v_mfma_f32_16x16x32_bf16 v[140:143], a[48:51], a[12:15], v[140:143]\n"
"v_mfma_f32_16x16x32_bf16 v[144:147], a[48:51], a[16:19], v[144:147]\n"
"ds_read_b128 a[120:123], v232 offset:20480\n"
"v_mfma_f32_16x16x32_bf16 v[148:151], a[48:51], a[20:23], v[148:151]\n"
"v_mfma_f32_16x16x32_bf16 v[152:155], a[48:51], a[24:27], v[152:155]\n"
"ds_read_b64_tr_b16 a[88:89], v230 offset:16384\n"
"v_mfma_f32_16x16x32_bf16 v[156:159], a[48:51], a[28:31], v[156:159]\n"
"v_mfma_f32_16x16x32_bf16 v[160:163], a[52:55], a[0:3], v[160:163]\n"
"ds_read_b64_tr_b16 a[90:91], v230 offset:17408\n"
"v_mfma_f32_16x16x32_bf16 v[164:167], a[52:55], a[4:7], v[164:167]\n"
"v_mfma_f32_16x16x32_bf16 v[168:171], a[52:55], a[8:11], v[168:171]\n"
"ds_read_b128 a[124:127], v232 offset:22528\n"
"v_mfma_f32_16x16x32_bf16 v[172:175], a[52:55], a[12:15], v[172:175]\n"
"buffer_load_dwordx4 v238, s[32:35], s55 offen offset:1024 lds\n"
"v_mfma_f32_16x16x32_bf16 v[176:179], a[52:55], a[16:19], v[176:179]\n"
"ds_read_b64_tr_b16 a[92:93], v231 offset:16384\n"
"v_mfma_f32_16x16x32_bf16 v[180:183], a[52:55], a[20:23], v[180:183]\n"
"v_mfma_f32_16x16x32_bf16 v[184:187], a[52:55], a[24:27], v[184:187]\n"
"ds_read_b64_tr_b16 a[94:95], v231 offset:17408\n"
"v_mfma_f32_16x16x32_bf16 v[188:191], a[52:55], a[28:31], v[188:191]\n"
"v_mfma_f32_16x16x32_bf16 v[192:195], a[56:59], a[0:3], v[192:195]\n"
"v_mfma_f32_16x16x32_bf16 v[196:199], a[56:59], a[4:7], v[196:199]\n"
"v_mfma_f32_16x16x32_bf16 v[200:203], a[56:59], a[8:11], v[200:203]\n"
"v_mfma_f32_16x16x32_bf16 v[204:207], a[56:59], a[12:15], v[204:207]\n"
"v_mfma_f32_16x16x32_bf16 v[208:211], a[56:59], a[16:19], v[208:211]\n"
"v_mfma_f32_16x16x32_bf16 v[212:215], a[56:59], a[20:23], v[212:215]\n"
"v_mfma_f32_16x16x32_bf16 v[216:219], a[56:59], a[24:27], v[216:219]\n"
"v_mfma_f32_16x16x32_bf16 v[220:223], a[56:59], a[28:31], v[220:223]\n"
"v_mfma_f32_16x16x32_bf16 a[128:131], a[60:63], a[0:3], a[128:131]\n"
"v_mfma_f32_16x16x32_bf16 a[132:135], a[60:63], a[4:7], a[132:135]\n"
"v_mfma_f32_16x16x32_bf16 a[136:139], a[60:63], a[8:11], a[136:139]\n"
"s_waitcnt vmcnt(8) lgkmcnt(0)\n"
"v_mfma_f32_16x16x32_bf16 a[140:143], a[60:63], a[12:15], a[140:143]\n"
"s_barrier\n"
"v_mfma_f32_16x16x32_bf16 a[144:147], a[60:63], a[16:19], a[144:147]\n"
"v_mfma_f32_16x16x32_bf16 a[148:151], a[60:63], a[20:23], a[148:151]\n"
"v_mfma_f32_16x16x32_bf16 a[152:155], a[60:63], a[24:27], a[152:155]\n"
"v_mfma_f32_16x16x32_bf16 a[156:159], a[60:63], a[28:31], a[156:159]\n"
"v_xor_b32 v232, 64, v232\n"
"v_mfma_f32_16x16x32_bf16 v[0:3], a[96:99], a[64:67], v[0:3]\n"
"ds_read_b128 a[32:35], v232 offset:32768\n"
"v_cmp_ne_u64 vcc, s[96:97], v[242:243]\n"
"v_mfma_f32_16x16x32_bf16 v[4:7], a[96:99], a[68:71], v[4:7]\n"
"s_cbranch_vccnz L_spin11_%=\n"
"L_spinr12_%=:\n"
"v_mfma_f32_16x16x32_bf16 v[8:11], a[96:99], a[72:75], v[8:11]\n"
"ds_read_b64_tr_b16 a[0:1], v224 offset:32768\n"
"s_lshl_b32 s58, s48, 10\n"
"s_add_u32 m0, s58, 155648\n"
"s_add_u32 s58, s58, 4096\n"
"v_mfma_f32_16x16x32_bf16 v[12:15], a[96:99], a[76:79], v[12:15]\n"
"v_mfma_f32_16x16x32_bf16 v[16:19], a[96:99], a[80:83], v[16:19]\n"
"ds_read_b64_tr_b16 a[2:3], v224 offset:33792\n"
"buffer_load_dwordx4 v255, s[44:47], s58 offen sc1 lds\n"
"v_mfma_f32_16x16x32_bf16 v[20:23], a[96:99], a[84:87], v[20:23]\n"
"v_mfma_f32_16x16x32_bf16 v[24:27], a[96:99], a[88:91], v[24:27]\n"
"ds_read_b128 a[36:39], v232 offset:34816\n"
"v_mfma_f32_16x16x32_bf16 v[28:31], a[96:99], a[92:95], v[28:31]\n"
"s_add_u32 m0, s52, 0\n"
"s_mov_b32 s54, 512\n"
"v_mfma_f32_16x16x32_bf16 v[32:35], a[100:103], a[64:67], v[32:35]\n"
"ds_read_b64_tr_b16 a[4:5], v225 offset:32768\n"
"v_mfma_f32_16x16x32_bf16 v[36:39], a[100:103], a[68:71], v[36:39]\n"
"buffer_load_dwordx4 v233, s[24:27], s54 offen offset:0 lds\n"
"v_mfma_f32_16x16x32_bf16 v[40:43], a[100:103], a[72:75], v[40:43]\n"
"ds_read_b64_tr_b16 a[6:7], v225 offset:33792\n"
"v_mfma_f32_16x16x32_bf16 v[44:47], a[100:103], a[76:79], v[44:47]\n"
"v_mfma_f32_16x16x32_bf16 v[48:51], a[100:103], a[80:83], v[48:51]\n"
"ds_read_b128 a[40:43], v232 offset:36864\n"
"v_mfma_f32_16x16x32_bf16 v[52:55], a[100:103], a[84:87], v[52:55]\n"
"buffer_load_dwordx4 v234, s[24:27], s54 offen offset:1024 lds\n"
"v_mfma_f32_16x16x32_bf16 v[56:59], a[100:103], a[88:91], v[56:59]\n"
"ds_read_b64_tr_b16 a[8:9], v226 offset:32768\n"
"v_mfma_f32_16x16x32_bf16 v[60:63], a[100:103], a[92:95], v[60:63]\n"
"buffer_load_dwordx4 v235, s[24:27], s54 offen offset:2048 lds\n"
"v_mfma_f32_16x16x32_bf16 v[64:67], a[104:107], a[64:67], v[64:67]\n"
"ds_read_b64_tr_b16 a[10:11], v226 offset:33792\n"
"v_mfma_f32_16x16x32_bf16 v[68:71], a[104:107], a[68:71], v[68:71]\n"
"v_mfma_f32_16x16x32_bf16 v[72:75], a[104:107], a[72:75], v[72:75]\n"
"ds_read_b128 a[44:47], v232 offset:38912\n"
"v_mfma_f32_16x16x32_bf16 v[76:79], a[104:107], a[76:79], v[76:79]\n"
"buffer_load_dwordx4 v236, s[24:27], s54 offen offset:3072 lds\n"
"s_add_u32 m0, s52, 4096\n"
"s_mov_b32 s54, 33280\n"
"v_mfma_f32_16x16x32_bf16 v[80:83], a[104:107], a[80:83], v[80:83]\n"
"ds_read_b64_tr_b16 a[12:13], v227 offset:32768\n"
"v_mfma_f32_16x16x32_bf16 v[84:87], a[104:107], a[84:87], v[84:87]\n"
"buffer_load_dwordx4 v233, s[24:27], s54 offen offset:0 lds\n"
"v_mfma_f32_16x16x32_bf16 v[88:91], a[104:107], a[88:91], v[88:91]\n"
"ds_read_b64_tr_b16 a[14:15], v227 offset:33792\n"
"v_mfma_f32_16x16x32_bf16 v[92:95], a[104:107], a[92:95], v[92:95]\n"
"v_mfma_f32_16x16x32_bf16 v[96:99], a[108:111], a[64:67], v[96:99]\n"
"ds_read_b128 a[48:51], v232 offset:49152\n"
"v_mfma_f32_16x16x32_bf16 v[100:103], a[108:111], a[68:71], v[100:103]\n"
"buffer_load_dwordx4 v234, s[24:27], s54 offen offset:1024 lds\n"
"v_mfma_f32_16x16x32_bf16 v[104:107], a[108:111], a[72:75], v[104:107]\n"
"ds_read_b64_tr_b16 a[16:17], v228 offset:32768\n"
"v_mfma_f32_16x16x32_bf16 v[108:111], a[108:111], a[76:79], v[108:111]\n"
"buffer_load_dwordx4 v235, s[24:27], s54 offen offset:2048 lds\n"
"v_mfma_f32_16x16x32_bf16 v[112:115], a[108:111], a[80:83], v[112:115]\n"
"ds_read_b64_tr_b16 a[18:19], v228 offset:33792\n"
"v_mfma_f32_16x16x32_bf16 v[116:119], a[108:111], a[84:87], v[116:119]\n"
"v_mfma_f32_16x16x32_bf16 v[120:123], a[108:111], a[88:91], v[120:123]\n"
"ds_read_b128 a[52:55], v232 offset:51200\n"
"v_mfma_f32_16x16x32_bf16 v[124:127], a[108:111], a[92:95], v[124:127]\n"
"buffer_load_dwordx4 v236, s[24:27], s54 offen offset:3072 lds\n"
"s_add_u32 m0, s53, 16384\n"
"s_mov_b32 s55, 2359296\n"
"v_mfma_f32_16x16x32_bf16 v[128:131], a[112:115], a[64:67], v[128:131]\n"
"ds_read_b64_tr_b16 a[20:21], v229 offset:32768\n"
"v_mfma_f32_16x16x32_bf16 v[132:135], a[112:115], a[68:71], v[132:135]\n"
"buffer_load_dwordx4 v237, s[28:31], s55 offen offset:0 lds\n"
"v_mfma_f32_16x16x32_bf16 v[136:139], a[112:115], a[72:75], v[136:139]\n"
"ds_read_b64_tr_b16 a[22:23], v229 offset:33792\n"
"v_mfma_f32_16x16x32_bf16 v[140:143], a[112:115], a[76:79], v[140:143]\n"
"v_mfma_f32_16x16x32_bf16 v[144:147], a[112:115], a[80:83], v[144:147]\n"
"ds_read_b128 a[56:59], v232 offset:53248\n"
"v_mfma_f32_16x16x32_bf16 v[148:151], a[112:115], a[84:87], v[148:151]\n"
"v_mfma_f32_16x16x32_bf16 v[152:155], a[112:115], a[88:91], v[152:155]\n"
"ds_read_b64_tr_b16 a[24:25], v230 offset:32768\n"
"v_mfma_f32_16x16x32_bf16 v[156:159], a[112:115], a[92:95], v[156:159]\n"
"buffer_load_dwordx4 v238, s[28:31], s55 offen offset:1024 lds\n"
"v_mfma_f32_16x16x32_bf16 v[160:163], a[116:119], a[64:67], v[160:163]\n"
"ds_read_b64_tr_b16 a[26:27], v230 offset:33792\n"
"v_mfma_f32_16x16x32_bf16 v[164:167], a[116:119], a[68:71], v[164:167]\n"
"v_mfma_f32_16x16x32_bf16 v[168:171], a[116:119], a[72:75], v[168:171]\n"
"ds_read_b128 a[60:63], v232 offset:55296\n"
"v_mfma_f32_16x16x32_bf16 v[172:175], a[116:119], a[76:79], v[172:175]\n"
"s_add_u32 m0, s53, 24576\n"
"v_mfma_f32_16x16x32_bf16 v[176:179], a[116:119], a[80:83], v[176:179]\n"
"ds_read_b64_tr_b16 a[28:29], v231 offset:32768\n"
"v_mfma_f32_16x16x32_bf16 v[180:183], a[116:119], a[84:87], v[180:183]\n"
"buffer_load_dwordx4 v237, s[32:35], s55 offen offset:0 lds\n"
"v_mfma_f32_16x16x32_bf16 v[184:187], a[116:119], a[88:91], v[184:187]\n"
"ds_read_b64_tr_b16 a[30:31], v231 offset:33792\n"
"v_mfma_f32_16x16x32_bf16 v[188:191], a[116:119], a[92:95], v[188:191]\n"
"v_mfma_f32_16x16x32_bf16 v[192:195], a[120:123], a[64:67], v[192:195]\n"
"v_mfma_f32_16x16x32_bf16 v[196:199], a[120:123], a[68:71], v[196:199]\n"
"v_mfma_f32_16x16x32_bf16 v[200:203], a[120:123], a[72:75], v[200:203]\n"
"v_mfma_f32_16x16x32_bf16 v[204:207], a[120:123], a[76:79], v[204:207]\n"
"buffer_load_dwordx4 v238, s[32:35], s55 offen offset:1024 lds\n"
"v_mfma_f32_16x16x32_bf16 v[208:211], a[120:123], a[80:83], v[208:211]\n"
"v_mfma_f32_16x16x32_bf16 v[212:215], a[120:123], a[84:87], v[212:215]\n"
"v_mfma_f32_16x16x32_bf16 v[216:219], a[120:123], a[88:91], v[216:219]\n"
"v_mfma_f32_16x16x32_bf16 v[220:223], a[120:123], a[92:95], v[220:223]\n"
"v_mfma_f32_16x16x32_bf16 a[128:131], a[124:127], a[64:67], a[128:131]\n"
"v_mfma_f32_16x16x32_bf16 a[132:135], a[124:127], a[68:71], a[132:135]\n"
"v_mfma_f32_16x16x32_bf16 a[136:139], a[124:127], a[72:75], a[136:139]\n"
"s_waitcnt vmcnt(17) lgkmcnt(0)\n"
"v_mfma_f32_16x16x32_bf16 a[140:143], a[124:127], a[76:79], a[140:143]\n"
"s_barrier\n"
"v_mfma_f32_16x16x32_bf16 a[144:147], a[124:127], a[80:83], a[144:147]\n"
"v_mfma_f32_16x16x32_bf16 a[148:151], a[124:127], a[84:87], a[148:151]\n"
"v_mfma_f32_16x16x32_bf16 a[152:155], a[124:127], a[88:91], a[152:155]\n"
"v_mfma_f32_16x16x32_bf16 a[156:159], a[124:127], a[92:95], a[156:159]\n"
"v_xor_b32 v232, 64, v232\n"
"v_mfma_f32_16x16x32_bf16 v[0:3], a[32:35], a[0:3], v[0:3]\n"
"ds_read_b128 a[96:99], v232 offset:32768\n"
"s_add_u32 m0, s53, 32768\n"
"s_mov_b32 s55, 2621440\n"
"v_mfma_f32_16x16x32_bf16 v[4:7], a[32:35], a[4:7], v[4:7]\n"
"buffer_load_dwordx4 v237, s[28:31], s55 offen offset:0 lds\n"
"v_mfma_f32_16x16x32_bf16 v[8:11], a[32:35], a[8:11], v[8:11]\n"
"ds_read_b64_tr_b16 a[64:65], v224 offset:49152\n"
"v_mfma_f32_16x16x32_bf16 v[12:15], a[32:35], a[12:15], v[12:15]\n"
"s_lshl_b32 s62, s48, 1\n"
"s_add_u32 s63, s62, 1\n"
"s_mov_b32 s61, 147456\n"
"v_mfma_f32_16x16x32_bf16 v[16:19], a[32:35], a[16:19], v[16:19]\n"
"ds_read_b64_tr_b16 a[66:67], v224 offset:50176\n"
"v_mfma_f32_16x16x32_bf16 v[20:23], a[32:35], a[20:23], v[20:23]\n"
"s_lshr_b32 s56, s49, 2\n"
"s_lshl_b32 s56, s56, 15\n"
"s_and_b32 s57, s49, 3\n"
"v_mfma_f32_16x16x32_bf16 v[24:27], a[32:35], a[24:27], v[24:27]\n"
"ds_read_b128 a[100:103], v232 offset:34816\n"
"v_mfma_f32_16x16x32_bf16 v[28:31], a[32:35], a[28:31], v[28:31]\n"
"s_lshl_b32 s57, s57, 7\n"
"s_add_u32 s56, s56, s57\n"
"s_lshl_b32 s57, s48, 13\n"
"v_mfma_f32_16x16x32_bf16 v[32:35], a[36:39], a[0:3], v[32:35]\n"
"ds_read_b64_tr_b16 a[68:69], v225 offset:49152\n"
"v_mfma_f32_16x16x32_bf16 v[36:39], a[36:39], a[4:7], v[36:39]\n"
"s_add_u32 s56, s56, s57\n"
"s_add_u32 s74, s56, 1048576\n"
"s_add_u32 s75, s74, 4096\n"
"v_mfma_f32_16x16x32_bf16 v[40:43], a[36:39], a[8:11], v[40:43]\n"
"ds_read_b64_tr_b16 a[70:71], v225 offset:50176\n"
"v_mfma_f32_16x16x32_bf16 v[44:47], a[36:39], a[12:15], v[44:47]\n"
"v_add_u32 v254, 155648, v239\n"
"v_mfma_f32_16x16x32_bf16 v[48:51], a[36:39], a[16:19], v[48:51]\n"
"ds_read_b128 a[104:107], v232 offset:36864\n"
"v_mfma_f32_16x16x32_bf16 v[52:55], a[36:39], a[20:23], v[52:55]\n"
"v_mfma_f32_16x16x32_bf16 v[56:59], a[36:39], a[24:27], v[56:59]\n"
"ds_read_b64_tr_b16 a[72:73], v226 offset:49152\n"
"v_mfma_f32_16x16x32_bf16 v[60:63], a[36:39], a[28:31], v[60:63]\n"
"buffer_load_dwordx4 v238, s[28:31], s55 offen offset:1024 lds\n"
"v_mfma_f32_16x16x32_bf16 v[64:67], a[40:43], a[0:3], v[64:67]\n"
"ds_read_b64_tr_b16 a[74:75], v226 offset:50176\n"
"v_mfma_f32_16x16x32_bf16 v[68:71], a[40:43], a[4:7], v[68:71]\n"
"v_mfma_f32_16x16x32_bf16 v[72:75], a[40:43], a[8:11], v[72:75]\n"
"ds_read_b128 a[108:111], v232 offset:38912\n"
"v_mfma_f32_16x16x32_bf16 v[76:79], a[40:43], a[12:15], v[76:79]\n"
"v_mfma_f32_16x16x32_bf16 v[80:83], a[40:43], a[16:19], v[80:83]\n"
"ds_read_b64_tr_b16 a[76:77], v227 offset:49152\n"
"v_mfma_f32_16x16x32_bf16 v[84:87], a[40:43], a[20:23], v[84:87]\n"
"v_mfma_f32_16x16x32_bf16 v[88:91], a[40:43], a[24:27], v[88:91]\n"
"ds_read_b64_tr_b16 a[78:79], v227 offset:50176\n"
"v_mfma_f32_16x16x32_bf16 v[92:95], a[40:43], a[28:31], v[92:95]\n"
"v_mfma_f32_16x16x32_bf16 v[96:99], a[44:47], a[0:3], v[96:99]\n"
"ds_read_b128 a[112:115], v232 offset:49152\n"
"v_mfma_f32_16x16x32_bf16 v[100:103], a[44:47], a[4:7], v[100:103]\n"
"v_mfma_f32_16x16x32_bf16 v[104:107], a[44:47], a[8:11], v[104:107]\n"
"ds_read_b64_tr_b16 a[80:81], v228 offset:49152\n"
"v_mfma_f32_16x16x32_bf16 v[108:111], a[44:47], a[12:15], v[108:111]\n"
"s_add_u32 m0, s53, 40960\n"
"v_mfma_f32_16x16x32_bf16 v[112:115], a[44:47], a[16:19], v[112:115]\n"
"ds_read_b64_tr_b16 a[82:83], v228 offset:50176\n"
"v_mfma_f32_16x16x32_bf16 v[116:119], a[44:47], a[20:23], v[116:119]\n"
"buffer_load_dwordx4 v237, s[32:35], s55 offen offset:0 lds\n"
"v_mfma_f32_16x16x32_bf16 v[120:123], a[44:47], a[24:27], v[120:123]\n"
"ds_read_b128 a[116:119], v232 offset:51200\n"
"v_mfma_f32_16x16x32_bf16 v[124:127], a[44:47], a[28:31], v[124:127]\n"
"v_mfma_f32_16x16x32_bf16 v[128:131], a[48:51], a[0:3], v[128:131]\n"
"ds_read_b64_tr_b16 a[84:85], v229 offset:49152\n"
"v_mfma_f32_16x16x32_bf16 v[132:135], a[48:51], a[4:7], v[132:135]\n"
"v_mfma_f32_16x16x32_bf16 v[136:139], a[48:51], a[8:11], v[136:139]\n"
"ds_read_b64_tr_b16 a[86:87], v229 offset:50176\n"
"v_mfma_f32_16x16x32_bf16 v[140:143], a[48:51], a[12:15], v[140:143]\n"
"v_mfma_f32_16x16x32_bf16 v[144:147], a[48:51], a[16:19], v[144:147]\n"
"ds_read_b128 a[120:123], v232 offset:53248\n"
"v_mfma_f32_16x16x32_bf16 v[148:151], a[48:51], a[20:23], v[148:151]\n"
"v_mfma_f32_16x16x32_bf16 v[152:155], a[48:51], a[24:27], v[152:155]\n"
"ds_read_b64_tr_b16 a[88:89], v230 offset:49152\n"
"v_mfma_f32_16x16x32_bf16 v[156:159], a[48:51], a[28:31], v[156:159]\n"
"v_mfma_f32_16x16x32_bf16 v[160:163], a[52:55], a[0:3], v[160:163]\n"
"ds_read_b64_tr_b16 a[90:91], v230 offset:50176\n"
"v_mfma_f32_16x16x32_bf16 v[164:167], a[52:55], a[4:7], v[164:167]\n"
"v_mfma_f32_16x16x32_bf16 v[168:171], a[52:55], a[8:11], v[168:171]\n"
"ds_read_b128 a[124:127], v232 offset:55296\n"
"v_mfma_f32_16x16x32_bf16 v[172:175], a[52:55], a[12:15], v[172:175]\n"
"buffer_load_dwordx4 v238, s[32:35], s55 offen offset:1024 lds\n"
"v_mfma_f32_16x16x32_bf16 v[176:179], a[52:55], a[16:19], v[176:179]\n"
"ds_read_b64_tr_b16 a[92:93], v231 offset:49152\n"
"v_mfma_f32_16x16x32_bf16 v[180:183], a[52:55], a[20:23], v[180:183]\n"
"v_mfma_f32_16x16x32_bf16 v[184:187], a[52:55], a[24:27], v[184:187]\n"
"ds_read_b64_tr_b16 a[94:95], v231 offset:50176\n"
"v_mfma_f32_16x16x32_bf16 v[188:191], a[52:55], a[28:31], v[188:191]\n"
"v_mfma_f32_16x16x32_bf16 v[192:195], a[56:59], a[0:3], v[192:195]\n"
"v_mfma_f32_16x16x32_bf16 v[196:199], a[56:59], a[4:7], v[196:199]\n"
"v_mfma_f32_16x16x32_bf16 v[200:203], a[56:59], a[8:11], v[200:203]\n"
"v_mfma_f32_16x16x32_bf16 v[204:207], a[56:59], a[12:15], v[204:207]\n"
"v_mfma_f32_16x16x32_bf16 v[208:211], a[56:59], a[16:19], v[208:211]\n"
"v_mfma_f32_16x16x32_bf16 v[212:215], a[56:59], a[20:23], v[212:215]\n"
"v_mfma_f32_16x16x32_bf16 v[216:219], a[56:59], a[24:27], v[216:219]\n"
"v_mfma_f32_16x16x32_bf16 v[220:223], a[56:59], a[28:31], v[220:223]\n"
"v_mfma_f32_16x16x32_bf16 a[128:131], a[60:63], a[0:3], a[128:131]\n"
"v_mfma_f32_16x16x32_bf16 a[132:135], a[60:63], a[4:7], a[132:135]\n"
"v_mfma_f32_16x16x32_bf16 a[136:139], a[60:63], a[8:11], a[136:139]\n"
"s_waitcnt vmcnt(8) lgkmcnt(0)\n"
"v_mfma_f32_16x16x32_bf16 a[140:143], a[60:63], a[12:15], a[140:143]\n"
"s_barrier\n"
"v_mfma_f32_16x16x32_bf16 a[144:147], a[60:63], a[16:19], a[144:147]\n"
"v_mfma_f32_16x16x32_bf16 a[148:151], a[60:63], a[20:23], a[148:151]\n"
"v_mfma_f32_16x16x32_bf16 a[152:155], a[60:63], a[24:27], a[152:155]\n"
"v_mfma_f32_16x16x32_bf16 a[156:159], a[60:63], a[28:31], a[156:159]\n"
"v_xor_b32 v232, 64, v232\n"
"v_mfma_f32_16x16x32_bf16 v[0:3], a[96:99], a[64:67], v[0:3]\n"
"ds_read_b128 a[200:203], v254\n"
"ds_read_b128 a[204:207], v254 offset:16\n"
"ds_read_b128 a[208:211], v254 offset:2048\n"
"ds_read_b128 a[212:215], v254 offset:2064\n"
"ds_read_b128 a[32:35], v232 offset:0\n"
"v_accvgpr_read_b32 v250, a192\n"
"v_lshlrev_b32 v240, 16, v250\n"
"s_add_u32 m0, s52, 32768\n"
"s_mov_b32 s54, 640\n"
"v_mfma_f32_16x16x32_bf16 v[4:7], a[96:99], a[68:71], v[4:7]\n"
"buffer_load_dwordx4 v233, s[24:27], s54 offen offset:0 lds\n"
"v_and_b32 v241, 0xffff0000, v250\n"
"v_accvgpr_read_b32 v251, a193\n"
"v_mfma_f32_16x16x32_bf16 v[8:11], a[96:99], a[72:75], v[8:11]\n"
"ds_read_b64_tr_b16 a[0:1], v224 offset:0\n"
"v_lshlrev_b32 v242, 16, v251\n"
"v_and_b32 v243, 0xffff0000, v251\n"
"v_mfma_f32_16x16x32_bf16 v[12:15], a[96:99], a[76:79], v[12:15]\n"
"v_accvgpr_read_b32 v250, a194\n"
"v_lshlrev_b32 v244, 16, v250\n"
"v_mfma_f32_16x16x32_bf16 v[16:19], a[96:99], a[80:83], v[16:19]\n"
"ds_read_b64_tr_b16 a[2:3], v224 offset:1024\n"
"v_and_b32 v245, 0xffff0000, v250\n"
"v_accvgpr_read_b32 v251, a195\n"
"v_mfma_f32_16x16x32_bf16 v[20:23], a[96:99], a[84:87], v[20:23]\n"
"buffer_load_dwordx4 v234, s[24:27], s54 offen offset:1024 lds\n"
"v_lshlrev_b32 v246, 16, v251\n"
"v_and_b32 v247, 0xffff0000, v251\n"
"v_mfma_f32_16x16x32_bf16 v[24:27], a[96:99], a[88:91], v[24:27]\n"
"ds_read_b128 a[36:39], v232 offset:2048\n"
"v_add_f32 v248, v240, v241\n"
"v_add_f32 v248, v248, v242\n"
"v_mfma_f32_16x16x32_bf16 v[28:31], a[96:99], a[92:95], v[28:31]\n"
"v_add_f32 v248, v248, v243\n"
"v_add_f32 v248, v248, v244\n"
"v_mfma_f32_16x16x32_bf16 v[32:35], a[100:103], a[64:67], v[32:35]\n"
"ds_read_b64_tr_b16 a[4:5], v225 offset:0\n"
"v_add_f32 v248, v248, v245\n"
"v_add_f32 v248, v248, v246\n"
"v_mfma_f32_16x16x32_bf16 v[36:39], a[100:103], a[68:71], v[36:39]\n"
"s_waitcnt lgkmcnt(5)\n"
"buffer_load_dwordx4 v235, s[24:27], s54 offen offset:2048 lds\n"
"v_add_f32 v248, v248, v247\n"
"s_nop 1\n"
"v_add_f32_dpp v248, v248, v248 quad_perm:[1,0,3,2] row_mask:0xf bank_mask:0xf\n"
"v_mfma_f32_16x16x32_bf16 v[40:43], a[100:103], a[72:75], v[40:43]\n"
"ds_read_b64_tr_b16 a[6:7], v225 offset:1024\n"
"v_add_f32_dpp v248, v248, v248 quad_perm:[2,3,0,1] row_mask:0xf bank_mask:0xf\n"
"s_nop 1\n"
"v_add_f32_dpp v248, v248, v248 row_half_mirror row_mask:0xf bank_mask:0xf\n"
"v_mfma_f32_16x16x32_bf16 v[44:47], a[100:103], a[76:79], v[44:47]\n"
"s_nop 0\n"
"v_add_f32_dpp v248, v248, v248 row_mirror row_mask:0xf bank_mask:0xf\n"
"v_mov_b32 v249, v248\n"
"v_mfma_f32_16x16x32_bf16 v[48:51], a[100:103], a[80:83], v[48:51]\n"
"ds_read_b128 a[40:43], v232 offset:4096\n"
"v_permlane16_swap_b32 v248, v249\n"
"v_add_f32 v248, v248, v249\n"
"v_mfma_f32_16x16x32_bf16 v[52:55], a[100:103], a[84:87], v[52:55]\n"
"buffer_load_dwordx4 v236, s[24:27], s54 offen offset:3072 lds\n"
"v_mov_b32 v249, v248\n"
"s_nop 1\n"
"v_permlane32_swap_b32 v248, v249\n"
"v_mfma_f32_16x16x32_bf16 v[56:59], a[100:103], a[88:91], v[56:59]\n"
"ds_read_b64_tr_b16 a[8:9], v226 offset:0\n"
"v_add_f32 v248, v248, v249\n"
"v_mfma_f32_16x16x32_bf16 v[60:63], a[100:103], a[92:95], v[60:63]\n"
"v_mul_f32 v248, 0x3b000000, v248\n"
"v_sub_f32 v240, v240, v248\n"
"s_add_u32 m0, s52, 36864\n"
"s_mov_b32 s54, 33408\n"
"v_mfma_f32_16x16x32_bf16 v[64:67], a[104:107], a[64:67], v[64:67]\n"
"ds_read_b64_tr_b16 a[10:11], v226 offset:1024\n"
"v_sub_f32 v241, v241, v248\n"
"v_sub_f32 v242, v242, v248\n"
"v_mfma_f32_16x16x32_bf16 v[68:71], a[104:107], a[68:71], v[68:71]\n"
"buffer_load_dwordx4 v233, s[24:27], s54 offen offset:0 lds\n"
"v_sub_f32 v243, v243, v248\n"
"v_sub_f32 v244, v244, v248\n"
"v_mfma_f32_16x16x32_bf16 v[72:75], a[104:107], a[72:75], v[72:75]\n"
"ds_read_b128 a[44:47], v232 offset:6144\n"
"v_sub_f32 v245, v245, v248\n"
"v_sub_f32 v246, v246, v248\n"
"v_mfma_f32_16x16x32_bf16 v[76:79], a[104:107], a[76:79], v[76:79]\n"
"v_sub_f32 v247, v247, v248\n"
"v_mul_f32 v248, v240, v240\n"
"v_mfma_f32_16x16x32_bf16 v[80:83], a[104:107], a[80:83], v[80:83]\n"
"ds_read_b64_tr_b16 a[12:13], v227 offset:0\n"
"v_fmac_f32 v248, v241, v241\n"
"v_fmac_f32 v248, v242, v242\n"
"v_mfma_f32_16x16x32_bf16 v[84:87], a[104:107], a[84:87], v[84:87]\n"
"buffer_load_dwordx4 v234, s[24:27], s54 offen offset:1024 lds\n"
"v_fmac_f32 v248, v243, v243\n"
"v_fmac_f32 v248, v244, v244\n"
"v_mfma_f32_16x16x32_bf16 v[88:91], a[104:107], a[88:91], v[88:91]\n"
"ds_read_b64_tr_b16 a[14:15], v227 offset:1024\n"
"v_fmac_f32 v248, v245, v245\n"
"v_fmac_f32 v248, v246, v246\n"
"v_mfma_f32_16x16x32_bf16 v[92:95], a[104:107], a[92:95], v[92:95]\n"
"v_fmac_f32 v248, v247, v247\n"
"s_nop 1\n"
"v_add_f32_dpp v248, v248, v248 quad_perm:[1,0,3,2] row_mask:0xf bank_mask:0xf\n"
"v_mfma_f32_16x16x32_bf16 v[96:99], a[108:111], a[64:67], v[96:99]\n"
"ds_read_b128 a[48:51], v232 offset:16384\n"
"v_add_f32_dpp v248, v248, v248 quad_perm:[2,3,0,1] row_mask:0xf bank_mask:0xf\n"
"s_nop 1\n"
"v_add_f32_dpp v248, v248, v248 row_half_mirror row_mask:0xf bank_mask:0xf\n"
"v_mfma_f32_16x16x32_bf16 v[100:103], a[108:111], a[68:71], v[100:103]\n"
"buffer_load_dwordx4 v235, s[24:27], s54 offen offset:2048 lds\n"
"v_add_f32_dpp v248, v248, v248 row_mirror row_mask:0xf bank_mask:0xf\n"
"v_mov_b32 v249, v248\n"
"v_mfma_f32_16x16x32_bf16 v[104:107], a[108:111], a[72:75], v[104:107]\n"
"ds_read_b64_tr_b16 a[16:17], v228 offset:0\n"
"v_permlane16_swap_b32 v248, v249\n"
"v_add_f32 v248, v248, v249\n"
"v_mfma_f32_16x16x32_bf16 v[108:111], a[108:111], a[76:79], v[108:111]\n"
"v_mov_b32 v249, v248\n"
"s_nop 1\n"
"v_permlane32_swap_b32 v248, v249\n"
"v_mfma_f32_16x16x32_bf16 v[112:115], a[108:111], a[80:83], v[112:115]\n"
"ds_read_b64_tr_b16 a[18:19], v228 offset:1024\n"
"v_add_f32 v248, v248, v249\n"
"v_mfma_f32_16x16x32_bf16 v[116:119], a[108:111], a[84:87], v[116:119]\n"
"buffer_load_dwordx4 v236, s[24:27], s54 offen offset:3072 lds\n"
"v_mul_f32 v248, 0x3b000000, v248\n"
"v_add_f32 v248, 0x3727c5ac, v248\n"
"s_add_u32 m0, s53, 49152\n"
"s_mov_b32 s55, 2883584\n"
"v_mfma_f32_16x16x32_bf16 v[120:123], a[108:111], a[88:91], v[120:123]\n"
"ds_read_b128 a[52:55], v232 offset:18432\n"
"v_rsq_f32 v248, v248\n"
"s_nop 0\n"
"v_mul_f32 v248, s59, v248\n"
"v_mfma_f32_16x16x32_bf16 v[124:127], a[108:111], a[92:95], v[124:127]\n"
"buffer_load_dwordx4 v237, s[28:31], s55 offen offset:0 lds\n"
"v_bfe_u32 v249, v239, 5, 3\n"
"v_xor_b32 v249, s62, v249\n"
"v_mfma_f32_16x16x32_bf16 v[128:131], a[112:115], a[64:67], v[128:131]\n"
"ds_read_b64_tr_b16 a[20:21], v229 offset:0\n"
"v_lshlrev_b32 v249, 4, v249\n"
"v_lshlrev_b32 v250, 2, v239\n"
"v_mfma_f32_16x16x32_bf16 v[132:135], a[112:115], a[68:71], v[132:135]\n"
"v_add3_u32 v249, v249, v250, s61\n"
"v_mul_f32 v240, v240, v248\n"
"v_mfma_f32_16x16x32_bf16 v[136:139], a[112:115], a[72:75], v[136:139]\n"
"ds_read_b64_tr_b16 a[22:23], v229 offset:1024\n"
"v_mul_f32 v241, v241, v248\n"
"v_mul_f32 v242, v242, v248\n"
"v_mfma_f32_16x16x32_bf16 v[140:143], a[112:115], a[76:79], v[140:143]\n"
"v_mul_f32 v243, v243, v248\n"
"v_mul_f32 v244, v244, v248\n"
"v_mfma_f32_16x16x32_bf16 v[144:147], a[112:115], a[80:83], v[144:147]\n"
"ds_read_b128 a[56:59], v232 offset:20480\n"
"v_mul_f32 v245, v245, v248\n"
"v_mul_f32 v246, v246, v248\n"
"v_mfma_f32_16x16x32_bf16 v[148:151], a[112:115], a[84:87], v[148:151]\n"
"buffer_load_dwordx4 v238, s[28:31], s55 offen offset:1024 lds\n"
"v_mul_f32 v247, v247, v248\n"
"v_accvgpr_read_b32 v250, a208\n"
"v_mfma_f32_16x16x32_bf16 v[152:155], a[112:115], a[88:91], v[152:155]\n"
"ds_read_b64_tr_b16 a[24:25], v230 offset:0\n"
"v_accvgpr_read_b32 v251, a200\n"
"v_fma_f32 v240, v240, v250, v240\n"
"v_mfma_f32_16x16x32_bf16 v[156:159], a[112:115], a[92:95], v[156:159]\n"
"v_fmac_f32 v240, s59, v251\n"
"v_accvgpr_read_b32 v250, a209\n"
"v_mfma_f32_16x16x32_bf16 v[160:163], a[116:119], a[64:67], v[160:163]\n"
"ds_read_b64_tr_b16 a[26:27], v230 offset:1024\n"
"v_accvgpr_read_b32 v251, a201\n"
"v_fma_f32 v241, v241, v250, v241\n"
"v_mfma_f32_16x16x32_bf16 v[164:167], a[116:119], a[68:71], v[164:167]\n"
"v_fmac_f32 v241, s59, v251\n"
"v_accvgpr_read_b32 v250, a210\n"
"v_mfma_f32_16x16x32_bf16 v[168:171], a[116:119], a[72:75], v[168:171]\n"
"ds_read_b128 a[60:63], v232 offset:22528\n"
"v_accvgpr_read_b32 v251, a202\n"
"v_mfma_f32_16x16x32_bf16 v[172:175], a[116:119], a[76:79], v[172:175]\n"
"v_fma_f32 v242, v242, v250, v242\n"
"v_fmac_f32 v242, s59, v251\n"
"s_add_u32 m0, s53, 57344\n"
"v_mfma_f32_16x16x32_bf16 v[176:179], a[116:119], a[80:83], v[176:179]\n"
"ds_read_b64_tr_b16 a[28:29], v231 offset:0\n"
"v_accvgpr_read_b32 v250, a211\n"
"v_accvgpr_read_b32 v251, a203\n"
"v_mfma_f32_16x16x32_bf16 v[180:183], a[116:119], a[84:87], v[180:183]\n"
"buffer_load_dwordx4 v237, s[32:35], s55 offen offset:0 lds\n"
"v_fma_f32 v243, v243, v250, v243\n"
"v_fmac_f32 v243, s59, v251\n"
"v_mfma_f32_16x16x32_bf16 v[184:187], a[116:119], a[88:91], v[184:187]\n"
"ds_read_b64_tr_b16 a[30:31], v231 offset:1024\n"
"v_accvgpr_read_b32 v250, a212\n"
"v_accvgpr_read_b32 v251, a204\n"
"v_mfma_f32_16x16x32_bf16 v[188:191], a[116:119], a[92:95], v[188:191]\n"
"v_fma_f32 v244, v244, v250, v244\n"
"v_fmac_f32 v244, s59, v251\n"
"v_mfma_f32_16x16x32_bf16 v[192:195], a[120:123], a[64:67], v[192:195]\n"
"v_accvgpr_read_b32 v250, a213\n"
"v_accvgpr_read_b32 v251, a205\n"
"v_mfma_f32_16x16x32_bf16 v[196:199], a[120:123], a[68:71], v[196:199]\n"
"v_fma_f32 v245, v245, v250, v245\n"
"v_fmac_f32 v245, s59, v251\n"
"v_mfma_f32_16x16x32_bf16 v[200:203], a[120:123], a[72:75], v[200:203]\n"
"v_accvgpr_read_b32 v250, a214\n"
"v_accvgpr_read_b32 v251, a206\n"
"v_mfma_f32_16x16x32_bf16 v[204:207], a[120:123], a[76:79], v[204:207]\n"
"buffer_load_dwordx4 v238, s[32:35], s55 offen offset:1024 lds\n"
"v_fma_f32 v246, v246, v250, v246\n"
"v_fmac_f32 v246, s59, v251\n"
"v_mfma_f32_16x16x32_bf16 v[208:211], a[120:123], a[80:83], v[208:211]\n"
"v_accvgpr_read_b32 v250, a215\n"
"v_accvgpr_read_b32 v251, a207\n"
"v_mfma_f32_16x16x32_bf16 v[212:215], a[120:123], a[84:87], v[212:215]\n"
"v_fma_f32 v247, v247, v250, v247\n"
"v_fmac_f32 v247, s59, v251\n"
"v_mfma_f32_16x16x32_bf16 v[216:219], a[120:123], a[88:91], v[216:219]\n"
"v_cvt_pk_bf16_f32 v252, v240, v241\n"
"v_cvt_pk_bf16_f32 v253, v242, v243\n"
"v_mfma_f32_16x16x32_bf16 v[220:223], a[120:123], a[92:95], v[220:223]\n"
"v_cvt_pk_bf16_f32 v254, v244, v245\n"
"v_cvt_pk_bf16_f32 v255, v246, v247\n"
"v_mfma_f32_16x16x32_bf16 a[128:131], a[124:127], a[64:67], a[128:131]\n"
"ds_write_b128 v249, v[252:255]\n"
"v_mfma_f32_16x16x32_bf16 a[132:135], a[124:127], a[68:71], a[132:135]\n"
"v_mfma_f32_16x16x32_bf16 a[136:139], a[124:127], a[72:75], a[136:139]\n"
"s_waitcnt vmcnt(16) lgkmcnt(0)\n"
"v_mfma_f32_16x16x32_bf16 a[140:143], a[124:127], a[76:79], a[140:143]\n"
"s_barrier\n"
"v_mfma_f32_16x16x32_bf16 a[144:147], a[124:127], a[80:83], a[144:147]\n"
"v_mfma_f32_16x16x32_bf16 a[148:151], a[124:127], a[84:87], a[148:151]\n"
"v_mfma_f32_16x16x32_bf16 a[152:155], a[124:127], a[88:91], a[152:155]\n"
"v_mfma_f32_16x16x32_bf16 a[156:159], a[124:127], a[92:95], a[156:159]\n"
"v_xor_b32 v232, 64, v232\n"
"v_mfma_f32_16x16x32_bf16 v[0:3], a[32:35], a[0:3], v[0:3]\n"
"ds_read_b128 a[96:99], v232 offset:0\n"
"v_accvgpr_read_b32 v250, a196\n"
"v_lshlrev_b32 v240, 16, v250\n"
"s_add_u32 m0, s53, 0\n"
"s_mov_b32 s55, 3145728\n"
"v_mfma_f32_16x16x32_bf16 v[4:7], a[32:35], a[4:7], v[4:7]\n"
"buffer_load_dwordx4 v237, s[28:31], s55 offen offset:0 lds\n"
"v_and_b32 v241, 0xffff0000, v250\n"
"v_accvgpr_read_b32 v251, a197\n"
"v_mfma_f32_16x16x32_bf16 v[8:11], a[32:35], a[8:11], v[8:11]\n"
"ds_read_b64_tr_b16 a[64:65], v224 offset:16384\n"
"v_lshlrev_b32 v242, 16, v251\n"
"v_and_b32 v243, 0xffff0000, v251\n"
"v_mfma_f32_16x16x32_bf16 v[12:15], a[32:35], a[12:15], v[12:15]\n"
"v_accvgpr_read_b32 v250, a198\n"
"v_lshlrev_b32 v244, 16, v250\n"
"v_mfma_f32_16x16x32_bf16 v[16:19], a[32:35], a[16:19], v[16:19]\n"
"ds_read_b64_tr_b16 a[66:67], v224 offset:17408\n"
"v_and_b32 v245, 0xffff0000, v250\n"
"v_accvgpr_read_b32 v251, a199\n"
"v_mfma_f32_16x16x32_bf16 v[20:23], a[32:35], a[20:23], v[20:23]\n"
"v_lshlrev_b32 v246, 16, v251\n"
"v_and_b32 v247, 0xffff0000, v251\n"
"v_mfma_f32_16x16x32_bf16 v[24:27], a[32:35], a[24:27], v[24:27]\n"
"ds_read_b128 a[100:103], v232 offset:2048\n"
"v_add_f32 v248, v240, v241\n"
"v_add_f32 v248, v248, v242\n"
"v_mfma_f32_16x16x32_bf16 v[28:31], a[32:35], a[28:31], v[28:31]\n"
"v_add_f32 v248, v248, v243\n"
"v_add_f32 v248, v248, v244\n"
"v_mfma_f32_16x16x32_bf16 v[32:35], a[36:39], a[0:3], v[32:35]\n"
"ds_read_b64_tr_b16 a[68:69], v225 offset:16384\n"
"v_add_f32 v248, v248, v245\n"
"v_add_f32 v248, v248, v246\n"
"v_mfma_f32_16x16x32_bf16 v[36:39], a[36:39], a[4:7], v[36:39]\n"
"v_add_f32 v248, v248, v247\n"
"s_nop 1\n"
"v_add_f32_dpp v248, v248, v248 quad_perm:[1,0,3,2] row_mask:0xf bank_mask:0xf\n"
"v_mfma_f32_16x16x32_bf16 v[40:43], a[36:39], a[8:11], v[40:43]\n"
"ds_read_b64_tr_b16 a[70:71], v225 offset:17408\n"
"v_add_f32_dpp v248, v248, v248 quad_perm:[2,3,0,1] row_mask:0xf bank_mask:0xf\n"
"s_nop 1\n"
"v_add_f32_dpp v248, v248, v248 row_half_mirror row_mask:0xf bank_mask:0xf\n"
"v_mfma_f32_16x16x32_bf16 v[44:47], a[36:39], a[12:15], v[44:47]\n"
"s_nop 0\n"
"v_add_f32_dpp v248, v248, v248 row_mirror row_mask:0xf bank_mask:0xf\n"
"v_mov_b32 v249, v248\n"
"v_mfma_f32_16x16x32_bf16 v[48:51], a[36:39], a[16:19], v[48:51]\n"
"ds_read_b128 a[104:107], v232 offset:4096\n"
"v_permlane16_swap_b32 v248, v249\n"
"v_add_f32 v248, v248, v249\n"
"v_mfma_f32_16x16x32_bf16 v[52:55], a[36:39], a[20:23], v[52:55]\n"
"v_mov_b32 v249, v248\n"
"s_nop 1\n"
"v_permlane32_swap_b32 v248, v249\n"
"v_mfma_f32_16x16x32_bf16 v[56:59], a[36:39], a[24:27], v[56:59]\n"
"ds_read_b64_tr_b16 a[72:73], v226 offset:16384\n"
"v_add_f32 v248, v248, v249\n"
"v_mfma_f32_16x16x32_bf16 v[60:63], a[36:39], a[28:31], v[60:63]\n"
"buffer_load_dwordx4 v238, s[28:31], s55 offen offset:1024 lds\n"
"v_mul_f32 v248, 0x3b000000, v248\n"
"v_sub_f32 v240, v240, v248\n"
"v_mfma_f32_16x16x32_bf16 v[64:67], a[40:43], a[0:3], v[64:67]\n"
"ds_read_b64_tr_b16 a[74:75], v226 offset:17408\n"
"v_sub_f32 v241, v241, v248\n"
"v_sub_f32 v242, v242, v248\n"
"v_mfma_f32_16x16x32_bf16 v[68:71], a[40:43], a[4:7], v[68:71]\n"
"v_sub_f32 v243, v243, v248\n"
"v_sub_f32 v244, v244, v248\n"
"v_mfma_f32_16x16x32_bf16 v[72:75], a[40:43], a[8:11], v[72:75]\n"
"ds_read_b128 a[108:111], v232 offset:6144\n"
"v_sub_f32 v245, v245, v248\n"
"v_sub_f32 v246, v246, v248\n"
"v_mfma_f32_16x16x32_bf16 v[76:79], a[40:43], a[12:15], v[76:79]\n"
"v_sub_f32 v247, v247, v248\n"
"v_mul_f32 v248, v240, v240\n"
"v_mfma_f32_16x16x32_bf16 v[80:83], a[40:43], a[16:19], v[80:83]\n"
"ds_read_b64_tr_b16 a[76:77], v227 offset:16384\n"
"v_fmac_f32 v248, v241, v241\n"
"v_fmac_f32 v248, v242, v242\n"
"v_mfma_f32_16x16x32_bf16 v[84:87], a[40:43], a[20:23], v[84:87]\n"
"v_fmac_f32 v248, v243, v243\n"
"v_fmac_f32 v248, v244, v244\n"
"v_mfma_f32_16x16x32_bf16 v[88:91], a[40:43], a[24:27], v[88:91]\n"
"ds_read_b64_tr_b16 a[78:79], v227 offset:17408\n"
"v_fmac_f32 v248, v245, v245\n"
"v_fmac_f32 v248, v246, v246\n"
"v_mfma_f32_16x16x32_bf16 v[92:95], a[40:43], a[28:31], v[92:95]\n"
"v_fmac_f32 v248, v247, v247\n"
"s_nop 1\n"
"v_add_f32_dpp v248, v248, v248 quad_perm:[1,0,3,2] row_mask:0xf bank_mask:0xf\n"
"v_mfma_f32_16x16x32_bf16 v[96:99], a[44:47], a[0:3], v[96:99]\n"
"ds_read_b128 a[112:115], v232 offset:16384\n"
"v_add_f32_dpp v248, v248, v248 quad_perm:[2,3,0,1] row_mask:0xf bank_mask:0xf\n"
"s_nop 1\n"
"v_add_f32_dpp v248, v248, v248 row_half_mirror row_mask:0xf bank_mask:0xf\n"
"v_mfma_f32_16x16x32_bf16 v[100:103], a[44:47], a[4:7], v[100:103]\n"
"s_nop 0\n"
"v_add_f32_dpp v248, v248, v248 row_mirror row_mask:0xf bank_mask:0xf\n"
"v_mov_b32 v249, v248\n"
"v_mfma_f32_16x16x32_bf16 v[104:107], a[44:47], a[8:11], v[104:107]\n"
"ds_read_b64_tr_b16 a[80:81], v228 offset:16384\n"
"v_permlane16_swap_b32 v248, v249\n"
"v_add_f32 v248, v248, v249\n"
"v_mfma_f32_16x16x32_bf16 v[108:111], a[44:47], a[12:15], v[108:111]\n"
"v_mov_b32 v249, v248\n"
"s_nop 1\n"
"v_permlane32_swap_b32 v248, v249\n"
"s_add_u32 m0, s53, 8192\n"
"v_mfma_f32_16x16x32_bf16 v[112:115], a[44:47], a[16:19], v[112:115]\n"
"ds_read_b64_tr_b16 a[82:83], v228 offset:17408\n"
"v_add_f32 v248, v248, v249\n"
"v_mfma_f32_16x16x32_bf16 v[116:119], a[44:47], a[20:23], v[116:119]\n"
"buffer_load_dwordx4 v237, s[32:35], s55 offen offset:0 lds\n"
"v_mul_f32 v248, 0x3b000000, v248\n"
"v_add_f32 v248, 0x3727c5ac, v248\n"
"v_mfma_f32_16x16x32_bf16 v[120:123], a[44:47], a[24:27], v[120:123]\n"
"ds_read_b128 a[116:119], v232 offset:18432\n"
"v_rsq_f32 v248, v248\n"
"s_nop 0\n"
"v_mul_f32 v248, s59, v248\n"
"v_mfma_f32_16x16x32_bf16 v[124:127], a[44:47], a[28:31], v[124:127]\n"
"v_bfe_u32 v249, v239, 5, 3\n"
"v_xor_b32 v249, s63, v249\n"
"v_mfma_f32_16x16x32_bf16 v[128:131], a[48:51], a[0:3], v[128:131]\n"
"ds_read_b64_tr_b16 a[84:85], v229 offset:16384\n"
"v_lshlrev_b32 v249, 4, v249\n"
"v_lshlrev_b32 v250, 2, v239\n"
"v_mfma_f32_16x16x32_bf16 v[132:135], a[48:51], a[4:7], v[132:135]\n"
"v_add3_u32 v249, v249, v250, s61\n"
"v_mul_f32 v240, v240, v248\n"
"v_mfma_f32_16x16x32_bf16 v[136:139], a[48:51], a[8:11], v[136:139]\n"
"ds_read_b64_tr_b16 a[86:87], v229 offset:17408\n"
"v_mul_f32 v241, v241, v248\n"
"v_mul_f32 v242, v242, v248\n"
"v_mfma_f32_16x16x32_bf16 v[140:143], a[48:51], a[12:15], v[140:143]\n"
"v_mul_f32 v243, v243, v248\n"
"v_mul_f32 v244, v244, v248\n"
"v_mfma_f32_16x16x32_bf16 v[144:147], a[48:51], a[16:19], v[144:147]\n"
"ds_read_b128 a[120:123], v232 offset:20480\n"
"v_mul_f32 v245, v245, v248\n"
"v_mul_f32 v246, v246, v248\n"
"v_mfma_f32_16x16x32_bf16 v[148:151], a[48:51], a[20:23], v[148:151]\n"
"v_mul_f32 v247, v247, v248\n"
"v_accvgpr_read_b32 v250, a208\n"
"v_mfma_f32_16x16x32_bf16 v[152:155], a[48:51], a[24:27], v[152:155]\n"
"ds_read_b64_tr_b16 a[88:89], v230 offset:16384\n"
"v_accvgpr_read_b32 v251, a200\n"
"v_fma_f32 v240, v240, v250, v240\n"
"v_mfma_f32_16x16x32_bf16 v[156:159], a[48:51], a[28:31], v[156:159]\n"
"v_fmac_f32 v240, s59, v251\n"
"v_accvgpr_read_b32 v250, a209\n"
"v_mfma_f32_16x16x32_bf16 v[160:163], a[52:55], a[0:3], v[160:163]\n"
"ds_read_b64_tr_b16 a[90:91], v230 offset:17408\n"
"v_accvgpr_read_b32 v251, a201\n"
"v_fma_f32 v241, v241, v250, v241\n"
"v_mfma_f32_16x16x32_bf16 v[164:167], a[52:55], a[4:7], v[164:167]\n"
"v_fmac_f32 v241, s59, v251\n"
"v_accvgpr_read_b32 v250, a210\n"
"v_mfma_f32_16x16x32_bf16 v[168:171], a[52:55], a[8:11], v[168:171]\n"
"ds_read_b128 a[124:127], v232 offset:22528\n"
"v_accvgpr_read_b32 v251, a202\n"
"v_mfma_f32_16x16x32_bf16 v[172:175], a[52:55], a[12:15], v[172:175]\n"
"buffer_load_dwordx4 v238, s[32:35], s55 offen offset:1024 lds\n"
"v_fma_f32 v242, v242, v250, v242\n"
"v_fmac_f32 v242, s59, v251\n"
"v_mfma_f32_16x16x32_bf16 v[176:179], a[52:55], a[16:19], v[176:179]\n"
"ds_read_b64_tr_b16 a[92:93], v231 offset:16384\n"
"v_accvgpr_read_b32 v250, a211\n"
"v_accvgpr_read_b32 v251, a203\n"
"v_mfma_f32_16x16x32_bf16 v[180:183], a[52:55], a[20:23], v[180:183]\n"
"v_fma_f32 v243, v243, v250, v243\n"
"v_fmac_f32 v243, s59, v251\n"
"v_mfma_f32_16x16x32_bf16 v[184:187], a[52:55], a[24:27], v[184:187]\n"
"ds_read_b64_tr_b16 a[94:95], v231 offset:17408\n"
"v_accvgpr_read_b32 v250, a212\n"
"v_accvgpr_read_b32 v251, a204\n"
"v_mfma_f32_16x16x32_bf16 v[188:191], a[52:55], a[28:31], v[188:191]\n"
"v_fma_f32 v244, v244, v250, v244\n"
"v_fmac_f32 v244, s59, v251\n"
"v_mfma_f32_16x16x32_bf16 v[192:195], a[56:59], a[0:3], v[192:195]\n"
"v_accvgpr_read_b32 v250, a213\n"
"v_accvgpr_read_b32 v251, a205\n"
"v_mfma_f32_16x16x32_bf16 v[196:199], a[56:59], a[4:7], v[196:199]\n"
"v_fma_f32 v245, v245, v250, v245\n"
"v_fmac_f32 v245, s59, v251\n"
"v_mfma_f32_16x16x32_bf16 v[200:203], a[56:59], a[8:11], v[200:203]\n"
"v_accvgpr_read_b32 v250, a214\n"
"v_accvgpr_read_b32 v251, a206\n"
"v_mfma_f32_16x16x32_bf16 v[204:207], a[56:59], a[12:15], v[204:207]\n"
"v_fma_f32 v246, v246, v250, v246\n"
"v_fmac_f32 v246, s59, v251\n"
"v_mfma_f32_16x16x32_bf16 v[208:211], a[56:59], a[16:19], v[208:211]\n"
"v_accvgpr_read_b32 v250, a215\n"
"v_accvgpr_read_b32 v251, a207\n"
"v_mfma_f32_16x16x32_bf16 v[212:215], a[56:59], a[20:23], v[212:215]\n"
"v_fma_f32 v247, v247, v250, v247\n"
"v_fmac_f32 v247, s59, v251\n"
"v_mfma_f32_16x16x32_bf16 v[216:219], a[56:59], a[24:27], v[216:219]\n"
"v_cvt_pk_bf16_f32 v252, v240, v241\n"
"v_cvt_pk_bf16_f32 v253, v242, v243\n"
"v_mfma_f32_16x16x32_bf16 v[220:223], a[56:59], a[28:31], v[220:223]\n"
"v_cvt_pk_bf16_f32 v254, v244, v245\n"
"v_cvt_pk_bf16_f32 v255, v246, v247\n"
"v_mfma_f32_16x16x32_bf16 a[128:131], a[60:63], a[0:3], a[128:131]\n"
"ds_write_b128 v249, v[252:255]\n"
"v_mfma_f32_16x16x32_bf16 a[132:135], a[60:63], a[4:7], a[132:135]\n"
"v_mfma_f32_16x16x32_bf16 a[136:139], a[60:63], a[8:11], a[136:139]\n"
"s_waitcnt vmcnt(8) lgkmcnt(0)\n"
"v_mfma_f32_16x16x32_bf16 a[140:143], a[60:63], a[12:15], a[140:143]\n"
"s_barrier\n"
"v_mfma_f32_16x16x32_bf16 a[144:147], a[60:63], a[16:19], a[144:147]\n"
"v_mfma_f32_16x16x32_bf16 a[148:151], a[60:63], a[20:23], a[148:151]\n"
"v_mfma_f32_16x16x32_bf16 a[152:155], a[60:63], a[24:27], a[152:155]\n"
"v_mfma_f32_16x16x32_bf16 a[156:159], a[60:63], a[28:31], a[156:159]\n"
"v_xor_b32 v232, 64, v232\n"
"s_lshl_b32 s62, s48, 11\n"
"s_add_u32 s62, s62, 147456\n"
"v_lshrrev_b32 v248, 1, v239\n"
"v_add_u32 v248, s62, v248\n"
"v_mfma_f32_16x16x32_bf16 v[0:3], a[96:99], a[64:67], v[0:3]\n"
"ds_read_b128 v[240:243], v248\n"
"ds_read_b128 v[244:247], v248 offset:1024\n"
"ds_read_b128 a[32:35], v232 offset:32768\n"
"s_add_u32 m0, s52, 0\n"
"s_mov_b32 s54, 768\n"
"v_mfma_f32_16x16x32_bf16 v[4:7], a[96:99], a[68:71], v[4:7]\n"
"buffer_load_dwordx4 v233, s[24:27], s54 offen offset:0 lds\n"
"v_lshrrev_b32 v249, 8, v239\n"
"v_mfma_f32_16x16x32_bf16 v[8:11], a[96:99], a[72:75], v[8:11]\n"
"ds_read_b64_tr_b16 a[0:1], v224 offset:32768\n"
"v_bfe_u32 v250, v239, 5, 3\n"
"v_mfma_f32_16x16x32_bf16 v[12:15], a[96:99], a[76:79], v[12:15]\n"
"v_xor_b32 v250, v250, v249\n"
"s_lshl_b32 s58, s49, 2\n"
"s_add_u32 s58, s58, s48\n"
"s_lshl_b32 s58, s58, 12\n"
"v_mfma_f32_16x16x32_bf16 v[16:19], a[96:99], a[80:83], v[16:19]\n"
"ds_read_b64_tr_b16 a[2:3], v224 offset:33792\n"
"v_lshlrev_b32 v249, 9, v249\n"
"v_mfma_f32_16x16x32_bf16 v[20:23], a[96:99], a[84:87], v[20:23]\n"
"buffer_load_dwordx4 v234, s[24:27], s54 offen offset:1024 lds\n"
"v_lshlrev_b32 v250, 4, v250\n"
"s_mov_b64 s[80:81], s[22:23]\n"
"s_add_u32 s80, s80, s58\n"
"s_addc_u32 s81, s81, 0\n"
"v_mfma_f32_16x16x32_bf16 v[24:27], a[96:99], a[88:91], v[24:27]\n"
"ds_read_b128 a[36:39], v232 offset:34816\n"
"v_or_b32 v249, v249, v250\n"
"v_mfma_f32_16x16x32_bf16 v[28:31], a[96:99], a[92:95], v[28:31]\n"
"s_and_b32 s81, s81, 0xffff\n"
"s_or_b32 s81, s81, 0x20000\n"
"s_mov_b32 s82, 0x10000\n"
"v_mfma_f32_16x16x32_bf16 v[32:35], a[100:103], a[64:67], v[32:35]\n"
"ds_read_b64_tr_b16 a[4:5], v225 offset:32768\n"
"v_mfma_f32_16x16x32_bf16 v[36:39], a[100:103], a[68:71], v[36:39]\n"
"s_waitcnt lgkmcnt(5)\n"
"buffer_load_dwordx4 v235, s[24:27], s54 offen offset:2048 lds\n"
"s_mov_b32 s83, 0x20000\n"
"v_mfma_f32_16x16x32_bf16 v[40:43], a[100:103], a[72:75], v[40:43]\n"
"ds_read_b64_tr_b16 a[6:7], v225 offset:33792\n"
"v_mfma_f32_16x16x32_bf16 v[44:47], a[100:103], a[76:79], v[44:47]\n"
"v_mfma_f32_16x16x32_bf16 v[48:51], a[100:103], a[80:83], v[48:51]\n"
"ds_read_b128 a[40:43], v232 offset:36864\n"
"v_mfma_f32_16x16x32_bf16 v[52:55], a[100:103], a[84:87], v[52:55]\n"
"buffer_load_dwordx4 v236, s[24:27], s54 offen offset:3072 lds\n"
"v_mfma_f32_16x16x32_bf16 v[56:59], a[100:103], a[88:91], v[56:59]\n"
"ds_read_b64_tr_b16 a[8:9], v226 offset:32768\n"
"v_mfma_f32_16x16x32_bf16 v[60:63], a[100:103], a[92:95], v[60:63]\n"
"s_add_u32 m0, s52, 4096\n"
"s_mov_b32 s54, 33536\n"
"v_mfma_f32_16x16x32_bf16 v[64:67], a[104:107], a[64:67], v[64:67]\n"
"ds_read_b64_tr_b16 a[10:11], v226 offset:33792\n"
"v_mfma_f32_16x16x32_bf16 v[68:71], a[104:107], a[68:71], v[68:71]\n"
"buffer_load_dwordx4 v233, s[24:27], s54 offen offset:0 lds\n"
"v_mfma_f32_16x16x32_bf16 v[72:75], a[104:107], a[72:75], v[72:75]\n"
"ds_read_b128 a[44:47], v232 offset:38912\n"
"v_mfma_f32_16x16x32_bf16 v[76:79], a[104:107], a[76:79], v[76:79]\n"
"v_mfma_f32_16x16x32_bf16 v[80:83], a[104:107], a[80:83], v[80:83]\n"
"ds_read_b64_tr_b16 a[12:13], v227 offset:32768\n"
"v_mfma_f32_16x16x32_bf16 v[84:87], a[104:107], a[84:87], v[84:87]\n"
"buffer_load_dwordx4 v234, s[24:27], s54 offen offset:1024 lds\n"
"v_mfma_f32_16x16x32_bf16 v[88:91], a[104:107], a[88:91], v[88:91]\n"
"ds_read_b64_tr_b16 a[14:15], v227 offset:33792\n"
"v_mfma_f32_16x16x32_bf16 v[92:95], a[104:107], a[92:95], v[92:95]\n"
"v_mfma_f32_16x16x32_bf16 v[96:99], a[108:111], a[64:67], v[96:99]\n"
"ds_read_b128 a[48:51], v232 offset:49152\n"
"v_mfma_f32_16x16x32_bf16 v[100:103], a[108:111], a[68:71], v[100:103]\n"
"buffer_load_dwordx4 v235, s[24:27], s54 offen offset:2048 lds\n"
"v_mfma_f32_16x16x32_bf16 v[104:107], a[108:111], a[72:75], v[104:107]\n"
"ds_read_b64_tr_b16 a[16:17], v228 offset:32768\n"
"v_mfma_f32_16x16x32_bf16 v[108:111], a[108:111], a[76:79], v[108:111]\n"
"v_mfma_f32_16x16x32_bf16 v[112:115], a[108:111], a[80:83], v[112:115]\n"
"ds_read_b64_tr_b16 a[18:19], v228 offset:33792\n"
"v_mfma_f32_16x16x32_bf16 v[116:119], a[108:111], a[84:87], v[116:119]\n"
"buffer_load_dwordx4 v236, s[24:27], s54 offen offset:3072 lds\n"
"v_mfma_f32_16x16x32_bf16 v[120:123], a[108:111], a[88:91], v[120:123]\n"
"ds_read_b128 a[52:55], v232 offset:51200\n"
"v_mfma_f32_16x16x32_bf16 v[124:127], a[108:111], a[92:95], v[124:127]\n"
"buffer_store_dwordx4 v[240:243], v249, s[44:47], s74 offen sc1\n"
"v_mfma_f32_16x16x32_bf16 v[128:131], a[112:115], a[64:67], v[128:131]\n"
"ds_read_b64_tr_b16 a[20:21], v229 offset:32768\n"
"v_mfma_f32_16x16x32_bf16 v[132:135], a[112:115], a[68:71], v[132:135]\n"
"v_mfma_f32_16x16x32_bf16 v[136:139], a[112:115], a[72:75], v[136:139]\n"
"ds_read_b64_tr_b16 a[22:23], v229 offset:33792\n"
"v_mfma_f32_16x16x32_bf16 v[140:143], a[112:115], a[76:79], v[140:143]\n"
"buffer_store_dwordx4 v[244:247], v249, s[44:47], s75 offen sc1\n"
"v_mfma_f32_16x16x32_bf16 v[144:147], a[112:115], a[80:83], v[144:147]\n"
"ds_read_b128 a[56:59], v232 offset:53248\n"
"v_mfma_f32_16x16x32_bf16 v[148:151], a[112:115], a[84:87], v[148:151]\n"
"s_add_u32 m0, s53, 16384\n"
"s_mov_b32 s55, 3407872\n"
"v_mfma_f32_16x16x32_bf16 v[152:155], a[112:115], a[88:91], v[152:155]\n"
"ds_read_b64_tr_b16 a[24:25], v230 offset:32768\n"
"v_mfma_f32_16x16x32_bf16 v[156:159], a[112:115], a[92:95], v[156:159]\n"
"buffer_load_dwordx4 v237, s[28:31], s55 offen offset:0 lds\n"
"v_mfma_f32_16x16x32_bf16 v[160:163], a[116:119], a[64:67], v[160:163]\n"
"ds_read_b64_tr_b16 a[26:27], v230 offset:33792\n"
"v_mfma_f32_16x16x32_bf16 v[164:167], a[116:119], a[68:71], v[164:167]\n"
"v_mfma_f32_16x16x32_bf16 v[168:171], a[116:119], a[72:75], v[168:171]\n"
"ds_read_b128 a[60:63], v232 offset:55296\n"
"v_mfma_f32_16x16x32_bf16 v[172:175], a[116:119], a[76:79], v[172:175]\n"
"buffer_load_dwordx4 v238, s[28:31], s55 offen offset:1024 lds\n"
"s_add_u32 m0, s53, 24576\n"
"v_mfma_f32_16x16x32_bf16 v[176:179], a[116:119], a[80:83], v[176:179]\n"
"ds_read_b64_tr_b16 a[28:29], v231 offset:32768\n"
"v_mfma_f32_16x16x32_bf16 v[180:183], a[116:119], a[84:87], v[180:183]\n"
"buffer_load_dwordx4 v237, s[32:35], s55 offen offset:0 lds\n"
"v_mfma_f32_16x16x32_bf16 v[184:187], a[116:119], a[88:91], v[184:187]\n"
"ds_read_b64_tr_b16 a[30:31], v231 offset:33792\n"
"v_mfma_f32_16x16x32_bf16 v[188:191], a[116:119], a[92:95], v[188:191]\n"
"v_mfma_f32_16x16x32_bf16 v[192:195], a[120:123], a[64:67], v[192:195]\n"
"v_mfma_f32_16x16x32_bf16 v[196:199], a[120:123], a[68:71], v[196:199]\n"
"buffer_load_dwordx4 v238, s[32:35], s55 offen offset:1024 lds\n"
"v_mfma_f32_16x16x32_bf16 v[200:203], a[120:123], a[72:75], v[200:203]\n"
"v_mfma_f32_16x16x32_bf16 v[204:207], a[120:123], a[76:79], v[204:207]\n"
"v_mfma_f32_16x16x32_bf16 v[208:211], a[120:123], a[80:83], v[208:211]\n"
"v_mfma_f32_16x16x32_bf16 v[212:215], a[120:123], a[84:87], v[212:215]\n"
"buffer_load_dword a255, v239, s[80:83], 0 idxen\n"
"v_mfma_f32_16x16x32_bf16 v[216:219], a[120:123], a[88:91], v[216:219]\n"
"v_mfma_f32_16x16x32_bf16 v[220:223], a[120:123], a[92:95], v[220:223]\n"
"v_mfma_f32_16x16x32_bf16 a[128:131], a[124:127], a[64:67], a[128:131]\n"
"v_mfma_f32_16x16x32_bf16 a[132:135], a[124:127], a[68:71], a[132:135]\n"
"v_mfma_f32_16x16x32_bf16 a[136:139], a[124:127], a[72:75], a[136:139]\n"
"s_waitcnt vmcnt(19) lgkmcnt(0)\n"
"v_mfma_f32_16x16x32_bf16 a[140:143], a[124:127], a[76:79], a[140:143]\n"
"s_barrier\n"
"v_mfma_f32_16x16x32_bf16 a[144:147], a[124:127], a[80:83], a[144:147]\n"
"v_mfma_f32_16x16x32_bf16 a[148:151], a[124:127], a[84:87], a[148:151]\n"
"v_mfma_f32_16x16x32_bf16 a[152:155], a[124:127], a[88:91], a[152:155]\n"
"v_mfma_f32_16x16x32_bf16 a[156:159], a[124:127], a[92:95], a[156:159]\n"
"v_xor_b32 v232, 64, v232\n"
"v_mfma_f32_16x16x32_bf16 v[0:3], a[32:35], a[0:3], v[0:3]\n"
"ds_read_b128 a[96:99], v232 offset:32768\n"
"s_add_u32 m0, s53, 32768\n"
"s_mov_b32 s55, 3670016\n"
"v_mfma_f32_16x16x32_bf16 v[4:7], a[32:35], a[4:7], v[4:7]\n"
"buffer_load_dwordx4 v237, s[28:31], s55 offen offset:0 lds\n"
"v_mfma_f32_16x16x32_bf16 v[8:11], a[32:35], a[8:11], v[8:11]\n"
"ds_read_b64_tr_b16 a[64:65], v224 offset:49152\n"
"v_mfma_f32_16x16x32_bf16 v[12:15], a[32:35], a[12:15], v[12:15]\n"
"v_mfma_f32_16x16x32_bf16 v[16:19], a[32:35], a[16:19], v[16:19]\n"
"ds_read_b64_tr_b16 a[66:67], v224 offset:50176\n"
"v_mfma_f32_16x16x32_bf16 v[20:23], a[32:35], a[20:23], v[20:23]\n"
"v_mfma_f32_16x16x32_bf16 v[24:27], a[32:35], a[24:27], v[24:27]\n"
"ds_read_b128 a[100:103], v232 offset:34816\n"
"v_mfma_f32_16x16x32_bf16 v[28:31], a[32:35], a[28:31], v[28:31]\n"
"v_mfma_f32_16x16x32_bf16 v[32:35], a[36:39], a[0:3], v[32:35]\n"
"ds_read_b64_tr_b16 a[68:69], v225 offset:49152\n"
"v_mfma_f32_16x16x32_bf16 v[36:39], a[36:39], a[4:7], v[36:39]\n"
"v_mfma_f32_16x16x32_bf16 v[40:43], a[36:39], a[8:11], v[40:43]\n"
"ds_read_b64_tr_b16 a[70:71], v225 offset:50176\n"
"v_mfma_f32_16x16x32_bf16 v[44:47], a[36:39], a[12:15], v[44:47]\n"
"v_mfma_f32_16x16x32_bf16 v[48:51], a[36:39], a[16:19], v[48:51]\n"
"ds_read_b128 a[104:107], v232 offset:36864\n"
"v_mfma_f32_16x16x32_bf16 v[52:55], a[36:39], a[20:23], v[52:55]\n"
"v_mfma_f32_16x16x32_bf16 v[56:59], a[36:39], a[24:27], v[56:59]\n"
"ds_read_b64_tr_b16 a[72:73], v226 offset:49152\n"
"v_mfma_f32_16x16x32_bf16 v[60:63], a[36:39], a[28:31], v[60:63]\n"
"buffer_load_dwordx4 v238, s[28:31], s55 offen offset:1024 lds\n"
"v_mfma_f32_16x16x32_bf16 v[64:67], a[40:43], a[0:3], v[64:67]\n"
"ds_read_b64_tr_b16 a[74:75], v226 offset:50176\n"
"v_mfma_f32_16x16x32_bf16 v[68:71], a[40:43], a[4:7], v[68:71]\n"
"v_mfma_f32_16x16x32_bf16 v[72:75], a[40:43], a[8:11], v[72:75]\n"
"ds_read_b128 a[108:111], v232 offset:38912\n"
"v_mfma_f32_16x16x32_bf16 v[76:79], a[40:43], a[12:15], v[76:79]\n"
"v_mfma_f32_16x16x32_bf16 v[80:83], a[40:43], a[16:19], v[80:83]\n"
"ds_read_b64_tr_b16 a[76:77], v227 offset:49152\n"
"v_mfma_f32_16x16x32_bf16 v[84:87], a[40:43], a[20:23], v[84:87]\n"
"v_mfma_f32_16x16x32_bf16 v[88:91], a[40:43], a[24:27], v[88:91]\n"
"ds_read_b64_tr_b16 a[78:79], v227 offset:50176\n"
"v_mfma_f32_16x16x32_bf16 v[92:95], a[40:43], a[28:31], v[92:95]\n"
"v_mfma_f32_16x16x32_bf16 v[96:99], a[44:47], a[0:3], v[96:99]\n"
"ds_read_b128 a[112:115], v232 offset:49152\n"
"v_mfma_f32_16x16x32_bf16 v[100:103], a[44:47], a[4:7], v[100:103]\n"
"v_mfma_f32_16x16x32_bf16 v[104:107], a[44:47], a[8:11], v[104:107]\n"
"ds_read_b64_tr_b16 a[80:81], v228 offset:49152\n"
"v_mfma_f32_16x16x32_bf16 v[108:111], a[44:47], a[12:15], v[108:111]\n"
"s_add_u32 m0, s53, 40960\n"
"v_mfma_f32_16x16x32_bf16 v[112:115], a[44:47], a[16:19], v[112:115]\n"
"ds_read_b64_tr_b16 a[82:83], v228 offset:50176\n"
"v_mfma_f32_16x16x32_bf16 v[116:119], a[44:47], a[20:23], v[116:119]\n"
"buffer_load_dwordx4 v237, s[32:35], s55 offen offset:0 lds\n"
"v_mfma_f32_16x16x32_bf16 v[120:123], a[44:47], a[24:27], v[120:123]\n"
"ds_read_b128 a[116:119], v232 offset:51200\n"
"v_mfma_f32_16x16x32_bf16 v[124:127], a[44:47], a[28:31], v[124:127]\n"
"v_mfma_f32_16x16x32_bf16 v[128:131], a[48:51], a[0:3], v[128:131]\n"
"ds_read_b64_tr_b16 a[84:85], v229 offset:49152\n"
"v_mfma_f32_16x16x32_bf16 v[132:135], a[48:51], a[4:7], v[132:135]\n"
"v_mfma_f32_16x16x32_bf16 v[136:139], a[48:51], a[8:11], v[136:139]\n"
"ds_read_b64_tr_b16 a[86:87], v229 offset:50176\n"
"v_mfma_f32_16x16x32_bf16 v[140:143], a[48:51], a[12:15], v[140:143]\n"
"v_mfma_f32_16x16x32_bf16 v[144:147], a[48:51], a[16:19], v[144:147]\n"
"ds_read_b128 a[120:123], v232 offset:53248\n"
"v_mfma_f32_16x16x32_bf16 v[148:151], a[48:51], a[20:23], v[148:151]\n"
"v_mfma_f32_16x16x32_bf16 v[152:155], a[48:51], a[24:27], v[152:155]\n"
"ds_read_b64_tr_b16 a[88:89], v230 offset:49152\n"
"v_mfma_f32_16x16x32_bf16 v[156:159], a[48:51], a[28:31], v[156:159]\n"
"v_mfma_f32_16x16x32_bf16 v[160:163], a[52:55], a[0:3], v[160:163]\n"
"ds_read_b64_tr_b16 a[90:91], v230 offset:50176\n"
"v_mfma_f32_16x16x32_bf16 v[164:167], a[52:55], a[4:7], v[164:167]\n"
"v_mfma_f32_16x16x32_bf16 v[168:171], a[52:55], a[8:11], v[168:171]\n"
"ds_read_b128 a[124:127], v232 offset:55296\n"
"v_mfma_f32_16x16x32_bf16 v[172:175], a[52:55], a[12:15], v[172:175]\n"
"buffer_load_dwordx4 v238, s[32:35], s55 offen offset:1024 lds\n"
"v_mfma_f32_16x16x32_bf16 v[176:179], a[52:55], a[16:19], v[176:179]\n"
"ds_read_b64_tr_b16 a[92:93], v231 offset:49152\n"
"v_mfma_f32_16x16x32_bf16 v[180:183], a[52:55], a[20:23], v[180:183]\n"
"v_mfma_f32_16x16x32_bf16 v[184:187], a[52:55], a[24:27], v[184:187]\n"
"ds_read_b64_tr_b16 a[94:95], v231 offset:50176\n"
"v_mfma_f32_16x16x32_bf16 v[188:191], a[52:55], a[28:31], v[188:191]\n"
"v_mfma_f32_16x16x32_bf16 v[192:195], a[56:59], a[0:3], v[192:195]\n"
"v_mfma_f32_16x16x32_bf16 v[196:199], a[56:59], a[4:7], v[196:199]\n"
"v_mfma_f32_16x16x32_bf16 v[200:203], a[56:59], a[8:11], v[200:203]\n"
"v_mfma_f32_16x16x32_bf16 v[204:207], a[56:59], a[12:15], v[204:207]\n"
"v_mfma_f32_16x16x32_bf16 v[208:211], a[56:59], a[16:19], v[208:211]\n"
"v_mfma_f32_16x16x32_bf16 v[212:215], a[56:59], a[20:23], v[212:215]\n"
"v_mfma_f32_16x16x32_bf16 v[216:219], a[56:59], a[24:27], v[216:219]\n"
"v_mfma_f32_16x16x32_bf16 v[220:223], a[56:59], a[28:31], v[220:223]\n"
"v_mfma_f32_16x16x32_bf16 a[128:131], a[60:63], a[0:3], a[128:131]\n"
"v_mfma_f32_16x16x32_bf16 a[132:135], a[60:63], a[4:7], a[132:135]\n"
"v_mfma_f32_16x16x32_bf16 a[136:139], a[60:63], a[8:11], a[136:139]\n"
"s_waitcnt vmcnt(9) lgkmcnt(0)\n"
"v_mfma_f32_16x16x32_bf16 a[140:143], a[60:63], a[12:15], a[140:143]\n"
"s_barrier\n"
"v_mfma_f32_16x16x32_bf16 a[144:147], a[60:63], a[16:19], a[144:147]\n"
"v_mfma_f32_16x16x32_bf16 a[148:151], a[60:63], a[20:23], a[148:151]\n"
"v_mfma_f32_16x16x32_bf16 a[152:155], a[60:63], a[24:27], a[152:155]\n"
"v_mfma_f32_16x16x32_bf16 a[156:159], a[60:63], a[28:31], a[156:159]\n"
"v_xor_b32 v232, 64, v232\n"
"v_mfma_f32_16x16x32_bf16 v[0:3], a[96:99], a[64:67], v[0:3]\n"
"ds_read_b128 a[32:35], v232 offset:0\n"
"s_add_u32 m0, s52, 32768\n"
"s_mov_b32 s54, 896\n"
"v_mfma_f32_16x16x32_bf16 v[4:7], a[96:99], a[68:71], v[4:7]\n"
"buffer_load_dwordx4 v233, s[24:27], s54 offen offset:0 lds\n"
"v_mfma_f32_16x16x32_bf16 v[8:11], a[96:99], a[72:75], v[8:11]\n"
"ds_read_b64_tr_b16 a[0:1], v224 offset:0\n"
"v_mfma_f32_16x16x32_bf16 v[12:15], a[96:99], a[76:79], v[12:15]\n"
"v_mfma_f32_16x16x32_bf16 v[16:19], a[96:99], a[80:83], v[16:19]\n"
"ds_read_b64_tr_b16 a[2:3], v224 offset:1024\n"
"v_mfma_f32_16x16x32_bf16 v[20:23], a[96:99], a[84:87], v[20:23]\n"
"buffer_load_dwordx4 v234, s[24:27], s54 offen offset:1024 lds\n"
"v_mfma_f32_16x16x32_bf16 v[24:27], a[96:99], a[88:91], v[24:27]\n"
"ds_read_b128 a[36:39], v232 offset:2048\n"
"v_mfma_f32_16x16x32_bf16 v[28:31], a[96:99], a[92:95], v[28:31]\n"
"v_mfma_f32_16x16x32_bf16 v[32:35], a[100:103], a[64:67], v[32:35]\n"
"ds_read_b64_tr_b16 a[4:5], v225 offset:0\n"
"v_mfma_f32_16x16x32_bf16 v[36:39], a[100:103], a[68:71], v[36:39]\n"
"buffer_load_dwordx4 v235, s[24:27], s54 offen offset:2048 lds\n"
"v_mfma_f32_16x16x32_bf16 v[40:43], a[100:103], a[72:75], v[40:43]\n"
"ds_read_b64_tr_b16 a[6:7], v225 offset:1024\n"
"v_mfma_f32_16x16x32_bf16 v[44:47], a[100:103], a[76:79], v[44:47]\n"
"v_mfma_f32_16x16x32_bf16 v[48:51], a[100:103], a[80:83], v[48:51]\n"
"ds_read_b128 a[40:43], v232 offset:4096\n"
"v_mfma_f32_16x16x32_bf16 v[52:55], a[100:103], a[84:87], v[52:55]\n"
"buffer_load_dwordx4 v236, s[24:27], s54 offen offset:3072 lds\n"
"v_mfma_f32_16x16x32_bf16 v[56:59], a[100:103], a[88:91], v[56:59]\n"
"ds_read_b64_tr_b16 a[8:9], v226 offset:0\n"
"v_mfma_f32_16x16x32_bf16 v[60:63], a[100:103], a[92:95], v[60:63]\n"
"s_add_u32 m0, s52, 36864\n"
"s_mov_b32 s54, 33664\n"
"v_mfma_f32_16x16x32_bf16 v[64:67], a[104:107], a[64:67], v[64:67]\n"
"ds_read_b64_tr_b16 a[10:11], v226 offset:1024\n"
"v_mfma_f32_16x16x32_bf16 v[68:71], a[104:107], a[68:71], v[68:71]\n"
"buffer_load_dwordx4 v233, s[24:27], s54 offen offset:0 lds\n"
"v_mfma_f32_16x16x32_bf16 v[72:75], a[104:107], a[72:75], v[72:75]\n"
"ds_read_b128 a[44:47], v232 offset:6144\n"
"v_mfma_f32_16x16x32_bf16 v[76:79], a[104:107], a[76:79], v[76:79]\n"
"v_mfma_f32_16x16x32_bf16 v[80:83], a[104:107], a[80:83], v[80:83]\n"
"ds_read_b64_tr_b16 a[12:13], v227 offset:0\n"
"v_mfma_f32_16x16x32_bf16 v[84:87], a[104:107], a[84:87], v[84:87]\n"
"buffer_load_dwordx4 v234, s[24:27], s54 offen offset:1024 lds\n"
"v_mfma_f32_16x16x32_bf16 v[88:91], a[104:107], a[88:91], v[88:91]\n"
"ds_read_b64_tr_b16 a[14:15], v227 offset:1024\n"
"v_mfma_f32_16x16x32_bf16 v[92:95], a[104:107], a[92:95], v[92:95]\n"
"v_mfma_f32_16x16x32_bf16 v[96:99], a[108:111], a[64:67], v[96:99]\n"
"ds_read_b128 a[48:51], v232 offset:16384\n"
"v_mfma_f32_16x16x32_bf16 v[100:103], a[108:111], a[68:71], v[100:103]\n"
"buffer_load_dwordx4 v235, s[24:27], s54 offen offset:2048 lds\n"
"v_mfma_f32_16x16x32_bf16 v[104:107], a[108:111], a[72:75], v[104:107]\n"
"ds_read_b64_tr_b16 a[16:17], v228 offset:0\n"
"v_mfma_f32_16x16x32_bf16 v[108:111], a[108:111], a[76:79], v[108:111]\n"
"v_mfma_f32_16x16x32_bf16 v[112:115], a[108:111], a[80:83], v[112:115]\n"
"ds_read_b64_tr_b16 a[18:19], v228 offset:1024\n"
"v_mfma_f32_16x16x32_bf16 v[116:119], a[108:111], a[84:87], v[116:119]\n"
"buffer_load_dwordx4 v236, s[24:27], s54 offen offset:3072 lds\n"
"s_add_u32 m0, s53, 49152\n"
"s_mov_b32 s55, 3932160\n"
"v_mfma_f32_16x16x32_bf16 v[120:123], a[108:111], a[88:91], v[120:123]\n"
"ds_read_b128 a[52:55], v232 offset:18432\n"
"v_mfma_f32_16x16x32_bf16 v[124:127], a[108:111], a[92:95], v[124:127]\n"
"buffer_load_dwordx4 v237, s[28:31], s55 offen offset:0 lds\n"
"v_mfma_f32_16x16x32_bf16 v[128:131], a[112:115], a[64:67], v[128:131]\n"
"ds_read_b64_tr_b16 a[20:21], v229 offset:0\n"
"v_mfma_f32_16x16x32_bf16 v[132:135], a[112:115], a[68:71], v[132:135]\n"
"v_mfma_f32_16x16x32_bf16 v[136:139], a[112:115], a[72:75], v[136:139]\n"
"ds_read_b64_tr_b16 a[22:23], v229 offset:1024\n"
"v_mfma_f32_16x16x32_bf16 v[140:143], a[112:115], a[76:79], v[140:143]\n"
"v_mfma_f32_16x16x32_bf16 v[144:147], a[112:115], a[80:83], v[144:147]\n"
"ds_read_b128 a[56:59], v232 offset:20480\n"
"v_mfma_f32_16x16x32_bf16 v[148:151], a[112:115], a[84:87], v[148:151]\n"
"buffer_load_dwordx4 v238, s[28:31], s55 offen offset:1024 lds\n"
"v_mfma_f32_16x16x32_bf16 v[152:155], a[112:115], a[88:91], v[152:155]\n"
"ds_read_b64_tr_b16 a[24:25], v230 offset:0\n"
"v_mfma_f32_16x16x32_bf16 v[156:159], a[112:115], a[92:95], v[156:159]\n"
"v_mfma_f32_16x16x32_bf16 v[160:163], a[116:119], a[64:67], v[160:163]\n"
"ds_read_b64_tr_b16 a[26:27], v230 offset:1024\n"
"v_mfma_f32_16x16x32_bf16 v[164:167], a[116:119], a[68:71], v[164:167]\n"
"v_mfma_f32_16x16x32_bf16 v[168:171], a[116:119], a[72:75], v[168:171]\n"
"ds_read_b128 a[60:63], v232 offset:22528\n"
"v_mfma_f32_16x16x32_bf16 v[172:175], a[116:119], a[76:79], v[172:175]\n"
"s_add_u32 m0, s53, 57344\n"
"v_mfma_f32_16x16x32_bf16 v[176:179], a[116:119], a[80:83], v[176:179]\n"
"ds_read_b64_tr_b16 a[28:29], v231 offset:0\n"
"v_mfma_f32_16x16x32_bf16 v[180:183], a[116:119], a[84:87], v[180:183]\n"
"buffer_load_dwordx4 v237, s[32:35], s55 offen offset:0 lds\n"
"v_mfma_f32_16x16x32_bf16 v[184:187], a[116:119], a[88:91], v[184:187]\n"
"ds_read_b64_tr_b16 a[30:31], v231 offset:1024\n"
"v_mfma_f32_16x16x32_bf16 v[188:191], a[116:119], a[92:95], v[188:191]\n"
"v_mfma_f32_16x16x32_bf16 v[192:195], a[120:123], a[64:67], v[192:195]\n"
"v_mfma_f32_16x16x32_bf16 v[196:199], a[120:123], a[68:71], v[196:199]\n"
"v_mfma_f32_16x16x32_bf16 v[200:203], a[120:123], a[72:75], v[200:203]\n"
"v_mfma_f32_16x16x32_bf16 v[204:207], a[120:123], a[76:79], v[204:207]\n"
"buffer_load_dwordx4 v238, s[32:35], s55 offen offset:1024 lds\n"
"v_mfma_f32_16x16x32_bf16 v[208:211], a[120:123], a[80:83], v[208:211]\n"
"v_mfma_f32_16x16x32_bf16 v[212:215], a[120:123], a[84:87], v[212:215]\n"
"v_mfma_f32_16x16x32_bf16 v[216:219], a[120:123], a[88:91], v[216:219]\n"
"v_mfma_f32_16x16x32_bf16 v[220:223], a[120:123], a[92:95], v[220:223]\n"
"v_mfma_f32_16x16x32_bf16 a[128:131], a[124:127], a[64:67], a[128:131]\n"
"v_mfma_f32_16x16x32_bf16 a[132:135], a[124:127], a[68:71], a[132:135]\n"
"v_mfma_f32_16x16x32_bf16 a[136:139], a[124:127], a[72:75], a[136:139]\n"
"s_waitcnt vmcnt(17) lgkmcnt(0)\n"
"v_mfma_f32_16x16x32_bf16 a[140:143], a[124:127], a[76:79], a[140:143]\n"
"s_barrier\n"
"v_mfma_f32_16x16x32_bf16 a[144:147], a[124:127], a[80:83], a[144:147]\n"
"v_mfma_f32_16x16x32_bf16 a[148:151], a[124:127], a[84:87], a[148:151]\n"
"v_mfma_f32_16x16x32_bf16 a[152:155], a[124:127], a[88:91], a[152:155]\n"
"v_mfma_f32_16x16x32_bf16 a[156:159], a[124:127], a[92:95], a[156:159]\n"
"v_xor_b32 v232, 64, v232\n"
"s_lshl_b32 s62, s48, 2\n"
"s_lshl_b32 s63, s51, 4\n"
"s_add_u32 s63, s63, s62\n"
"s_lshl_b32 s57, s51, 5\n"
"s_sub_u32 s57, 240, s57\n"
"v_mfma_f32_16x16x32_bf16 v[0:3], a[32:35], a[0:3], v[0:3]\n"
"ds_read_b128 a[96:99], v232 offset:0\n"
"v_lshrrev_b32 v236, 5, v239\n"
"v_bfe_u32 v251, v236, 2, 1\n"
"v_mul_u32_u24 v251, s57, v251\n"
"v_mfma_f32_16x16x32_bf16 v[4:7], a[32:35], a[4:7], v[4:7]\n"
"v_and_b32 v252, 3, v236\n"
"v_mfma_f32_16x16x32_bf16 v[8:11], a[32:35], a[8:11], v[8:11]\n"
"ds_read_b64_tr_b16 a[64:65], v224 offset:16384\n"
"v_add3_u32 v250, v251, v252, s63\n"
"s_lshl_b32 s56, s50, 8\n"
"v_mfma_f32_16x16x32_bf16 v[12:15], a[32:35], a[12:15], v[12:15]\n"
"v_lshlrev_b32 v250, 3, v250\n"
"s_add_u32 s28, s22, s56\n"
"s_addc_u32 s29, s23, 0\n"
"v_mfma_f32_16x16x32_bf16 v[16:19], a[32:35], a[16:19], v[16:19]\n"
"ds_read_b64_tr_b16 a[66:67], v224 offset:17408\n"
"v_lshlrev_b32 v234, 4, v236\n"
"s_and_b32 s29, s29, 0xffff\n"
"v_mfma_f32_16x16x32_bf16 v[20:23], a[32:35], a[20:23], v[20:23]\n"
"v_lshrrev_b32 v251, 4, v236\n"
"s_mov_b32 s30, 0x3fe100\n"
"v_mfma_f32_16x16x32_bf16 v[24:27], a[32:35], a[24:27], v[24:27]\n"
"ds_read_b128 a[100:103], v232 offset:2048\n"
"v_add_u32 v233, s62, v251\n"
"s_mov_b32 s31, 0x20000\n"
"v_mfma_f32_16x16x32_bf16 v[28:31], a[32:35], a[28:31], v[28:31]\n"
"v_lshlrev_b32 v233, 13, v233\n"
"s_lshl_b32 s18, s48, 10\n"
"v_mfma_f32_16x16x32_bf16 v[32:35], a[36:39], a[0:3], v[32:35]\n"
"ds_read_b64_tr_b16 a[68:69], v225 offset:16384\n"
"v_lshlrev_b32 v251, 2, v251\n"
"s_add_u32 s32, s20, 1048576\n"
"s_addc_u32 s33, s21, 0\n"
"v_mfma_f32_16x16x32_bf16 v[36:39], a[36:39], a[4:7], v[36:39]\n"
"v_or_b32 v251, s48, v251\n"
"s_and_b32 s33, s33, 0xffff\n"
"v_mfma_f32_16x16x32_bf16 v[40:43], a[36:39], a[8:11], v[40:43]\n"
"ds_read_b64_tr_b16 a[70:71], v225 offset:17408\n"
"v_and_b32 v252, 15, v236\n"
"s_mov_b32 s34, 0x200000\n"
"v_mfma_f32_16x16x32_bf16 v[44:47], a[36:39], a[12:15], v[44:47]\n"
"v_xor_b32 v251, v251, v252\n"
"s_mov_b32 s35, 0x20000\n"
"v_mfma_f32_16x16x32_bf16 v[48:51], a[36:39], a[16:19], v[48:51]\n"
"ds_read_b128 a[104:107], v232 offset:4096\n"
"v_lshlrev_b32 v251, 4, v251\n"
"s_lshl_b32 s57, s51, 2\n"
"s_add_u32 s8, s57, s48\n"
"v_mfma_f32_16x16x32_bf16 v[52:55], a[36:39], a[20:23], v[52:55]\n"
"v_add_u32 v233, v233, v251\n"
"s_sub_u32 s9, 60, s57\n"
"s_add_u32 s9, s9, s48\n"
"v_mfma_f32_16x16x32_bf16 v[56:59], a[36:39], a[24:27], v[56:59]\n"
"ds_read_b64_tr_b16 a[72:73], v226 offset:16384\n"
"s_lshl_b32 s16, s8, 15\n"
"s_lshl_b32 s17, s9, 15\n"
"v_mfma_f32_16x16x32_bf16 v[60:63], a[36:39], a[28:31], v[60:63]\n"
"s_load_dwordx4 s[80:83], %0, 0x40\n"
"s_load_dwordx2 s[84:85], %0, 0x50\n"
"v_mfma_f32_16x16x32_bf16 v[64:67], a[40:43], a[0:3], v[64:67]\n"
"ds_read_b64_tr_b16 a[74:75], v226 offset:17408\n"
"v_mfma_f32_16x16x32_bf16 v[68:71], a[40:43], a[4:7], v[68:71]\n"
"s_movk_i32 s58, 16384\n"
"v_mfma_f32_16x16x32_bf16 v[72:75], a[40:43], a[8:11], v[72:75]\n"
"ds_read_b128 a[108:111], v232 offset:6144\n"
"v_mfma_f32_16x16x32_bf16 v[76:79], a[40:43], a[12:15], v[76:79]\n"
"buffer_load_dwordx2 v[248:249], v250, s[44:47], s58 offen sc1\n"
"v_mfma_f32_16x16x32_bf16 v[80:83], a[40:43], a[16:19], v[80:83]\n"
"ds_read_b64_tr_b16 a[76:77], v227 offset:16384\n"
"v_mfma_f32_16x16x32_bf16 v[84:87], a[40:43], a[20:23], v[84:87]\n"
"s_add_u32 m0, s18, 131072\n"
"s_mov_b32 s19, 3145728\n"
"v_mfma_f32_16x16x32_bf16 v[88:91], a[40:43], a[24:27], v[88:91]\n"
"ds_read_b64_tr_b16 a[78:79], v227 offset:17408\n"
"v_mfma_f32_16x16x32_bf16 v[92:95], a[40:43], a[28:31], v[92:95]\n"
"buffer_load_dwordx4 v233, s[28:31], s19 offen lds\n"
"v_mfma_f32_16x16x32_bf16 v[96:99], a[44:47], a[0:3], v[96:99]\n"
"ds_read_b128 a[112:115], v232 offset:16384\n"
"v_mfma_f32_16x16x32_bf16 v[100:103], a[44:47], a[4:7], v[100:103]\n"
"s_add_u32 m0, s18, 135168\n"
"s_mov_b32 s19, 3276800\n"
"v_mfma_f32_16x16x32_bf16 v[104:107], a[44:47], a[8:11], v[104:107]\n"
"ds_read_b64_tr_b16 a[80:81], v228 offset:16384\n"
"v_mfma_f32_16x16x32_bf16 v[108:111], a[44:47], a[12:15], v[108:111]\n"
"buffer_load_dwordx4 v233, s[28:31], s19 offen lds\n"
"v_mfma_f32_16x16x32_bf16 v[112:115], a[44:47], a[16:19], v[112:115]\n"
"ds_read_b64_tr_b16 a[82:83], v228 offset:17408\n"
"v_mfma_f32_16x16x32_bf16 v[116:119], a[44:47], a[20:23], v[116:119]\n"
"s_add_u32 m0, s18, 139264\n"
"s_mov_b32 s19, 3407872\n"
"v_mfma_f32_16x16x32_bf16 v[120:123], a[44:47], a[24:27], v[120:123]\n"
"ds_read_b128 a[116:119], v232 offset:18432\n"
"v_mfma_f32_16x16x32_bf16 v[124:127], a[44:47], a[28:31], v[124:127]\n"
"buffer_load_dwordx4 v233, s[28:31], s19 offen lds\n"
"v_mfma_f32_16x16x32_bf16 v[128:131], a[48:51], a[0:3], v[128:131]\n"
"ds_read_b64_tr_b16 a[84:85], v229 offset:16384\n"
"v_mfma_f32_16x16x32_bf16 v[132:135], a[48:51], a[4:7], v[132:135]\n"
"s_add_u32 m0, s18, 143360\n"
"s_mov_b32 s19, 3538944\n"
"v_mfma_f32_16x16x32_bf16 v[136:139], a[48:51], a[8:11], v[136:139]\n"
"ds_read_b64_tr_b16 a[86:87], v229 offset:17408\n"
"v_mfma_f32_16x16x32_bf16 v[140:143], a[48:51], a[12:15], v[140:143]\n"
"buffer_load_dwordx4 v233, s[28:31], s19 offen lds\n"
"v_mfma_f32_16x16x32_bf16 v[144:147], a[48:51], a[16:19], v[144:147]\n"
"ds_read_b128 a[120:123], v232 offset:20480\n"
"v_mfma_f32_16x16x32_bf16 v[148:151], a[48:51], a[20:23], v[148:151]\n"
"v_mfma_f32_16x16x32_bf16 v[152:155], a[48:51], a[24:27], v[152:155]\n"
"ds_read_b64_tr_b16 a[88:89], v230 offset:16384\n"
"v_mfma_f32_16x16x32_bf16 v[156:159], a[48:51], a[28:31], v[156:159]\n"
"s_add_u32 m0, s18, 147456\n"
"s_mov_b32 s19, 3670016\n"
"v_mfma_f32_16x16x32_bf16 v[160:163], a[52:55], a[0:3], v[160:163]\n"
"ds_read_b64_tr_b16 a[90:91], v230 offset:17408\n"
"v_mfma_f32_16x16x32_bf16 v[164:167], a[52:55], a[4:7], v[164:167]\n"
"buffer_load_dwordx4 v233, s[28:31], s19 offen lds\n"
"v_mfma_f32_16x16x32_bf16 v[168:171], a[52:55], a[8:11], v[168:171]\n"
"ds_read_b128 a[124:127], v232 offset:22528\n"
"v_mfma_f32_16x16x32_bf16 v[172:175], a[52:55], a[12:15], v[172:175]\n"
"s_add_u32 m0, s18, 151552\n"
"s_mov_b32 s19, 3801088\n"
"v_mfma_f32_16x16x32_bf16 v[176:179], a[52:55], a[16:19], v[176:179]\n"
"ds_read_b64_tr_b16 a[92:93], v231 offset:16384\n"
"v_mfma_f32_16x16x32_bf16 v[180:183], a[52:55], a[20:23], v[180:183]\n"
"buffer_load_dwordx4 v233, s[28:31], s19 offen lds\n"
"v_mfma_f32_16x16x32_bf16 v[184:187], a[52:55], a[24:27], v[184:187]\n"
"ds_read_b64_tr_b16 a[94:95], v231 offset:17408\n"
"v_mfma_f32_16x16x32_bf16 v[188:191], a[52:55], a[28:31], v[188:191]\n"
"s_add_u32 m0, s18, 155648\n"
"s_mov_b32 s19, 3932160\n"
"v_mfma_f32_16x16x32_bf16 v[192:195], a[56:59], a[0:3], v[192:195]\n"
"v_mfma_f32_16x16x32_bf16 v[196:199], a[56:59], a[4:7], v[196:199]\n"
"buffer_load_dwordx4 v233, s[28:31], s19 offen lds\n"
"v_mfma_f32_16x16x32_bf16 v[200:203], a[56:59], a[8:11], v[200:203]\n"
"v_mfma_f32_16x16x32_bf16 v[204:207], a[56:59], a[12:15], v[204:207]\n"
"s_add_u32 m0, s18, 159744\n"
"s_mov_b32 s19, 4063232\n"
"v_mfma_f32_16x16x32_bf16 v[208:211], a[56:59], a[16:19], v[208:211]\n"
"v_mfma_f32_16x16x32_bf16 v[212:215], a[56:59], a[20:23], v[212:215]\n"
"buffer_load_dwordx4 v233, s[28:31], s19 offen lds\n"
"v_mfma_f32_16x16x32_bf16 v[216:219], a[56:59], a[24:27], v[216:219]\n"
"v_mfma_f32_16x16x32_bf16 v[220:223], a[56:59], a[28:31], v[220:223]\n"
"v_mfma_f32_16x16x32_bf16 a[128:131], a[60:63], a[0:3], a[128:131]\n"
"v_mfma_f32_16x16x32_bf16 a[132:135], a[60:63], a[4:7], a[132:135]\n"
"v_mfma_f32_16x16x32_bf16 a[136:139], a[60:63], a[8:11], a[136:139]\n"
"s_waitcnt vmcnt(13) lgkmcnt(0)\n"
"v_mfma_f32_16x16x32_bf16 a[140:143], a[60:63], a[12:15], a[140:143]\n"
"s_barrier\n"
"v_mfma_f32_16x16x32_bf16 a[144:147], a[60:63], a[16:19], a[144:147]\n"
"v_mfma_f32_16x16x32_bf16 a[148:151], a[60:63], a[20:23], a[148:151]\n"
"v_mfma_f32_16x16x32_bf16 a[152:155], a[60:63], a[24:27], a[152:155]\n"
"v_mfma_f32_16x16x32_bf16 a[156:159], a[60:63], a[28:31], a[156:159]\n"
"v_xor_b32 v232, 64, v232\n"
"v_mfma_f32_16x16x32_bf16 v[0:3], a[96:99], a[64:67], v[0:3]\n"
"ds_read_b128 a[32:35], v232 offset:32768\n"
"s_add_u32 m0, s18, 16384\n"
"s_mov_b32 s19, 0\n"
"v_mfma_f32_16x16x32_bf16 v[4:7], a[96:99], a[68:71], v[4:7]\n"
"buffer_load_dwordx4 v233, s[28:31], s19 offen lds\n"
"v_lshrrev_b32 v252, 4, v236\n"
"v_mfma_f32_16x16x32_bf16 v[8:11], a[96:99], a[72:75], v[8:11]\n"
"ds_read_b64_tr_b16 a[0:1], v224 offset:32768\n"
"v_and_b32 v253, 15, v236\n"
"v_mfma_f32_16x16x32_bf16 v[12:15], a[96:99], a[76:79], v[12:15]\n"
"v_lshrrev_b32 v254, 1, v252\n"
"v_mfma_f32_16x16x32_bf16 v[16:19], a[96:99], a[80:83], v[16:19]\n"
"ds_read_b64_tr_b16 a[2:3], v224 offset:33792\n"
"v_and_b32 v252, 1, v252\n"
"v_mfma_f32_16x16x32_bf16 v[20:23], a[96:99], a[84:87], v[20:23]\n"
"v_lshlrev_b32 v254, 4, v254\n"
"v_mfma_f32_16x16x32_bf16 v[24:27], a[96:99], a[88:91], v[24:27]\n"
"ds_read_b128 a[36:39], v232 offset:34816\n"
"v_lshlrev_b32 v252, 5, v252\n"
"v_mfma_f32_16x16x32_bf16 v[28:31], a[96:99], a[92:95], v[28:31]\n"
"v_add_u32 v253, v253, v254\n"
"s_add_u32 m0, s18, 20480\n"
"s_mov_b32 s19, 131072\n"
"v_mfma_f32_16x16x32_bf16 v[32:35], a[100:103], a[64:67], v[32:35]\n"
"ds_read_b64_tr_b16 a[4:5], v225 offset:32768\n"
"v_add_u32 v253, v253, v252\n"
"v_mfma_f32_16x16x32_bf16 v[36:39], a[100:103], a[68:71], v[36:39]\n"
"buffer_load_dwordx4 v233, s[28:31], s19 offen lds\n"
"v_lshlrev_b32 v235, 4, v253\n"
"v_mfma_f32_16x16x32_bf16 v[40:43], a[100:103], a[72:75], v[40:43]\n"
"ds_read_b64_tr_b16 a[6:7], v225 offset:33792\n"
"v_lshrrev_b32 v251, 5, v236\n"
"v_mfma_f32_16x16x32_bf16 v[44:47], a[100:103], a[76:79], v[44:47]\n"
"v_bfe_u32 v252, v236, 4, 1\n"
"v_mfma_f32_16x16x32_bf16 v[48:51], a[100:103], a[80:83], v[48:51]\n"
"ds_read_b128 a[40:43], v232 offset:36864\n"
"v_bfe_u32 v253, v236, 2, 2\n"
"v_mfma_f32_16x16x32_bf16 v[52:55], a[100:103], a[84:87], v[52:55]\n"
"v_and_b32 v254, 3, v236\n"
"s_add_u32 m0, s18, 24576\n"
"s_mov_b32 s19, 262144\n"
"v_mfma_f32_16x16x32_bf16 v[56:59], a[100:103], a[88:91], v[56:59]\n"
"ds_read_b64_tr_b16 a[8:9], v226 offset:32768\n"
"v_lshlrev_b32 v255, 3, v251\n"
"v_mfma_f32_16x16x32_bf16 v[60:63], a[100:103], a[92:95], v[60:63]\n"
"buffer_load_dwordx4 v233, s[28:31], s19 offen lds\n"
"v_add_u32 v255, v255, v253\n"
"v_mfma_f32_16x16x32_bf16 v[64:67], a[104:107], a[64:67], v[64:67]\n"
"ds_read_b64_tr_b16 a[10:11], v226 offset:33792\n"
"v_lshlrev_b32 v240, 8, v255\n"
"v_mfma_f32_16x16x32_bf16 v[68:71], a[104:107], a[68:71], v[68:71]\n"
"v_lshlrev_b32 v255, 1, v252\n"
"v_mfma_f32_16x16x32_bf16 v[72:75], a[104:107], a[72:75], v[72:75]\n"
"ds_read_b128 a[44:47], v232 offset:38912\n"
"v_lshrrev_b32 v237, 1, v254\n"
"v_mfma_f32_16x16x32_bf16 v[76:79], a[104:107], a[76:79], v[76:79]\n"
"v_add_u32 v255, v255, v237\n"
"v_mfma_f32_16x16x32_bf16 v[80:83], a[104:107], a[80:83], v[80:83]\n"
"ds_read_b64_tr_b16 a[12:13], v227 offset:32768\n"
"v_lshlrev_b32 v237, 1, v251\n"
"v_mfma_f32_16x16x32_bf16 v[84:87], a[104:107], a[84:87], v[84:87]\n"
"v_xor_b32 v255, v255, v237\n"
"s_add_u32 m0, s18, 28672\n"
"s_mov_b32 s19, 393216\n"
"v_mfma_f32_16x16x32_bf16 v[88:91], a[104:107], a[88:91], v[88:91]\n"
"ds_read_b64_tr_b16 a[14:15], v227 offset:33792\n"
"v_lshlrev_b32 v237, 2, v253\n"
"v_mfma_f32_16x16x32_bf16 v[92:95], a[104:107], a[92:95], v[92:95]\n"
"buffer_load_dwordx4 v233, s[28:31], s19 offen lds\n"
"v_or_b32 v255, v255, v237\n"
"v_mfma_f32_16x16x32_bf16 v[96:99], a[108:111], a[64:67], v[96:99]\n"
"ds_read_b128 a[48:51], v232 offset:49152\n"
"v_lshlrev_b32 v255, 4, v255\n"
"v_mfma_f32_16x16x32_bf16 v[100:103], a[108:111], a[68:71], v[100:103]\n"
"v_add_u32 v240, v240, v255\n"
"v_mfma_f32_16x16x32_bf16 v[104:107], a[108:111], a[72:75], v[104:107]\n"
"ds_read_b64_tr_b16 a[16:17], v228 offset:32768\n"
"v_and_b32 v237, 1, v254\n"
"v_mfma_f32_16x16x32_bf16 v[108:111], a[108:111], a[76:79], v[108:111]\n"
"v_lshlrev_b32 v237, 3, v237\n"
"s_add_u32 m0, s18, 81920\n"
"s_mov_b32 s19, 1572864\n"
"v_mfma_f32_16x16x32_bf16 v[112:115], a[108:111], a[80:83], v[112:115]\n"
"ds_read_b64_tr_b16 a[18:19], v228 offset:33792\n"
"v_add_u32 v240, v240, v237\n"
"v_mfma_f32_16x16x32_bf16 v[116:119], a[108:111], a[84:87], v[116:119]\n"
"buffer_load_dwordx4 v233, s[28:31], s19 offen lds\n"
"v_add_u32 v241, 1024, v240\n"
"v_mfma_f32_16x16x32_bf16 v[120:123], a[108:111], a[88:91], v[120:123]\n"
"ds_read_b128 a[52:55], v232 offset:51200\n"
"v_xor_b32 v241, 16, v241\n"
"v_mfma_f32_16x16x32_bf16 v[124:127], a[108:111], a[92:95], v[124:127]\n"
"v_xor_b32 v242, 64, v240\n"
"v_mfma_f32_16x16x32_bf16 v[128:131], a[112:115], a[64:67], v[128:131]\n"
"ds_read_b64_tr_b16 a[20:21], v229 offset:32768\n"
"v_xor_b32 v243, 64, v241\n"
"v_mfma_f32_16x16x32_bf16 v[132:135], a[112:115], a[68:71], v[132:135]\n"
"v_xor_b32 v244, 128, v240\n"
"v_mfma_f32_16x16x32_bf16 v[136:139], a[112:115], a[72:75], v[136:139]\n"
"ds_read_b64_tr_b16 a[22:23], v229 offset:33792\n"
"v_xor_b32 v245, 128, v241\n"
"v_mfma_f32_16x16x32_bf16 v[140:143], a[112:115], a[76:79], v[140:143]\n"
"v_xor_b32 v246, 192, v240\n"
"s_add_u32 m0, s18, 86016\n"
"s_mov_b32 s19, 1703936\n"
"v_mfma_f32_16x16x32_bf16 v[144:147], a[112:115], a[80:83], v[144:147]\n"
"ds_read_b128 a[56:59], v232 offset:53248\n"
"v_xor_b32 v247, 192, v241\n"
"v_mfma_f32_16x16x32_bf16 v[148:151], a[112:115], a[84:87], v[148:151]\n"
"buffer_load_dwordx4 v233, s[28:31], s19 offen lds\n"
"v_mfma_f32_16x16x32_bf16 v[152:155], a[112:115], a[88:91], v[152:155]\n"
"ds_read_b64_tr_b16 a[24:25], v230 offset:32768\n"
"v_mfma_f32_16x16x32_bf16 v[156:159], a[112:115], a[92:95], v[156:159]\n"
"v_mfma_f32_16x16x32_bf16 v[160:163], a[116:119], a[64:67], v[160:163]\n"
"ds_read_b64_tr_b16 a[26:27], v230 offset:33792\n"
"s_lshl_b32 s56, s49, 2\n"
"s_add_u32 s56, s56, s48\n"
"s_min_u32 s56, s56, 959\n"
"v_mfma_f32_16x16x32_bf16 v[164:167], a[116:119], a[68:71], v[164:167]\n"
"s_add_u32 m0, s18, 90112\n"
"s_mov_b32 s19, 1835008\n"
"v_mfma_f32_16x16x32_bf16 v[168:171], a[116:119], a[72:75], v[168:171]\n"
"ds_read_b128 a[60:63], v232 offset:55296\n"
"s_cmp_ge_u32 s56, 512\n"
"s_cselect_b64 s[22:23], s[82:83], s[80:81]\n"
"s_cselect_b32 s57, 512, 0\n"
"v_mfma_f32_16x16x32_bf16 v[172:175], a[116:119], a[76:79], v[172:175]\n"
"buffer_load_dwordx4 v233, s[28:31], s19 offen lds\n"
"v_mfma_f32_16x16x32_bf16 v[176:179], a[116:119], a[80:83], v[176:179]\n"
"ds_read_b64_tr_b16 a[28:29], v231 offset:32768\n"
"s_cmp_ge_u32 s56, 736\n"
"s_cselect_b64 s[22:23], s[84:85], s[22:23]\n"
"s_cselect_b32 s57, 736, s57\n"
"v_mfma_f32_16x16x32_bf16 v[180:183], a[116:119], a[84:87], v[180:183]\n"
"v_mfma_f32_16x16x32_bf16 v[184:187], a[116:119], a[88:91], v[184:187]\n"
"ds_read_b64_tr_b16 a[30:31], v231 offset:33792\n"
"s_sub_u32 s56, s56, s57\n"
"s_lshl_b32 s56, s56, 13\n"
"v_mfma_f32_16x16x32_bf16 v[188:191], a[116:119], a[92:95], v[188:191]\n"
"v_mfma_f32_16x16x32_bf16 v[192:195], a[120:123], a[64:67], v[192:195]\n"
"s_add_u32 s22, s22, s56\n"
"s_addc_u32 s23, s23, 0\n"
"v_mfma_f32_16x16x32_bf16 v[196:199], a[120:123], a[68:71], v[196:199]\n"
"s_add_u32 m0, s18, 94208\n"
"s_mov_b32 s19, 1966080\n"
"v_mfma_f32_16x16x32_bf16 v[200:203], a[120:123], a[72:75], v[200:203]\n"
"v_mfma_f32_16x16x32_bf16 v[204:207], a[120:123], a[76:79], v[204:207]\n"
"buffer_load_dwordx4 v233, s[28:31], s19 offen lds\n"
"v_mfma_f32_16x16x32_bf16 v[208:211], a[120:123], a[80:83], v[208:211]\n"
"v_mfma_f32_16x16x32_bf16 v[212:215], a[120:123], a[84:87], v[212:215]\n"
"v_mfma_f32_16x16x32_bf16 v[216:219], a[120:123], a[88:91], v[216:219]\n"
"v_mfma_f32_16x16x32_bf16 v[220:223], a[120:123], a[92:95], v[220:223]\n"
"v_mfma_f32_16x16x32_bf16 a[128:131], a[124:127], a[64:67], a[128:131]\n"
"v_mfma_f32_16x16x32_bf16 a[132:135], a[124:127], a[68:71], a[132:135]\n"
"v_mfma_f32_16x16x32_bf16 a[136:139], a[124:127], a[72:75], a[136:139]\n"
"s_waitcnt vmcnt(16) lgkmcnt(0)\n"
"v_mfma_f32_16x16x32_bf16 a[140:143], a[124:127], a[76:79], a[140:143]\n"
"s_barrier\n"
"v_mfma_f32_16x16x32_bf16 a[144:147], a[124:127], a[80:83], a[144:147]\n"
"v_mfma_f32_16x16x32_bf16 a[148:151], a[124:127], a[84:87], a[148:151]\n"
"v_mfma_f32_16x16x32_bf16 a[152:155], a[124:127], a[88:91], a[152:155]\n"
"v_mfma_f32_16x16x32_bf16 a[156:159], a[124:127], a[92:95], a[156:159]\n"
"v_xor_b32 v232, 64, v232\n"
"v_mfma_f32_16x16x32_bf16 v[0:3], a[32:35], a[0:3], v[0:3]\n"
"ds_read_b128 a[96:99], v232 offset:32768\n"
"v_cmp_ne_u64 vcc, s[96:97], v[248:249]\n"
"v_mfma_f32_16x16x32_bf16 v[4:7], a[32:35], a[4:7], v[4:7]\n"
"s_cbranch_vccnz L_hspin14_%=\n"
"L_hspinr15_%=:\n"
"v_mfma_f32_16x16x32_bf16 v[8:11], a[32:35], a[8:11], v[8:11]\n"
"ds_read_b64_tr_b16 a[64:65], v224 offset:49152\n"
"v_mfma_f32_16x16x32_bf16 v[12:15], a[32:35], a[12:15], v[12:15]\n"
"buffer_load_dwordx4 a[160:163], v234, s[32:35], s16 offen offset:0 sc1\n"
"v_mfma_f32_16x16x32_bf16 v[16:19], a[32:35], a[16:19], v[16:19]\n"
"ds_read_b64_tr_b16 a[66:67], v224 offset:50176\n"
"v_mfma_f32_16x16x32_bf16 v[20:23], a[32:35], a[20:23], v[20:23]\n"
"v_mfma_f32_16x16x32_bf16 v[24:27], a[32:35], a[24:27], v[24:27]\n"
"ds_read_b128 a[100:103], v232 offset:34816\n"
"v_mfma_f32_16x16x32_bf16 v[28:31], a[32:35], a[28:31], v[28:31]\n"
"buffer_load_dwordx4 a[164:167], v234, s[32:35], s16 offen offset:1024 sc1\n"
"v_mfma_f32_16x16x32_bf16 v[32:35], a[36:39], a[0:3], v[32:35]\n"
"ds_read_b64_tr_b16 a[68:69], v225 offset:49152\n"
"v_mfma_f32_16x16x32_bf16 v[36:39], a[36:39], a[4:7], v[36:39]\n"
"v_mfma_f32_16x16x32_bf16 v[40:43], a[36:39], a[8:11], v[40:43]\n"
"ds_read_b64_tr_b16 a[70:71], v225 offset:50176\n"
"v_mfma_f32_16x16x32_bf16 v[44:47], a[36:39], a[12:15], v[44:47]\n"
"v_mfma_f32_16x16x32_bf16 v[48:51], a[36:39], a[16:19], v[48:51]\n"
"ds_read_b128 a[104:107], v232 offset:36864\n"
"v_mfma_f32_16x16x32_bf16 v[52:55], a[36:39], a[20:23], v[52:55]\n"
"buffer_load_dwordx4 a[168:171], v234, s[32:35], s16 offen offset:2048 sc1\n"
"v_mfma_f32_16x16x32_bf16 v[56:59], a[36:39], a[24:27], v[56:59]\n"
"ds_read_b64_tr_b16 a[72:73], v226 offset:49152\n"
"v_mfma_f32_16x16x32_bf16 v[60:63], a[36:39], a[28:31], v[60:63]\n"
"v_mfma_f32_16x16x32_bf16 v[64:67], a[40:43], a[0:3], v[64:67]\n"
"ds_read_b64_tr_b16 a[74:75], v226 offset:50176\n"
"v_mfma_f32_16x16x32_bf16 v[68:71], a[40:43], a[4:7], v[68:71]\n"
"buffer_load_dwordx4 a[172:175], v234, s[32:35], s16 offen offset:3072 sc1\n"
"v_mfma_f32_16x16x32_bf16 v[72:75], a[40:43], a[8:11], v[72:75]\n"
"ds_read_b128 a[108:111], v232 offset:38912\n"
"v_mfma_f32_16x16x32_bf16 v[76:79], a[40:43], a[12:15], v[76:79]\n"
"v_mfma_f32_16x16x32_bf16 v[80:83], a[40:43], a[16:19], v[80:83]\n"
"ds_read_b64_tr_b16 a[76:77], v227 offset:49152\n"
"v_mfma_f32_16x16x32_bf16 v[84:87], a[40:43], a[20:23], v[84:87]\n"
"buffer_load_dwordx4 a[176:179], v234, s[32:35], s17 offen offset:0 sc1\n"
"v_mfma_f32_16x16x32_bf16 v[88:91], a[40:43], a[24:27], v[88:91]\n"
"ds_read_b64_tr_b16 a[78:79], v227 offset:50176\n"
"v_mfma_f32_16x16x32_bf16 v[92:95], a[40:43], a[28:31], v[92:95]\n"
"v_mfma_f32_16x16x32_bf16 v[96:99], a[44:47], a[0:3], v[96:99]\n"
"ds_read_b128 a[112:115], v232 offset:49152\n"
"v_mfma_f32_16x16x32_bf16 v[100:103], a[44:47], a[4:7], v[100:103]\n"
"buffer_load_dwordx4 a[180:183], v234, s[32:35], s17 offen offset:1024 sc1\n"
"v_mfma_f32_16x16x32_bf16 v[104:107], a[44:47], a[8:11], v[104:107]\n"
"ds_read_b64_tr_b16 a[80:81], v228 offset:49152\n"
"v_mfma_f32_16x16x32_bf16 v[108:111], a[44:47], a[12:15], v[108:111]\n"
"v_mfma_f32_16x16x32_bf16 v[112:115], a[44:47], a[16:19], v[112:115]\n"
"ds_read_b64_tr_b16 a[82:83], v228 offset:50176\n"
"v_mfma_f32_16x16x32_bf16 v[116:119], a[44:47], a[20:23], v[116:119]\n"
"v_mfma_f32_16x16x32_bf16 v[120:123], a[44:47], a[24:27], v[120:123]\n"
"ds_read_b128 a[116:119], v232 offset:51200\n"
"v_mfma_f32_16x16x32_bf16 v[124:127], a[44:47], a[28:31], v[124:127]\n"
"buffer_load_dwordx4 a[184:187], v234, s[32:35], s17 offen offset:2048 sc1\n"
"v_mfma_f32_16x16x32_bf16 v[128:131], a[48:51], a[0:3], v[128:131]\n"
"ds_read_b64_tr_b16 a[84:85], v229 offset:49152\n"
"v_mfma_f32_16x16x32_bf16 v[132:135], a[48:51], a[4:7], v[132:135]\n"
"v_mfma_f32_16x16x32_bf16 v[136:139], a[48:51], a[8:11], v[136:139]\n"
"ds_read_b64_tr_b16 a[86:87], v229 offset:50176\n"
"v_mfma_f32_16x16x32_bf16 v[140:143], a[48:51], a[12:15], v[140:143]\n"
"buffer_load_dwordx4 a[188:191], v234, s[32:35], s17 offen offset:3072 sc1\n"
"s_add_u32 s16, s16, 4096\n"
"s_add_u32 s17, s17, 4096\n"
"v_mfma_f32_16x16x32_bf16 v[144:147], a[48:51], a[16:19], v[144:147]\n"
"ds_read_b128 a[120:123], v232 offset:53248\n"
"v_mfma_f32_16x16x32_bf16 v[148:151], a[48:51], a[20:23], v[148:151]\n"
"s_add_u32 m0, s18, 32768\n"
"s_mov_b32 s19, 524288\n"
"v_mfma_f32_16x16x32_bf16 v[152:155], a[48:51], a[24:27], v[152:155]\n"
"ds_read_b64_tr_b16 a[88:89], v230 offset:49152\n"
"v_mfma_f32_16x16x32_bf16 v[156:159], a[48:51], a[28:31], v[156:159]\n"
"buffer_load_dwordx4 v233, s[28:31], s19 offen lds\n"
"v_mfma_f32_16x16x32_bf16 v[160:163], a[52:55], a[0:3], v[160:163]\n"
"ds_read_b64_tr_b16 a[90:91], v230 offset:50176\n"
"v_mfma_f32_16x16x32_bf16 v[164:167], a[52:55], a[4:7], v[164:167]\n"
"s_add_u32 m0, s18, 36864\n"
"s_mov_b32 s19, 655360\n"
"v_mfma_f32_16x16x32_bf16 v[168:171], a[52:55], a[8:11], v[168:171]\n"
"ds_read_b128 a[124:127], v232 offset:55296\n"
"v_mfma_f32_16x16x32_bf16 v[172:175], a[52:55], a[12:15], v[172:175]\n"
"buffer_load_dwordx4 v233, s[28:31], s19 offen lds\n"
"v_mfma_f32_16x16x32_bf16 v[176:179], a[52:55], a[16:19], v[176:179]\n"
"ds_read_b64_tr_b16 a[92:93], v231 offset:49152\n"
"v_mfma_f32_16x16x32_bf16 v[180:183], a[52:55], a[20:23], v[180:183]\n"
"v_mfma_f32_16x16x32_bf16 v[184:187], a[52:55], a[24:27], v[184:187]\n"
"ds_read_b64_tr_b16 a[94:95], v231 offset:50176\n"
"v_mfma_f32_16x16x32_bf16 v[188:191], a[52:55], a[28:31], v[188:191]\n"
"s_add_u32 m0, s18, 40960\n"
"s_mov_b32 s19, 786432\n"
"v_mfma_f32_16x16x32_bf16 v[192:195], a[56:59], a[0:3], v[192:195]\n"
"v_mfma_f32_16x16x32_bf16 v[196:199], a[56:59], a[4:7], v[196:199]\n"
"buffer_load_dwordx4 v233, s[28:31], s19 offen lds\n"
"v_mfma_f32_16x16x32_bf16 v[200:203], a[56:59], a[8:11], v[200:203]\n"
"v_mfma_f32_16x16x32_bf16 v[204:207], a[56:59], a[12:15], v[204:207]\n"
"s_add_u32 m0, s18, 45056\n"
"s_mov_b32 s19, 917504\n"
"v_mfma_f32_16x16x32_bf16 v[208:211], a[56:59], a[16:19], v[208:211]\n"
"v_mfma_f32_16x16x32_bf16 v[212:215], a[56:59], a[20:23], v[212:215]\n"
"buffer_load_dwordx4 v233, s[28:31], s19 offen lds\n"
"v_mfma_f32_16x16x32_bf16 v[216:219], a[56:59], a[24:27], v[216:219]\n"
"v_mfma_f32_16x16x32_bf16 v[220:223], a[56:59], a[28:31], v[220:223]\n"
"v_mfma_f32_16x16x32_bf16 a[128:131], a[60:63], a[0:3], a[128:131]\n"
"v_mfma_f32_16x16x32_bf16 a[132:135], a[60:63], a[4:7], a[132:135]\n"
"v_mfma_f32_16x16x32_bf16 a[136:139], a[60:63], a[8:11], a[136:139]\n"
"s_waitcnt lgkmcnt(0)\n"
"v_mfma_f32_16x16x32_bf16 a[140:143], a[60:63], a[12:15], a[140:143]\n"
"s_barrier\n"
"v_mfma_f32_16x16x32_bf16 a[144:147], a[60:63], a[16:19], a[144:147]\n"
"v_mfma_f32_16x16x32_bf16 a[148:151], a[60:63], a[20:23], a[148:151]\n"
"v_mfma_f32_16x16x32_bf16 a[152:155], a[60:63], a[24:27], a[152:155]\n"
"v_mfma_f32_16x16x32_bf16 a[156:159], a[60:63], a[28:31], a[156:159]\n"
"v_mfma_f32_16x16x32_bf16 v[0:3], a[96:99], a[64:67], v[0:3]\n"
"buffer_load_dwordx4 a[192:195], v234, s[32:35], s16 offen offset:0 sc1\n"
"v_mfma_f32_16x16x32_bf16 v[4:7], a[96:99], a[68:71], v[4:7]\n"
"buffer_load_dwordx4 a[196:199], v234, s[32:35], s16 offen offset:1024 sc1\n"
"v_mfma_f32_16x16x32_bf16 v[8:11], a[96:99], a[72:75], v[8:11]\n"
"buffer_load_dwordx4 a[200:203], v234, s[32:35], s16 offen offset:2048 sc1\n"
"v_mfma_f32_16x16x32_bf16 v[12:15], a[96:99], a[76:79], v[12:15]\n"
"buffer_load_dwordx4 a[204:207], v234, s[32:35], s16 offen offset:3072 sc1\n"
"v_mfma_f32_16x16x32_bf16 v[16:19], a[96:99], a[80:83], v[16:19]\n"
"buffer_load_dwordx4 a[208:211], v234, s[32:35], s17 offen offset:0 sc1\n"
"v_mfma_f32_16x16x32_bf16 v[20:23], a[96:99], a[84:87], v[20:23]\n"
"buffer_load_dwordx4 a[212:215], v234, s[32:35], s17 offen offset:1024 sc1\n"
"v_mfma_f32_16x16x32_bf16 v[24:27], a[96:99], a[88:91], v[24:27]\n"
"buffer_load_dwordx4 a[216:219], v234, s[32:35], s17 offen offset:2048 sc1\n"
"v_mfma_f32_16x16x32_bf16 v[28:31], a[96:99], a[92:95], v[28:31]\n"
"buffer_load_dwordx4 a[220:223], v234, s[32:35], s17 offen offset:3072 sc1\n"
"s_add_u32 s16, s16, 4096\n"
"s_add_u32 s17, s17, 4096\n"
"s_add_u32 m0, s18, 49152\n"
"s_mov_b32 s19, 1048576\n"
"v_mfma_f32_16x16x32_bf16 v[32:35], a[100:103], a[64:67], v[32:35]\n"
"buffer_load_dwordx4 v233, s[28:31], s19 offen lds\n"
"s_add_u32 m0, s18, 53248\n"
"s_mov_b32 s19, 1179648\n"
"v_mfma_f32_16x16x32_bf16 v[36:39], a[100:103], a[68:71], v[36:39]\n"
"buffer_load_dwordx4 v233, s[28:31], s19 offen lds\n"
"s_add_u32 m0, s18, 57344\n"
"s_mov_b32 s19, 1310720\n"
"v_mfma_f32_16x16x32_bf16 v[40:43], a[100:103], a[72:75], v[40:43]\n"
"buffer_load_dwordx4 v233, s[28:31], s19 offen lds\n"
"s_add_u32 m0, s18, 61440\n"
"s_mov_b32 s19, 1441792\n"
"v_mfma_f32_16x16x32_bf16 v[44:47], a[100:103], a[76:79], v[44:47]\n"
"buffer_load_dwordx4 v233, s[28:31], s19 offen lds\n"
"v_mfma_f32_16x16x32_bf16 v[48:51], a[100:103], a[80:83], v[48:51]\n"
"v_cvt_pk_bf16_f32 v0, v0, v1\n"
"v_cvt_pk_bf16_f32 v1, v2, v3\n"
"v_cvt_pk_bf16_f32 v2, v4, v5\n"
"v_mfma_f32_16x16x32_bf16 v[52:55], a[100:103], a[84:87], v[52:55]\n"
"v_cvt_pk_bf16_f32 v3, v6, v7\n"
"v_cvt_pk_bf16_f32 v8, v8, v9\n"
"v_cvt_pk_bf16_f32 v9, v10, v11\n"
"v_mfma_f32_16x16x32_bf16 v[56:59], a[100:103], a[88:91], v[56:59]\n"
"v_cvt_pk_bf16_f32 v10, v12, v13\n"
"v_cvt_pk_bf16_f32 v11, v14, v15\n"
"v_permlane32_swap_b32 v0, v2\n"
"v_mfma_f32_16x16x32_bf16 v[60:63], a[100:103], a[92:95], v[60:63]\n"
"v_permlane32_swap_b32 v1, v3\n"
"buffer_store_dwordx4 v[0:3], v235, s[36:39], s64 offen offset:0 sc1\n"
"v_cvt_pk_bf16_f32 v16, v16, v17\n"
"v_mfma_f32_16x16x32_bf16 v[64:67], a[104:107], a[64:67], v[64:67]\n"
"v_cvt_pk_bf16_f32 v17, v18, v19\n"
"v_cvt_pk_bf16_f32 v18, v20, v21\n"
"v_cvt_pk_bf16_f32 v19, v22, v23\n"
"v_mfma_f32_16x16x32_bf16 v[68:71], a[104:107], a[68:71], v[68:71]\n"
"v_permlane32_swap_b32 v8, v10\n"
"v_permlane32_swap_b32 v9, v11\n"
"buffer_store_dwordx4 v[8:11], v235, s[36:39], s64 offen offset:1024 sc1\n"
"v_mfma_f32_16x16x32_bf16 v[72:75], a[104:107], a[72:75], v[72:75]\n"
"v_cvt_pk_bf16_f32 v24, v24, v25\n"
"v_cvt_pk_bf16_f32 v25, v26, v27\n"
"v_cvt_pk_bf16_f32 v26, v28, v29\n"
"v_mfma_f32_16x16x32_bf16 v[76:79], a[104:107], a[76:79], v[76:79]\n"
"v_cvt_pk_bf16_f32 v27, v30, v31\n"
"v_permlane32_swap_b32 v16, v18\n"
"v_permlane32_swap_b32 v17, v19\n"
"v_mfma_f32_16x16x32_bf16 v[80:83], a[104:107], a[80:83], v[80:83]\n"
"buffer_store_dwordx4 v[16:19], v235, s[36:39], s64 offen offset:2048 sc1\n"
"v_cvt_pk_bf16_f32 v32, v32, v33\n"
"v_cvt_pk_bf16_f32 v33, v34, v35\n"
"v_mfma_f32_16x16x32_bf16 v[84:87], a[104:107], a[84:87], v[84:87]\n"
"v_cvt_pk_bf16_f32 v34, v36, v37\n"
"v_cvt_pk_bf16_f32 v35, v38, v39\n"
"v_permlane32_swap_b32 v24, v26\n"
"v_mfma_f32_16x16x32_bf16 v[88:91], a[104:107], a[88:91], v[88:91]\n"
"v_permlane32_swap_b32 v25, v27\n"
"buffer_store_dwordx4 v[24:27], v235, s[36:39], s64 offen offset:3072 sc1\n"
"v_cvt_pk_bf16_f32 v40, v40, v41\n"
"v_mfma_f32_16x16x32_bf16 v[92:95], a[104:107], a[92:95], v[92:95]\n"
"v_cvt_pk_bf16_f32 v41, v42, v43\n"
"v_cvt_pk_bf16_f32 v42, v44, v45\n"
"v_cvt_pk_bf16_f32 v43, v46, v47\n"
"v_mfma_f32_16x16x32_bf16 v[96:99], a[108:111], a[64:67], v[96:99]\n"
"v_permlane32_swap_b32 v32, v34\n"
"v_permlane32_swap_b32 v33, v35\n"
"buffer_store_dwordx4 v[32:35], v235, s[36:39], s65 offen offset:0 sc1\n"
"v_mfma_f32_16x16x32_bf16 v[100:103], a[108:111], a[68:71], v[100:103]\n"
"v_cvt_pk_bf16_f32 v48, v48, v49\n"
"v_cvt_pk_bf16_f32 v49, v50, v51\n"
"v_cvt_pk_bf16_f32 v50, v52, v53\n"
"v_mfma_f32_16x16x32_bf16 v[104:107], a[108:111], a[72:75], v[104:107]\n"
"v_cvt_pk_bf16_f32 v51, v54, v55\n"
"v_permlane32_swap_b32 v40, v42\n"
"v_permlane32_swap_b32 v41, v43\n"
"v_mfma_f32_16x16x32_bf16 v[108:111], a[108:111], a[76:79], v[108:111]\n"
"buffer_store_dwordx4 v[40:43], v235, s[36:39], s65 offen offset:1024 sc1\n"
"v_cvt_pk_bf16_f32 v56, v56, v57\n"
"v_cvt_pk_bf16_f32 v57, v58, v59\n"
"v_mfma_f32_16x16x32_bf16 v[112:115], a[108:111], a[80:83], v[112:115]\n"
"v_cvt_pk_bf16_f32 v58, v60, v61\n"
"v_cvt_pk_bf16_f32 v59, v62, v63\n"
"v_permlane32_swap_b32 v48, v50\n"
"v_mfma_f32_16x16x32_bf16 v[116:119], a[108:111], a[84:87], v[116:119]\n"
"v_permlane32_swap_b32 v49, v51\n"
"buffer_store_dwordx4 v[48:51], v235, s[36:39], s65 offen offset:2048 sc1\n"
"v_cvt_pk_bf16_f32 v64, v64, v65\n"
"v_mfma_f32_16x16x32_bf16 v[120:123], a[108:111], a[88:91], v[120:123]\n"
"v_cvt_pk_bf16_f32 v65, v66, v67\n"
"v_cvt_pk_bf16_f32 v66, v68, v69\n"
"v_cvt_pk_bf16_f32 v67, v70, v71\n"
"v_mfma_f32_16x16x32_bf16 v[124:127], a[108:111], a[92:95], v[124:127]\n"
"v_permlane32_swap_b32 v56, v58\n"
"v_permlane32_swap_b32 v57, v59\n"
"buffer_store_dwordx4 v[56:59], v235, s[36:39], s65 offen offset:3072 sc1\n"
"v_mfma_f32_16x16x32_bf16 v[128:131], a[112:115], a[64:67], v[128:131]\n"
"v_cvt_pk_bf16_f32 v72, v72, v73\n"
"v_cvt_pk_bf16_f32 v73, v74, v75\n"
"v_cvt_pk_bf16_f32 v74, v76, v77\n"
"v_mfma_f32_16x16x32_bf16 v[132:135], a[112:115], a[68:71], v[132:135]\n"
"v_cvt_pk_bf16_f32 v75, v78, v79\n"
"v_permlane32_swap_b32 v64, v66\n"
"v_permlane32_swap_b32 v65, v67\n"
"v_mfma_f32_16x16x32_bf16 v[136:139], a[112:115], a[72:75], v[136:139]\n"
"v_accvgpr_write_b32 a32, v64\n"
"v_accvgpr_write_b32 a33, v65\n"
"v_accvgpr_write_b32 a34, v66\n"
"v_mfma_f32_16x16x32_bf16 v[140:143], a[112:115], a[76:79], v[140:143]\n"
"v_accvgpr_write_b32 a35, v67\n"
"v_cvt_pk_bf16_f32 v80, v80, v81\n"
"v_cvt_pk_bf16_f32 v81, v82, v83\n"
"v_mfma_f32_16x16x32_bf16 v[144:147], a[112:115], a[80:83], v[144:147]\n"
"v_cvt_pk_bf16_f32 v82, v84, v85\n"
"v_cvt_pk_bf16_f32 v83, v86, v87\n"
"v_permlane32_swap_b32 v72, v74\n"
"v_mfma_f32_16x16x32_bf16 v[148:151], a[112:115], a[84:87], v[148:151]\n"
"v_permlane32_swap_b32 v73, v75\n"
"v_accvgpr_write_b32 a36, v72\n"
"v_accvgpr_write_b32 a37, v73\n"
"v_mfma_f32_16x16x32_bf16 v[152:155], a[112:115], a[88:91], v[152:155]\n"
"v_accvgpr_write_b32 a38, v74\n"
"v_accvgpr_write_b32 a39, v75\n"
"v_cvt_pk_bf16_f32 v88, v88, v89\n"
"v_mfma_f32_16x16x32_bf16 v[156:159], a[112:115], a[92:95], v[156:159]\n"
"v_cvt_pk_bf16_f32 v89, v90, v91\n"
"v_cvt_pk_bf16_f32 v90, v92, v93\n"
"v_cvt_pk_bf16_f32 v91, v94, v95\n"
"v_mfma_f32_16x16x32_bf16 v[160:163], a[116:119], a[64:67], v[160:163]\n"
"v_permlane32_swap_b32 v80, v82\n"
"v_permlane32_swap_b32 v81, v83\n"
"v_accvgpr_write_b32 a40, v80\n"
"v_mfma_f32_16x16x32_bf16 v[164:167], a[116:119], a[68:71], v[164:167]\n"
"v_accvgpr_write_b32 a41, v81\n"
"v_accvgpr_write_b32 a42, v82\n"
"v_accvgpr_write_b32 a43, v83\n"
"v_mfma_f32_16x16x32_bf16 v[168:171], a[116:119], a[72:75], v[168:171]\n"
"v_cvt_pk_bf16_f32 v96, v96, v97\n"
"v_cvt_pk_bf16_f32 v97, v98, v99\n"
"v_cvt_pk_bf16_f32 v98, v100, v101\n"
"v_mfma_f32_16x16x32_bf16 v[172:175], a[116:119], a[76:79], v[172:175]\n"
"v_cvt_pk_bf16_f32 v99, v102, v103\n"
"v_permlane32_swap_b32 v88, v90\n"
"v_permlane32_swap_b32 v89, v91\n"
"v_mfma_f32_16x16x32_bf16 v[176:179], a[116:119], a[80:83], v[176:179]\n"
"v_accvgpr_write_b32 a44, v88\n"
"v_accvgpr_write_b32 a45, v89\n"
"v_accvgpr_write_b32 a46, v90\n"
"v_mfma_f32_16x16x32_bf16 v[180:183], a[116:119], a[84:87], v[180:183]\n"
"v_accvgpr_write_b32 a47, v91\n"
"v_cvt_pk_bf16_f32 v104, v104, v105\n"
"v_cvt_pk_bf16_f32 v105, v106, v107\n"
"v_mfma_f32_16x16x32_bf16 v[184:187], a[116:119], a[88:91], v[184:187]\n"
"v_cvt_pk_bf16_f32 v106, v108, v109\n"
"v_cvt_pk_bf16_f32 v107, v110, v111\n"
"v_permlane32_swap_b32 v96, v98\n"
"v_mfma_f32_16x16x32_bf16 v[188:191], a[116:119], a[92:95], v[188:191]\n"
"v_permlane32_swap_b32 v97, v99\n"
"v_accvgpr_write_b32 a48, v96\n"
"v_accvgpr_write_b32 a49, v97\n"
"v_mfma_f32_16x16x32_bf16 v[192:195], a[120:123], a[64:67], v[192:195]\n"
"v_accvgpr_write_b32 a50, v98\n"
"v_accvgpr_write_b32 a51, v99\n"
"v_cvt_pk_bf16_f32 v112, v112, v113\n"
"v_mfma_f32_16x16x32_bf16 v[196:199], a[120:123], a[68:71], v[196:199]\n"
"v_cvt_pk_bf16_f32 v113, v114, v115\n"
"v_cvt_pk_bf16_f32 v114, v116, v117\n"
"v_cvt_pk_bf16_f32 v115, v118, v119\n"
"v_mfma_f32_16x16x32_bf16 v[200:203], a[120:123], a[72:75], v[200:203]\n"
"v_permlane32_swap_b32 v104, v106\n"
"v_permlane32_swap_b32 v105, v107\n"
"v_accvgpr_write_b32 a52, v104\n"
"v_mfma_f32_16x16x32_bf16 v[204:207], a[120:123], a[76:79], v[204:207]\n"
"v_accvgpr_write_b32 a53, v105\n"
"v_accvgpr_write_b32 a54, v106\n"
"v_accvgpr_write_b32 a55, v107\n"
"v_mfma_f32_16x16x32_bf16 v[208:211], a[120:123], a[80:83], v[208:211]\n"
"v_cvt_pk_bf16_f32 v120, v120, v121\n"
"v_cvt_pk_bf16_f32 v121, v122, v123\n"
"v_cvt_pk_bf16_f32 v122, v124, v125\n"
"v_mfma_f32_16x16x32_bf16 v[212:215], a[120:123], a[84:87], v[212:215]\n"
"v_cvt_pk_bf16_f32 v123, v126, v127\n"
"v_permlane32_swap_b32 v112, v114\n"
"v_permlane32_swap_b32 v113, v115\n"
"v_mfma_f32_16x16x32_bf16 v[216:219], a[120:123], a[88:91], v[216:219]\n"
"v_accvgpr_write_b32 a56, v112\n"
"v_accvgpr_write_b32 a57, v113\n"
"v_accvgpr_write_b32 a58, v114\n"
"v_mfma_f32_16x16x32_bf16 v[220:223], a[120:123], a[92:95], v[220:223]\n"
"v_accvgpr_write_b32 a59, v115\n"
"v_cvt_pk_bf16_f32 v0, v128, v129\n"
"v_cvt_pk_bf16_f32 v1, v130, v131\n"
"v_mfma_f32_16x16x32_bf16 a[128:131], a[124:127], a[64:67], a[128:131]\n"
"v_cvt_pk_bf16_f32 v2, v132, v133\n"
"v_cvt_pk_bf16_f32 v3, v134, v135\n"
"v_permlane32_swap_b32 v120, v122\n"
"v_mfma_f32_16x16x32_bf16 a[132:135], a[124:127], a[68:71], a[132:135]\n"
"v_permlane32_swap_b32 v121, v123\n"
"v_accvgpr_write_b32 a60, v120\n"
"v_accvgpr_write_b32 a61, v121\n"
"v_mfma_f32_16x16x32_bf16 a[136:139], a[124:127], a[72:75], a[136:139]\n"
"v_accvgpr_write_b32 a62, v122\n"
"v_accvgpr_write_b32 a63, v123\n"
"v_cvt_pk_bf16_f32 v8, v136, v137\n"
"v_mfma_f32_16x16x32_bf16 a[140:143], a[124:127], a[76:79], a[140:143]\n"
"v_cvt_pk_bf16_f32 v9, v138, v139\n"
"v_cvt_pk_bf16_f32 v10, v140, v141\n"
"v_cvt_pk_bf16_f32 v11, v142, v143\n"
"v_mfma_f32_16x16x32_bf16 a[144:147], a[124:127], a[80:83], a[144:147]\n"
"v_permlane32_swap_b32 v0, v2\n"
"v_permlane32_swap_b32 v1, v3\n"
"v_cvt_pk_bf16_f32 v16, v144, v145\n"
"v_mfma_f32_16x16x32_bf16 a[148:151], a[124:127], a[84:87], a[148:151]\n"
"v_cvt_pk_bf16_f32 v17, v146, v147\n"
"v_cvt_pk_bf16_f32 v18, v148, v149\n"
"v_cvt_pk_bf16_f32 v19, v150, v151\n"
"v_mfma_f32_16x16x32_bf16 a[152:155], a[124:127], a[88:91], a[152:155]\n"
"v_permlane32_swap_b32 v8, v10\n"
"v_permlane32_swap_b32 v9, v11\n"
"v_cvt_pk_bf16_f32 v24, v152, v153\n"
"v_mfma_f32_16x16x32_bf16 a[156:159], a[124:127], a[92:95], a[156:159]\n"
"v_cvt_pk_bf16_f32 v25, v154, v155\n"
"v_cvt_pk_bf16_f32 v26, v156, v157\n"
"v_cvt_pk_bf16_f32 v27, v158, v159\n"
"s_nop 7\n"
"v_permlane32_swap_b32 v16, v18\n"
"v_permlane32_swap_b32 v17, v19\n"
"v_cvt_pk_bf16_f32 v32, v160, v161\n"
"v_cvt_pk_bf16_f32 v33, v162, v163\n"
"v_cvt_pk_bf16_f32 v34, v164, v165\n"
"v_cvt_pk_bf16_f32 v35, v166, v167\n"
"v_permlane32_swap_b32 v24, v26\n"
"v_permlane32_swap_b32 v25, v27\n"
"v_cvt_pk_bf16_f32 v40, v168, v169\n"
"v_cvt_pk_bf16_f32 v41, v170, v171\n"
"v_cvt_pk_bf16_f32 v42, v172, v173\n"
"v_cvt_pk_bf16_f32 v43, v174, v175\n"
"v_permlane32_swap_b32 v32, v34\n"
"v_permlane32_swap_b32 v33, v35\n"
"v_cvt_pk_bf16_f32 v48, v176, v177\n"
"v_cvt_pk_bf16_f32 v49, v178, v179\n"
"v_cvt_pk_bf16_f32 v50, v180, v181\n"
"v_cvt_pk_bf16_f32 v51, v182, v183\n"
"v_permlane32_swap_b32 v40, v42\n"
"v_permlane32_swap_b32 v41, v43\n"
"v_cvt_pk_bf16_f32 v56, v184, v185\n"
"v_cvt_pk_bf16_f32 v57, v186, v187\n"
"v_cvt_pk_bf16_f32 v58, v188, v189\n"
"v_cvt_pk_bf16_f32 v59, v190, v191\n"
"v_permlane32_swap_b32 v48, v50\n"
"v_permlane32_swap_b32 v49, v51\n"
"v_cvt_pk_bf16_f32 v4, v192, v193\n"
"v_cvt_pk_bf16_f32 v5, v194, v195\n"
"v_cvt_pk_bf16_f32 v6, v196, v197\n"
"v_cvt_pk_bf16_f32 v7, v198, v199\n"
"v_permlane32_swap_b32 v56, v58\n"
"v_permlane32_swap_b32 v57, v59\n"
"v_cvt_pk_bf16_f32 v12, v200, v201\n"
"v_cvt_pk_bf16_f32 v13, v202, v203\n"
"v_cvt_pk_bf16_f32 v14, v204, v205\n"
"v_cvt_pk_bf16_f32 v15, v206, v207\n"
"v_permlane32_swap_b32 v4, v6\n"
"v_permlane32_swap_b32 v5, v7\n"
"v_cvt_pk_bf16_f32 v20, v208, v209\n"
"v_cvt_pk_bf16_f32 v21, v210, v211\n"
"v_cvt_pk_bf16_f32 v22, v212, v213\n"
"v_cvt_pk_bf16_f32 v23, v214, v215\n"
"v_permlane32_swap_b32 v12, v14\n"
"v_permlane32_swap_b32 v13, v15\n"
"v_cvt_pk_bf16_f32 v28, v216, v217\n"
"v_cvt_pk_bf16_f32 v29, v218, v219\n"
"v_cvt_pk_bf16_f32 v30, v220, v221\n"
"v_cvt_pk_bf16_f32 v31, v222, v223\n"
"v_permlane32_swap_b32 v20, v22\n"
"v_permlane32_swap_b32 v21, v23\n"
"v_accvgpr_read_b32 v64, a128\n"
"v_accvgpr_read_b32 v65, a129\n"
"v_accvgpr_read_b32 v66, a130\n"
"v_accvgpr_read_b32 v67, a131\n"
"v_accvgpr_read_b32 v68, a132\n"
"v_accvgpr_read_b32 v69, a133\n"
"v_accvgpr_read_b32 v70, a134\n"
"v_accvgpr_read_b32 v71, a135\n"
"v_cvt_pk_bf16_f32 v36, v64, v65\n"
"v_cvt_pk_bf16_f32 v37, v66, v67\n"
"v_cvt_pk_bf16_f32 v38, v68, v69\n"
"v_cvt_pk_bf16_f32 v39, v70, v71\n"
"v_permlane32_swap_b32 v28, v30\n"
"v_permlane32_swap_b32 v29, v31\n"
"v_accvgpr_read_b32 v72, a136\n"
"v_accvgpr_read_b32 v73, a137\n"
"v_accvgpr_read_b32 v74, a138\n"
"v_accvgpr_read_b32 v75, a139\n"
"v_accvgpr_read_b32 v76, a140\n"
"v_accvgpr_read_b32 v77, a141\n"
"v_accvgpr_read_b32 v78, a142\n"
"v_accvgpr_read_b32 v79, a143\n"
"v_cvt_pk_bf16_f32 v44, v72, v73\n"
"v_cvt_pk_bf16_f32 v45, v74, v75\n"
"v_cvt_pk_bf16_f32 v46, v76, v77\n"
"v_cvt_pk_bf16_f32 v47, v78, v79\n"
"v_permlane32_swap_b32 v36, v38\n"
"v_permlane32_swap_b32 v37, v39\n"
"v_accvgpr_read_b32 v80, a144\n"
"v_accvgpr_read_b32 v81, a145\n"
"v_accvgpr_read_b32 v82, a146\n"
"v_accvgpr_read_b32 v83, a147\n"
"v_accvgpr_read_b32 v84, a148\n"
"v_accvgpr_read_b32 v85, a149\n"
"v_accvgpr_read_b32 v86, a150\n"
"v_accvgpr_read_b32 v87, a151\n"
"v_cvt_pk_bf16_f32 v52, v80, v81\n"
"v_cvt_pk_bf16_f32 v53, v82, v83\n"
"v_cvt_pk_bf16_f32 v54, v84, v85\n"
"v_cvt_pk_bf16_f32 v55, v86, v87\n"
"v_permlane32_swap_b32 v44, v46\n"
"v_permlane32_swap_b32 v45, v47\n"
"v_accvgpr_read_b32 v88, a152\n"
"v_accvgpr_read_b32 v89, a153\n"
"v_accvgpr_read_b32 v90, a154\n"
"v_accvgpr_read_b32 v91, a155\n"
"v_accvgpr_read_b32 v92, a156\n"
"v_accvgpr_read_b32 v93, a157\n"
"v_accvgpr_read_b32 v94, a158\n"
"v_accvgpr_read_b32 v95, a159\n"
"v_cvt_pk_bf16_f32 v60, v88, v89\n"
"v_cvt_pk_bf16_f32 v61, v90, v91\n"
"v_cvt_pk_bf16_f32 v62, v92, v93\n"
"v_cvt_pk_bf16_f32 v63, v94, v95\n"
"v_permlane32_swap_b32 v52, v54\n"
"v_permlane32_swap_b32 v53, v55\n"
"v_permlane32_swap_b32 v60, v62\n"
"v_permlane32_swap_b32 v61, v63\n"
"L_join_%=:\n"
"s_waitcnt vmcnt(24)\n"
"s_barrier\n"
"ds_read_b64_tr_b16 a[224:225], v240 offset:16384\n"
"ds_read_b64_tr_b16 a[226:227], v241 offset:16384\n"
"ds_read_b64_tr_b16 a[228:229], v242 offset:16384\n"
"ds_read_b64_tr_b16 a[230:231], v243 offset:16384\n"
"ds_read_b64_tr_b16 a[232:233], v244 offset:16384\n"
"ds_read_b64_tr_b16 a[234:235], v245 offset:16384\n"
"ds_read_b64_tr_b16 a[236:237], v246 offset:16384\n"
"s_waitcnt lgkmcnt(5)\n"
"v_mfma_f32_32x32x16_bf16 v[64:79], a[224:227], a[160:163], 0\n"
"ds_read_b64_tr_b16 a[238:239], v247 offset:16384\n"
"v_mfma_f32_32x32x16_bf16 v[128:143], a[224:227], a[176:179], 0\n"
"s_waitcnt lgkmcnt(4)\n"
"ds_read_b64_tr_b16 a[240:241], v240 offset:20480\n"
"buffer_load_dwordx4 a[64:67], v234, s[32:35], s16 offen offset:0 sc1\n"
"v_mfma_f32_32x32x16_bf16 v[80:95], a[228:231], a[160:163], 0\n"
"ds_read_b64_tr_b16 a[242:243], v241 offset:20480\n"
"buffer_load_dwordx4 a[68:71], v234, s[32:35], s16 offen offset:1024 sc1\n"
"v_mfma_f32_32x32x16_bf16 v[144:159], a[228:231], a[176:179], 0\n"
"s_waitcnt lgkmcnt(4)\n"
"ds_read_b64_tr_b16 a[244:245], v242 offset:20480\n"
"buffer_load_dwordx4 a[72:75], v234, s[32:35], s16 offen offset:2048 sc1\n"
"v_mfma_f32_32x32x16_bf16 v[96:111], a[232:235], a[160:163], 0\n"
"ds_read_b64_tr_b16 a[246:247], v243 offset:20480\n"
"v_mfma_f32_32x32x16_bf16 v[160:175], a[232:235], a[176:179], 0\n"
"s_waitcnt lgkmcnt(4)\n"
"ds_read_b64_tr_b16 a[248:249], v244 offset:20480\n"
"buffer_load_dwordx4 a[76:79], v234, s[32:35], s16 offen offset:3072 sc1\n"
"v_mfma_f32_32x32x16_bf16 v[112:127], a[236:239], a[160:163], 0\n"
"ds_read_b64_tr_b16 a[250:251], v245 offset:20480\n"
"buffer_load_dwordx4 a[80:83], v234, s[32:35], s17 offen offset:0 sc1\n"
"v_mfma_f32_32x32x16_bf16 v[176:191], a[236:239], a[176:179], 0\n"
"s_waitcnt lgkmcnt(4)\n"
"ds_read_b64_tr_b16 a[252:253], v246 offset:20480\n"
"buffer_load_dwordx4 a[84:87], v234, s[32:35], s17 offen offset:1024 sc1\n"
"v_mfma_f32_32x32x16_bf16 v[64:79], a[240:243], a[164:167], v[64:79]\n"
"ds_read_b64_tr_b16 a[254:255], v247 offset:20480\n"
"v_mfma_f32_32x32x16_bf16 v[128:143], a[240:243], a[180:183], v[128:143]\n"
"s_waitcnt lgkmcnt(4)\n"
"ds_read_b64_tr_b16 a[224:225], v240 offset:24576\n"
"buffer_load_dwordx4 a[88:91], v234, s[32:35], s17 offen offset:2048 sc1\n"
"v_mfma_f32_32x32x16_bf16 v[80:95], a[244:247], a[164:167], v[80:95]\n"
"ds_read_b64_tr_b16 a[226:227], v241 offset:24576\n"
"buffer_load_dwordx4 a[92:95], v234, s[32:35], s17 offen offset:3072 sc1\n"
"v_mfma_f32_32x32x16_bf16 v[144:159], a[244:247], a[180:183], v[144:159]\n"
"s_waitcnt lgkmcnt(4)\n"
"s_add_u32 s16, s16, 4096\n"
"s_add_u32 s17, s17, 4096\n"
"ds_read_b64_tr_b16 a[228:229], v242 offset:24576\n"
"v_mfma_f32_32x32x16_bf16 v[96:111], a[248:251], a[164:167], v[96:111]\n"
"s_add_u32 m0, s18, 98304\n"
"s_mov_b32 s19, 2097152\n"
"ds_read_b64_tr_b16 a[230:231], v243 offset:24576\n"
"buffer_load_dwordx4 v233, s[28:31], s19 offen lds\n"
"v_mfma_f32_32x32x16_bf16 v[160:175], a[248:251], a[180:183], v[160:175]\n"
"s_waitcnt lgkmcnt(4)\n"
"s_add_u32 m0, s18, 102400\n"
"s_mov_b32 s19, 2228224\n"
"ds_read_b64_tr_b16 a[232:233], v244 offset:24576\n"
"buffer_load_dwordx4 v233, s[28:31], s19 offen lds\n"
"v_mfma_f32_32x32x16_bf16 v[112:127], a[252:255], a[164:167], v[112:127]\n"
"s_add_u32 m0, s18, 106496\n"
"s_mov_b32 s19, 2359296\n"
"ds_read_b64_tr_b16 a[234:235], v245 offset:24576\n"
"buffer_load_dwordx4 v233, s[28:31], s19 offen lds\n"
"v_mfma_f32_32x32x16_bf16 v[176:191], a[252:255], a[180:183], v[176:191]\n"
"s_waitcnt lgkmcnt(4)\n"
"ds_read_b64_tr_b16 a[236:237], v246 offset:24576\n"
"v_mfma_f32_32x32x16_bf16 v[64:79], a[224:227], a[168:171], v[64:79]\n"
"s_add_u32 m0, s18, 110592\n"
"s_mov_b32 s19, 2490368\n"
"ds_read_b64_tr_b16 a[238:239], v247 offset:24576\n"
"buffer_load_dwordx4 v233, s[28:31], s19 offen lds\n"
"v_mfma_f32_32x32x16_bf16 v[128:143], a[224:227], a[184:187], v[128:143]\n"
"s_waitcnt lgkmcnt(4)\n"
"ds_read_b64_tr_b16 a[240:241], v240 offset:28672\n"
"buffer_store_dwordx4 a[32:35], v235, s[36:39], s66 offen offset:0 sc1\n"
"v_mfma_f32_32x32x16_bf16 v[80:95], a[228:231], a[168:171], v[80:95]\n"
"ds_read_b64_tr_b16 a[242:243], v241 offset:28672\n"
"buffer_store_dwordx4 a[36:39], v235, s[36:39], s66 offen offset:1024 sc1\n"
"v_mfma_f32_32x32x16_bf16 v[144:159], a[228:231], a[184:187], v[144:159]\n"
"s_waitcnt lgkmcnt(4)\n"
"ds_read_b64_tr_b16 a[244:245], v242 offset:28672\n"
"v_mfma_f32_32x32x16_bf16 v[96:111], a[232:235], a[168:171], v[96:111]\n"
"ds_read_b64_tr_b16 a[246:247], v243 offset:28672\n"
"buffer_store_dwordx4 a[40:43], v235, s[36:39], s66 offen offset:2048 sc1\n"
"v_mfma_f32_32x32x16_bf16 v[160:175], a[232:235], a[184:187], v[160:175]\n"
"s_waitcnt lgkmcnt(4)\n"
"ds_read_b64_tr_b16 a[248:249], v244 offset:28672\n"
"buffer_store_dwordx4 a[44:47], v235, s[36:39], s66 offen offset:3072 sc1\n"
"v_mfma_f32_32x32x16_bf16 v[112:127], a[236:239], a[168:171], v[112:127]\n"
"ds_read_b64_tr_b16 a[250:251], v245 offset:28672\n"
"v_mfma_f32_32x32x16_bf16 v[176:191], a[236:239], a[184:187], v[176:191]\n"
"s_waitcnt lgkmcnt(4)\n"
"ds_read_b64_tr_b16 a[252:253], v246 offset:28672\n"
"s_waitcnt vmcnt(28)\n"
"v_mfma_f32_32x32x16_bf16 v[64:79], a[240:243], a[172:175], v[64:79]\n"
"s_barrier\n"
"ds_read_b64_tr_b16 a[254:255], v247 offset:28672\n"
"v_mfma_f32_32x32x16_bf16 v[128:143], a[240:243], a[188:191], v[128:143]\n"
"s_waitcnt lgkmcnt(4)\n"
"ds_read_b64_tr_b16 a[224:225], v240 offset:32768\n"
"v_mfma_f32_32x32x16_bf16 v[80:95], a[244:247], a[172:175], v[80:95]\n"
"ds_read_b64_tr_b16 a[226:227], v241 offset:32768\n"
"v_mfma_f32_32x32x16_bf16 v[144:159], a[244:247], a[188:191], v[144:159]\n"
"s_waitcnt lgkmcnt(4)\n"
"ds_read_b64_tr_b16 a[228:229], v242 offset:32768\n"
"v_mfma_f32_32x32x16_bf16 v[96:111], a[248:251], a[172:175], v[96:111]\n"
"ds_read_b64_tr_b16 a[230:231], v243 offset:32768\n"
"v_mfma_f32_32x32x16_bf16 v[160:175], a[248:251], a[188:191], v[160:175]\n"
"s_waitcnt lgkmcnt(4)\n"
"ds_read_b64_tr_b16 a[232:233], v244 offset:32768\n"
"v_mfma_f32_32x32x16_bf16 v[112:127], a[252:255], a[172:175], v[112:127]\n"
"ds_read_b64_tr_b16 a[234:235], v245 offset:32768\n"
"v_mfma_f32_32x32x16_bf16 v[176:191], a[252:255], a[188:191], v[176:191]\n"
"s_waitcnt lgkmcnt(4)\n"
"ds_read_b64_tr_b16 a[236:237], v246 offset:32768\n"
"v_mfma_f32_32x32x16_bf16 v[64:79], a[224:227], a[192:195], v[64:79]\n"
"ds_read_b64_tr_b16 a[238:239], v247 offset:32768\n"
"v_add_u32 v248, 81920, v240\n"
"v_add_u32 v249, 81920, v241\n"
"v_mfma_f32_32x32x16_bf16 v[128:143], a[224:227], a[208:211], v[128:143]\n"
"s_waitcnt lgkmcnt(4)\n"
"ds_read_b64_tr_b16 a[240:241], v240 offset:36864\n"
"buffer_load_dwordx4 a[96:99], v234, s[32:35], s16 offen offset:0 sc1\n"
"v_mfma_f32_32x32x16_bf16 v[80:95], a[228:231], a[192:195], v[80:95]\n"
"ds_read_b64_tr_b16 a[242:243], v241 offset:36864\n"
"buffer_load_dwordx4 a[100:103], v234, s[32:35], s16 offen offset:1024 sc1\n"
"s_lshl_b32 s56, s50, 19\n"
"v_mfma_f32_32x32x16_bf16 v[144:159], a[228:231], a[208:211], v[144:159]\n"
"s_waitcnt lgkmcnt(4)\n"
"ds_read_b64_tr_b16 a[244:245], v242 offset:36864\n"
"buffer_load_dwordx4 a[104:107], v234, s[32:35], s16 offen offset:2048 sc1\n"
"s_add_u32 s40, s20, s56\n"
"s_addc_u32 s41, s21, 0\n"
"v_mfma_f32_32x32x16_bf16 v[96:111], a[232:235], a[192:195], v[96:111]\n"
"ds_read_b64_tr_b16 a[246:247], v243 offset:36864\n"
"s_add_u32 s40, s40, 4194304\n"
"s_addc_u32 s41, s41, 0\n"
"v_mfma_f32_32x32x16_bf16 v[160:175], a[232:235], a[208:211], v[160:175]\n"
"s_waitcnt lgkmcnt(4)\n"
"ds_read_b64_tr_b16 a[248:249], v244 offset:36864\n"
"buffer_load_dwordx4 a[108:111], v234, s[32:35], s16 offen offset:3072 sc1\n"
"s_and_b32 s41, s41, 0xffff\n"
"v_mfma_f32_32x32x16_bf16 v[112:127], a[236:239], a[192:195], v[112:127]\n"
"ds_read_b64_tr_b16 a[250:251], v245 offset:36864\n"
"buffer_load_dwordx4 a[112:115], v234, s[32:35], s17 offen offset:0 sc1\n"
"s_lshl_b32 s57, s51, 15\n"
"v_mfma_f32_32x32x16_bf16 v[176:191], a[236:239], a[208:211], v[176:191]\n"
"s_waitcnt lgkmcnt(4)\n"
"ds_read_b64_tr_b16 a[252:253], v246 offset:36864\n"
"buffer_load_dwordx4 a[116:119], v234, s[32:35], s17 offen offset:1024 sc1\n"
"s_sub_u32 s42, 0x80000, s57\n"
"v_mfma_f32_32x32x16_bf16 v[64:79], a[240:243], a[196:199], v[64:79]\n"
"ds_read_b64_tr_b16 a[254:255], v247 offset:36864\n"
"v_add_u32 v250, 81920, v242\n"
"s_mov_b32 s43, 0x20000\n"
"v_mfma_f32_32x32x16_bf16 v[128:143], a[240:243], a[212:215], v[128:143]\n"
"s_waitcnt lgkmcnt(4)\n"
"ds_read_b64_tr_b16 a[224:225], v240 offset:40960\n"
"buffer_load_dwordx4 a[120:123], v234, s[32:35], s17 offen offset:2048 sc1\n"
"v_mfma_f32_32x32x16_bf16 v[80:95], a[244:247], a[196:199], v[80:95]\n"
"ds_read_b64_tr_b16 a[226:227], v241 offset:40960\n"
"buffer_load_dwordx4 a[124:127], v234, s[32:35], s17 offen offset:3072 sc1\n"
"v_add_u32 v251, 81920, v243\n"
"v_mfma_f32_32x32x16_bf16 v[144:159], a[244:247], a[212:215], v[144:159]\n"
"s_waitcnt lgkmcnt(4)\n"
"s_add_u32 s16, s16, 4096\n"
"s_add_u32 s17, s17, 4096\n"
"ds_read_b64_tr_b16 a[228:229], v242 offset:40960\n"
"v_mfma_f32_32x32x16_bf16 v[96:111], a[248:251], a[196:199], v[96:111]\n"
"s_add_u32 m0, s18, 114688\n"
"s_mov_b32 s19, 2621440\n"
"ds_read_b64_tr_b16 a[230:231], v243 offset:40960\n"
"buffer_load_dwordx4 v233, s[28:31], s19 offen lds\n"
"v_mfma_f32_32x32x16_bf16 v[160:175], a[248:251], a[212:215], v[160:175]\n"
"s_waitcnt lgkmcnt(4)\n"
"s_add_u32 m0, s18, 118784\n"
"s_mov_b32 s19, 2752512\n"
"ds_read_b64_tr_b16 a[232:233], v244 offset:40960\n"
"buffer_load_dwordx4 v233, s[28:31], s19 offen lds\n"
"v_mfma_f32_32x32x16_bf16 v[112:127], a[252:255], a[196:199], v[112:127]\n"
"s_add_u32 m0, s18, 122880\n"
"s_mov_b32 s19, 2883584\n"
"ds_read_b64_tr_b16 a[234:235], v245 offset:40960\n"
"buffer_load_dwordx4 v233, s[28:31], s19 offen lds\n"
"v_mfma_f32_32x32x16_bf16 v[176:191], a[252:255], a[212:215], v[176:191]\n"
"s_waitcnt lgkmcnt(4)\n"
"ds_read_b64_tr_b16 a[236:237], v246 offset:40960\n"
"v_add_u32 v252, 81920, v244\n"
"v_mfma_f32_32x32x16_bf16 v[64:79], a[224:227], a[200:203], v[64:79]\n"
"s_add_u32 m0, s18, 126976\n"
"s_mov_b32 s19, 3014656\n"
"ds_read_b64_tr_b16 a[238:239], v247 offset:40960\n"
"buffer_load_dwordx4 v233, s[28:31], s19 offen lds\n"
"v_mfma_f32_32x32x16_bf16 v[128:143], a[224:227], a[216:219], v[128:143]\n"
"s_waitcnt lgkmcnt(4)\n"
"ds_read_b64_tr_b16 a[240:241], v240 offset:45056\n"
"buffer_store_dwordx4 a[48:51], v235, s[36:39], s67 offen offset:0 sc1\n"
"v_mfma_f32_32x32x16_bf16 v[80:95], a[228:231], a[200:203], v[80:95]\n"
"ds_read_b64_tr_b16 a[242:243], v241 offset:45056\n"
"buffer_store_dwordx4 a[52:55], v235, s[36:39], s67 offen offset:1024 sc1\n"
"v_add_u32 v253, 81920, v245\n"
"v_mfma_f32_32x32x16_bf16 v[144:159], a[228:231], a[216:219], v[144:159]\n"
"s_waitcnt lgkmcnt(4)\n"
"ds_read_b64_tr_b16 a[244:245], v242 offset:45056\n"
"v_mfma_f32_32x32x16_bf16 v[96:111], a[232:235], a[200:203], v[96:111]\n"
"ds_read_b64_tr_b16 a[246:247], v243 offset:45056\n"
"buffer_store_dwordx4 a[56:59], v235, s[36:39], s67 offen offset:2048 sc1\n"
"v_add_u32 v254, 81920, v246\n"
"v_mfma_f32_32x32x16_bf16 v[160:175], a[232:235], a[216:219], v[160:175]\n"
"s_waitcnt lgkmcnt(4)\n"
"ds_read_b64_tr_b16 a[248:249], v244 offset:45056\n"
"buffer_store_dwordx4 a[60:63], v235, s[36:39], s67 offen offset:3072 sc1\n"
"v_mfma_f32_32x32x16_bf16 v[112:127], a[236:239], a[200:203], v[112:127]\n"
"ds_read_b64_tr_b16 a[250:251], v245 offset:45056\n"
"v_add_u32 v255, 81920, v247\n"
"v_mfma_f32_32x32x16_bf16 v[176:191], a[236:239], a[216:219], v[176:191]\n"
"s_waitcnt lgkmcnt(4)\n"
"ds_read_b64_tr_b16 a[252:253], v246 offset:45056\n"
"s_waitcnt vmcnt(24)\n"
"v_mfma_f32_32x32x16_bf16 v[64:79], a[240:243], a[204:207], v[64:79]\n"
"s_barrier\n"
"ds_read_b64_tr_b16 a[254:255], v247 offset:45056\n"
"v_mfma_f32_32x32x16_bf16 v[128:143], a[240:243], a[220:223], v[128:143]\n"
"s_waitcnt lgkmcnt(4)\n"
"ds_read_b64_tr_b16 a[224:225], v240 offset:49152\n"
"v_mfma_f32_32x32x16_bf16 v[80:95], a[244:247], a[204:207], v[80:95]\n"
"ds_read_b64_tr_b16 a[226:227], v241 offset:49152\n"
"v_mfma_f32_32x32x16_bf16 v[144:159], a[244:247], a[220:223], v[144:159]\n"
"s_waitcnt lgkmcnt(4)\n"
"ds_read_b64_tr_b16 a[228:229], v242 offset:49152\n"
"v_mfma_f32_32x32x16_bf16 v[96:111], a[248:251], a[204:207], v[96:111]\n"
"ds_read_b64_tr_b16 a[230:231], v243 offset:49152\n"
"v_mfma_f32_32x32x16_bf16 v[160:175], a[248:251], a[220:223], v[160:175]\n"
"s_waitcnt lgkmcnt(4)\n"
"ds_read_b64_tr_b16 a[232:233], v244 offset:49152\n"
"v_mfma_f32_32x32x16_bf16 v[112:127], a[252:255], a[204:207], v[112:127]\n"
"ds_read_b64_tr_b16 a[234:235], v245 offset:49152\n"
"v_mfma_f32_32x32x16_bf16 v[176:191], a[252:255], a[220:223], v[176:191]\n"
"s_waitcnt lgkmcnt(4)\n"
"ds_read_b64_tr_b16 a[236:237], v246 offset:49152\n"
"v_mfma_f32_32x32x16_bf16 v[64:79], a[224:227], a[64:67], v[64:79]\n"
"ds_read_b64_tr_b16 a[238:239], v247 offset:49152\n"
"v_add_u32 v224, 147456, v240\n"
"v_add_u32 v225, 147456, v241\n"
"v_mfma_f32_32x32x16_bf16 v[128:143], a[224:227], a[80:83], v[128:143]\n"
"s_waitcnt lgkmcnt(4)\n"
"ds_read_b64_tr_b16 a[240:241], v240 offset:53248\n"
"buffer_load_dwordx4 a[128:131], v234, s[32:35], s16 offen offset:0 sc1\n"
"v_mfma_f32_32x32x16_bf16 v[80:95], a[228:231], a[64:67], v[80:95]\n"
"ds_read_b64_tr_b16 a[242:243], v241 offset:53248\n"
"buffer_load_dwordx4 a[132:135], v234, s[32:35], s16 offen offset:1024 sc1\n"
"s_lshl_b32 s56, s50, 19\n"
"v_mfma_f32_32x32x16_bf16 v[144:159], a[228:231], a[80:83], v[144:159]\n"
"s_waitcnt lgkmcnt(4)\n"
"ds_read_b64_tr_b16 a[244:245], v242 offset:53248\n"
"s_add_u32 s24, s20, s56\n"
"s_addc_u32 s25, s21, 0\n"
"v_mfma_f32_32x32x16_bf16 v[96:111], a[232:235], a[64:67], v[96:111]\n"
"ds_read_b64_tr_b16 a[246:247], v243 offset:53248\n"
"buffer_load_dwordx4 a[136:139], v234, s[32:35], s16 offen offset:2048 sc1\n"
"s_add_u32 s24, s24, 22020096\n"
"s_addc_u32 s25, s25, 0\n"
"v_mfma_f32_32x32x16_bf16 v[160:175], a[232:235], a[80:83], v[160:175]\n"
"s_waitcnt lgkmcnt(4)\n"
"ds_read_b64_tr_b16 a[248:249], v244 offset:53248\n"
"s_and_b32 s25, s25, 0xffff\n"
"v_mfma_f32_32x32x16_bf16 v[112:127], a[236:239], a[64:67], v[112:127]\n"
"ds_read_b64_tr_b16 a[250:251], v245 offset:53248\n"
"buffer_load_dwordx4 a[140:143], v234, s[32:35], s16 offen offset:3072 sc1\n"
"s_lshl_b32 s57, s51, 15\n"
"v_mfma_f32_32x32x16_bf16 v[176:191], a[236:239], a[80:83], v[176:191]\n"
"s_waitcnt lgkmcnt(4)\n"
"ds_read_b64_tr_b16 a[252:253], v246 offset:53248\n"
"s_sub_u32 s26, 0x80000, s57\n"
"v_mfma_f32_32x32x16_bf16 v[64:79], a[240:243], a[68:71], v[64:79]\n"
"ds_read_b64_tr_b16 a[254:255], v247 offset:53248\n"
"buffer_load_dwordx4 a[144:147], v234, s[32:35], s17 offen offset:0 sc1\n"
"s_mov_b32 s27, 0x20000\n"
"v_mfma_f32_32x32x16_bf16 v[128:143], a[240:243], a[84:87], v[128:143]\n"
"s_waitcnt lgkmcnt(4)\n"
"ds_read_b64_tr_b16 a[224:225], v240 offset:57344\n"
"v_add_u32 v226, 147456, v242\n"
"v_mfma_f32_32x32x16_bf16 v[80:95], a[244:247], a[68:71], v[80:95]\n"
"ds_read_b64_tr_b16 a[226:227], v241 offset:57344\n"
"buffer_load_dwordx4 a[148:151], v234, s[32:35], s17 offen offset:1024 sc1\n"
"v_add_u32 v227, 147456, v243\n"
"v_mfma_f32_32x32x16_bf16 v[144:159], a[244:247], a[84:87], v[144:159]\n"
"s_waitcnt lgkmcnt(4)\n"
"ds_read_b64_tr_b16 a[228:229], v242 offset:57344\n"
"v_add_u32 v228, 147456, v244\n"
"v_mfma_f32_32x32x16_bf16 v[96:111], a[248:251], a[68:71], v[96:111]\n"
"ds_read_b64_tr_b16 a[230:231], v243 offset:57344\n"
"buffer_load_dwordx4 a[152:155], v234, s[32:35], s17 offen offset:2048 sc1\n"
"v_add_u32 v229, 147456, v245\n"
"v_mfma_f32_32x32x16_bf16 v[160:175], a[248:251], a[84:87], v[160:175]\n"
"s_waitcnt lgkmcnt(4)\n"
"ds_read_b64_tr_b16 a[232:233], v244 offset:57344\n"
"buffer_load_dwordx4 a[156:159], v234, s[32:35], s17 offen offset:3072 sc1\n"
"v_mfma_f32_32x32x16_bf16 v[112:127], a[252:255], a[68:71], v[112:127]\n"
"s_add_u32 s16, s16, 4096\n"
"s_add_u32 s17, s17, 4096\n"
"ds_read_b64_tr_b16 a[234:235], v245 offset:57344\n"
"v_mfma_f32_32x32x16_bf16 v[176:191], a[252:255], a[84:87], v[176:191]\n"
"s_waitcnt lgkmcnt(4)\n"
"ds_read_b64_tr_b16 a[236:237], v246 offset:57344\n"
"buffer_store_dwordx4 v[0:3], v235, s[36:39], s68 offen offset:0 sc1\n"
"v_mfma_f32_32x32x16_bf16 v[64:79], a[224:227], a[72:75], v[64:79]\n"
"ds_read_b64_tr_b16 a[238:239], v247 offset:57344\n"
"v_add_u32 v230, 147456, v246\n"
"v_add_u32 v231, 147456, v247\n"
"v_mfma_f32_32x32x16_bf16 v[128:143], a[224:227], a[88:91], v[128:143]\n"
"s_waitcnt lgkmcnt(4)\n"
"ds_read_b64_tr_b16 a[240:241], v240 offset:61440\n"
"buffer_store_dwordx4 v[8:11], v235, s[36:39], s68 offen offset:1024 sc1\n"
"v_mfma_f32_32x32x16_bf16 v[80:95], a[228:231], a[72:75], v[80:95]\n"
"ds_read_b64_tr_b16 a[242:243], v241 offset:61440\n"
"v_mov_b32 v214, v234\n"
"v_add_u32 v217, 0x10000, v234\n"
"v_mfma_f32_32x32x16_bf16 v[144:159], a[228:231], a[88:91], v[144:159]\n"
"s_waitcnt lgkmcnt(4)\n"
"ds_read_b64_tr_b16 a[244:245], v242 offset:61440\n"
"buffer_store_dwordx4 v[16:19], v235, s[36:39], s68 offen offset:2048 sc1\n"
"v_mfma_f32_32x32x16_bf16 v[96:111], a[232:235], a[72:75], v[96:111]\n"
"ds_read_b64_tr_b16 a[246:247], v243 offset:61440\n"
"v_mov_b32 v215, v217\n"
"v_mov_b32 v210, 0\n"
"v_mfma_f32_32x32x16_bf16 v[160:175], a[232:235], a[88:91], v[160:175]\n"
"s_waitcnt lgkmcnt(4)\n"
"ds_read_b64_tr_b16 a[248:249], v244 offset:61440\n"
"buffer_store_dwordx4 v[24:27], v235, s[36:39], s68 offen offset:3072 sc1\n"
"v_mfma_f32_32x32x16_bf16 v[112:127], a[236:239], a[72:75], v[112:127]\n"
"ds_read_b64_tr_b16 a[250:251], v245 offset:61440\n"
"v_mov_b32 v211, 0\n"
"v_mov_b32 v220, 0x47800000\n"
"v_mfma_f32_32x32x16_bf16 v[176:191], a[236:239], a[88:91], v[176:191]\n"
"s_waitcnt lgkmcnt(4)\n"
"ds_read_b64_tr_b16 a[252:253], v246 offset:61440\n"
"s_waitcnt vmcnt(20)\n"
"v_mfma_f32_32x32x16_bf16 v[64:79], a[240:243], a[76:79], v[64:79]\n"
"s_barrier\n"
"ds_read_b64_tr_b16 a[254:255], v247 offset:61440\n"
"v_mfma_f32_32x32x16_bf16 v[128:143], a[240:243], a[92:95], v[128:143]\n"
"s_waitcnt lgkmcnt(4)\n"
"ds_read_b64_tr_b16 a[224:225], v248 offset:0\n"
"v_mfma_f32_32x32x16_bf16 v[80:95], a[244:247], a[76:79], v[80:95]\n"
"ds_read_b64_tr_b16 a[226:227], v249 offset:0\n"
"v_mfma_f32_32x32x16_bf16 v[144:159], a[244:247], a[92:95], v[144:159]\n"
"s_waitcnt lgkmcnt(4)\n"
"ds_read_b64_tr_b16 a[228:229], v250 offset:0\n"
"v_mfma_f32_32x32x16_bf16 v[96:111], a[248:251], a[76:79], v[96:111]\n"
"ds_read_b64_tr_b16 a[230:231], v251 offset:0\n"
"v_mfma_f32_32x32x16_bf16 v[160:175], a[248:251], a[92:95], v[160:175]\n"
"s_waitcnt lgkmcnt(4)\n"
"ds_read_b64_tr_b16 a[232:233], v252 offset:0\n"
"v_mfma_f32_32x32x16_bf16 v[112:127], a[252:255], a[76:79], v[112:127]\n"
"ds_read_b64_tr_b16 a[234:235], v253 offset:0\n"
"v_mfma_f32_32x32x16_bf16 v[176:191], a[252:255], a[92:95], v[176:191]\n"
"s_waitcnt lgkmcnt(4)\n"
"ds_read_b64_tr_b16 a[236:237], v254 offset:0\n"
"v_mfma_f32_32x32x16_bf16 v[64:79], a[224:227], a[96:99], v[64:79]\n"
"ds_read_b64_tr_b16 a[238:239], v255 offset:0\n"
"v_mfma_f32_32x32x16_bf16 v[128:143], a[224:227], a[112:115], v[128:143]\n"
"s_waitcnt lgkmcnt(4)\n"
"ds_read_b64_tr_b16 a[240:241], v248 offset:4096\n"
"buffer_load_dwordx4 a[160:163], v234, s[32:35], s16 offen offset:0 sc1\n"
"v_mfma_f32_32x32x16_bf16 v[80:95], a[228:231], a[96:99], v[80:95]\n"
"ds_read_b64_tr_b16 a[242:243], v249 offset:4096\n"
"buffer_load_dwordx4 a[164:167], v234, s[32:35], s16 offen offset:1024 sc1\n"
"s_lshl_b32 s56, s48, 12\n"
"v_mfma_f32_32x32x16_bf16 v[144:159], a[228:231], a[112:115], v[144:159]\n"
"s_waitcnt lgkmcnt(4)\n"
"ds_read_b64_tr_b16 a[244:245], v250 offset:4096\n"
"v_add_u32 v216, s56, v234\n"
"s_mov_b32 s52, s56\n"
"v_mfma_f32_32x32x16_bf16 v[96:111], a[232:235], a[96:99], v[96:111]\n"
"ds_read_b64_tr_b16 a[246:247], v251 offset:4096\n"
"buffer_load_dwordx4 a[168:171], v234, s[32:35], s16 offen offset:2048 sc1\n"
"s_mov_b32 s54, s56\n"
"v_mfma_f32_32x32x16_bf16 v[160:175], a[232:235], a[112:115], v[160:175]\n"
"s_waitcnt lgkmcnt(4)\n"
"ds_read_b64_tr_b16 a[248:249], v252 offset:4096\n"
"v_add_u32 v218, 0x4000, v216\n"
"s_mov_b32 s53, 0\n"
"v_mfma_f32_32x32x16_bf16 v[112:127], a[236:239], a[96:99], v[112:127]\n"
"ds_read_b64_tr_b16 a[250:251], v253 offset:4096\n"
"buffer_load_dwordx4 a[172:175], v234, s[32:35], s16 offen offset:3072 sc1\n"
"s_mov_b32 s55, 0\n"
"v_mfma_f32_32x32x16_bf16 v[176:191], a[236:239], a[112:115], v[176:191]\n"
"s_waitcnt lgkmcnt(4)\n"
"ds_read_b64_tr_b16 a[252:253], v254 offset:4096\n"
"s_mov_b32 s10, 0\n"
"v_mfma_f32_32x32x16_bf16 v[64:79], a[240:243], a[100:103], v[64:79]\n"
"ds_read_b64_tr_b16 a[254:255], v255 offset:4096\n"
"buffer_load_dwordx4 a[176:179], v234, s[32:35], s17 offen offset:0 sc1\n"
"s_mov_b32 s60, s8\n"
"v_mfma_f32_32x32x16_bf16 v[128:143], a[240:243], a[116:119], v[128:143]\n"
"s_waitcnt lgkmcnt(4)\n"
"ds_read_b64_tr_b16 a[224:225], v248 offset:8192\n"
"s_sub_u32 s61, s9, 1\n"
"v_mfma_f32_32x32x16_bf16 v[80:95], a[244:247], a[100:103], v[80:95]\n"
"ds_read_b64_tr_b16 a[226:227], v249 offset:8192\n"
"buffer_load_dwordx4 a[180:183], v234, s[32:35], s17 offen offset:1024 sc1\n"
"s_lshl_b32 s11, s51, 1\n"
"v_mfma_f32_32x32x16_bf16 v[144:159], a[244:247], a[116:119], v[144:159]\n"
"s_waitcnt lgkmcnt(4)\n"
"ds_read_b64_tr_b16 a[228:229], v250 offset:8192\n"
"v_mfma_f32_32x32x16_bf16 v[96:111], a[248:251], a[100:103], v[96:111]\n"
"ds_read_b64_tr_b16 a[230:231], v251 offset:8192\n"
"buffer_load_dwordx4 a[184:187], v234, s[32:35], s17 offen offset:2048 sc1\n"
"v_mfma_f32_32x32x16_bf16 v[160:175], a[248:251], a[116:119], v[160:175]\n"
"s_waitcnt lgkmcnt(4)\n"
"ds_read_b64_tr_b16 a[232:233], v252 offset:8192\n"
"buffer_load_dwordx4 a[188:191], v234, s[32:35], s17 offen offset:3072 sc1\n"
"v_mfma_f32_32x32x16_bf16 v[112:127], a[252:255], a[100:103], v[112:127]\n"
"s_add_u32 s16, s16, 4096\n"
"s_add_u32 s17, s17, 4096\n"
"ds_read_b64_tr_b16 a[234:235], v253 offset:8192\n"
"v_mfma_f32_32x32x16_bf16 v[176:191], a[252:255], a[116:119], v[176:191]\n"
"s_waitcnt lgkmcnt(4)\n"
"ds_read_b64_tr_b16 a[236:237], v254 offset:8192\n"
"buffer_store_dwordx4 v[32:35], v235, s[36:39], s69 offen offset:0 sc1\n"
"v_mfma_f32_32x32x16_bf16 v[64:79], a[224:227], a[104:107], v[64:79]\n"
"ds_read_b64_tr_b16 a[238:239], v255 offset:8192\n"
"v_mfma_f32_32x32x16_bf16 v[128:143], a[224:227], a[120:123], v[128:143]\n"
"s_waitcnt lgkmcnt(4)\n"
"ds_read_b64_tr_b16 a[240:241], v248 offset:12288\n"
"buffer_store_dwordx4 v[40:43], v235, s[36:39], s69 offen offset:1024 sc1\n"
"v_mfma_f32_32x32x16_bf16 v[80:95], a[228:231], a[104:107], v[80:95]\n"
"ds_read_b64_tr_b16 a[242:243], v249 offset:12288\n"
"v_mfma_f32_32x32x16_bf16 v[144:159], a[228:231], a[120:123], v[144:159]\n"
"s_waitcnt lgkmcnt(4)\n"
"ds_read_b64_tr_b16 a[244:245], v250 offset:12288\n"
"buffer_store_dwordx4 v[48:51], v235, s[36:39], s69 offen offset:2048 sc1\n"
"v_mfma_f32_32x32x16_bf16 v[96:111], a[232:235], a[104:107], v[96:111]\n"
"ds_read_b64_tr_b16 a[246:247], v251 offset:12288\n"
"v_mfma_f32_32x32x16_bf16 v[160:175], a[232:235], a[120:123], v[160:175]\n"
"s_waitcnt lgkmcnt(4)\n"
"ds_read_b64_tr_b16 a[248:249], v252 offset:12288\n"
"buffer_store_dwordx4 v[56:59], v235, s[36:39], s69 offen offset:3072 sc1\n"
"v_mfma_f32_32x32x16_bf16 v[112:127], a[236:239], a[104:107], v[112:127]\n"
"ds_read_b64_tr_b16 a[250:251], v253 offset:12288\n"
"v_mfma_f32_32x32x16_bf16 v[176:191], a[236:239], a[120:123], v[176:191]\n"
"s_waitcnt lgkmcnt(4)\n"
"ds_read_b64_tr_b16 a[252:253], v254 offset:12288\n"
"s_waitcnt vmcnt(16)\n"
"v_mfma_f32_32x32x16_bf16 v[64:79], a[240:243], a[108:111], v[64:79]\n"
"s_barrier\n"
"ds_read_b64_tr_b16 a[254:255], v255 offset:12288\n"
"v_mfma_f32_32x32x16_bf16 v[128:143], a[240:243], a[124:127], v[128:143]\n"
"s_waitcnt lgkmcnt(4)\n"
"ds_read_b64_tr_b16 a[224:225], v248 offset:16384\n"
"s_cmp_lg_u32 s48, 0\n"
"s_cbranch_scc1 L_anofl1_%=\n"
"v_mov_b32 v238, s96\n"
"v_mov_b32 v239, s97\n"
"s_lshl_b32 s58, s50, 7\n"
"s_lshl_b32 s59, s51, 4\n"
"s_add_u32 s58, s58, s59\n"
"s_add_u32 s58, s58, 20480\n"
"buffer_store_dwordx2 v[238:239], off, s[44:47], s58 sc1\n"
"L_anofl1_%=:\n"
"v_mfma_f32_32x32x16_bf16 v[80:95], a[244:247], a[108:111], v[80:95]\n"
"ds_read_b64_tr_b16 a[226:227], v249 offset:16384\n"
"v_mfma_f32_32x32x16_bf16 v[144:159], a[244:247], a[124:127], v[144:159]\n"
"s_waitcnt lgkmcnt(4)\n"
"ds_read_b64_tr_b16 a[228:229], v250 offset:16384\n"
"v_mfma_f32_32x32x16_bf16 v[96:111], a[248:251], a[108:111], v[96:111]\n"
"ds_read_b64_tr_b16 a[230:231], v251 offset:16384\n"
"v_mfma_f32_32x32x16_bf16 v[160:175], a[248:251], a[124:127], v[160:175]\n"
"s_waitcnt lgkmcnt(4)\n"
"ds_read_b64_tr_b16 a[232:233], v252 offset:16384\n"
"v_mfma_f32_32x32x16_bf16 v[112:127], a[252:255], a[108:111], v[112:127]\n"
"ds_read_b64_tr_b16 a[234:235], v253 offset:16384\n"
"v_mfma_f32_32x32x16_bf16 v[176:191], a[252:255], a[124:127], v[176:191]\n"
"s_waitcnt lgkmcnt(4)\n"
"ds_read_b64_tr_b16 a[236:237], v254 offset:16384\n"
"v_mfma_f32_32x32x16_bf16 v[64:79], a[224:227], a[128:131], v[64:79]\n"
"ds_read_b64_tr_b16 a[238:239], v255 offset:16384\n"
"v_mfma_f32_32x32x16_bf16 v[128:143], a[224:227], a[144:147], v[128:143]\n"
"s_waitcnt lgkmcnt(4)\n"
"ds_read_b64_tr_b16 a[240:241], v248 offset:20480\n"
"buffer_load_dwordx4 a[192:195], v234, s[32:35], s16 offen offset:0 sc1\n"
"v_mfma_f32_32x32x16_bf16 v[80:95], a[228:231], a[128:131], v[80:95]\n"
"ds_read_b64_tr_b16 a[242:243], v249 offset:20480\n"
"buffer_load_dwordx4 a[196:199], v234, s[32:35], s16 offen offset:1024 sc1\n"
"v_mfma_f32_32x32x16_bf16 v[144:159], a[228:231], a[144:147], v[144:159]\n"
"s_waitcnt lgkmcnt(4)\n"
"ds_read_b64_tr_b16 a[244:245], v250 offset:20480\n"
"v_mfma_f32_32x32x16_bf16 v[96:111], a[232:235], a[128:131], v[96:111]\n"
"ds_read_b64_tr_b16 a[246:247], v251 offset:20480\n"
"buffer_load_dwordx4 a[200:203], v234, s[32:35], s16 offen offset:2048 sc1\n"
"v_mfma_f32_32x32x16_bf16 v[160:175], a[232:235], a[144:147], v[160:175]\n"
"s_waitcnt lgkmcnt(4)\n"
"ds_read_b64_tr_b16 a[248:249], v252 offset:20480\n"
"v_mfma_f32_32x32x16_bf16 v[112:127], a[236:239], a[128:131], v[112:127]\n"
"ds_read_b64_tr_b16 a[250:251], v253 offset:20480\n"
"buffer_load_dwordx4 a[204:207], v234, s[32:35], s16 offen offset:3072 sc1\n"
"v_mfma_f32_32x32x16_bf16 v[176:191], a[236:239], a[144:147], v[176:191]\n"
"s_waitcnt lgkmcnt(4)\n"
"ds_read_b64_tr_b16 a[252:253], v254 offset:20480\n"
"buffer_load_dwordx4 a[208:211], v234, s[32:35], s17 offen offset:0 sc1\n"
"v_mfma_f32_32x32x16_bf16 v[64:79], a[240:243], a[132:135], v[64:79]\n"
"ds_read_b64_tr_b16 a[254:255], v255 offset:20480\n"
"v_mfma_f32_32x32x16_bf16 v[128:143], a[240:243], a[148:151], v[128:143]\n"
"s_waitcnt lgkmcnt(4)\n"
"ds_read_b64_tr_b16 a[224:225], v248 offset:24576\n"
"buffer_load_dwordx4 a[212:215], v234, s[32:35], s17 offen offset:1024 sc1\n"
"v_mfma_f32_32x32x16_bf16 v[80:95], a[244:247], a[132:135], v[80:95]\n"
"ds_read_b64_tr_b16 a[226:227], v249 offset:24576\n"
"v_mfma_f32_32x32x16_bf16 v[144:159], a[244:247], a[148:151], v[144:159]\n"
"s_waitcnt lgkmcnt(4)\n"
"ds_read_b64_tr_b16 a[228:229], v250 offset:24576\n"
"buffer_load_dwordx4 a[216:219], v234, s[32:35], s17 offen offset:2048 sc1\n"
"v_mfma_f32_32x32x16_bf16 v[96:111], a[248:251], a[132:135], v[96:111]\n"
"ds_read_b64_tr_b16 a[230:231], v251 offset:24576\n"
"buffer_load_dwordx4 a[220:223], v234, s[32:35], s17 offen offset:3072 sc1\n"
"v_mfma_f32_32x32x16_bf16 v[160:175], a[248:251], a[148:151], v[160:175]\n"
"s_waitcnt lgkmcnt(4)\n"
"s_add_u32 s16, s16, 4096\n"
"s_add_u32 s17, s17, 4096\n"
"ds_read_b64_tr_b16 a[232:233], v252 offset:24576\n"
"v_mfma_f32_32x32x16_bf16 v[112:127], a[252:255], a[132:135], v[112:127]\n"
"ds_read_b64_tr_b16 a[234:235], v253 offset:24576\n"
"buffer_store_dwordx4 v[4:7], v235, s[36:39], s70 offen offset:0 sc1\n"
"v_mfma_f32_32x32x16_bf16 v[176:191], a[252:255], a[148:151], v[176:191]\n"
"s_waitcnt lgkmcnt(4)\n"
"ds_read_b64_tr_b16 a[236:237], v254 offset:24576\n"
"v_mfma_f32_32x32x16_bf16 v[64:79], a[224:227], a[136:139], v[64:79]\n"
"ds_read_b64_tr_b16 a[238:239], v255 offset:24576\n"
"buffer_store_dwordx4 v[12:15], v235, s[36:39], s70 offen offset:1024 sc1\n"
"v_mfma_f32_32x32x16_bf16 v[128:143], a[224:227], a[152:155], v[128:143]\n"
"s_waitcnt lgkmcnt(4)\n"
"ds_read_b64_tr_b16 a[240:241], v248 offset:28672\n"
"buffer_store_dwordx4 v[20:23], v235, s[36:39], s70 offen offset:2048 sc1\n"
"v_mfma_f32_32x32x16_bf16 v[80:95], a[228:231], a[136:139], v[80:95]\n"
"ds_read_b64_tr_b16 a[242:243], v249 offset:28672\n"
"v_mfma_f32_32x32x16_bf16 v[144:159], a[228:231], a[152:155], v[144:159]\n"
"s_waitcnt lgkmcnt(4)\n"
"ds_read_b64_tr_b16 a[244:245], v250 offset:28672\n"
"buffer_store_dwordx4 v[28:31], v235, s[36:39], s70 offen offset:3072 sc1\n"
"v_mfma_f32_32x32x16_bf16 v[96:111], a[232:235], a[136:139], v[96:111]\n"
"ds_read_b64_tr_b16 a[246:247], v251 offset:28672\n"
"v_mfma_f32_32x32x16_bf16 v[160:175], a[232:235], a[152:155], v[160:175]\n"
"s_waitcnt lgkmcnt(4)\n"
"s_lshl_b32 s58, s50, 7\n"
"s_add_u32 s58, s58, 20480\n"
"ds_read_b64_tr_b16 a[248:249], v252 offset:28672\n"
"buffer_load_dwordx2 v[240:241], off, s[44:47], s58 sc1\n"
"v_mfma_f32_32x32x16_bf16 v[112:127], a[236:239], a[136:139], v[112:127]\n"
"ds_read_b64_tr_b16 a[250:251], v253 offset:28672\n"
"v_mfma_f32_32x32x16_bf16 v[176:191], a[236:239], a[152:155], v[176:191]\n"
"s_waitcnt lgkmcnt(4)\n"
"ds_read_b64_tr_b16 a[252:253], v254 offset:28672\n"
"s_waitcnt vmcnt(17)\n"
"v_mfma_f32_32x32x16_bf16 v[64:79], a[240:243], a[140:143], v[64:79]\n"
"s_barrier\n"
"ds_read_b64_tr_b16 a[254:255], v255 offset:28672\n"
"v_mfma_f32_32x32x16_bf16 v[128:143], a[240:243], a[156:159], v[128:143]\n"
"s_waitcnt lgkmcnt(4)\n"
"ds_read_b64_tr_b16 a[224:225], v248 offset:32768\n"
"v_mfma_f32_32x32x16_bf16 v[80:95], a[244:247], a[140:143], v[80:95]\n"
"ds_read_b64_tr_b16 a[226:227], v249 offset:32768\n"
"v_mfma_f32_32x32x16_bf16 v[144:159], a[244:247], a[156:159], v[144:159]\n"
"s_waitcnt lgkmcnt(4)\n"
"ds_read_b64_tr_b16 a[228:229], v250 offset:32768\n"
"v_mfma_f32_32x32x16_bf16 v[96:111], a[248:251], a[140:143], v[96:111]\n"
"ds_read_b64_tr_b16 a[230:231], v251 offset:32768\n"
"v_mfma_f32_32x32x16_bf16 v[160:175], a[248:251], a[156:159], v[160:175]\n"
"s_waitcnt lgkmcnt(4)\n"
"ds_read_b64_tr_b16 a[232:233], v252 offset:32768\n"
"v_mfma_f32_32x32x16_bf16 v[112:127], a[252:255], a[140:143], v[112:127]\n"
"ds_read_b64_tr_b16 a[234:235], v253 offset:32768\n"
"v_mfma_f32_32x32x16_bf16 v[176:191], a[252:255], a[156:159], v[176:191]\n"
"s_waitcnt lgkmcnt(4)\n"
"ds_read_b64_tr_b16 a[236:237], v254 offset:32768\n"
"v_mfma_f32_32x32x16_bf16 v[64:79], a[224:227], a[160:163], v[64:79]\n"
"ds_read_b64_tr_b16 a[238:239], v255 offset:32768\n"
"v_mfma_f32_32x32x16_bf16 v[128:143], a[224:227], a[176:179], v[128:143]\n"
"s_waitcnt lgkmcnt(4)\n"
"ds_read_b64_tr_b16 a[240:241], v248 offset:36864\n"
"buffer_load_dwordx4 a[128:131], v234, s[32:35], s16 offen offset:0 sc1\n"
"v_mfma_f32_32x32x16_bf16 v[80:95], a[228:231], a[160:163], v[80:95]\n"
"ds_read_b64_tr_b16 a[242:243], v249 offset:36864\n"
"buffer_load_dwordx4 a[132:135], v234, s[32:35], s16 offen offset:1024 sc1\n"
"v_accvgpr_write_b32 a0, 0\n"
"v_accvgpr_write_b32 a1, 0\n"
"v_mfma_f32_32x32x16_bf16 v[144:159], a[228:231], a[176:179], v[144:159]\n"
"s_waitcnt lgkmcnt(4)\n"
"ds_read_b64_tr_b16 a[244:245], v250 offset:36864\n"
"v_accvgpr_write_b32 a2, 0\n"
"v_accvgpr_write_b32 a3, 0\n"
"v_mfma_f32_32x32x16_bf16 v[96:111], a[232:235], a[160:163], v[96:111]\n"
"ds_read_b64_tr_b16 a[246:247], v251 offset:36864\n"
"buffer_load_dwordx4 a[136:139], v234, s[32:35], s16 offen offset:2048 sc1\n"
"v_accvgpr_write_b32 a4, 0\n"
"v_accvgpr_write_b32 a5, 0\n"
"v_mfma_f32_32x32x16_bf16 v[160:175], a[232:235], a[176:179], v[160:175]\n"
"s_waitcnt lgkmcnt(4)\n"
"ds_read_b64_tr_b16 a[248:249], v252 offset:36864\n"
"v_accvgpr_write_b32 a6, 0\n"
"v_accvgpr_write_b32 a7, 0\n"
"v_mfma_f32_32x32x16_bf16 v[112:127], a[236:239], a[160:163], v[112:127]\n"
"ds_read_b64_tr_b16 a[250:251], v253 offset:36864\n"
"buffer_load_dwordx4 a[140:143], v234, s[32:35], s16 offen offset:3072 sc1\n"
"v_accvgpr_write_b32 a8, 0\n"
"v_accvgpr_write_b32 a9, 0\n"
"v_mfma_f32_32x32x16_bf16 v[176:191], a[236:239], a[176:179], v[176:191]\n"
"s_waitcnt lgkmcnt(4)\n"
"ds_read_b64_tr_b16 a[252:253], v254 offset:36864\n"
"v_accvgpr_write_b32 a10, 0\n"
"v_accvgpr_write_b32 a11, 0\n"
"v_mfma_f32_32x32x16_bf16 v[64:79], a[240:243], a[164:167], v[64:79]\n"
"ds_read_b64_tr_b16 a[254:255], v255 offset:36864\n"
"buffer_load_dwordx4 a[144:147], v234, s[32:35], s17 offen offset:0 sc1\n"
"v_accvgpr_write_b32 a12, 0\n"
"v_accvgpr_write_b32 a13, 0\n"
"v_mfma_f32_32x32x16_bf16 v[128:143], a[240:243], a[180:183], v[128:143]\n"
"s_waitcnt lgkmcnt(4)\n"
"ds_read_b64_tr_b16 a[224:225], v248 offset:40960\n"
"v_accvgpr_write_b32 a14, 0\n"
"v_accvgpr_write_b32 a15, 0\n"
"v_mfma_f32_32x32x16_bf16 v[80:95], a[244:247], a[164:167], v[80:95]\n"
"ds_read_b64_tr_b16 a[226:227], v249 offset:40960\n"
"buffer_load_dwordx4 a[148:151], v234, s[32:35], s17 offen offset:1024 sc1\n"
"v_accvgpr_write_b32 a16, 0\n"
"v_accvgpr_write_b32 a17, 0\n"
"v_mfma_f32_32x32x16_bf16 v[144:159], a[244:247], a[180:183], v[144:159]\n"
"s_waitcnt lgkmcnt(4)\n"
"ds_read_b64_tr_b16 a[228:229], v250 offset:40960\n"
"v_accvgpr_write_b32 a18, 0\n"
"v_accvgpr_write_b32 a19, 0\n"
"v_mfma_f32_32x32x16_bf16 v[96:111], a[248:251], a[164:167], v[96:111]\n"
"ds_read_b64_tr_b16 a[230:231], v251 offset:40960\n"
"buffer_load_dwordx4 a[152:155], v234, s[32:35], s17 offen offset:2048 sc1\n"
"v_accvgpr_write_b32 a20, 0\n"
"v_accvgpr_write_b32 a21, 0\n"
"v_mfma_f32_32x32x16_bf16 v[160:175], a[248:251], a[180:183], v[160:175]\n"
"s_waitcnt lgkmcnt(4)\n"
"ds_read_b64_tr_b16 a[232:233], v252 offset:40960\n"
"buffer_load_dwordx4 a[156:159], v234, s[32:35], s17 offen offset:3072 sc1\n"
"v_accvgpr_write_b32 a22, 0\n"
"v_mfma_f32_32x32x16_bf16 v[112:127], a[252:255], a[164:167], v[112:127]\n"
"s_add_u32 s16, s16, 4096\n"
"s_add_u32 s17, s17, 4096\n"
"ds_read_b64_tr_b16 a[234:235], v253 offset:40960\n"
"v_accvgpr_write_b32 a23, 0\n"
"v_mfma_f32_32x32x16_bf16 v[176:191], a[252:255], a[180:183], v[176:191]\n"
"s_waitcnt lgkmcnt(4)\n"
"ds_read_b64_tr_b16 a[236:237], v254 offset:40960\n"
"buffer_store_dwordx4 v[36:39], v235, s[36:39], s71 offen offset:0 sc1\n"
"v_accvgpr_write_b32 a24, 0\n"
"v_mfma_f32_32x32x16_bf16 v[64:79], a[224:227], a[168:171], v[64:79]\n"
"ds_read_b64_tr_b16 a[238:239], v255 offset:40960\n"
"v_accvgpr_write_b32 a25, 0\n"
"v_accvgpr_write_b32 a26, 0\n"
"v_accvgpr_write_b32 a27, 0\n"
"v_mfma_f32_32x32x16_bf16 v[128:143], a[224:227], a[184:187], v[128:143]\n"
"s_waitcnt lgkmcnt(4)\n"
"ds_read_b64_tr_b16 a[240:241], v248 offset:45056\n"
"buffer_store_dwordx4 v[44:47], v235, s[36:39], s71 offen offset:1024 sc1\n"
"v_accvgpr_write_b32 a28, 0\n"
"v_mfma_f32_32x32x16_bf16 v[80:95], a[228:231], a[168:171], v[80:95]\n"
"ds_read_b64_tr_b16 a[242:243], v249 offset:45056\n"
"v_accvgpr_write_b32 a29, 0\n"
"v_accvgpr_write_b32 a30, 0\n"
"v_accvgpr_write_b32 a31, 0\n"
"v_mfma_f32_32x32x16_bf16 v[144:159], a[228:231], a[184:187], v[144:159]\n"
"s_waitcnt lgkmcnt(4)\n"
"ds_read_b64_tr_b16 a[244:245], v250 offset:45056\n"
"buffer_store_dwordx4 v[52:55], v235, s[36:39], s71 offen offset:2048 sc1\n"
"v_accvgpr_write_b32 a32, 0\n"
"v_mfma_f32_32x32x16_bf16 v[96:111], a[232:235], a[168:171], v[96:111]\n"
"ds_read_b64_tr_b16 a[246:247], v251 offset:45056\n"
"v_accvgpr_write_b32 a33, 0\n"
"v_accvgpr_write_b32 a34, 0\n"
"v_accvgpr_write_b32 a35, 0\n"
"v_mfma_f32_32x32x16_bf16 v[160:175], a[232:235], a[184:187], v[160:175]\n"
"s_waitcnt lgkmcnt(4)\n"
"ds_read_b64_tr_b16 a[248:249], v252 offset:45056\n"
"buffer_store_dwordx4 v[60:63], v235, s[36:39], s71 offen offset:3072 sc1\n"
"v_accvgpr_write_b32 a36, 0\n"
"v_mfma_f32_32x32x16_bf16 v[112:127], a[236:239], a[168:171], v[112:127]\n"
"ds_read_b64_tr_b16 a[250:251], v253 offset:45056\n"
"v_accvgpr_write_b32 a37, 0\n"
"v_accvgpr_write_b32 a38, 0\n"
"v_accvgpr_write_b32 a39, 0\n"
"v_mfma_f32_32x32x16_bf16 v[176:191], a[236:239], a[184:187], v[176:191]\n"
"s_waitcnt lgkmcnt(4)\n"
"ds_read_b64_tr_b16 a[252:253], v254 offset:45056\n"
"v_accvgpr_write_b32 a40, 0\n"
"s_waitcnt vmcnt(12)\n"
"v_mfma_f32_32x32x16_bf16 v[64:79], a[240:243], a[172:175], v[64:79]\n"
"s_barrier\n"
"ds_read_b64_tr_b16 a[254:255], v255 offset:45056\n"
"v_accvgpr_write_b32 a41, 0\n"
"v_accvgpr_write_b32 a42, 0\n"
"v_mfma_f32_32x32x16_bf16 v[128:143], a[240:243], a[188:191], v[128:143]\n"
"s_waitcnt lgkmcnt(4)\n"
"ds_read_b64_tr_b16 a[224:225], v248 offset:49152\n"
"v_accvgpr_write_b32 a43, 0\n"
"v_accvgpr_write_b32 a44, 0\n"
"v_mfma_f32_32x32x16_bf16 v[80:95], a[244:247], a[172:175], v[80:95]\n"
"ds_read_b64_tr_b16 a[226:227], v249 offset:49152\n"
"v_accvgpr_write_b32 a45, 0\n"
"v_accvgpr_write_b32 a46, 0\n"
"v_accvgpr_write_b32 a47, 0\n"
"v_mfma_f32_32x32x16_bf16 v[144:159], a[244:247], a[188:191], v[144:159]\n"
"s_waitcnt lgkmcnt(4)\n"
"ds_read_b64_tr_b16 a[228:229], v250 offset:49152\n"
"v_accvgpr_write_b32 a48, 0\n"
"v_accvgpr_write_b32 a49, 0\n"
"v_mfma_f32_32x32x16_bf16 v[96:111], a[248:251], a[172:175], v[96:111]\n"
"ds_read_b64_tr_b16 a[230:231], v251 offset:49152\n"
"v_accvgpr_write_b32 a50, 0\n"
"v_accvgpr_write_b32 a51, 0\n"
"v_accvgpr_write_b32 a52, 0\n"
"v_mfma_f32_32x32x16_bf16 v[160:175], a[248:251], a[188:191], v[160:175]\n"
"s_waitcnt lgkmcnt(4)\n"
"ds_read_b64_tr_b16 a[232:233], v252 offset:49152\n"
"v_accvgpr_write_b32 a53, 0\n"
"v_accvgpr_write_b32 a54, 0\n"
"v_mfma_f32_32x32x16_bf16 v[112:127], a[252:255], a[172:175], v[112:127]\n"
"ds_read_b64_tr_b16 a[234:235], v253 offset:49152\n"
"v_accvgpr_write_b32 a55, 0\n"
"v_accvgpr_write_b32 a56, 0\n"
"v_accvgpr_write_b32 a57, 0\n"
"v_mfma_f32_32x32x16_bf16 v[176:191], a[252:255], a[188:191], v[176:191]\n"
"s_waitcnt lgkmcnt(4)\n"
"ds_read_b64_tr_b16 a[236:237], v254 offset:49152\n"
"v_accvgpr_write_b32 a58, 0\n"
"v_accvgpr_write_b32 a59, 0\n"
"v_mfma_f32_32x32x16_bf16 v[64:79], a[224:227], a[192:195], v[64:79]\n"
"ds_read_b64_tr_b16 a[238:239], v255 offset:49152\n"
"v_cmp_ne_u64 vcc, s[96:97], v[240:241]\n"
"v_accvgpr_write_b32 a60, 0\n"
"v_accvgpr_write_b32 a61, 0\n"
"v_mfma_f32_32x32x16_bf16 v[128:143], a[224:227], a[208:211], v[128:143]\n"
"s_waitcnt lgkmcnt(4)\n"
"ds_read_b64_tr_b16 a[240:241], v248 offset:53248\n"
"v_accvgpr_write_b32 a62, 0\n"
"s_cbranch_vccnz L_aspin3_%=\n"
"L_ac1r2_%=:\n"
"v_mfma_f32_32x32x16_bf16 v[80:95], a[228:231], a[192:195], v[80:95]\n"
"ds_read_b64_tr_b16 a[242:243], v249 offset:53248\n"
"v_accvgpr_write_b32 a63, 0\n"
"v_accvgpr_write_b32 a64, 0\n"
"v_accvgpr_write_b32 a65, 0\n"
"v_mfma_f32_32x32x16_bf16 v[144:159], a[228:231], a[208:211], v[144:159]\n"
"s_waitcnt lgkmcnt(4)\n"
"s_mov_b32 m0, s52\n"
"ds_read_b64_tr_b16 a[244:245], v250 offset:53248\n"
"buffer_load_dwordx4 v216, s[40:43], s53 offen offset:0 sc1 lds\n"
"v_mfma_f32_32x32x16_bf16 v[96:111], a[232:235], a[192:195], v[96:111]\n"
"ds_read_b64_tr_b16 a[246:247], v251 offset:53248\n"
"buffer_load_dwordx4 v216, s[40:43], s53 offen offset:1024 sc1 lds\n"
"v_accvgpr_write_b32 a66, 0\n"
"v_accvgpr_write_b32 a67, 0\n"
"v_accvgpr_write_b32 a68, 0\n"
"v_mfma_f32_32x32x16_bf16 v[160:175], a[232:235], a[208:211], v[160:175]\n"
"s_waitcnt lgkmcnt(4)\n"
"ds_read_b64_tr_b16 a[248:249], v252 offset:53248\n"
"v_accvgpr_write_b32 a69, 0\n"
"v_accvgpr_write_b32 a70, 0\n"
"v_mfma_f32_32x32x16_bf16 v[112:127], a[236:239], a[192:195], v[112:127]\n"
"ds_read_b64_tr_b16 a[250:251], v253 offset:53248\n"
"buffer_load_dwordx4 v216, s[40:43], s53 offen offset:2048 sc1 lds\n"
"v_accvgpr_write_b32 a71, 0\n"
"v_accvgpr_write_b32 a72, 0\n"
"v_accvgpr_write_b32 a73, 0\n"
"v_mfma_f32_32x32x16_bf16 v[176:191], a[236:239], a[208:211], v[176:191]\n"
"s_waitcnt lgkmcnt(4)\n"
"ds_read_b64_tr_b16 a[252:253], v254 offset:53248\n"
"v_accvgpr_write_b32 a74, 0\n"
"v_accvgpr_write_b32 a75, 0\n"
"v_mfma_f32_32x32x16_bf16 v[64:79], a[240:243], a[196:199], v[64:79]\n"
"ds_read_b64_tr_b16 a[254:255], v255 offset:53248\n"
"buffer_load_dwordx4 v216, s[40:43], s53 offen offset:3072 sc1 lds\n"
"v_accvgpr_write_b32 a76, 0\n"
"v_accvgpr_write_b32 a77, 0\n"
"v_accvgpr_write_b32 a78, 0\n"
"v_mfma_f32_32x32x16_bf16 v[128:143], a[240:243], a[212:215], v[128:143]\n"
"s_waitcnt lgkmcnt(4)\n"
"s_add_u32 s52, s52, 0x4000\n"
"s_add_u32 s53, s53, 0x4000\n"
"s_mov_b32 m0, s52\n"
"ds_read_b64_tr_b16 a[224:225], v248 offset:57344\n"
"buffer_load_dwordx4 v216, s[40:43], s53 offen offset:0 sc1 lds\n"
"v_mfma_f32_32x32x16_bf16 v[80:95], a[244:247], a[196:199], v[80:95]\n"
"ds_read_b64_tr_b16 a[226:227], v249 offset:57344\n"
"v_accvgpr_write_b32 a79, 0\n"
"v_accvgpr_write_b32 a80, 0\n"
"v_accvgpr_write_b32 a81, 0\n"
"v_mfma_f32_32x32x16_bf16 v[144:159], a[244:247], a[212:215], v[144:159]\n"
"s_waitcnt lgkmcnt(4)\n"
"ds_read_b64_tr_b16 a[228:229], v250 offset:57344\n"
"buffer_load_dwordx4 v216, s[40:43], s53 offen offset:1024 sc1 lds\n"
"v_accvgpr_write_b32 a82, 0\n"
"v_accvgpr_write_b32 a83, 0\n"
"v_mfma_f32_32x32x16_bf16 v[96:111], a[248:251], a[196:199], v[96:111]\n"
"ds_read_b64_tr_b16 a[230:231], v251 offset:57344\n"
"v_accvgpr_write_b32 a84, 0\n"
"v_accvgpr_write_b32 a85, 0\n"
"v_accvgpr_write_b32 a86, 0\n"
"v_mfma_f32_32x32x16_bf16 v[160:175], a[248:251], a[212:215], v[160:175]\n"
"s_waitcnt lgkmcnt(4)\n"
"ds_read_b64_tr_b16 a[232:233], v252 offset:57344\n"
"buffer_load_dwordx4 v216, s[40:43], s53 offen offset:2048 sc1 lds\n"
"v_accvgpr_write_b32 a87, 0\n"
"v_accvgpr_write_b32 a88, 0\n"
"v_mfma_f32_32x32x16_bf16 v[112:127], a[252:255], a[196:199], v[112:127]\n"
"ds_read_b64_tr_b16 a[234:235], v253 offset:57344\n"
"buffer_load_dwordx4 v216, s[40:43], s53 offen offset:3072 sc1 lds\n"
"v_accvgpr_write_b32 a89, 0\n"
"v_accvgpr_write_b32 a90, 0\n"
"v_mfma_f32_32x32x16_bf16 v[176:191], a[252:255], a[212:215], v[176:191]\n"
"s_waitcnt lgkmcnt(4)\n"
"s_add_u32 s52, s52, 0x4000\n"
"s_add_u32 s53, s53, 0x4000\n"
"ds_read_b64_tr_b16 a[236:237], v254 offset:57344\n"
"v_accvgpr_write_b32 a91, 0\n"
"v_mfma_f32_32x32x16_bf16 v[64:79], a[224:227], a[200:203], v[64:79]\n"
"s_add_u32 m0, s54, 0x10000\n"
"ds_read_b64_tr_b16 a[238:239], v255 offset:57344\n"
"buffer_load_dwordx4 v216, s[24:27], s55 offen offset:0 sc1 lds\n"
"v_accvgpr_write_b32 a92, 0\n"
"v_mfma_f32_32x32x16_bf16 v[128:143], a[224:227], a[216:219], v[128:143]\n"
"s_waitcnt lgkmcnt(4)\n"
"ds_read_b64_tr_b16 a[240:241], v248 offset:61440\n"
"v_accvgpr_write_b32 a93, 0\n"
"v_accvgpr_write_b32 a94, 0\n"
"v_accvgpr_write_b32 a95, 0\n"
"v_mfma_f32_32x32x16_bf16 v[80:95], a[228:231], a[200:203], v[80:95]\n"
"ds_read_b64_tr_b16 a[242:243], v249 offset:61440\n"
"buffer_load_dwordx4 v216, s[24:27], s55 offen offset:1024 sc1 lds\n"
"v_accvgpr_write_b32 a96, 0\n"
"v_accvgpr_write_b32 a97, 0\n"
"v_mfma_f32_32x32x16_bf16 v[144:159], a[228:231], a[216:219], v[144:159]\n"
"s_waitcnt lgkmcnt(4)\n"
"ds_read_b64_tr_b16 a[244:245], v250 offset:61440\n"
"buffer_load_dwordx4 v216, s[24:27], s55 offen offset:2048 sc1 lds\n"
"v_accvgpr_write_b32 a98, 0\n"
"v_accvgpr_write_b32 a99, 0\n"
"v_mfma_f32_32x32x16_bf16 v[96:111], a[232:235], a[200:203], v[96:111]\n"
"ds_read_b64_tr_b16 a[246:247], v251 offset:61440\n"
"v_accvgpr_write_b32 a100, 0\n"
"v_accvgpr_write_b32 a101, 0\n"
"v_accvgpr_write_b32 a102, 0\n"
"v_mfma_f32_32x32x16_bf16 v[160:175], a[232:235], a[216:219], v[160:175]\n"
"s_waitcnt lgkmcnt(4)\n"
"ds_read_b64_tr_b16 a[248:249], v252 offset:61440\n"
"buffer_load_dwordx4 v216, s[24:27], s55 offen offset:3072 sc1 lds\n"
"v_accvgpr_write_b32 a103, 0\n"
"v_accvgpr_write_b32 a104, 0\n"
"v_mfma_f32_32x32x16_bf16 v[112:127], a[236:239], a[200:203], v[112:127]\n"
"s_add_u32 s54, s54, 0x4000\n"
"s_add_u32 s55, s55, 0x4000\n"
"ds_read_b64_tr_b16 a[250:251], v253 offset:61440\n"
"v_accvgpr_write_b32 a105, 0\n"
"v_mfma_f32_32x32x16_bf16 v[176:191], a[236:239], a[216:219], v[176:191]\n"
"s_waitcnt lgkmcnt(4)\n"
"ds_read_b64_tr_b16 a[252:253], v254 offset:61440\n"
"v_accvgpr_write_b32 a106, 0\n"
"v_accvgpr_write_b32 a107, 0\n"
"s_waitcnt vmcnt(12)\n"
"v_mfma_f32_32x32x16_bf16 v[64:79], a[240:243], a[204:207], v[64:79]\n"
"s_barrier\n"
"ds_read_b64_tr_b16 a[254:255], v255 offset:61440\n"
"v_accvgpr_write_b32 a108, 0\n"
"v_accvgpr_write_b32 a109, 0\n"
"v_mfma_f32_32x32x16_bf16 v[128:143], a[240:243], a[220:223], v[128:143]\n"
"s_waitcnt lgkmcnt(4)\n"
"ds_read_b64_tr_b16 a[224:225], v224 offset:0\n"
"s_cmp_lg_u32 s48, 0\n"
"s_cbranch_scc1 L_anofl5_%=\n"
"v_mov_b32 v238, s96\n"
"v_mov_b32 v239, s97\n"
"s_lshl_b32 s58, s50, 7\n"
"s_lshl_b32 s59, s51, 4\n"
"s_add_u32 s58, s58, s59\n"
"s_add_u32 s58, s58, 20488\n"
"buffer_store_dwordx2 v[238:239], off, s[44:47], s58 sc1\n"
"L_anofl5_%=:\n"
"v_mfma_f32_32x32x16_bf16 v[80:95], a[244:247], a[204:207], v[80:95]\n"
"ds_read_b64_tr_b16 a[226:227], v225 offset:0\n"
"v_accvgpr_write_b32 a110, 0\n"
"v_accvgpr_write_b32 a111, 0\n"
"v_accvgpr_write_b32 a112, 0\n"
"v_accvgpr_write_b32 a113, 0\n"
"v_mfma_f32_32x32x16_bf16 v[144:159], a[244:247], a[220:223], v[144:159]\n"
"s_waitcnt lgkmcnt(4)\n"
"ds_read_b64_tr_b16 a[228:229], v224 offset:4096\n"
"v_accvgpr_write_b32 a114, 0\n"
"v_accvgpr_write_b32 a115, 0\n"
"v_mfma_f32_32x32x16_bf16 v[96:111], a[248:251], a[204:207], v[96:111]\n"
"ds_read_b64_tr_b16 a[230:231], v225 offset:4096\n"
"v_accvgpr_write_b32 a116, 0\n"
"v_accvgpr_write_b32 a117, 0\n"
"v_accvgpr_write_b32 a118, 0\n"
"v_accvgpr_write_b32 a119, 0\n"
"v_mfma_f32_32x32x16_bf16 v[160:175], a[248:251], a[220:223], v[160:175]\n"
"s_waitcnt lgkmcnt(4)\n"
"ds_read_b64_tr_b16 a[232:233], v224 offset:8192\n"
"v_accvgpr_write_b32 a120, 0\n"
"v_accvgpr_write_b32 a121, 0\n"
"v_mfma_f32_32x32x16_bf16 v[112:127], a[252:255], a[204:207], v[112:127]\n"
"ds_read_b64_tr_b16 a[234:235], v225 offset:8192\n"
"v_accvgpr_write_b32 a122, 0\n"
"v_accvgpr_write_b32 a123, 0\n"
"v_accvgpr_write_b32 a124, 0\n"
"v_accvgpr_write_b32 a125, 0\n"
"v_mfma_f32_32x32x16_bf16 v[176:191], a[252:255], a[220:223], v[176:191]\n"
"s_waitcnt lgkmcnt(4)\n"
"ds_read_b64_tr_b16 a[236:237], v224 offset:12288\n"
"v_accvgpr_write_b32 a126, 0\n"
"v_accvgpr_write_b32 a127, 0\n"
"v_mfma_f32_32x32x16_bf16 v[64:79], a[224:227], a[128:131], v[64:79]\n"
"ds_read_b64_tr_b16 a[238:239], v225 offset:12288\n"
"v_and_b32 v242, 7, v236\n"
"v_lshlrev_b32 v242, 4, v242\n"
"v_lshrrev_b32 v241, 5, v236\n"
"v_mfma_f32_32x32x16_bf16 v[128:143], a[224:227], a[144:147], v[128:143]\n"
"s_waitcnt lgkmcnt(4)\n"
"s_add_u32 m0, s54, 0x10000\n"
"ds_read_b64_tr_b16 a[240:241], v226 offset:0\n"
"buffer_load_dwordx4 v216, s[24:27], s55 offen offset:0 sc1 lds\n"
"v_mfma_f32_32x32x16_bf16 v[64:79], a[228:231], a[132:135], v[64:79]\n"
"ds_read_b64_tr_b16 a[242:243], v227 offset:0\n"
"v_and_b32 v240, 31, v236\n"
"v_lshlrev_b32 v241, 2, v241\n"
"v_sub_u32 v240, v240, v241\n"
"v_mfma_f32_32x32x16_bf16 v[128:143], a[228:231], a[148:151], v[128:143]\n"
"s_waitcnt lgkmcnt(4)\n"
"ds_read_b64_tr_b16 a[244:245], v226 offset:4096\n"
"v_cmp_gt_i32_e64 s[64:65], 0, v240\n"
"v_cmp_gt_i32_e64 s[66:67], 1, v240\n"
"v_mfma_f32_32x32x16_bf16 v[64:79], a[232:235], a[136:139], v[64:79]\n"
"ds_read_b64_tr_b16 a[246:247], v227 offset:4096\n"
"v_cmp_gt_i32_e64 s[68:69], 2, v240\n"
"v_cmp_gt_i32_e64 s[70:71], 3, v240\n"
"v_cmp_gt_i32_e64 s[72:73], 8, v240\n"
"v_mfma_f32_32x32x16_bf16 v[128:143], a[232:235], a[152:155], v[128:143]\n"
"s_waitcnt lgkmcnt(4)\n"
"ds_read_b64_tr_b16 a[248:249], v226 offset:8192\n"
"v_cmp_gt_i32_e64 s[74:75], 9, v240\n"
"v_cmp_gt_i32_e64 s[76:77], 10, v240\n"
"v_mfma_f32_32x32x16_bf16 v[64:79], a[236:239], a[140:143], v[64:79]\n"
"ds_read_b64_tr_b16 a[250:251], v227 offset:8192\n"
"buffer_load_dwordx4 v216, s[24:27], s55 offen offset:1024 sc1 lds\n"
"v_cmp_gt_i32_e64 s[78:79], 11, v240\n"
"v_mfma_f32_32x32x16_bf16 v[128:143], a[236:239], a[156:159], v[128:143]\n"
"s_waitcnt lgkmcnt(4)\n"
"ds_read_b64_tr_b16 a[252:253], v226 offset:12288\n"
"v_cmp_gt_i32_e64 s[80:81], 16, v240\n"
"v_cmp_gt_i32_e64 s[82:83], 17, v240\n"
"v_mfma_f32_32x32x16_bf16 v[80:95], a[240:243], a[128:131], v[80:95]\n"
"ds_read_b64_tr_b16 a[254:255], v227 offset:12288\n"
"v_cmp_gt_i32_e64 s[84:85], 18, v240\n"
"v_cmp_gt_i32_e64 s[86:87], 19, v240\n"
"v_mfma_f32_32x32x16_bf16 v[144:159], a[240:243], a[144:147], v[144:159]\n"
"s_waitcnt lgkmcnt(4)\n"
"ds_read_b64_tr_b16 a[224:225], v228 offset:0\n"
"v_cmp_gt_i32_e64 s[88:89], 24, v240\n"
"v_cmp_gt_i32_e64 s[90:91], 25, v240\n"
"v_mfma_f32_32x32x16_bf16 v[80:95], a[244:247], a[132:135], v[80:95]\n"
"ds_read_b64_tr_b16 a[226:227], v229 offset:0\n"
"v_cmp_gt_i32_e64 s[92:93], 26, v240\n"
"v_cmp_gt_i32_e64 s[94:95], 27, v240\n"
"v_mfma_f32_32x32x16_bf16 v[144:159], a[244:247], a[148:151], v[144:159]\n"
"s_waitcnt lgkmcnt(4)\n"
"ds_read_b64_tr_b16 a[228:229], v228 offset:4096\n"
"v_cvt_pk_bf16_f32 v0, v64, v65\n"
"v_cvt_pk_bf16_f32 v1, v66, v67\n"
"v_mfma_f32_32x32x16_bf16 v[80:95], a[248:251], a[136:139], v[80:95]\n"
"ds_read_b64_tr_b16 a[230:231], v229 offset:4096\n"
"buffer_load_dwordx4 v216, s[24:27], s55 offen offset:2048 sc1 lds\n"
"v_cvt_pk_bf16_f32 v2, v68, v69\n"
"v_cvt_pk_bf16_f32 v3, v70, v71\n"
"v_mfma_f32_32x32x16_bf16 v[144:159], a[248:251], a[152:155], v[144:159]\n"
"s_waitcnt lgkmcnt(4)\n"
"ds_read_b64_tr_b16 a[232:233], v228 offset:8192\n"
"v_cvt_pk_bf16_f32 v4, v72, v73\n"
"v_cvt_pk_bf16_f32 v5, v74, v75\n"
"v_mfma_f32_32x32x16_bf16 v[80:95], a[252:255], a[140:143], v[80:95]\n"
"ds_read_b64_tr_b16 a[234:235], v229 offset:8192\n"
"v_cvt_pk_bf16_f32 v6, v76, v77\n"
"v_cvt_pk_bf16_f32 v7, v78, v79\n"
"v_cvt_pk_bf16_f32 v32, v128, v129\n"
"v_mfma_f32_32x32x16_bf16 v[144:159], a[252:255], a[156:159], v[144:159]\n"
"s_waitcnt lgkmcnt(4)\n"
"ds_read_b64_tr_b16 a[236:237], v228 offset:12288\n"
"v_cvt_pk_bf16_f32 v33, v130, v131\n"
"v_cvt_pk_bf16_f32 v34, v132, v133\n"
"v_mfma_f32_32x32x16_bf16 v[96:111], a[224:227], a[128:131], v[96:111]\n"
"ds_read_b64_tr_b16 a[238:239], v229 offset:12288\n"
"v_cvt_pk_bf16_f32 v35, v134, v135\n"
"v_cvt_pk_bf16_f32 v36, v136, v137\n"
"v_cvt_pk_bf16_f32 v37, v138, v139\n"
"v_mfma_f32_32x32x16_bf16 v[160:175], a[224:227], a[144:147], v[160:175]\n"
"s_waitcnt lgkmcnt(4)\n"
"ds_read_b64_tr_b16 a[240:241], v230 offset:0\n"
"buffer_load_dwordx4 v216, s[24:27], s55 offen offset:3072 sc1 lds\n"
"v_cvt_pk_bf16_f32 v38, v140, v141\n"
"v_mfma_f32_32x32x16_bf16 v[96:111], a[228:231], a[132:135], v[96:111]\n"
"s_add_u32 s54, s54, 0x4000\n"
"s_add_u32 s55, s55, 0x4000\n"
"ds_read_b64_tr_b16 a[242:243], v231 offset:0\n"
"v_cvt_pk_bf16_f32 v39, v142, v143\n"
"v_mfma_f32_32x32x16_bf16 v[160:175], a[228:231], a[148:151], v[160:175]\n"
"s_waitcnt lgkmcnt(4)\n"
"ds_read_b64_tr_b16 a[244:245], v230 offset:4096\n"
"v_cvt_pk_bf16_f32 v8, v80, v81\n"
"v_cvt_pk_bf16_f32 v9, v82, v83\n"
"v_mfma_f32_32x32x16_bf16 v[96:111], a[232:235], a[136:139], v[96:111]\n"
"ds_read_b64_tr_b16 a[246:247], v231 offset:4096\n"
"v_cvt_pk_bf16_f32 v10, v84, v85\n"
"v_cvt_pk_bf16_f32 v11, v86, v87\n"
"v_cvt_pk_bf16_f32 v12, v88, v89\n"
"v_mfma_f32_32x32x16_bf16 v[160:175], a[232:235], a[152:155], v[160:175]\n"
"s_waitcnt lgkmcnt(4)\n"
"ds_read_b64_tr_b16 a[248:249], v230 offset:8192\n"
"v_cvt_pk_bf16_f32 v13, v90, v91\n"
"v_cvt_pk_bf16_f32 v14, v92, v93\n"
"v_mfma_f32_32x32x16_bf16 v[96:111], a[236:239], a[140:143], v[96:111]\n"
"ds_read_b64_tr_b16 a[250:251], v231 offset:8192\n"
"v_cvt_pk_bf16_f32 v15, v94, v95\n"
"v_cvt_pk_bf16_f32 v40, v144, v145\n"
"v_mfma_f32_32x32x16_bf16 v[160:175], a[236:239], a[156:159], v[160:175]\n"
"s_waitcnt lgkmcnt(4)\n"
"ds_read_b64_tr_b16 a[252:253], v230 offset:12288\n"
"v_cvt_pk_bf16_f32 v41, v146, v147\n"
"s_waitcnt vmcnt(4)\n"
"v_mfma_f32_32x32x16_bf16 v[112:127], a[240:243], a[128:131], v[112:127]\n"
"s_barrier\n"
"ds_read_b64_tr_b16 a[254:255], v231 offset:12288\n"
"v_cvt_pk_bf16_f32 v42, v148, v149\n"
"v_mfma_f32_32x32x16_bf16 v[176:191], a[240:243], a[144:147], v[176:191]\n"
"s_waitcnt lgkmcnt(4)\n"
"v_cvt_pk_bf16_f32 v43, v150, v151\n"
"v_cvt_pk_bf16_f32 v44, v152, v153\n"
"v_cvt_pk_bf16_f32 v45, v154, v155\n"
"v_mfma_f32_32x32x16_bf16 v[112:127], a[244:247], a[132:135], v[112:127]\n"
"ds_read_b128 a[224:227], v214 offset:0\n"
"v_cvt_pk_bf16_f32 v46, v156, v157\n"
"v_cvt_pk_bf16_f32 v47, v158, v159\n"
"v_mfma_f32_32x32x16_bf16 v[176:191], a[244:247], a[148:151], v[176:191]\n"
"s_waitcnt lgkmcnt(3)\n"
"v_cvt_pk_bf16_f32 v16, v96, v97\n"
"v_cvt_pk_bf16_f32 v17, v98, v99\n"
"v_mfma_f32_32x32x16_bf16 v[112:127], a[248:251], a[136:139], v[112:127]\n"
"ds_read_b128 a[228:231], v214 offset:1024\n"
"v_cvt_pk_bf16_f32 v18, v100, v101\n"
"v_cvt_pk_bf16_f32 v19, v102, v103\n"
"v_mfma_f32_32x32x16_bf16 v[176:191], a[248:251], a[152:155], v[176:191]\n"
"s_waitcnt lgkmcnt(2)\n"
"v_cvt_pk_bf16_f32 v20, v104, v105\n"
"v_cvt_pk_bf16_f32 v21, v106, v107\n"
"v_mfma_f32_32x32x16_bf16 v[112:127], a[252:255], a[140:143], v[112:127]\n"
"ds_read_b128 a[232:235], v214 offset:2048\n"
"v_cvt_pk_bf16_f32 v22, v108, v109\n"
"v_cvt_pk_bf16_f32 v23, v110, v111\n"
"v_mfma_f32_32x32x16_bf16 v[176:191], a[252:255], a[156:159], v[176:191]\n"
"v_cvt_pk_bf16_f32 v48, v160, v161\n"
"v_cvt_pk_bf16_f32 v49, v162, v163\n"
"v_cvt_pk_bf16_f32 v50, v164, v165\n"
"s_waitcnt lgkmcnt(2)\n"
"v_mfma_f32_32x32x16_bf16 v[64:79], a[224:227], v[0:3], 0\n"
"ds_read_b128 a[236:239], v214 offset:3072\n"
"v_cvt_pk_bf16_f32 v51, v166, v167\n"
"v_cvt_pk_bf16_f32 v52, v168, v169\n"
"v_mfma_f32_32x32x16_bf16 v[80:95], a[224:227], v[32:35], 0\n"
"s_waitcnt lgkmcnt(2)\n"
"v_cvt_pk_bf16_f32 v53, v170, v171\n"
"v_cvt_pk_bf16_f32 v54, v172, v173\n"
"v_mfma_f32_32x32x16_bf16 v[64:79], a[228:231], v[4:7], v[64:79]\n"
"ds_read_b128 a[240:243], v214 offset:4096\n"
"v_cvt_pk_bf16_f32 v55, v174, v175\n"
"v_mfma_f32_32x32x16_bf16 v[80:95], a[228:231], v[36:39], v[80:95]\n"
"s_waitcnt lgkmcnt(2)\n"
"v_cvt_pk_bf16_f32 v24, v112, v113\n"
"v_cvt_pk_bf16_f32 v25, v114, v115\n"
"v_cvt_pk_bf16_f32 v26, v116, v117\n"
"s_lshl_b32 s58, s50, 7\n"
"s_add_u32 s58, s58, 20488\n"
"v_mfma_f32_32x32x16_bf16 v[64:79], a[232:235], v[8:11], v[64:79]\n"
"ds_read_b128 a[244:247], v214 offset:5120\n"
"buffer_load_dwordx2 v[240:241], v242, s[44:47], s58 offen sc1\n"
"v_cvt_pk_bf16_f32 v27, v118, v119\n"
"v_cvt_pk_bf16_f32 v28, v120, v121\n"
"v_cvt_pk_bf16_f32 v29, v122, v123\n"
"v_mfma_f32_32x32x16_bf16 v[80:95], a[232:235], v[40:43], v[80:95]\n"
"s_waitcnt lgkmcnt(2)\n"
"v_cvt_pk_bf16_f32 v30, v124, v125\n"
"v_cvt_pk_bf16_f32 v31, v126, v127\n"
"v_cvt_pk_bf16_f32 v56, v176, v177\n"
"s_or_b32 s59, s49, s48\n"
"s_cmp_lg_u32 s59, 0\n"
"s_cbranch_scc1 L_anob6_%=\n"
"v_lshlrev_b32 v252, 3, v236\n"
"s_movk_i32 s59, 16384\n"
"buffer_load_dwordx2 v[244:245], v252, s[44:47], s59 offen offset:0 sc1\n"
"buffer_load_dwordx2 v[246:247], v252, s[44:47], s59 offen offset:512 sc1\n"
"buffer_load_dwordx2 v[248:249], v252, s[44:47], s59 offen offset:1024 sc1\n"
"buffer_load_dwordx2 v[250:251], v252, s[44:47], s59 offen offset:1536 sc1\n"
"L_anob6_%=:\n"
"v_mfma_f32_32x32x16_bf16 v[64:79], a[236:239], v[12:15], v[64:79]\n"
"ds_read_b128 a[248:251], v214 offset:6144\n"
"v_cvt_pk_bf16_f32 v57, v178, v179\n"
"v_cvt_pk_bf16_f32 v58, v180, v181\n"
"v_mfma_f32_32x32x16_bf16 v[80:95], a[236:239], v[44:47], v[80:95]\n"
"s_waitcnt lgkmcnt(2)\n"
"v_cvt_pk_bf16_f32 v59, v182, v183\n"
"v_cvt_pk_bf16_f32 v60, v184, v185\n"
"v_cvt_pk_bf16_f32 v61, v186, v187\n"
"v_mfma_f32_32x32x16_bf16 v[64:79], a[240:243], v[16:19], v[64:79]\n"
"ds_read_b128 a[252:255], v214 offset:7168\n"
"v_cvt_pk_bf16_f32 v62, v188, v189\n"
"v_cvt_pk_bf16_f32 v63, v190, v191\n"
"v_mfma_f32_32x32x16_bf16 v[80:95], a[240:243], v[48:51], v[80:95]\n"
"s_waitcnt lgkmcnt(2)\n"
"v_mfma_f32_32x32x16_bf16 v[64:79], a[244:247], v[20:23], v[64:79]\n"
"v_mfma_f32_32x32x16_bf16 v[80:95], a[244:247], v[52:55], v[80:95]\n"
"s_waitcnt lgkmcnt(1)\n"
"v_mfma_f32_32x32x16_bf16 v[64:79], a[248:251], v[24:27], v[64:79]\n"
"v_mfma_f32_32x32x16_bf16 v[80:95], a[248:251], v[56:59], v[80:95]\n"
"s_waitcnt lgkmcnt(0)\n"
"v_mfma_f32_32x32x16_bf16 v[64:79], a[252:255], v[28:31], v[64:79]\n"
"v_mfma_f32_32x32x16_bf16 v[80:95], a[252:255], v[60:63], v[80:95]\n"
"s_nop 7\n"
"s_nop 3\n"
"s_cmp_lg_u32 s8, 0\n"
"s_cbranch_scc1 L_anomask14_%=\n"
"v_mov_b32 v221, 0xff800000\n"
"v_cndmask_b32_e64 v64, v64, v221, s[64:65]\n"
"v_cndmask_b32_e64 v65, v65, v221, s[66:67]\n"
"v_cndmask_b32_e64 v66, v66, v221, s[68:69]\n"
"v_cndmask_b32_e64 v67, v67, v221, s[70:71]\n"
"v_cndmask_b32_e64 v68, v68, v221, s[72:73]\n"
"v_cndmask_b32_e64 v69, v69, v221, s[74:75]\n"
"v_cndmask_b32_e64 v70, v70, v221, s[76:77]\n"
"v_cndmask_b32_e64 v71, v71, v221, s[78:79]\n"
"v_cndmask_b32_e64 v72, v72, v221, s[80:81]\n"
"v_cndmask_b32_e64 v73, v73, v221, s[82:83]\n"
"v_cndmask_b32_e64 v74, v74, v221, s[84:85]\n"
"v_cndmask_b32_e64 v75, v75, v221, s[86:87]\n"
"v_cndmask_b32_e64 v76, v76, v221, s[88:89]\n"
"v_cndmask_b32_e64 v77, v77, v221, s[90:91]\n"
"v_cndmask_b32_e64 v78, v78, v221, s[92:93]\n"
"v_cndmask_b32_e64 v79, v79, v221, s[94:95]\n"
"L_anomask14_%=:\n"
"v_max3_f32 v222, v64, v65, v66\n"
"v_max3_f32 v222, v222, v67, v68\n"
"v_max3_f32 v222, v222, v69, v70\n"
"v_max3_f32 v222, v222, v71, v72\n"
"v_max3_f32 v222, v222, v73, v74\n"
"v_max3_f32 v222, v222, v75, v76\n"
"v_max3_f32 v222, v222, v77, v78\n"
"v_max_f32 v222, v222, v79\n"
"v_mov_b32 v223, v222\n"
"s_nop 1\n"
"v_permlane32_swap_b32 v222, v223\n"
"v_max_f32 v222, v222, v223\n"
"v_max3_f32 v224, v80, v81, v82\n"
"v_max3_f32 v224, v224, v83, v84\n"
"v_max3_f32 v224, v224, v85, v86\n"
"v_max3_f32 v224, v224, v87, v88\n"
"v_max3_f32 v224, v224, v89, v90\n"
"v_max3_f32 v224, v224, v91, v92\n"
"v_max3_f32 v224, v224, v93, v94\n"
"v_max_f32 v224, v224, v95\n"
"v_mov_b32 v225, v224\n"
"s_nop 1\n"
"v_permlane32_swap_b32 v224, v225\n"
"v_max_f32 v224, v224, v225\n"
"v_sub_f32 v64, v64, v222\n"
"v_sub_f32 v65, v65, v222\n"
"v_sub_f32 v66, v66, v222\n"
"v_sub_f32 v67, v67, v222\n"
"v_sub_f32 v68, v68, v222\n"
"v_sub_f32 v69, v69, v222\n"
"v_sub_f32 v70, v70, v222\n"
"v_sub_f32 v71, v71, v222\n"
"v_sub_f32 v72, v72, v222\n"
"v_sub_f32 v73, v73, v222\n"
"v_sub_f32 v74, v74, v222\n"
"v_sub_f32 v75, v75, v222\n"
"v_sub_f32 v76, v76, v222\n"
"v_sub_f32 v77, v77, v222\n"
"v_sub_f32 v78, v78, v222\n"
"v_sub_f32 v79, v79, v222\n"
"v_sub_f32 v176, 0, v222\n"
"v_sub_f32 v177, 0, v222\n"
"v_sub_f32 v178, 0, v222\n"
"v_sub_f32 v179, 0, v222\n"
"v_sub_f32 v180, 0, v222\n"
"v_sub_f32 v181, 0, v222\n"
"v_sub_f32 v182, 0, v222\n"
"v_sub_f32 v183, 0, v222\n"
"v_sub_f32 v184, 0, v222\n"
"v_sub_f32 v185, 0, v222\n"
"v_sub_f32 v186, 0, v222\n"
"v_sub_f32 v187, 0, v222\n"
"v_sub_f32 v188, 0, v222\n"
"v_sub_f32 v189, 0, v222\n"
"v_sub_f32 v190, 0, v222\n"
"v_sub_f32 v191, 0, v222\n"
"v_sub_f32 v80, v80, v224\n"
"v_sub_f32 v81, v81, v224\n"
"v_sub_f32 v82, v82, v224\n"
"v_sub_f32 v83, v83, v224\n"
"v_sub_f32 v84, v84, v224\n"
"v_sub_f32 v85, v85, v224\n"
"v_sub_f32 v86, v86, v224\n"
"v_sub_f32 v87, v87, v224\n"
"v_sub_f32 v88, v88, v224\n"
"v_sub_f32 v89, v89, v224\n"
"v_sub_f32 v90, v90, v224\n"
"v_sub_f32 v91, v91, v224\n"
"v_sub_f32 v92, v92, v224\n"
"v_sub_f32 v93, v93, v224\n"
"v_sub_f32 v94, v94, v224\n"
"v_sub_f32 v95, v95, v224\n"
"v_sub_f32 v192, 0, v224\n"
"v_sub_f32 v193, 0, v224\n"
"v_sub_f32 v194, 0, v224\n"
"v_sub_f32 v195, 0, v224\n"
"v_sub_f32 v196, 0, v224\n"
"v_sub_f32 v197, 0, v224\n"
"v_sub_f32 v198, 0, v224\n"
"v_sub_f32 v199, 0, v224\n"
"v_sub_f32 v200, 0, v224\n"
"v_sub_f32 v201, 0, v224\n"
"v_sub_f32 v202, 0, v224\n"
"v_sub_f32 v203, 0, v224\n"
"v_sub_f32 v204, 0, v224\n"
"v_sub_f32 v205, 0, v224\n"
"v_sub_f32 v206, 0, v224\n"
"v_sub_f32 v207, 0, v224\n"
"v_exp_f32 v112, v64\n"
"v_exp_f32 v113, v65\n"
"v_exp_f32 v114, v66\n"
"v_exp_f32 v115, v67\n"
"v_add_f32 v208, v112, v113\n"
"v_cvt_pk_bf16_f32 v96, v112, v113\n"
"v_exp_f32 v116, v68\n"
"v_add_f32 v208, v208, v114\n"
"v_exp_f32 v117, v69\n"
"v_add_f32 v208, v208, v115\n"
"s_mov_b32 s12, 2\n"
"v_cvt_pk_bf16_f32 v97, v114, v115\n"
"v_exp_f32 v118, v70\n"
"v_add_f32 v208, v208, v116\n"
"v_exp_f32 v119, v71\n"
"v_add_f32 v208, v208, v117\n"
"v_cvt_pk_bf16_f32 v98, v116, v117\n"
"v_exp_f32 v120, v72\n"
"v_add_f32 v208, v208, v118\n"
"v_exp_f32 v121, v73\n"
"v_add_f32 v208, v208, v119\n"
"v_cvt_pk_bf16_f32 v99, v118, v119\n"
"v_exp_f32 v122, v74\n"
"v_add_f32 v208, v208, v120\n"
"v_exp_f32 v123, v75\n"
"v_add_f32 v208, v208, v121\n"
"v_cvt_pk_bf16_f32 v100, v120, v121\n"
"v_exp_f32 v124, v76\n"
"v_add_f32 v208, v208, v122\n"
"v_exp_f32 v125, v77\n"
"ds_read_b128 a[224:227], v214 offset:8192\n"
"v_add_f32 v208, v208, v123\n"
"v_cvt_pk_bf16_f32 v101, v122, v123\n"
"v_exp_f32 v126, v78\n"
"v_add_f32 v208, v208, v124\n"
"v_exp_f32 v127, v79\n"
"v_add_f32 v208, v208, v125\n"
"ds_read_b128 a[228:231], v214 offset:9216\n"
"v_cvt_pk_bf16_f32 v102, v124, v125\n"
"v_exp_f32 v112, v80\n"
"v_add_f32 v208, v208, v126\n"
"v_exp_f32 v113, v81\n"
"v_add_f32 v208, v208, v127\n"
"ds_read_b128 a[232:235], v214 offset:10240\n"
"v_cvt_pk_bf16_f32 v103, v126, v127\n"
"v_exp_f32 v114, v82\n"
"s_waitcnt lgkmcnt(2)\n"
"v_exp_f32 v115, v83\n"
"v_add_f32 v209, v112, v113\n"
"v_cvt_pk_bf16_f32 v104, v112, v113\n"
"v_mfma_f32_32x32x16_bf16 v[128:143], a[224:227], v[0:3], v[176:191]\n"
"ds_read_b128 a[236:239], v214 offset:11264\n"
"v_exp_f32 v116, v84\n"
"v_add_f32 v209, v209, v114\n"
"v_mfma_f32_32x32x16_bf16 v[144:159], a[224:227], v[32:35], v[192:207]\n"
"s_waitcnt lgkmcnt(2)\n"
"v_exp_f32 v117, v85\n"
"v_add_f32 v209, v209, v115\n"
"v_cvt_pk_bf16_f32 v105, v114, v115\n"
"v_mfma_f32_32x32x16_bf16 v[128:143], a[228:231], v[4:7], v[128:143]\n"
"ds_read_b128 a[240:243], v214 offset:12288\n"
"v_exp_f32 v118, v86\n"
"v_add_f32 v209, v209, v116\n"
"v_mfma_f32_32x32x16_bf16 v[144:159], a[228:231], v[36:39], v[144:159]\n"
"s_waitcnt lgkmcnt(2)\n"
"v_exp_f32 v119, v87\n"
"v_add_f32 v209, v209, v117\n"
"v_cvt_pk_bf16_f32 v106, v116, v117\n"
"v_mfma_f32_32x32x16_bf16 v[128:143], a[232:235], v[8:11], v[128:143]\n"
"ds_read_b128 a[244:247], v214 offset:13312\n"
"v_exp_f32 v120, v88\n"
"v_add_f32 v209, v209, v118\n"
"v_mfma_f32_32x32x16_bf16 v[144:159], a[232:235], v[40:43], v[144:159]\n"
"s_waitcnt lgkmcnt(2)\n"
"v_exp_f32 v121, v89\n"
"v_add_f32 v209, v209, v119\n"
"v_cvt_pk_bf16_f32 v107, v118, v119\n"
"v_mfma_f32_32x32x16_bf16 v[128:143], a[236:239], v[12:15], v[128:143]\n"
"ds_read_b128 a[248:251], v214 offset:14336\n"
"v_exp_f32 v122, v90\n"
"v_add_f32 v209, v209, v120\n"
"v_mfma_f32_32x32x16_bf16 v[144:159], a[236:239], v[44:47], v[144:159]\n"
"s_waitcnt lgkmcnt(2)\n"
"v_exp_f32 v123, v91\n"
"v_add_f32 v209, v209, v121\n"
"v_cvt_pk_bf16_f32 v108, v120, v121\n"
"v_mfma_f32_32x32x16_bf16 v[128:143], a[240:243], v[16:19], v[128:143]\n"
"ds_read_b128 a[252:255], v214 offset:15360\n"
"v_exp_f32 v124, v92\n"
"v_add_f32 v209, v209, v122\n"
"v_mfma_f32_32x32x16_bf16 v[144:159], a[240:243], v[48:51], v[144:159]\n"
"s_waitcnt lgkmcnt(2)\n"
"v_exp_f32 v125, v93\n"
"v_add_f32 v209, v209, v123\n"
"v_cvt_pk_bf16_f32 v109, v122, v123\n"
"v_mfma_f32_32x32x16_bf16 v[128:143], a[244:247], v[20:23], v[128:143]\n"
"ds_read_b128 a[224:227], v215 offset:0\n"
"v_exp_f32 v126, v94\n"
"v_add_f32 v209, v209, v124\n"
"v_mfma_f32_32x32x16_bf16 v[144:159], a[244:247], v[52:55], v[144:159]\n"
"s_waitcnt lgkmcnt(2)\n"
"v_exp_f32 v127, v95\n"
"v_add_f32 v209, v209, v125\n"
"s_waitcnt vmcnt(0)\n"
"v_mfma_f32_32x32x16_bf16 v[128:143], a[248:251], v[24:27], v[128:143]\n"
"ds_read_b128 a[228:231], v215 offset:1024\n"
"v_cmp_ne_u64_e64 s[56:57], s[96:97], v[240:241]\n"
"v_cvt_pk_bf16_f32 v110, v124, v125\n"
"v_add_f32 v209, v209, v126\n"
"v_add_f32 v209, v209, v127\n"
"v_mfma_f32_32x32x16_bf16 v[144:159], a[248:251], v[56:59], v[144:159]\n"
"s_waitcnt lgkmcnt(2)\n"
"v_max_f32 v219, v208, v209\n"
"s_cmp_lg_u64 s[56:57], 0\n"
"s_cbranch_scc1 L_aspin12_%=\n"
"L_ac2r11_%=:\n"
"v_mfma_f32_32x32x16_bf16 v[128:143], a[252:255], v[28:31], v[128:143]\n"
"ds_read_b128 a[232:235], v215 offset:2048\n"
"v_cmp_nle_f32 vcc, v219, v220\n"
"s_or_b32 s59, s49, s48\n"
"s_cmp_eq_u32 s59, 0\n"
"s_cbranch_scc1 L_abump7_%=\n"
"L_abumpr8_%=:\n"
"v_mfma_f32_32x32x16_bf16 v[144:159], a[252:255], v[60:63], v[144:159]\n"
"v_cvt_pk_bf16_f32 v111, v126, v127\n"
"s_cbranch_vccnz L_aft15_%=\n"
"L_afr16_%=:\n"
"v_add_f32 v210, v210, v208\n"
"v_add_f32 v211, v211, v209\n"
"s_sub_u32 s11, s11, 1\n"
"s_cbranch_scc1 L_ss2_%=\n"
"L_ss1_%=:\n"
"s_waitcnt lgkmcnt(2)\n"
"v_mfma_f32_32x32x16_bf16 a[0:15], a[224:227], v[96:99], a[0:15]\n"
"ds_read_b128 a[236:239], v215 offset:3072\n"
"v_exp_f32 v112, v128\n"
"s_waitcnt vmcnt(0)\n"
"s_barrier\n"
"v_mfma_f32_32x32x16_bf16 a[64:79], a[224:227], v[104:107], a[64:79]\n"
"s_waitcnt lgkmcnt(2)\n"
"s_mov_b32 m0, s52\n"
"v_add_u32 v214, 0x4000, v214\n"
"v_and_b32 v214, 0xffff, v214\n"
"v_exp_f32 v113, v129\n"
"v_mfma_f32_32x32x16_bf16 a[16:31], a[228:231], v[96:99], a[16:31]\n"
"s_add_u32 s58, s52, 0x4000\n"
"ds_read_b128 a[240:243], v215 offset:4096\n"
"buffer_load_dwordx4 v216, s[40:43], s53 offen offset:0 sc1 lds\n"
"v_exp_f32 v114, v130\n"
"v_mfma_f32_32x32x16_bf16 a[80:95], a[228:231], v[104:107], a[80:95]\n"
"s_waitcnt lgkmcnt(2)\n"
"v_exp_f32 v115, v131\n"
"v_add_f32 v208, v112, v113\n"
"v_cvt_pk_bf16_f32 v160, v112, v113\n"
"v_mfma_f32_32x32x16_bf16 a[32:47], a[232:235], v[96:99], a[32:47]\n"
"s_and_b32 s52, s58, 0xffff\n"
"ds_read_b128 a[244:247], v215 offset:5120\n"
"v_exp_f32 v116, v132\n"
"v_add_f32 v208, v208, v114\n"
"v_mfma_f32_32x32x16_bf16 a[96:111], a[232:235], v[104:107], a[96:111]\n"
"s_waitcnt lgkmcnt(2)\n"
"buffer_load_dwordx4 v216, s[40:43], s53 offen offset:1024 sc1 lds\n"
"v_exp_f32 v117, v133\n"
"v_add_f32 v208, v208, v115\n"
"v_mfma_f32_32x32x16_bf16 a[48:63], a[236:239], v[96:99], a[48:63]\n"
"ds_read_b128 a[248:251], v215 offset:6144\n"
"v_cvt_pk_bf16_f32 v161, v114, v115\n"
"v_exp_f32 v118, v134\n"
"v_add_f32 v208, v208, v116\n"
"v_mfma_f32_32x32x16_bf16 a[112:127], a[236:239], v[104:107], a[112:127]\n"
"s_waitcnt lgkmcnt(2)\n"
"v_exp_f32 v119, v135\n"
"v_add_f32 v208, v208, v117\n"
"v_cvt_pk_bf16_f32 v162, v116, v117\n"
"v_mfma_f32_32x32x16_bf16 a[0:15], a[240:243], v[100:103], a[0:15]\n"
"ds_read_b128 a[252:255], v215 offset:7168\n"
"v_exp_f32 v120, v136\n"
"v_add_f32 v208, v208, v118\n"
"v_exp_f32 v121, v137\n"
"v_mfma_f32_32x32x16_bf16 a[64:79], a[240:243], v[108:111], a[64:79]\n"
"s_waitcnt lgkmcnt(2)\n"
"buffer_load_dwordx4 v216, s[40:43], s53 offen offset:2048 sc1 lds\n"
"v_add_f32 v208, v208, v119\n"
"v_cvt_pk_bf16_f32 v163, v118, v119\n"
"v_exp_f32 v122, v138\n"
"v_mfma_f32_32x32x16_bf16 a[16:31], a[244:247], v[100:103], a[16:31]\n"
"ds_read_b128 a[224:227], v214 offset:0\n"
"v_add_f32 v208, v208, v120\n"
"v_exp_f32 v123, v139\n"
"v_add_f32 v208, v208, v121\n"
"v_mfma_f32_32x32x16_bf16 a[80:95], a[244:247], v[108:111], a[80:95]\n"
"s_waitcnt lgkmcnt(2)\n"
"v_cvt_pk_bf16_f32 v164, v120, v121\n"
"v_exp_f32 v124, v140\n"
"v_add_f32 v208, v208, v122\n"
"v_mfma_f32_32x32x16_bf16 a[32:47], a[248:251], v[100:103], a[32:47]\n"
"ds_read_b128 a[228:231], v214 offset:1024\n"
"buffer_load_dwordx4 v216, s[40:43], s53 offen offset:3072 sc1 lds\n"
"v_exp_f32 v125, v141\n"
"v_add_f32 v208, v208, v123\n"
"v_mfma_f32_32x32x16_bf16 a[96:111], a[248:251], v[108:111], a[96:111]\n"
"s_waitcnt lgkmcnt(2)\n"
"v_cvt_pk_bf16_f32 v165, v122, v123\n"
"v_exp_f32 v126, v142\n"
"v_add_f32 v208, v208, v124\n"
"v_mfma_f32_32x32x16_bf16 a[48:63], a[252:255], v[100:103], a[48:63]\n"
"s_add_u32 s53, s53, 16384\n"
"ds_read_b128 a[232:235], v214 offset:2048\n"
"v_exp_f32 v127, v143\n"
"v_add_f32 v208, v208, v125\n"
"v_mfma_f32_32x32x16_bf16 a[112:127], a[252:255], v[108:111], a[112:127]\n"
"s_waitcnt lgkmcnt(2)\n"
"s_add_u32 m0, s54, 0x10000\n"
"v_cvt_pk_bf16_f32 v166, v124, v125\n"
"v_exp_f32 v112, v144\n"
"v_mfma_f32_32x32x16_bf16 v[64:79], a[224:227], v[0:3], v[176:191]\n"
"s_add_u32 s56, s54, 0x4000\n"
"ds_read_b128 a[236:239], v214 offset:3072\n"
"buffer_load_dwordx4 v216, s[24:27], s55 offen offset:0 sc1 lds\n"
"v_add_f32 v208, v208, v126\n"
"v_exp_f32 v113, v145\n"
"v_mfma_f32_32x32x16_bf16 v[80:95], a[224:227], v[32:35], v[192:207]\n"
"s_waitcnt lgkmcnt(2)\n"
"v_add_f32 v208, v208, v127\n"
"v_cvt_pk_bf16_f32 v167, v126, v127\n"
"v_exp_f32 v114, v146\n"
"v_mfma_f32_32x32x16_bf16 v[64:79], a[228:231], v[4:7], v[64:79]\n"
"s_sub_u32 s57, s56, 0x14000\n"
"ds_read_b128 a[240:243], v214 offset:4096\n"
"v_exp_f32 v115, v147\n"
"v_add_f32 v209, v112, v113\n"
"v_mfma_f32_32x32x16_bf16 v[80:95], a[228:231], v[36:39], v[80:95]\n"
"s_waitcnt lgkmcnt(2)\n"
"v_cvt_pk_bf16_f32 v168, v112, v113\n"
"v_exp_f32 v116, v148\n"
"v_add_f32 v209, v209, v114\n"
"v_mfma_f32_32x32x16_bf16 v[64:79], a[232:235], v[8:11], v[64:79]\n"
"s_min_u32 s54, s56, s57\n"
"ds_read_b128 a[244:247], v214 offset:5120\n"
"buffer_load_dwordx4 v216, s[24:27], s55 offen offset:1024 sc1 lds\n"
"v_exp_f32 v117, v149\n"
"v_mfma_f32_32x32x16_bf16 v[80:95], a[232:235], v[40:43], v[80:95]\n"
"s_waitcnt lgkmcnt(2)\n"
"v_add_f32 v209, v209, v115\n"
"v_cvt_pk_bf16_f32 v169, v114, v115\n"
"v_exp_f32 v118, v150\n"
"v_mfma_f32_32x32x16_bf16 v[64:79], a[236:239], v[12:15], v[64:79]\n"
"ds_read_b128 a[248:251], v214 offset:6144\n"
"v_add_f32 v209, v209, v116\n"
"v_exp_f32 v119, v151\n"
"v_add_f32 v209, v209, v117\n"
"v_mfma_f32_32x32x16_bf16 v[80:95], a[236:239], v[44:47], v[80:95]\n"
"s_waitcnt lgkmcnt(2)\n"
"buffer_load_dwordx4 v216, s[24:27], s55 offen offset:2048 sc1 lds\n"
"v_cvt_pk_bf16_f32 v170, v116, v117\n"
"v_exp_f32 v120, v152\n"
"v_mfma_f32_32x32x16_bf16 v[64:79], a[240:243], v[16:19], v[64:79]\n"
"ds_read_b128 a[252:255], v214 offset:7168\n"
"v_add_f32 v209, v209, v118\n"
"v_exp_f32 v121, v153\n"
"v_add_f32 v209, v209, v119\n"
"v_cvt_pk_bf16_f32 v171, v118, v119\n"
"v_mfma_f32_32x32x16_bf16 v[80:95], a[240:243], v[48:51], v[80:95]\n"
"s_waitcnt lgkmcnt(2)\n"
"v_exp_f32 v122, v154\n"
"v_add_f32 v209, v209, v120\n"
"v_exp_f32 v123, v155\n"
"v_mfma_f32_32x32x16_bf16 v[64:79], a[244:247], v[20:23], v[64:79]\n"
"ds_read_b128 a[224:227], v215 offset:8192\n"
"v_add_f32 v209, v209, v121\n"
"v_cvt_pk_bf16_f32 v172, v120, v121\n"
"v_exp_f32 v124, v156\n"
"v_mfma_f32_32x32x16_bf16 v[80:95], a[244:247], v[52:55], v[80:95]\n"
"s_waitcnt lgkmcnt(2)\n"
"buffer_load_dwordx4 v216, s[24:27], s55 offen offset:3072 sc1 lds\n"
"v_add_f32 v209, v209, v122\n"
"v_exp_f32 v125, v157\n"
"v_mfma_f32_32x32x16_bf16 v[64:79], a[248:251], v[24:27], v[64:79]\n"
"s_add_u32 s55, s55, 16384\n"
"ds_read_b128 a[228:231], v215 offset:9216\n"
"v_add_f32 v209, v209, v123\n"
"v_cvt_pk_bf16_f32 v173, v122, v123\n"
"v_exp_f32 v126, v158\n"
"v_mfma_f32_32x32x16_bf16 v[80:95], a[248:251], v[56:59], v[80:95]\n"
"s_waitcnt lgkmcnt(2)\n"
"v_add_f32 v209, v209, v124\n"
"v_exp_f32 v127, v159\n"
"v_add_f32 v209, v209, v125\n"
"v_mfma_f32_32x32x16_bf16 v[64:79], a[252:255], v[28:31], v[64:79]\n"
"ds_read_b128 a[232:235], v215 offset:10240\n"
"v_cvt_pk_bf16_f32 v174, v124, v125\n"
"v_add_f32 v209, v209, v126\n"
"v_add_f32 v209, v209, v127\n"
"v_max_f32 v219, v208, v209\n"
"v_cmp_nle_f32 vcc, v219, v220\n"
"v_mfma_f32_32x32x16_bf16 v[80:95], a[252:255], v[60:63], v[80:95]\n"
"s_waitcnt lgkmcnt(2)\n"
"v_cvt_pk_bf16_f32 v175, v126, v127\n"
"s_cbranch_vccnz L_aft21_%=\n"
"L_afr22_%=:\n"
"v_add_f32 v210, v210, v208\n"
"v_add_f32 v211, v211, v209\n"
"v_mfma_f32_32x32x16_bf16 a[0:15], a[224:227], v[160:163], a[0:15]\n"
"s_cmp_ge_u32 s12, s60\n"
"s_cbranch_scc1 L_amask17_%=\n"
"L_amret18_%=:\n"
"ds_read_b128 a[236:239], v215 offset:11264\n"
"v_mfma_f32_32x32x16_bf16 a[64:79], a[224:227], v[168:171], a[64:79]\n"
"s_waitcnt lgkmcnt(2)\n"
"v_exp_f32 v112, v64\n"
"v_exp_f32 v113, v65\n"
"v_mfma_f32_32x32x16_bf16 a[16:31], a[228:231], v[160:163], a[16:31]\n"
"ds_read_b128 a[240:243], v215 offset:12288\n"
"v_exp_f32 v114, v66\n"
"v_exp_f32 v115, v67\n"
"v_mfma_f32_32x32x16_bf16 a[80:95], a[228:231], v[168:171], a[80:95]\n"
"s_waitcnt lgkmcnt(2)\n"
"v_add_f32 v208, v112, v113\n"
"v_cvt_pk_bf16_f32 v96, v112, v113\n"
"v_exp_f32 v116, v68\n"
"v_mfma_f32_32x32x16_bf16 a[32:47], a[232:235], v[160:163], a[32:47]\n"
"s_add_u32 s12, s12, 2\n"
"ds_read_b128 a[244:247], v215 offset:13312\n"
"v_add_f32 v208, v208, v114\n"
"v_exp_f32 v117, v69\n"
"v_mfma_f32_32x32x16_bf16 a[96:111], a[232:235], v[168:171], a[96:111]\n"
"s_waitcnt lgkmcnt(2)\n"
"v_add_f32 v208, v208, v115\n"
"v_cvt_pk_bf16_f32 v97, v114, v115\n"
"v_exp_f32 v118, v70\n"
"v_mfma_f32_32x32x16_bf16 a[48:63], a[236:239], v[160:163], a[48:63]\n"
"ds_read_b128 a[248:251], v215 offset:14336\n"
"v_add_f32 v208, v208, v116\n"
"v_exp_f32 v119, v71\n"
"v_add_f32 v208, v208, v117\n"
"v_mfma_f32_32x32x16_bf16 a[112:127], a[236:239], v[168:171], a[112:127]\n"
"s_waitcnt lgkmcnt(2)\n"
"v_cvt_pk_bf16_f32 v98, v116, v117\n"
"v_exp_f32 v120, v72\n"
"v_add_f32 v208, v208, v118\n"
"v_mfma_f32_32x32x16_bf16 a[0:15], a[240:243], v[164:167], a[0:15]\n"
"ds_read_b128 a[252:255], v215 offset:15360\n"
"v_exp_f32 v121, v73\n"
"v_add_f32 v208, v208, v119\n"
"v_cvt_pk_bf16_f32 v99, v118, v119\n"
"v_mfma_f32_32x32x16_bf16 a[64:79], a[240:243], v[172:175], a[64:79]\n"
"s_waitcnt lgkmcnt(2)\n"
"v_exp_f32 v122, v74\n"
"v_add_f32 v208, v208, v120\n"
"v_mfma_f32_32x32x16_bf16 a[16:31], a[244:247], v[164:167], a[16:31]\n"
"s_add_u32 s59, s10, 0x4000\n"
"ds_read_b128 a[224:227], v214 offset:8192\n"
"v_exp_f32 v123, v75\n"
"v_add_f32 v208, v208, v121\n"
"v_mfma_f32_32x32x16_bf16 a[80:95], a[244:247], v[172:175], a[80:95]\n"
"s_waitcnt lgkmcnt(2)\n"
"v_cvt_pk_bf16_f32 v100, v120, v121\n"
"v_exp_f32 v124, v76\n"
"v_add_f32 v208, v208, v122\n"
"v_mfma_f32_32x32x16_bf16 a[32:47], a[248:251], v[164:167], a[32:47]\n"
"s_sub_u32 s15, s59, 0x14000\n"
"ds_read_b128 a[228:231], v214 offset:9216\n"
"v_exp_f32 v125, v77\n"
"v_add_f32 v208, v208, v123\n"
"v_mfma_f32_32x32x16_bf16 a[96:111], a[248:251], v[172:175], a[96:111]\n"
"s_waitcnt lgkmcnt(2)\n"
"v_cvt_pk_bf16_f32 v101, v122, v123\n"
"v_exp_f32 v126, v78\n"
"v_add_f32 v208, v208, v124\n"
"v_mfma_f32_32x32x16_bf16 a[48:63], a[252:255], v[164:167], a[48:63]\n"
"s_min_u32 s10, s59, s15\n"
"ds_read_b128 a[232:235], v214 offset:10240\n"
"v_exp_f32 v127, v79\n"
"v_add_f32 v208, v208, v125\n"
"v_mfma_f32_32x32x16_bf16 a[112:127], a[252:255], v[172:175], a[112:127]\n"
"s_waitcnt lgkmcnt(2)\n"
"v_add_u32 v215, s10, v217\n"
"v_cvt_pk_bf16_f32 v102, v124, v125\n"
"v_exp_f32 v112, v80\n"
"v_mfma_f32_32x32x16_bf16 v[128:143], a[224:227], v[0:3], v[176:191]\n"
"ds_read_b128 a[236:239], v214 offset:11264\n"
"v_add_f32 v208, v208, v126\n"
"v_exp_f32 v113, v81\n"
"v_add_f32 v208, v208, v127\n"
"v_mfma_f32_32x32x16_bf16 v[144:159], a[224:227], v[32:35], v[192:207]\n"
"s_waitcnt lgkmcnt(2)\n"
"v_cvt_pk_bf16_f32 v103, v126, v127\n"
"v_exp_f32 v114, v82\n"
"v_mfma_f32_32x32x16_bf16 v[128:143], a[228:231], v[4:7], v[128:143]\n"
"ds_read_b128 a[240:243], v214 offset:12288\n"
"v_exp_f32 v115, v83\n"
"v_add_f32 v209, v112, v113\n"
"v_cvt_pk_bf16_f32 v104, v112, v113\n"
"v_mfma_f32_32x32x16_bf16 v[144:159], a[228:231], v[36:39], v[144:159]\n"
"s_waitcnt lgkmcnt(2)\n"
"v_exp_f32 v116, v84\n"
"v_add_f32 v209, v209, v114\n"
"v_mfma_f32_32x32x16_bf16 v[128:143], a[232:235], v[8:11], v[128:143]\n"
"ds_read_b128 a[244:247], v214 offset:13312\n"
"v_exp_f32 v117, v85\n"
"v_add_f32 v209, v209, v115\n"
"v_cvt_pk_bf16_f32 v105, v114, v115\n"
"v_mfma_f32_32x32x16_bf16 v[144:159], a[232:235], v[40:43], v[144:159]\n"
"s_waitcnt lgkmcnt(2)\n"
"v_exp_f32 v118, v86\n"
"v_add_f32 v209, v209, v116\n"
"v_exp_f32 v119, v87\n"
"v_mfma_f32_32x32x16_bf16 v[128:143], a[236:239], v[12:15], v[128:143]\n"
"ds_read_b128 a[248:251], v214 offset:14336\n"
"v_add_f32 v209, v209, v117\n"
"v_cvt_pk_bf16_f32 v106, v116, v117\n"
"v_exp_f32 v120, v88\n"
"v_mfma_f32_32x32x16_bf16 v[144:159], a[236:239], v[44:47], v[144:159]\n"
"s_waitcnt lgkmcnt(2)\n"
"v_add_f32 v209, v209, v118\n"
"v_exp_f32 v121, v89\n"
"v_add_f32 v209, v209, v119\n"
"v_mfma_f32_32x32x16_bf16 v[128:143], a[240:243], v[16:19], v[128:143]\n"
"ds_read_b128 a[252:255], v214 offset:15360\n"
"v_cvt_pk_bf16_f32 v107, v118, v119\n"
"v_exp_f32 v122, v90\n"
"v_add_f32 v209, v209, v120\n"
"v_mfma_f32_32x32x16_bf16 v[144:159], a[240:243], v[48:51], v[144:159]\n"
"s_waitcnt lgkmcnt(2)\n"
"v_exp_f32 v123, v91\n"
"v_add_f32 v209, v209, v121\n"
"v_cvt_pk_bf16_f32 v108, v120, v121\n"
"v_mfma_f32_32x32x16_bf16 v[128:143], a[244:247], v[20:23], v[128:143]\n"
"ds_read_b128 a[224:227], v215 offset:0\n"
"v_exp_f32 v124, v92\n"
"v_add_f32 v209, v209, v122\n"
"v_mfma_f32_32x32x16_bf16 v[144:159], a[244:247], v[52:55], v[144:159]\n"
"s_waitcnt lgkmcnt(2)\n"
"v_exp_f32 v125, v93\n"
"v_add_f32 v209, v209, v123\n"
"v_cvt_pk_bf16_f32 v109, v122, v123\n"
"v_mfma_f32_32x32x16_bf16 v[128:143], a[248:251], v[24:27], v[128:143]\n"
"ds_read_b128 a[228:231], v215 offset:1024\n"
"v_exp_f32 v126, v94\n"
"v_add_f32 v209, v209, v124\n"
"v_exp_f32 v127, v95\n"
"v_mfma_f32_32x32x16_bf16 v[144:159], a[248:251], v[56:59], v[144:159]\n"
"s_waitcnt lgkmcnt(2)\n"
"v_add_f32 v209, v209, v125\n"
"v_cvt_pk_bf16_f32 v110, v124, v125\n"
"v_add_f32 v209, v209, v126\n"
"v_add_f32 v209, v209, v127\n"
"v_mfma_f32_32x32x16_bf16 v[128:143], a[252:255], v[28:31], v[128:143]\n"
"ds_read_b128 a[232:235], v215 offset:2048\n"
"v_max_f32 v219, v208, v209\n"
"v_cmp_nle_f32 vcc, v219, v220\n"
"v_cvt_pk_bf16_f32 v111, v126, v127\n"
"v_mfma_f32_32x32x16_bf16 v[144:159], a[252:255], v[60:63], v[144:159]\n"
"s_cbranch_vccnz L_aft23_%=\n"
"L_afr24_%=:\n"
"v_add_f32 v210, v210, v208\n"
"v_add_f32 v211, v211, v209\n"
"s_sub_u32 s11, s11, 1\n"
"s_cbranch_scc0 L_ss1_%=\n"
"L_ss2_%=:\n"
"s_sub_u32 s12, s12, 1\n"
"s_waitcnt lgkmcnt(2)\n"
"v_mfma_f32_32x32x16_bf16 a[0:15], a[224:227], v[96:99], a[0:15]\n"
"s_cmp_ge_u32 s12, s60\n"
"s_cbranch_scc1 L_amask25_%=\n"
"L_amret26_%=:\n"
"ds_read_b128 a[236:239], v215 offset:3072\n"
"s_waitcnt vmcnt(0)\n"
"s_barrier\n"
"v_mfma_f32_32x32x16_bf16 a[64:79], a[224:227], v[104:107], a[64:79]\n"
"s_waitcnt lgkmcnt(2)\n"
"s_mov_b32 m0, s52\n"
"v_add_u32 v214, 0x4000, v214\n"
"v_and_b32 v214, 0xffff, v214\n"
"v_exp_f32 v112, v128\n"
"v_mfma_f32_32x32x16_bf16 a[16:31], a[228:231], v[96:99], a[16:31]\n"
"s_add_u32 s12, s12, 1\n"
"ds_read_b128 a[240:243], v215 offset:4096\n"
"buffer_load_dwordx4 v216, s[40:43], s53 offen offset:0 sc1 lds\n"
"v_exp_f32 v113, v129\n"
"v_mfma_f32_32x32x16_bf16 a[80:95], a[228:231], v[104:107], a[80:95]\n"
"s_waitcnt lgkmcnt(2)\n"
"v_exp_f32 v114, v130\n"
"v_exp_f32 v115, v131\n"
"v_mfma_f32_32x32x16_bf16 a[32:47], a[232:235], v[96:99], a[32:47]\n"
"ds_read_b128 a[244:247], v215 offset:5120\n"
"buffer_load_dwordx4 v216, s[40:43], s53 offen offset:1024 sc1 lds\n"
"v_add_f32 v208, v112, v113\n"
"v_cvt_pk_bf16_f32 v160, v112, v113\n"
"v_exp_f32 v116, v132\n"
"v_mfma_f32_32x32x16_bf16 a[96:111], a[232:235], v[104:107], a[96:111]\n"
"s_waitcnt lgkmcnt(2)\n"
"v_add_f32 v208, v208, v114\n"
"v_exp_f32 v117, v133\n"
"v_add_f32 v208, v208, v115\n"
"v_mfma_f32_32x32x16_bf16 a[48:63], a[236:239], v[96:99], a[48:63]\n"
"ds_read_b128 a[248:251], v215 offset:6144\n"
"buffer_load_dwordx4 v216, s[40:43], s53 offen offset:2048 sc1 lds\n"
"v_cvt_pk_bf16_f32 v161, v114, v115\n"
"v_exp_f32 v118, v134\n"
"v_add_f32 v208, v208, v116\n"
"v_mfma_f32_32x32x16_bf16 a[112:127], a[236:239], v[104:107], a[112:127]\n"
"s_waitcnt lgkmcnt(2)\n"
"v_exp_f32 v119, v135\n"
"v_add_f32 v208, v208, v117\n"
"v_cvt_pk_bf16_f32 v162, v116, v117\n"
"v_mfma_f32_32x32x16_bf16 a[0:15], a[240:243], v[100:103], a[0:15]\n"
"ds_read_b128 a[252:255], v215 offset:7168\n"
"v_exp_f32 v120, v136\n"
"v_add_f32 v208, v208, v118\n"
"v_exp_f32 v121, v137\n"
"v_mfma_f32_32x32x16_bf16 a[64:79], a[240:243], v[108:111], a[64:79]\n"
"s_waitcnt lgkmcnt(2)\n"
"buffer_load_dwordx4 v216, s[40:43], s53 offen offset:3072 sc1 lds\n"
"v_add_f32 v208, v208, v119\n"
"v_cvt_pk_bf16_f32 v163, v118, v119\n"
"v_exp_f32 v122, v138\n"
"v_mfma_f32_32x32x16_bf16 a[16:31], a[244:247], v[100:103], a[16:31]\n"
"s_add_u32 m0, s54, 0x10000\n"
"ds_read_b128 a[224:227], v214 offset:0\n"
"v_add_f32 v208, v208, v120\n"
"v_exp_f32 v123, v139\n"
"v_mfma_f32_32x32x16_bf16 a[80:95], a[244:247], v[108:111], a[80:95]\n"
"s_waitcnt lgkmcnt(2)\n"
"buffer_load_dwordx4 v216, s[24:27], s55 offen offset:0 sc1 lds\n"
"v_add_f32 v208, v208, v121\n"
"v_cvt_pk_bf16_f32 v164, v120, v121\n"
"v_exp_f32 v124, v140\n"
"v_mfma_f32_32x32x16_bf16 a[32:47], a[248:251], v[100:103], a[32:47]\n"
"s_add_u32 s56, s54, 0x4000\n"
"ds_read_b128 a[228:231], v214 offset:1024\n"
"v_add_f32 v208, v208, v122\n"
"v_exp_f32 v125, v141\n"
"v_mfma_f32_32x32x16_bf16 a[96:111], a[248:251], v[108:111], a[96:111]\n"
"s_waitcnt lgkmcnt(2)\n"
"v_add_f32 v208, v208, v123\n"
"v_cvt_pk_bf16_f32 v165, v122, v123\n"
"v_exp_f32 v126, v142\n"
"v_mfma_f32_32x32x16_bf16 a[48:63], a[252:255], v[100:103], a[48:63]\n"
"s_sub_u32 s57, s56, 0x14000\n"
"ds_read_b128 a[232:235], v214 offset:2048\n"
"buffer_load_dwordx4 v216, s[24:27], s55 offen offset:1024 sc1 lds\n"
"v_add_f32 v208, v208, v124\n"
"v_exp_f32 v127, v143\n"
"v_mfma_f32_32x32x16_bf16 a[112:127], a[252:255], v[108:111], a[112:127]\n"
"s_waitcnt lgkmcnt(2)\n"
"v_add_f32 v208, v208, v125\n"
"v_cvt_pk_bf16_f32 v166, v124, v125\n"
"v_exp_f32 v112, v144\n"
"v_mfma_f32_32x32x16_bf16 v[64:79], a[224:227], v[0:3], v[176:191]\n"
"s_min_u32 s54, s56, s57\n"
"ds_read_b128 a[236:239], v214 offset:3072\n"
"buffer_load_dwordx4 v216, s[24:27], s55 offen offset:2048 sc1 lds\n"
"v_add_f32 v208, v208, v126\n"
"v_exp_f32 v113, v145\n"
"v_mfma_f32_32x32x16_bf16 v[80:95], a[224:227], v[32:35], v[192:207]\n"
"s_waitcnt lgkmcnt(2)\n"
"v_add_f32 v208, v208, v127\n"
"v_cvt_pk_bf16_f32 v167, v126, v127\n"
"v_exp_f32 v114, v146\n"
"v_mfma_f32_32x32x16_bf16 v[64:79], a[228:231], v[4:7], v[64:79]\n"
"ds_read_b128 a[240:243], v214 offset:4096\n"
"buffer_load_dwordx4 v216, s[24:27], s55 offen offset:3072 sc1 lds\n"
"v_exp_f32 v115, v147\n"
"v_add_f32 v209, v112, v113\n"
"v_cvt_pk_bf16_f32 v168, v112, v113\n"
"v_mfma_f32_32x32x16_bf16 v[80:95], a[228:231], v[36:39], v[80:95]\n"
"s_waitcnt lgkmcnt(2)\n"
"v_exp_f32 v116, v148\n"
"v_add_f32 v209, v209, v114\n"
"v_exp_f32 v117, v149\n"
"v_mfma_f32_32x32x16_bf16 v[64:79], a[232:235], v[8:11], v[64:79]\n"
"s_add_u32 s55, s55, 16384\n"
"s_add_u32 m0, s52, 0x4000\n"
"ds_read_b128 a[244:247], v214 offset:5120\n"
"v_add_f32 v209, v209, v115\n"
"v_cvt_pk_bf16_f32 v169, v114, v115\n"
"v_mfma_f32_32x32x16_bf16 v[80:95], a[232:235], v[40:43], v[80:95]\n"
"s_waitcnt lgkmcnt(2)\n"
"buffer_load_dwordx4 v218, s[40:43], s53 offen offset:0 sc1 lds\n"
"v_exp_f32 v118, v150\n"
"v_add_f32 v209, v209, v116\n"
"v_mfma_f32_32x32x16_bf16 v[64:79], a[236:239], v[12:15], v[64:79]\n"
"s_xor_b32 s52, s52, 0x8000\n"
"ds_read_b128 a[248:251], v214 offset:6144\n"
"v_exp_f32 v119, v151\n"
"v_add_f32 v209, v209, v117\n"
"v_cvt_pk_bf16_f32 v170, v116, v117\n"
"v_mfma_f32_32x32x16_bf16 v[80:95], a[236:239], v[44:47], v[80:95]\n"
"s_waitcnt lgkmcnt(2)\n"
"buffer_load_dwordx4 v218, s[40:43], s53 offen offset:1024 sc1 lds\n"
"v_exp_f32 v120, v152\n"
"v_add_f32 v209, v209, v118\n"
"v_mfma_f32_32x32x16_bf16 v[64:79], a[240:243], v[16:19], v[64:79]\n"
"ds_read_b128 a[252:255], v214 offset:7168\n"
"v_exp_f32 v121, v153\n"
"v_add_f32 v209, v209, v119\n"
"v_cvt_pk_bf16_f32 v171, v118, v119\n"
"v_mfma_f32_32x32x16_bf16 v[80:95], a[240:243], v[48:51], v[80:95]\n"
"s_waitcnt lgkmcnt(2)\n"
"v_exp_f32 v122, v154\n"
"v_add_f32 v209, v209, v120\n"
"v_exp_f32 v123, v155\n"
"v_mfma_f32_32x32x16_bf16 v[64:79], a[244:247], v[20:23], v[64:79]\n"
"ds_read_b128 a[224:227], v215 offset:8192\n"
"buffer_load_dwordx4 v218, s[40:43], s53 offen offset:2048 sc1 lds\n"
"v_add_f32 v209, v209, v121\n"
"v_cvt_pk_bf16_f32 v172, v120, v121\n"
"v_exp_f32 v124, v156\n"
"v_mfma_f32_32x32x16_bf16 v[80:95], a[244:247], v[52:55], v[80:95]\n"
"s_waitcnt lgkmcnt(2)\n"
"v_add_f32 v209, v209, v122\n"
"v_exp_f32 v125, v157\n"
"v_add_f32 v209, v209, v123\n"
"v_mfma_f32_32x32x16_bf16 v[64:79], a[248:251], v[24:27], v[64:79]\n"
"ds_read_b128 a[228:231], v215 offset:9216\n"
"buffer_load_dwordx4 v218, s[40:43], s53 offen offset:3072 sc1 lds\n"
"v_cvt_pk_bf16_f32 v173, v122, v123\n"
"v_exp_f32 v126, v158\n"
"v_add_f32 v209, v209, v124\n"
"v_mfma_f32_32x32x16_bf16 v[80:95], a[248:251], v[56:59], v[80:95]\n"
"s_waitcnt lgkmcnt(2)\n"
"v_exp_f32 v127, v159\n"
"v_add_f32 v209, v209, v125\n"
"v_cvt_pk_bf16_f32 v174, v124, v125\n"
"v_mfma_f32_32x32x16_bf16 v[64:79], a[252:255], v[28:31], v[64:79]\n"
"s_add_u32 s53, s53, 32768\n"
"ds_read_b128 a[232:235], v215 offset:10240\n"
"v_add_f32 v209, v209, v126\n"
"v_add_f32 v209, v209, v127\n"
"v_max_f32 v219, v208, v209\n"
"v_cmp_nle_f32 vcc, v219, v220\n"
"v_mfma_f32_32x32x16_bf16 v[80:95], a[252:255], v[60:63], v[80:95]\n"
"s_waitcnt lgkmcnt(2)\n"
"v_cvt_pk_bf16_f32 v175, v126, v127\n"
"s_cbranch_vccnz L_aft33_%=\n"
"L_afr34_%=:\n"
"v_add_f32 v210, v210, v208\n"
"v_add_f32 v211, v211, v209\n"
"v_mfma_f32_32x32x16_bf16 a[0:15], a[224:227], v[160:163], a[0:15]\n"
"s_cmp_ge_u32 s12, s60\n"
"s_cbranch_scc1 L_amask29_%=\n"
"L_amret30_%=:\n"
"ds_read_b128 a[236:239], v215 offset:11264\n"
"v_mfma_f32_32x32x16_bf16 a[64:79], a[224:227], v[168:171], a[64:79]\n"
"s_waitcnt lgkmcnt(2)\n"
"v_exp_f32 v112, v64\n"
"v_exp_f32 v113, v65\n"
"v_mfma_f32_32x32x16_bf16 a[16:31], a[228:231], v[160:163], a[16:31]\n"
"ds_read_b128 a[240:243], v215 offset:12288\n"
"v_exp_f32 v114, v66\n"
"v_exp_f32 v115, v67\n"
"v_mfma_f32_32x32x16_bf16 a[80:95], a[228:231], v[168:171], a[80:95]\n"
"s_waitcnt lgkmcnt(2)\n"
"v_add_f32 v208, v112, v113\n"
"v_cvt_pk_bf16_f32 v96, v112, v113\n"
"v_exp_f32 v116, v68\n"
"v_mfma_f32_32x32x16_bf16 a[32:47], a[232:235], v[160:163], a[32:47]\n"
"s_add_u32 s12, s12, 1\n"
"ds_read_b128 a[244:247], v215 offset:13312\n"
"v_add_f32 v208, v208, v114\n"
"v_exp_f32 v117, v69\n"
"v_mfma_f32_32x32x16_bf16 a[96:111], a[232:235], v[168:171], a[96:111]\n"
"s_waitcnt lgkmcnt(2)\n"
"v_add_f32 v208, v208, v115\n"
"v_cvt_pk_bf16_f32 v97, v114, v115\n"
"v_exp_f32 v118, v70\n"
"v_mfma_f32_32x32x16_bf16 a[48:63], a[236:239], v[160:163], a[48:63]\n"
"ds_read_b128 a[248:251], v215 offset:14336\n"
"v_add_f32 v208, v208, v116\n"
"v_exp_f32 v119, v71\n"
"v_add_f32 v208, v208, v117\n"
"v_mfma_f32_32x32x16_bf16 a[112:127], a[236:239], v[168:171], a[112:127]\n"
"s_waitcnt lgkmcnt(2)\n"
"v_cvt_pk_bf16_f32 v98, v116, v117\n"
"v_exp_f32 v120, v72\n"
"v_add_f32 v208, v208, v118\n"
"v_mfma_f32_32x32x16_bf16 a[0:15], a[240:243], v[164:167], a[0:15]\n"
"ds_read_b128 a[252:255], v215 offset:15360\n"
"v_exp_f32 v121, v73\n"
"v_add_f32 v208, v208, v119\n"
"v_cvt_pk_bf16_f32 v99, v118, v119\n"
"v_mfma_f32_32x32x16_bf16 a[64:79], a[240:243], v[172:175], a[64:79]\n"
"s_waitcnt lgkmcnt(2)\n"
"v_exp_f32 v122, v74\n"
"v_add_f32 v208, v208, v120\n"
"v_mfma_f32_32x32x16_bf16 a[16:31], a[244:247], v[164:167], a[16:31]\n"
"s_add_u32 s59, s10, 0x4000\n"
"ds_read_b128 a[224:227], v214 offset:8192\n"
"v_exp_f32 v123, v75\n"
"v_add_f32 v208, v208, v121\n"
"v_mfma_f32_32x32x16_bf16 a[80:95], a[244:247], v[172:175], a[80:95]\n"
"s_waitcnt lgkmcnt(2)\n"
"v_cvt_pk_bf16_f32 v100, v120, v121\n"
"v_exp_f32 v124, v76\n"
"v_add_f32 v208, v208, v122\n"
"v_mfma_f32_32x32x16_bf16 a[32:47], a[248:251], v[164:167], a[32:47]\n"
"s_sub_u32 s15, s59, 0x14000\n"
"ds_read_b128 a[228:231], v214 offset:9216\n"
"v_exp_f32 v125, v77\n"
"v_add_f32 v208, v208, v123\n"
"v_mfma_f32_32x32x16_bf16 a[96:111], a[248:251], v[172:175], a[96:111]\n"
"s_waitcnt lgkmcnt(2)\n"
"v_cvt_pk_bf16_f32 v101, v122, v123\n"
"v_exp_f32 v126, v78\n"
"v_add_f32 v208, v208, v124\n"
"v_mfma_f32_32x32x16_bf16 a[48:63], a[252:255], v[164:167], a[48:63]\n"
"s_min_u32 s10, s59, s15\n"
"ds_read_b128 a[232:235], v214 offset:10240\n"
"v_exp_f32 v127, v79\n"
"v_add_f32 v208, v208, v125\n"
"v_mfma_f32_32x32x16_bf16 a[112:127], a[252:255], v[172:175], a[112:127]\n"
"s_waitcnt lgkmcnt(2)\n"
"v_add_u32 v215, s10, v217\n"
"v_cvt_pk_bf16_f32 v102, v124, v125\n"
"v_exp_f32 v112, v80\n"
"v_mfma_f32_32x32x16_bf16 v[128:143], a[224:227], v[0:3], v[176:191]\n"
"ds_read_b128 a[236:239], v214 offset:11264\n"
"v_add_f32 v208, v208, v126\n"
"v_exp_f32 v113, v81\n"
"v_add_f32 v208, v208, v127\n"
"v_mfma_f32_32x32x16_bf16 v[144:159], a[224:227], v[32:35], v[192:207]\n"
"s_waitcnt lgkmcnt(2)\n"
"v_cvt_pk_bf16_f32 v103, v126, v127\n"
"v_exp_f32 v114, v82\n"
"v_mfma_f32_32x32x16_bf16 v[128:143], a[228:231], v[4:7], v[128:143]\n"
"ds_read_b128 a[240:243], v214 offset:12288\n"
"v_exp_f32 v115, v83\n"
"v_add_f32 v209, v112, v113\n"
"v_cvt_pk_bf16_f32 v104, v112, v113\n"
"v_mfma_f32_32x32x16_bf16 v[144:159], a[228:231], v[36:39], v[144:159]\n"
"s_waitcnt lgkmcnt(2)\n"
"v_exp_f32 v116, v84\n"
"v_add_f32 v209, v209, v114\n"
"v_mfma_f32_32x32x16_bf16 v[128:143], a[232:235], v[8:11], v[128:143]\n"
"ds_read_b128 a[244:247], v214 offset:13312\n"
"v_exp_f32 v117, v85\n"
"v_add_f32 v209, v209, v115\n"
"v_cvt_pk_bf16_f32 v105, v114, v115\n"
"v_mfma_f32_32x32x16_bf16 v[144:159], a[232:235], v[40:43], v[144:159]\n"
"s_waitcnt lgkmcnt(2)\n"
"v_exp_f32 v118, v86\n"
"v_add_f32 v209, v209, v116\n"
"v_mfma_f32_32x32x16_bf16 v[128:143], a[236:239], v[12:15], v[128:143]\n"
"ds_read_b128 a[248:251], v214 offset:14336\n"
"v_exp_f32 v119, v87\n"
"v_add_f32 v209, v209, v117\n"
"v_cvt_pk_bf16_f32 v106, v116, v117\n"
"v_mfma_f32_32x32x16_bf16 v[144:159], a[236:239], v[44:47], v[144:159]\n"
"s_waitcnt lgkmcnt(2)\n"
"v_exp_f32 v120, v88\n"
"v_add_f32 v209, v209, v118\n"
"v_mfma_f32_32x32x16_bf16 v[128:143], a[240:243], v[16:19], v[128:143]\n"
"ds_read_b128 a[252:255], v214 offset:15360\n"
"v_exp_f32 v121, v89\n"
"v_add_f32 v209, v209, v119\n"
"v_cvt_pk_bf16_f32 v107, v118, v119\n"
"v_mfma_f32_32x32x16_bf16 v[144:159], a[240:243], v[48:51], v[144:159]\n"
"s_waitcnt lgkmcnt(2)\n"
"v_exp_f32 v122, v90\n"
"v_add_f32 v209, v209, v120\n"
"v_exp_f32 v123, v91\n"
"v_mfma_f32_32x32x16_bf16 v[128:143], a[244:247], v[20:23], v[128:143]\n"
"ds_read_b128 a[224:227], v215 offset:0\n"
"v_add_f32 v209, v209, v121\n"
"v_cvt_pk_bf16_f32 v108, v120, v121\n"
"v_exp_f32 v124, v92\n"
"v_mfma_f32_32x32x16_bf16 v[144:159], a[244:247], v[52:55], v[144:159]\n"
"s_waitcnt lgkmcnt(2)\n"
"v_add_f32 v209, v209, v122\n"
"v_exp_f32 v125, v93\n"
"v_add_f32 v209, v209, v123\n"
"v_mfma_f32_32x32x16_bf16 v[128:143], a[248:251], v[24:27], v[128:143]\n"
"ds_read_b128 a[228:231], v215 offset:1024\n"
"v_cvt_pk_bf16_f32 v109, v122, v123\n"
"v_exp_f32 v126, v94\n"
"v_add_f32 v209, v209, v124\n"
"v_mfma_f32_32x32x16_bf16 v[144:159], a[248:251], v[56:59], v[144:159]\n"
"s_waitcnt lgkmcnt(2)\n"
"v_exp_f32 v127, v95\n"
"v_add_f32 v209, v209, v125\n"
"v_cvt_pk_bf16_f32 v110, v124, v125\n"
"v_mfma_f32_32x32x16_bf16 v[128:143], a[252:255], v[28:31], v[128:143]\n"
"ds_read_b128 a[232:235], v215 offset:2048\n"
"v_add_f32 v209, v209, v126\n"
"v_add_f32 v209, v209, v127\n"
"v_max_f32 v219, v208, v209\n"
"v_cmp_nle_f32 vcc, v219, v220\n"
"v_mfma_f32_32x32x16_bf16 v[144:159], a[252:255], v[60:63], v[144:159]\n"
"v_cvt_pk_bf16_f32 v111, v126, v127\n"
"s_cbranch_vccnz L_aft35_%=\n"
"L_afr36_%=:\n"
"v_add_f32 v210, v210, v208\n"
"v_add_f32 v211, v211, v209\n"
"s_waitcnt lgkmcnt(2)\n"
"v_mfma_f32_32x32x16_bf16 a[0:15], a[224:227], v[96:99], a[0:15]\n"
"s_cmp_ge_u32 s12, s60\n"
"s_cbranch_scc1 L_amask37_%=\n"
"L_amret38_%=:\n"
"ds_read_b128 a[236:239], v215 offset:3072\n"
"s_waitcnt vmcnt(0)\n"
"s_barrier\n"
"v_mfma_f32_32x32x16_bf16 a[64:79], a[224:227], v[104:107], a[64:79]\n"
"s_waitcnt lgkmcnt(2)\n"
"s_mov_b32 m0, s52\n"
"v_add_u32 v214, 0x4000, v214\n"
"v_and_b32 v214, 0xffff, v214\n"
"v_exp_f32 v112, v128\n"
"v_mfma_f32_32x32x16_bf16 a[16:31], a[228:231], v[96:99], a[16:31]\n"
"s_add_u32 s12, s12, 1\n"
"ds_read_b128 a[240:243], v215 offset:4096\n"
"buffer_load_dwordx4 v216, s[40:43], s53 offen offset:0 sc1 lds\n"
"v_exp_f32 v113, v129\n"
"v_exp_f32 v114, v130\n"
"v_mfma_f32_32x32x16_bf16 a[80:95], a[228:231], v[104:107], a[80:95]\n"
"s_waitcnt lgkmcnt(2)\n"
"buffer_load_dwordx4 v216, s[40:43], s53 offen offset:1024 sc1 lds\n"
"v_exp_f32 v115, v131\n"
"v_add_f32 v208, v112, v113\n"
"v_cvt_pk_bf16_f32 v160, v112, v113\n"
"v_mfma_f32_32x32x16_bf16 a[32:47], a[232:235], v[96:99], a[32:47]\n"
"s_mov_b64 s[28:29], s[22:23]\n"
"ds_read_b128 a[244:247], v215 offset:5120\n"
"v_exp_f32 v116, v132\n"
"v_add_f32 v208, v208, v114\n"
"v_exp_f32 v117, v133\n"
"v_mfma_f32_32x32x16_bf16 a[96:111], a[232:235], v[104:107], a[96:111]\n"
"s_waitcnt lgkmcnt(2)\n"
"buffer_load_dwordx4 v216, s[40:43], s53 offen offset:2048 sc1 lds\n"
"v_add_f32 v208, v208, v115\n"
"v_cvt_pk_bf16_f32 v161, v114, v115\n"
"v_exp_f32 v118, v134\n"
"v_mfma_f32_32x32x16_bf16 a[48:63], a[236:239], v[96:99], a[48:63]\n"
"s_and_b32 s29, s29, 0xffff\n"
"s_or_b32 s29, s29, 0x80000\n"
"ds_read_b128 a[248:251], v215 offset:6144\n"
"v_add_f32 v208, v208, v116\n"
"v_exp_f32 v119, v135\n"
"v_add_f32 v208, v208, v117\n"
"v_cvt_pk_bf16_f32 v162, v116, v117\n"
"v_mfma_f32_32x32x16_bf16 a[112:127], a[236:239], v[104:107], a[112:127]\n"
"s_waitcnt lgkmcnt(2)\n"
"s_mov_b32 s30, 0x10000\n"
"buffer_load_dwordx4 v216, s[40:43], s53 offen offset:3072 sc1 lds\n"
"v_exp_f32 v120, v136\n"
"v_add_f32 v208, v208, v118\n"
"v_exp_f32 v121, v137\n"
"v_mfma_f32_32x32x16_bf16 a[0:15], a[240:243], v[100:103], a[0:15]\n"
"s_add_u32 m0, s54, 0x10000\n"
"s_mov_b32 s31, 0x20000\n"
"ds_read_b128 a[252:255], v215 offset:7168\n"
"v_add_f32 v208, v208, v119\n"
"v_cvt_pk_bf16_f32 v163, v118, v119\n"
"v_exp_f32 v122, v138\n"
"v_mfma_f32_32x32x16_bf16 a[64:79], a[240:243], v[108:111], a[64:79]\n"
"s_waitcnt lgkmcnt(2)\n"
"buffer_load_dwordx4 v216, s[24:27], s55 offen offset:0 sc1 lds\n"
"v_add_f32 v208, v208, v120\n"
"v_exp_f32 v123, v139\n"
"v_add_f32 v208, v208, v121\n"
"v_mfma_f32_32x32x16_bf16 a[16:31], a[244:247], v[100:103], a[16:31]\n"
"s_add_u32 s56, s54, 0x4000\n"
"ds_read_b128 a[224:227], v214 offset:0\n"
"buffer_load_dword v226, v234, s[28:31], 0 idxen\n"
"v_cvt_pk_bf16_f32 v164, v120, v121\n"
"v_exp_f32 v124, v140\n"
"v_add_f32 v208, v208, v122\n"
"v_mfma_f32_32x32x16_bf16 a[80:95], a[244:247], v[108:111], a[80:95]\n"
"s_waitcnt lgkmcnt(2)\n"
"ds_read_b128 a[228:231], v214 offset:8192\n"
"buffer_load_dwordx4 v216, s[24:27], s55 offen offset:1024 sc1 lds\n"
"v_exp_f32 v125, v141\n"
"v_add_f32 v208, v208, v123\n"
"v_mfma_f32_32x32x16_bf16 a[32:47], a[248:251], v[100:103], a[32:47]\n"
"s_sub_u32 s57, s56, 0x14000\n"
"ds_read_b128 a[232:235], v214 offset:1024\n"
"buffer_load_dwordx4 v216, s[24:27], s55 offen offset:2048 sc1 lds\n"
"buffer_load_dword v226, v234, s[28:31], 0 idxen offset:64\n"
"v_cvt_pk_bf16_f32 v165, v122, v123\n"
"v_exp_f32 v126, v142\n"
"v_mfma_f32_32x32x16_bf16 a[96:111], a[248:251], v[108:111], a[96:111]\n"
"s_waitcnt lgkmcnt(3)\n"
"ds_read_b128 a[236:239], v214 offset:9216\n"
"v_add_f32 v208, v208, v124\n"
"v_exp_f32 v127, v143\n"
"v_add_f32 v208, v208, v125\n"
"v_mfma_f32_32x32x16_bf16 a[48:63], a[252:255], v[100:103], a[48:63]\n"
"s_min_u32 s54, s56, s57\n"
"ds_read_b128 a[240:243], v214 offset:2048\n"
"buffer_load_dwordx4 v216, s[24:27], s55 offen offset:3072 sc1 lds\n"
"v_cvt_pk_bf16_f32 v166, v124, v125\n"
"v_exp_f32 v112, v144\n"
"v_mfma_f32_32x32x16_bf16 a[112:127], a[252:255], v[108:111], a[112:127]\n"
"s_waitcnt lgkmcnt(3)\n"
"s_add_u32 m0, s52, 0x4000\n"
"ds_read_b128 a[244:247], v214 offset:10240\n"
"v_add_f32 v208, v208, v126\n"
"v_exp_f32 v113, v145\n"
"v_mfma_f32_32x32x16_bf16 v[64:79], a[224:227], v[32:35], v[192:207]\n"
"ds_read_b128 a[248:251], v214 offset:3072\n"
"buffer_load_dwordx4 v218, s[40:43], s53 offen offset:0 sc1 lds\n"
"v_add_f32 v208, v208, v127\n"
"v_cvt_pk_bf16_f32 v167, v126, v127\n"
"v_exp_f32 v114, v146\n"
"v_mfma_f32_32x32x16_bf16 v[80:95], a[228:231], v[32:35], v[192:207]\n"
"s_waitcnt lgkmcnt(3)\n"
"ds_read_b128 a[252:255], v214 offset:11264\n"
"v_exp_f32 v115, v147\n"
"v_add_f32 v209, v112, v113\n"
"v_cvt_pk_bf16_f32 v168, v112, v113\n"
"v_mfma_f32_32x32x16_bf16 v[64:79], a[232:235], v[36:39], v[64:79]\n"
"s_xor_b32 s52, s52, 0x8000\n"
"ds_read_b128 a[224:227], v214 offset:4096\n"
"buffer_load_dwordx4 v218, s[40:43], s53 offen offset:1024 sc1 lds\n"
"v_exp_f32 v116, v148\n"
"v_add_f32 v209, v209, v114\n"
"v_mfma_f32_32x32x16_bf16 v[80:95], a[236:239], v[36:39], v[80:95]\n"
"s_waitcnt lgkmcnt(3)\n"
"ds_read_b128 a[228:231], v214 offset:12288\n"
"v_exp_f32 v117, v149\n"
"v_add_f32 v209, v209, v115\n"
"v_cvt_pk_bf16_f32 v169, v114, v115\n"
"v_mfma_f32_32x32x16_bf16 v[64:79], a[240:243], v[40:43], v[64:79]\n"
"ds_read_b128 a[232:235], v214 offset:5120\n"
"buffer_load_dwordx4 v218, s[40:43], s53 offen offset:2048 sc1 lds\n"
"v_exp_f32 v118, v150\n"
"v_add_f32 v209, v209, v116\n"
"v_exp_f32 v119, v151\n"
"v_mfma_f32_32x32x16_bf16 v[80:95], a[244:247], v[40:43], v[80:95]\n"
"s_waitcnt lgkmcnt(3)\n"
"ds_read_b128 a[236:239], v214 offset:13312\n"
"buffer_load_dwordx4 v218, s[40:43], s53 offen offset:3072 sc1 lds\n"
"v_add_f32 v209, v209, v117\n"
"v_cvt_pk_bf16_f32 v170, v116, v117\n"
"v_mfma_f32_32x32x16_bf16 v[64:79], a[248:251], v[44:47], v[64:79]\n"
"s_add_u32 s53, s53, 32768\n"
"s_add_u32 m0, s54, 0x10000\n"
"ds_read_b128 a[240:243], v214 offset:6144\n"
"v_exp_f32 v120, v152\n"
"v_add_f32 v209, v209, v118\n"
"v_mfma_f32_32x32x16_bf16 v[80:95], a[252:255], v[44:47], v[80:95]\n"
"s_waitcnt lgkmcnt(3)\n"
"ds_read_b128 a[244:247], v214 offset:14336\n"
"buffer_load_dwordx4 v218, s[24:27], s55 offen offset:0 sc1 lds\n"
"v_exp_f32 v121, v153\n"
"v_add_f32 v209, v209, v119\n"
"v_mfma_f32_32x32x16_bf16 v[64:79], a[224:227], v[48:51], v[64:79]\n"
"s_add_u32 s56, s54, 0x4000\n"
"ds_read_b128 a[248:251], v214 offset:7168\n"
"v_cvt_pk_bf16_f32 v171, v118, v119\n"
"v_exp_f32 v122, v154\n"
"v_add_f32 v209, v209, v120\n"
"v_mfma_f32_32x32x16_bf16 v[80:95], a[228:231], v[48:51], v[80:95]\n"
"s_waitcnt lgkmcnt(3)\n"
"ds_read_b128 a[252:255], v214 offset:15360\n"
"buffer_load_dwordx4 v218, s[24:27], s55 offen offset:1024 sc1 lds\n"
"v_exp_f32 v123, v155\n"
"v_add_f32 v209, v209, v121\n"
"v_mfma_f32_32x32x16_bf16 v[64:79], a[232:235], v[52:55], v[64:79]\n"
"s_sub_u32 s57, s56, 0x14000\n"
"ds_read_b128 a[224:227], v215 offset:8192\n"
"v_cvt_pk_bf16_f32 v172, v120, v121\n"
"v_exp_f32 v124, v156\n"
"v_add_f32 v209, v209, v122\n"
"v_mfma_f32_32x32x16_bf16 v[80:95], a[236:239], v[52:55], v[80:95]\n"
"s_waitcnt lgkmcnt(3)\n"
"buffer_load_dwordx4 v218, s[24:27], s55 offen offset:2048 sc1 lds\n"
"v_exp_f32 v125, v157\n"
"v_add_f32 v209, v209, v123\n"
"v_cvt_pk_bf16_f32 v173, v122, v123\n"
"v_mfma_f32_32x32x16_bf16 v[64:79], a[240:243], v[56:59], v[64:79]\n"
"s_min_u32 s54, s56, s57\n"
"ds_read_b128 a[228:231], v215 offset:9216\n"
"v_exp_f32 v126, v158\n"
"v_add_f32 v209, v209, v124\n"
"v_exp_f32 v127, v159\n"
"v_mfma_f32_32x32x16_bf16 v[80:95], a[244:247], v[56:59], v[80:95]\n"
"s_waitcnt lgkmcnt(2)\n"
"buffer_load_dwordx4 v218, s[24:27], s55 offen offset:3072 sc1 lds\n"
"v_add_f32 v209, v209, v125\n"
"v_cvt_pk_bf16_f32 v174, v124, v125\n"
"v_add_f32 v209, v209, v126\n"
"v_add_f32 v209, v209, v127\n"
"v_mfma_f32_32x32x16_bf16 v[64:79], a[248:251], v[60:63], v[64:79]\n"
"s_add_u32 s55, s55, 32768\n"
"ds_read_b128 a[232:235], v215 offset:10240\n"
"v_max_f32 v219, v208, v209\n"
"v_cmp_nle_f32 vcc, v219, v220\n"
"v_cvt_pk_bf16_f32 v175, v126, v127\n"
"v_mfma_f32_32x32x16_bf16 v[80:95], a[252:255], v[60:63], v[80:95]\n"
"s_waitcnt lgkmcnt(2)\n"
"s_cbranch_vccnz L_aft47_%=\n"
"L_afr48_%=:\n"
"v_add_f32 v210, v210, v208\n"
"v_add_f32 v211, v211, v209\n"
"v_mov_b32 v212, v210\n"
"v_mov_b32 v210, 0\n"
"v_mfma_f32_32x32x16_bf16 a[0:15], a[224:227], v[160:163], a[0:15]\n"
"s_cmp_ge_u32 s12, s61\n"
"s_cbranch_scc1 L_amask41_%=\n"
"L_amret42_%=:\n"
"ds_read_b128 a[236:239], v215 offset:11264\n"
"v_exp_f32 v112, v64\n"
"v_mfma_f32_32x32x16_bf16 a[64:79], a[224:227], v[168:171], a[64:79]\n"
"s_waitcnt lgkmcnt(2)\n"
"v_exp_f32 v113, v65\n"
"v_exp_f32 v114, v66\n"
"v_mfma_f32_32x32x16_bf16 a[16:31], a[228:231], v[160:163], a[16:31]\n"
"ds_read_b128 a[240:243], v215 offset:12288\n"
"v_exp_f32 v115, v67\n"
"v_add_f32 v208, v112, v113\n"
"v_cvt_pk_bf16_f32 v96, v112, v113\n"
"v_mfma_f32_32x32x16_bf16 a[80:95], a[228:231], v[168:171], a[80:95]\n"
"s_waitcnt lgkmcnt(2)\n"
"v_exp_f32 v116, v68\n"
"v_add_f32 v208, v208, v114\n"
"v_exp_f32 v117, v69\n"
"v_mfma_f32_32x32x16_bf16 a[32:47], a[232:235], v[160:163], a[32:47]\n"
"s_add_u32 s12, s12, 4\n"
"ds_read_b128 a[244:247], v215 offset:13312\n"
"v_add_f32 v208, v208, v115\n"
"v_cvt_pk_bf16_f32 v97, v114, v115\n"
"v_exp_f32 v118, v70\n"
"v_mfma_f32_32x32x16_bf16 a[96:111], a[232:235], v[168:171], a[96:111]\n"
"s_waitcnt lgkmcnt(2)\n"
"v_add_f32 v208, v208, v116\n"
"v_exp_f32 v119, v71\n"
"v_add_f32 v208, v208, v117\n"
"v_mfma_f32_32x32x16_bf16 a[48:63], a[236:239], v[160:163], a[48:63]\n"
"ds_read_b128 a[248:251], v215 offset:14336\n"
"v_cvt_pk_bf16_f32 v98, v116, v117\n"
"v_exp_f32 v120, v72\n"
"v_add_f32 v208, v208, v118\n"
"v_mfma_f32_32x32x16_bf16 a[112:127], a[236:239], v[168:171], a[112:127]\n"
"s_waitcnt lgkmcnt(2)\n"
"v_exp_f32 v121, v73\n"
"v_add_f32 v208, v208, v119\n"
"v_cvt_pk_bf16_f32 v99, v118, v119\n"
"v_mfma_f32_32x32x16_bf16 a[0:15], a[240:243], v[164:167], a[0:15]\n"
"ds_read_b128 a[252:255], v215 offset:15360\n"
"v_exp_f32 v122, v74\n"
"v_add_f32 v208, v208, v120\n"
"v_exp_f32 v123, v75\n"
"v_mfma_f32_32x32x16_bf16 a[64:79], a[240:243], v[172:175], a[64:79]\n"
"s_waitcnt lgkmcnt(2)\n"
"v_add_f32 v208, v208, v121\n"
"v_cvt_pk_bf16_f32 v100, v120, v121\n"
"v_exp_f32 v124, v76\n"
"v_mfma_f32_32x32x16_bf16 a[16:31], a[244:247], v[164:167], a[16:31]\n"
"s_add_u32 s59, s10, 0x4000\n"
"ds_read_b128 a[224:227], v214 offset:16384\n"
"v_add_f32 v208, v208, v122\n"
"v_exp_f32 v125, v77\n"
"v_mfma_f32_32x32x16_bf16 a[80:95], a[244:247], v[172:175], a[80:95]\n"
"s_waitcnt lgkmcnt(2)\n"
"ds_read_b128 a[228:231], v214 offset:24576\n"
"v_add_f32 v208, v208, v123\n"
"v_cvt_pk_bf16_f32 v101, v122, v123\n"
"v_exp_f32 v126, v78\n"
"v_mfma_f32_32x32x16_bf16 a[32:47], a[248:251], v[164:167], a[32:47]\n"
"s_sub_u32 s15, s59, 0x14000\n"
"ds_read_b128 a[232:235], v214 offset:17408\n"
"v_add_f32 v208, v208, v124\n"
"v_exp_f32 v127, v79\n"
"v_mfma_f32_32x32x16_bf16 a[96:111], a[248:251], v[172:175], a[96:111]\n"
"s_waitcnt lgkmcnt(3)\n"
"ds_read_b128 a[236:239], v214 offset:25600\n"
"v_add_f32 v208, v208, v125\n"
"v_cvt_pk_bf16_f32 v102, v124, v125\n"
"v_exp_f32 v112, v80\n"
"v_mfma_f32_32x32x16_bf16 a[48:63], a[252:255], v[164:167], a[48:63]\n"
"s_min_u32 s10, s59, s15\n"
"ds_read_b128 a[240:243], v214 offset:18432\n"
"v_add_f32 v208, v208, v126\n"
"v_exp_f32 v113, v81\n"
"v_mfma_f32_32x32x16_bf16 a[112:127], a[252:255], v[172:175], a[112:127]\n"
"s_waitcnt lgkmcnt(3)\n"
"ds_read_b128 a[244:247], v214 offset:26624\n"
"v_add_u32 v215, s10, v217\n"
"v_add_f32 v208, v208, v127\n"
"v_cvt_pk_bf16_f32 v103, v126, v127\n"
"v_mfma_f32_32x32x16_bf16 v[128:143], a[224:227], v[32:35], v[192:207]\n"
"ds_read_b128 a[248:251], v214 offset:19456\n"
"v_exp_f32 v114, v82\n"
"v_exp_f32 v115, v83\n"
"v_mfma_f32_32x32x16_bf16 v[144:159], a[228:231], v[32:35], v[192:207]\n"
"s_waitcnt lgkmcnt(3)\n"
"ds_read_b128 a[252:255], v214 offset:27648\n"
"v_add_f32 v209, v112, v113\n"
"v_cvt_pk_bf16_f32 v104, v112, v113\n"
"v_mfma_f32_32x32x16_bf16 v[128:143], a[232:235], v[36:39], v[128:143]\n"
"ds_read_b128 a[224:227], v214 offset:20480\n"
"v_exp_f32 v116, v84\n"
"v_add_f32 v209, v209, v114\n"
"v_exp_f32 v117, v85\n"
"v_mfma_f32_32x32x16_bf16 v[144:159], a[236:239], v[36:39], v[144:159]\n"
"s_waitcnt lgkmcnt(3)\n"
"ds_read_b128 a[228:231], v214 offset:28672\n"
"v_add_f32 v209, v209, v115\n"
"v_cvt_pk_bf16_f32 v105, v114, v115\n"
"v_mfma_f32_32x32x16_bf16 v[128:143], a[240:243], v[40:43], v[128:143]\n"
"ds_read_b128 a[232:235], v214 offset:21504\n"
"v_exp_f32 v118, v86\n"
"v_add_f32 v209, v209, v116\n"
"v_exp_f32 v119, v87\n"
"v_mfma_f32_32x32x16_bf16 v[144:159], a[244:247], v[40:43], v[144:159]\n"
"s_waitcnt lgkmcnt(3)\n"
"ds_read_b128 a[236:239], v214 offset:29696\n"
"v_add_f32 v209, v209, v117\n"
"v_cvt_pk_bf16_f32 v106, v116, v117\n"
"v_mfma_f32_32x32x16_bf16 v[128:143], a[248:251], v[44:47], v[128:143]\n"
"ds_read_b128 a[240:243], v214 offset:22528\n"
"v_exp_f32 v120, v88\n"
"v_add_f32 v209, v209, v118\n"
"v_exp_f32 v121, v89\n"
"v_mfma_f32_32x32x16_bf16 v[144:159], a[252:255], v[44:47], v[144:159]\n"
"s_waitcnt lgkmcnt(3)\n"
"ds_read_b128 a[244:247], v214 offset:30720\n"
"v_add_f32 v209, v209, v119\n"
"v_cvt_pk_bf16_f32 v107, v118, v119\n"
"v_mfma_f32_32x32x16_bf16 v[128:143], a[224:227], v[48:51], v[128:143]\n"
"ds_read_b128 a[248:251], v214 offset:23552\n"
"v_exp_f32 v122, v90\n"
"v_add_f32 v209, v209, v120\n"
"v_exp_f32 v123, v91\n"
"v_mfma_f32_32x32x16_bf16 v[144:159], a[228:231], v[48:51], v[144:159]\n"
"s_waitcnt lgkmcnt(3)\n"
"ds_read_b128 a[252:255], v214 offset:31744\n"
"v_add_f32 v209, v209, v121\n"
"v_cvt_pk_bf16_f32 v108, v120, v121\n"
"v_mfma_f32_32x32x16_bf16 v[128:143], a[232:235], v[52:55], v[128:143]\n"
"ds_read_b128 a[224:227], v215 offset:0\n"
"v_exp_f32 v124, v92\n"
"v_add_f32 v209, v209, v122\n"
"v_exp_f32 v125, v93\n"
"v_mfma_f32_32x32x16_bf16 v[144:159], a[236:239], v[52:55], v[144:159]\n"
"s_waitcnt lgkmcnt(3)\n"
"ds_read_b128 a[228:231], v215 offset:1024\n"
"v_add_f32 v209, v209, v123\n"
"v_cvt_pk_bf16_f32 v109, v122, v123\n"
"v_mfma_f32_32x32x16_bf16 v[128:143], a[240:243], v[56:59], v[128:143]\n"
"ds_read_b128 a[232:235], v215 offset:2048\n"
"v_exp_f32 v126, v94\n"
"v_add_f32 v209, v209, v124\n"
"v_exp_f32 v127, v95\n"
"v_mfma_f32_32x32x16_bf16 v[144:159], a[244:247], v[56:59], v[144:159]\n"
"s_waitcnt lgkmcnt(3)\n"
"ds_read_b128 a[236:239], v215 offset:3072\n"
"v_add_f32 v209, v209, v125\n"
"v_cvt_pk_bf16_f32 v110, v124, v125\n"
"v_add_f32 v209, v209, v126\n"
"v_mfma_f32_32x32x16_bf16 v[128:143], a[248:251], v[60:63], v[128:143]\n"
"ds_read_b128 a[240:243], v215 offset:4096\n"
"v_add_f32 v209, v209, v127\n"
"v_max_f32 v219, v208, v209\n"
"v_cmp_nle_f32 vcc, v219, v220\n"
"v_cvt_pk_bf16_f32 v111, v126, v127\n"
"v_mfma_f32_32x32x16_bf16 v[144:159], a[252:255], v[60:63], v[144:159]\n"
"ds_read_b128 a[244:247], v215 offset:5120\n"
"s_cbranch_vccnz L_aft49_%=\n"
"L_afr50_%=:\n"
"v_add_f32 v210, v210, v208\n"
"v_add_f32 v211, v211, v209\n"
"s_lshl_b32 s11, s51, 1\n"
"s_sub_u32 s11, 13, s11\n"
"s_cbranch_scc1 L_od_%=\n"
"L_oo_%=:\n"
"s_waitcnt lgkmcnt(4)\n"
"v_mfma_f32_32x32x16_bf16 a[64:79], a[224:227], v[96:99], a[64:79]\n"
"ds_read_b128 a[248:251], v215 offset:6144\n"
"v_exp_f32 v112, v128\n"
"v_exp_f32 v113, v129\n"
"s_waitcnt vmcnt(0)\n"
"s_barrier\n"
"v_mfma_f32_32x32x16_bf16 a[80:95], a[228:231], v[96:99], a[80:95]\n"
"s_waitcnt lgkmcnt(3)\n"
"s_mov_b32 m0, s52\n"
"ds_read_b128 a[252:255], v215 offset:7168\n"
"v_xor_b32 v214, 0x8000, v214\n"
"v_exp_f32 v114, v130\n"
"v_mfma_f32_32x32x16_bf16 a[96:111], a[232:235], v[96:99], a[96:111]\n"
"ds_read_b128 a[224:227], v215 offset:8192\n"
"buffer_load_dwordx4 v216, s[40:43], s53 offen offset:0 sc1 lds\n"
"v_exp_f32 v115, v131\n"
"v_add_f32 v208, v112, v113\n"
"v_cvt_pk_bf16_f32 v160, v112, v113\n"
"v_mfma_f32_32x32x16_bf16 a[112:127], a[236:239], v[96:99], a[112:127]\n"
"s_waitcnt lgkmcnt(3)\n"
"ds_read_b128 a[228:231], v215 offset:9216\n"
"buffer_load_dwordx4 v216, s[40:43], s53 offen offset:1024 sc1 lds\n"
"v_exp_f32 v116, v132\n"
"v_add_f32 v208, v208, v114\n"
"v_mfma_f32_32x32x16_bf16 a[64:79], a[240:243], v[100:103], a[64:79]\n"
"ds_read_b128 a[232:235], v215 offset:10240\n"
"v_exp_f32 v117, v133\n"
"v_add_f32 v208, v208, v115\n"
"v_cvt_pk_bf16_f32 v161, v114, v115\n"
"v_exp_f32 v118, v134\n"
"v_mfma_f32_32x32x16_bf16 a[80:95], a[244:247], v[100:103], a[80:95]\n"
"s_waitcnt lgkmcnt(3)\n"
"ds_read_b128 a[236:239], v215 offset:11264\n"
"buffer_load_dwordx4 v216, s[40:43], s53 offen offset:2048 sc1 lds\n"
"v_add_f32 v208, v208, v116\n"
"v_exp_f32 v119, v135\n"
"v_mfma_f32_32x32x16_bf16 a[96:111], a[248:251], v[100:103], a[96:111]\n"
"ds_read_b128 a[240:243], v215 offset:12288\n"
"v_add_f32 v208, v208, v117\n"
"v_cvt_pk_bf16_f32 v162, v116, v117\n"
"v_exp_f32 v120, v136\n"
"v_add_f32 v208, v208, v118\n"
"v_mfma_f32_32x32x16_bf16 a[112:127], a[252:255], v[100:103], a[112:127]\n"
"s_waitcnt lgkmcnt(3)\n"
"ds_read_b128 a[244:247], v215 offset:13312\n"
"buffer_load_dwordx4 v216, s[40:43], s53 offen offset:3072 sc1 lds\n"
"v_exp_f32 v121, v137\n"
"v_add_f32 v208, v208, v119\n"
"v_mfma_f32_32x32x16_bf16 a[64:79], a[224:227], v[104:107], a[64:79]\n"
"s_add_u32 m0, s54, 0x10000\n"
"ds_read_b128 a[248:251], v215 offset:14336\n"
"v_cvt_pk_bf16_f32 v163, v118, v119\n"
"v_exp_f32 v122, v138\n"
"v_add_f32 v208, v208, v120\n"
"v_mfma_f32_32x32x16_bf16 a[80:95], a[228:231], v[104:107], a[80:95]\n"
"s_waitcnt lgkmcnt(3)\n"
"ds_read_b128 a[252:255], v215 offset:15360\n"
"buffer_load_dwordx4 v216, s[24:27], s55 offen offset:0 sc1 lds\n"
"v_exp_f32 v123, v139\n"
"v_add_f32 v208, v208, v121\n"
"v_mfma_f32_32x32x16_bf16 a[96:111], a[232:235], v[104:107], a[96:111]\n"
"s_add_u32 s56, s54, 0x4000\n"
"ds_read_b128 a[224:227], v214 offset:0\n"
"v_cvt_pk_bf16_f32 v164, v120, v121\n"
"v_exp_f32 v124, v140\n"
"v_add_f32 v208, v208, v122\n"
"v_mfma_f32_32x32x16_bf16 a[112:127], a[236:239], v[104:107], a[112:127]\n"
"s_waitcnt lgkmcnt(3)\n"
"ds_read_b128 a[228:231], v214 offset:8192\n"
"buffer_load_dwordx4 v216, s[24:27], s55 offen offset:1024 sc1 lds\n"
"v_exp_f32 v125, v141\n"
"v_add_f32 v208, v208, v123\n"
"v_mfma_f32_32x32x16_bf16 a[64:79], a[240:243], v[108:111], a[64:79]\n"
"s_sub_u32 s57, s56, 0x14000\n"
"ds_read_b128 a[232:235], v214 offset:1024\n"
"buffer_load_dwordx4 v216, s[24:27], s55 offen offset:2048 sc1 lds\n"
"v_cvt_pk_bf16_f32 v165, v122, v123\n"
"v_exp_f32 v126, v142\n"
"v_mfma_f32_32x32x16_bf16 a[80:95], a[244:247], v[108:111], a[80:95]\n"
"s_waitcnt lgkmcnt(3)\n"
"ds_read_b128 a[236:239], v214 offset:9216\n"
"v_add_f32 v208, v208, v124\n"
"v_exp_f32 v127, v143\n"
"v_add_f32 v208, v208, v125\n"
"v_mfma_f32_32x32x16_bf16 a[96:111], a[248:251], v[108:111], a[96:111]\n"
"s_min_u32 s54, s56, s57\n"
"ds_read_b128 a[240:243], v214 offset:2048\n"
"buffer_load_dwordx4 v216, s[24:27], s55 offen offset:3072 sc1 lds\n"
"v_cvt_pk_bf16_f32 v166, v124, v125\n"
"v_exp_f32 v112, v144\n"
"v_mfma_f32_32x32x16_bf16 a[112:127], a[252:255], v[108:111], a[112:127]\n"
"s_waitcnt lgkmcnt(3)\n"
"s_add_u32 m0, s52, 0x4000\n"
"ds_read_b128 a[244:247], v214 offset:10240\n"
"v_add_f32 v208, v208, v126\n"
"v_exp_f32 v113, v145\n"
"v_mfma_f32_32x32x16_bf16 v[64:79], a[224:227], v[32:35], v[192:207]\n"
"s_add_u32 s59, s10, 0x4000\n"
"ds_read_b128 a[248:251], v214 offset:3072\n"
"buffer_load_dwordx4 v218, s[40:43], s53 offen offset:0 sc1 lds\n"
"v_add_f32 v208, v208, v127\n"
"v_cvt_pk_bf16_f32 v167, v126, v127\n"
"v_exp_f32 v114, v146\n"
"v_mfma_f32_32x32x16_bf16 v[80:95], a[228:231], v[32:35], v[192:207]\n"
"s_waitcnt lgkmcnt(3)\n"
"ds_read_b128 a[252:255], v214 offset:11264\n"
"v_exp_f32 v115, v147\n"
"v_add_f32 v209, v112, v113\n"
"v_cvt_pk_bf16_f32 v168, v112, v113\n"
"v_mfma_f32_32x32x16_bf16 v[64:79], a[232:235], v[36:39], v[64:79]\n"
"s_xor_b32 s52, s52, 0x8000\n"
"ds_read_b128 a[224:227], v214 offset:4096\n"
"buffer_load_dwordx4 v218, s[40:43], s53 offen offset:1024 sc1 lds\n"
"v_exp_f32 v116, v148\n"
"v_add_f32 v209, v209, v114\n"
"v_mfma_f32_32x32x16_bf16 v[80:95], a[236:239], v[36:39], v[80:95]\n"
"s_waitcnt lgkmcnt(3)\n"
"ds_read_b128 a[228:231], v214 offset:12288\n"
"v_exp_f32 v117, v149\n"
"v_add_f32 v209, v209, v115\n"
"v_cvt_pk_bf16_f32 v169, v114, v115\n"
"v_mfma_f32_32x32x16_bf16 v[64:79], a[240:243], v[40:43], v[64:79]\n"
"s_sub_u32 s15, s59, 0x14000\n"
"s_min_u32 s10, s59, s15\n"
"ds_read_b128 a[232:235], v214 offset:5120\n"
"buffer_load_dwordx4 v218, s[40:43], s53 offen offset:2048 sc1 lds\n"
"v_exp_f32 v118, v150\n"
"v_mfma_f32_32x32x16_bf16 v[80:95], a[244:247], v[40:43], v[80:95]\n"
"s_waitcnt lgkmcnt(3)\n"
"ds_read_b128 a[236:239], v214 offset:13312\n"
"buffer_load_dwordx4 v218, s[40:43], s53 offen offset:3072 sc1 lds\n"
"v_add_u32 v215, s10, v217\n"
"v_add_f32 v209, v209, v116\n"
"v_exp_f32 v119, v151\n"
"v_mfma_f32_32x32x16_bf16 v[64:79], a[248:251], v[44:47], v[64:79]\n"
"s_add_u32 s53, s53, 32768\n"
"s_add_u32 m0, s54, 0x10000\n"
"ds_read_b128 a[240:243], v214 offset:6144\n"
"v_add_f32 v209, v209, v117\n"
"v_cvt_pk_bf16_f32 v170, v116, v117\n"
"v_exp_f32 v120, v152\n"
"v_mfma_f32_32x32x16_bf16 v[80:95], a[252:255], v[44:47], v[80:95]\n"
"s_waitcnt lgkmcnt(3)\n"
"ds_read_b128 a[244:247], v214 offset:14336\n"
"buffer_load_dwordx4 v218, s[24:27], s55 offen offset:0 sc1 lds\n"
"v_add_f32 v209, v209, v118\n"
"v_exp_f32 v121, v153\n"
"v_mfma_f32_32x32x16_bf16 v[64:79], a[224:227], v[48:51], v[64:79]\n"
"s_add_u32 s56, s54, 0x4000\n"
"ds_read_b128 a[248:251], v214 offset:7168\n"
"v_add_f32 v209, v209, v119\n"
"v_cvt_pk_bf16_f32 v171, v118, v119\n"
"v_exp_f32 v122, v154\n"
"v_mfma_f32_32x32x16_bf16 v[80:95], a[228:231], v[48:51], v[80:95]\n"
"s_waitcnt lgkmcnt(3)\n"
"ds_read_b128 a[252:255], v214 offset:15360\n"
"buffer_load_dwordx4 v218, s[24:27], s55 offen offset:1024 sc1 lds\n"
"v_add_f32 v209, v209, v120\n"
"v_exp_f32 v123, v155\n"
"v_mfma_f32_32x32x16_bf16 v[64:79], a[232:235], v[52:55], v[64:79]\n"
"s_sub_u32 s57, s56, 0x14000\n"
"ds_read_b128 a[224:227], v215 offset:0\n"
"v_add_f32 v209, v209, v121\n"
"v_cvt_pk_bf16_f32 v172, v120, v121\n"
"v_exp_f32 v124, v156\n"
"v_mfma_f32_32x32x16_bf16 v[80:95], a[236:239], v[52:55], v[80:95]\n"
"s_waitcnt lgkmcnt(3)\n"
"ds_read_b128 a[228:231], v215 offset:1024\n"
"buffer_load_dwordx4 v218, s[24:27], s55 offen offset:2048 sc1 lds\n"
"v_add_f32 v209, v209, v122\n"
"v_exp_f32 v125, v157\n"
"v_mfma_f32_32x32x16_bf16 v[64:79], a[240:243], v[56:59], v[64:79]\n"
"s_min_u32 s54, s56, s57\n"
"ds_read_b128 a[232:235], v215 offset:2048\n"
"v_add_f32 v209, v209, v123\n"
"v_cvt_pk_bf16_f32 v173, v122, v123\n"
"v_exp_f32 v126, v158\n"
"v_add_f32 v209, v209, v124\n"
"v_mfma_f32_32x32x16_bf16 v[80:95], a[244:247], v[56:59], v[80:95]\n"
"s_waitcnt lgkmcnt(3)\n"
"ds_read_b128 a[236:239], v215 offset:3072\n"
"buffer_load_dwordx4 v218, s[24:27], s55 offen offset:3072 sc1 lds\n"
"v_exp_f32 v127, v159\n"
"v_add_f32 v209, v209, v125\n"
"v_mfma_f32_32x32x16_bf16 v[64:79], a[248:251], v[60:63], v[64:79]\n"
"s_add_u32 s55, s55, 32768\n"
"ds_read_b128 a[240:243], v215 offset:4096\n"
"v_cvt_pk_bf16_f32 v174, v124, v125\n"
"v_add_f32 v209, v209, v126\n"
"v_add_f32 v209, v209, v127\n"
"v_max_f32 v219, v208, v209\n"
"v_cmp_nle_f32 vcc, v219, v220\n"
"v_mfma_f32_32x32x16_bf16 v[80:95], a[252:255], v[60:63], v[80:95]\n"
"s_waitcnt lgkmcnt(3)\n"
"ds_read_b128 a[244:247], v215 offset:5120\n"
"v_cvt_pk_bf16_f32 v175, v126, v127\n"
"s_cbranch_vccnz L_aft57_%=\n"
"L_afr58_%=:\n"
"v_add_f32 v210, v210, v208\n"
"v_add_f32 v211, v211, v209\n"
"v_mfma_f32_32x32x16_bf16 a[64:79], a[224:227], v[160:163], a[64:79]\n"
"s_cmp_ge_u32 s12, s61\n"
"s_cbranch_scc1 L_amask51_%=\n"
"L_amret52_%=:\n"
"ds_read_b128 a[248:251], v215 offset:6144\n"
"v_exp_f32 v112, v64\n"
"v_mfma_f32_32x32x16_bf16 a[80:95], a[228:231], v[160:163], a[80:95]\n"
"s_waitcnt lgkmcnt(3)\n"
"ds_read_b128 a[252:255], v215 offset:7168\n"
"v_exp_f32 v113, v65\n"
"v_exp_f32 v114, v66\n"
"v_mfma_f32_32x32x16_bf16 a[96:111], a[232:235], v[160:163], a[96:111]\n"
"ds_read_b128 a[224:227], v215 offset:8192\n"
"v_exp_f32 v115, v67\n"
"v_add_f32 v208, v112, v113\n"
"v_cvt_pk_bf16_f32 v96, v112, v113\n"
"v_mfma_f32_32x32x16_bf16 a[112:127], a[236:239], v[160:163], a[112:127]\n"
"s_waitcnt lgkmcnt(3)\n"
"ds_read_b128 a[228:231], v215 offset:9216\n"
"v_exp_f32 v116, v68\n"
"v_add_f32 v208, v208, v114\n"
"v_mfma_f32_32x32x16_bf16 a[64:79], a[240:243], v[164:167], a[64:79]\n"
"s_add_u32 s12, s12, 4\n"
"ds_read_b128 a[232:235], v215 offset:10240\n"
"v_exp_f32 v117, v69\n"
"v_add_f32 v208, v208, v115\n"
"v_mfma_f32_32x32x16_bf16 a[80:95], a[244:247], v[164:167], a[80:95]\n"
"s_waitcnt lgkmcnt(3)\n"
"ds_read_b128 a[236:239], v215 offset:11264\n"
"v_cvt_pk_bf16_f32 v97, v114, v115\n"
"v_exp_f32 v118, v70\n"
"v_mfma_f32_32x32x16_bf16 a[96:111], a[248:251], v[164:167], a[96:111]\n"
"ds_read_b128 a[240:243], v215 offset:12288\n"
"v_add_f32 v208, v208, v116\n"
"v_exp_f32 v119, v71\n"
"v_add_f32 v208, v208, v117\n"
"v_mfma_f32_32x32x16_bf16 a[112:127], a[252:255], v[164:167], a[112:127]\n"
"s_waitcnt lgkmcnt(3)\n"
"ds_read_b128 a[244:247], v215 offset:13312\n"
"v_cvt_pk_bf16_f32 v98, v116, v117\n"
"v_exp_f32 v120, v72\n"
"v_mfma_f32_32x32x16_bf16 a[64:79], a[224:227], v[168:171], a[64:79]\n"
"ds_read_b128 a[248:251], v215 offset:14336\n"
"v_add_f32 v208, v208, v118\n"
"v_exp_f32 v121, v73\n"
"v_add_f32 v208, v208, v119\n"
"v_mfma_f32_32x32x16_bf16 a[80:95], a[228:231], v[168:171], a[80:95]\n"
"s_waitcnt lgkmcnt(3)\n"
"ds_read_b128 a[252:255], v215 offset:15360\n"
"v_cvt_pk_bf16_f32 v99, v118, v119\n"
"v_exp_f32 v122, v74\n"
"v_mfma_f32_32x32x16_bf16 a[96:111], a[232:235], v[168:171], a[96:111]\n"
"s_add_u32 s59, s10, 0x4000\n"
"ds_read_b128 a[224:227], v214 offset:16384\n"
"v_add_f32 v208, v208, v120\n"
"v_exp_f32 v123, v75\n"
"v_mfma_f32_32x32x16_bf16 a[112:127], a[236:239], v[168:171], a[112:127]\n"
"s_waitcnt lgkmcnt(3)\n"
"ds_read_b128 a[228:231], v214 offset:24576\n"
"v_add_f32 v208, v208, v121\n"
"v_cvt_pk_bf16_f32 v100, v120, v121\n"
"v_exp_f32 v124, v76\n"
"v_mfma_f32_32x32x16_bf16 a[64:79], a[240:243], v[172:175], a[64:79]\n"
"s_sub_u32 s15, s59, 0x14000\n"
"ds_read_b128 a[232:235], v214 offset:17408\n"
"v_add_f32 v208, v208, v122\n"
"v_exp_f32 v125, v77\n"
"v_mfma_f32_32x32x16_bf16 a[80:95], a[244:247], v[172:175], a[80:95]\n"
"s_waitcnt lgkmcnt(3)\n"
"ds_read_b128 a[236:239], v214 offset:25600\n"
"v_add_f32 v208, v208, v123\n"
"v_cvt_pk_bf16_f32 v101, v122, v123\n"
"v_exp_f32 v126, v78\n"
"v_mfma_f32_32x32x16_bf16 a[96:111], a[248:251], v[172:175], a[96:111]\n"
"s_min_u32 s10, s59, s15\n"
"ds_read_b128 a[240:243], v214 offset:18432\n"
"v_add_f32 v208, v208, v124\n"
"v_exp_f32 v127, v79\n"
"v_mfma_f32_32x32x16_bf16 a[112:127], a[252:255], v[172:175], a[112:127]\n"
"s_waitcnt lgkmcnt(3)\n"
"ds_read_b128 a[244:247], v214 offset:26624\n"
"v_add_u32 v215, s10, v217\n"
"v_add_f32 v208, v208, v125\n"
"v_cvt_pk_bf16_f32 v102, v124, v125\n"
"v_mfma_f32_32x32x16_bf16 v[128:143], a[224:227], v[32:35], v[192:207]\n"
"ds_read_b128 a[248:251], v214 offset:19456\n"
"v_exp_f32 v112, v80\n"
"v_add_f32 v208, v208, v126\n"
"v_exp_f32 v113, v81\n"
"v_mfma_f32_32x32x16_bf16 v[144:159], a[228:231], v[32:35], v[192:207]\n"
"s_waitcnt lgkmcnt(3)\n"
"ds_read_b128 a[252:255], v214 offset:27648\n"
"v_add_f32 v208, v208, v127\n"
"v_cvt_pk_bf16_f32 v103, v126, v127\n"
"v_exp_f32 v114, v82\n"
"v_mfma_f32_32x32x16_bf16 v[128:143], a[232:235], v[36:39], v[128:143]\n"
"ds_read_b128 a[224:227], v214 offset:20480\n"
"v_exp_f32 v115, v83\n"
"v_add_f32 v209, v112, v113\n"
"v_cvt_pk_bf16_f32 v104, v112, v113\n"
"v_mfma_f32_32x32x16_bf16 v[144:159], a[236:239], v[36:39], v[144:159]\n"
"s_waitcnt lgkmcnt(3)\n"
"ds_read_b128 a[228:231], v214 offset:28672\n"
"v_exp_f32 v116, v84\n"
"v_add_f32 v209, v209, v114\n"
"v_mfma_f32_32x32x16_bf16 v[128:143], a[240:243], v[40:43], v[128:143]\n"
"ds_read_b128 a[232:235], v214 offset:21504\n"
"v_exp_f32 v117, v85\n"
"v_add_f32 v209, v209, v115\n"
"v_cvt_pk_bf16_f32 v105, v114, v115\n"
"v_mfma_f32_32x32x16_bf16 v[144:159], a[244:247], v[40:43], v[144:159]\n"
"s_waitcnt lgkmcnt(3)\n"
"ds_read_b128 a[236:239], v214 offset:29696\n"
"v_exp_f32 v118, v86\n"
"v_add_f32 v209, v209, v116\n"
"v_mfma_f32_32x32x16_bf16 v[128:143], a[248:251], v[44:47], v[128:143]\n"
"ds_read_b128 a[240:243], v214 offset:22528\n"
"v_exp_f32 v119, v87\n"
"v_add_f32 v209, v209, v117\n"
"v_cvt_pk_bf16_f32 v106, v116, v117\n"
"v_mfma_f32_32x32x16_bf16 v[144:159], a[252:255], v[44:47], v[144:159]\n"
"s_waitcnt lgkmcnt(3)\n"
"ds_read_b128 a[244:247], v214 offset:30720\n"
"v_exp_f32 v120, v88\n"
"v_add_f32 v209, v209, v118\n"
"v_mfma_f32_32x32x16_bf16 v[128:143], a[224:227], v[48:51], v[128:143]\n"
"ds_read_b128 a[248:251], v214 offset:23552\n"
"v_exp_f32 v121, v89\n"
"v_add_f32 v209, v209, v119\n"
"v_cvt_pk_bf16_f32 v107, v118, v119\n"
"v_mfma_f32_32x32x16_bf16 v[144:159], a[228:231], v[48:51], v[144:159]\n"
"s_waitcnt lgkmcnt(3)\n"
"ds_read_b128 a[252:255], v214 offset:31744\n"
"v_exp_f32 v122, v90\n"
"v_add_f32 v209, v209, v120\n"
"v_mfma_f32_32x32x16_bf16 v[128:143], a[232:235], v[52:55], v[128:143]\n"
"ds_read_b128 a[224:227], v215 offset:0\n"
"v_exp_f32 v123, v91\n"
"v_add_f32 v209, v209, v121\n"
"v_cvt_pk_bf16_f32 v108, v120, v121\n"
"v_exp_f32 v124, v92\n"
"v_mfma_f32_32x32x16_bf16 v[144:159], a[236:239], v[52:55], v[144:159]\n"
"s_waitcnt lgkmcnt(3)\n"
"ds_read_b128 a[228:231], v215 offset:1024\n"
"v_add_f32 v209, v209, v122\n"
"v_exp_f32 v125, v93\n"
"v_add_f32 v209, v209, v123\n"
"v_mfma_f32_32x32x16_bf16 v[128:143], a[240:243], v[56:59], v[128:143]\n"
"ds_read_b128 a[232:235], v215 offset:2048\n"
"v_cvt_pk_bf16_f32 v109, v122, v123\n"
"v_exp_f32 v126, v94\n"
"v_add_f32 v209, v209, v124\n"
"v_mfma_f32_32x32x16_bf16 v[144:159], a[244:247], v[56:59], v[144:159]\n"
"s_waitcnt lgkmcnt(3)\n"
"ds_read_b128 a[236:239], v215 offset:3072\n"
"v_exp_f32 v127, v95\n"
"v_add_f32 v209, v209, v125\n"
"v_cvt_pk_bf16_f32 v110, v124, v125\n"
"v_mfma_f32_32x32x16_bf16 v[128:143], a[248:251], v[60:63], v[128:143]\n"
"ds_read_b128 a[240:243], v215 offset:4096\n"
"v_add_f32 v209, v209, v126\n"
"v_add_f32 v209, v209, v127\n"
"v_max_f32 v219, v208, v209\n"
"v_cmp_nle_f32 vcc, v219, v220\n"
"v_cvt_pk_bf16_f32 v111, v126, v127\n"
"v_mfma_f32_32x32x16_bf16 v[144:159], a[252:255], v[60:63], v[144:159]\n"
"ds_read_b128 a[244:247], v215 offset:5120\n"
"s_cbranch_vccnz L_aft59_%=\n"
"L_afr60_%=:\n"
"v_add_f32 v210, v210, v208\n"
"v_add_f32 v211, v211, v209\n"
"s_sub_u32 s11, s11, 1\n"
"s_cbranch_scc0 L_oo_%=\n"
"L_od_%=:\n"
"s_sub_u32 s12, s12, 2\n"
"s_lshl_b32 s56, s50, 13\n"
"s_add_u32 s56, s56, 39845888\n"
"s_add_u32 s28, s20, s56\n"
"s_addc_u32 s29, s21, 0\n"
"s_and_b32 s29, s29, 0xffff\n"
"s_mov_b32 s30, 0x1000000\n"
"s_mov_b32 s31, 0x20000\n"
"s_lshl_b32 s16, s8, 18\n"
"s_lshl_b32 s17, s9, 18\n"
"v_mov_b32 v222, v212\n"
"s_nop 1\n"
"v_permlane32_swap_b32 v212, v222\n"
"v_add_f32 v212, v212, v222\n"
"v_rcp_f32 v222, v212\n"
"s_add_u32 s59, s16, 4096\n"
"s_nop 0\n"
"v_mov_b32 v223, v222\n"
"v_accvgpr_read_b32 v0, a0\n"
"v_accvgpr_read_b32 v1, a1\n"
"v_accvgpr_read_b32 v2, a2\n"
"v_accvgpr_read_b32 v3, a3\n"
"v_accvgpr_read_b32 v4, a4\n"
"v_accvgpr_read_b32 v5, a5\n"
"v_accvgpr_read_b32 v6, a6\n"
"v_accvgpr_read_b32 v7, a7\n"
"v_accvgpr_read_b32 v8, a8\n"
"v_accvgpr_read_b32 v9, a9\n"
"v_accvgpr_read_b32 v10, a10\n"
"v_accvgpr_read_b32 v11, a11\n"
"v_accvgpr_read_b32 v12, a12\n"
"v_accvgpr_read_b32 v13, a13\n"
"v_accvgpr_read_b32 v14, a14\n"
"v_accvgpr_read_b32 v15, a15\n"
"v_pk_mul_f32 v[0:1], v[0:1], v[222:223]\n"
"v_pk_mul_f32 v[2:3], v[2:3], v[222:223]\n"
"v_pk_mul_f32 v[4:5], v[4:5], v[222:223]\n"
"v_pk_mul_f32 v[6:7], v[6:7], v[222:223]\n"
"v_pk_mul_f32 v[8:9], v[8:9], v[222:223]\n"
"v_pk_mul_f32 v[10:11], v[10:11], v[222:223]\n"
"v_pk_mul_f32 v[12:13], v[12:13], v[222:223]\n"
"v_pk_mul_f32 v[14:15], v[14:15], v[222:223]\n"
"v_cvt_pk_bf16_f32 v16, v0, v1\n"
"v_cvt_pk_bf16_f32 v17, v2, v3\n"
"v_cvt_pk_bf16_f32 v18, v4, v5\n"
"v_cvt_pk_bf16_f32 v19, v6, v7\n"
"v_cvt_pk_bf16_f32 v20, v8, v9\n"
"v_cvt_pk_bf16_f32 v21, v10, v11\n"
"v_cvt_pk_bf16_f32 v22, v12, v13\n"
"v_cvt_pk_bf16_f32 v23, v14, v15\n"
"buffer_store_dwordx4 v[16:19], v234, s[28:31], s16 offen offset:0 sc1\n"
"buffer_store_dwordx4 v[20:23], v234, s[28:31], s16 offen offset:1024 sc1\n"
"v_accvgpr_read_b32 v0, a16\n"
"v_accvgpr_read_b32 v1, a17\n"
"v_accvgpr_read_b32 v2, a18\n"
"v_accvgpr_read_b32 v3, a19\n"
"v_accvgpr_read_b32 v4, a20\n"
"v_accvgpr_read_b32 v5, a21\n"
"v_accvgpr_read_b32 v6, a22\n"
"v_accvgpr_read_b32 v7, a23\n"
"v_accvgpr_read_b32 v8, a24\n"
"v_accvgpr_read_b32 v9, a25\n"
"v_accvgpr_read_b32 v10, a26\n"
"v_accvgpr_read_b32 v11, a27\n"
"v_accvgpr_read_b32 v12, a28\n"
"v_accvgpr_read_b32 v13, a29\n"
"v_accvgpr_read_b32 v14, a30\n"
"v_accvgpr_read_b32 v15, a31\n"
"v_pk_mul_f32 v[0:1], v[0:1], v[222:223]\n"
"v_pk_mul_f32 v[2:3], v[2:3], v[222:223]\n"
"v_pk_mul_f32 v[4:5], v[4:5], v[222:223]\n"
"v_pk_mul_f32 v[6:7], v[6:7], v[222:223]\n"
"v_pk_mul_f32 v[8:9], v[8:9], v[222:223]\n"
"v_pk_mul_f32 v[10:11], v[10:11], v[222:223]\n"
"v_pk_mul_f32 v[12:13], v[12:13], v[222:223]\n"
"v_pk_mul_f32 v[14:15], v[14:15], v[222:223]\n"
"v_cvt_pk_bf16_f32 v16, v0, v1\n"
"v_cvt_pk_bf16_f32 v17, v2, v3\n"
"v_cvt_pk_bf16_f32 v18, v4, v5\n"
"v_cvt_pk_bf16_f32 v19, v6, v7\n"
"v_cvt_pk_bf16_f32 v20, v8, v9\n"
"v_cvt_pk_bf16_f32 v21, v10, v11\n"
"v_cvt_pk_bf16_f32 v22, v12, v13\n"
"v_cvt_pk_bf16_f32 v23, v14, v15\n"
"buffer_store_dwordx4 v[16:19], v234, s[28:31], s16 offen offset:2048 sc1\n"
"buffer_store_dwordx4 v[20:23], v234, s[28:31], s16 offen offset:3072 sc1\n"
"v_accvgpr_read_b32 v0, a32\n"
"v_accvgpr_read_b32 v1, a33\n"
"v_accvgpr_read_b32 v2, a34\n"
"v_accvgpr_read_b32 v3, a35\n"
"v_accvgpr_read_b32 v4, a36\n"
"v_accvgpr_read_b32 v5, a37\n"
"v_accvgpr_read_b32 v6, a38\n"
"v_accvgpr_read_b32 v7, a39\n"
"v_accvgpr_read_b32 v8, a40\n"
"v_accvgpr_read_b32 v9, a41\n"
"v_accvgpr_read_b32 v10, a42\n"
"v_accvgpr_read_b32 v11, a43\n"
"v_accvgpr_read_b32 v12, a44\n"
"v_accvgpr_read_b32 v13, a45\n"
"v_accvgpr_read_b32 v14, a46\n"
"v_accvgpr_read_b32 v15, a47\n"
"v_pk_mul_f32 v[0:1], v[0:1], v[222:223]\n"
"v_pk_mul_f32 v[2:3], v[2:3], v[222:223]\n"
"v_pk_mul_f32 v[4:5], v[4:5], v[222:223]\n"
"v_pk_mul_f32 v[6:7], v[6:7], v[222:223]\n"
"v_pk_mul_f32 v[8:9], v[8:9], v[222:223]\n"
"v_pk_mul_f32 v[10:11], v[10:11], v[222:223]\n"
"v_pk_mul_f32 v[12:13], v[12:13], v[222:223]\n"
"v_pk_mul_f32 v[14:15], v[14:15], v[222:223]\n"
"v_cvt_pk_bf16_f32 v16, v0, v1\n"
"v_cvt_pk_bf16_f32 v17, v2, v3\n"
"v_cvt_pk_bf16_f32 v18, v4, v5\n"
"v_cvt_pk_bf16_f32 v19, v6, v7\n"
"v_cvt_pk_bf16_f32 v20, v8, v9\n"
"v_cvt_pk_bf16_f32 v21, v10, v11\n"
"v_cvt_pk_bf16_f32 v22, v12, v13\n"
"v_cvt_pk_bf16_f32 v23, v14, v15\n"
"buffer_store_dwordx4 v[16:19], v234, s[28:31], s59 offen offset:0 sc1\n"
"buffer_store_dwordx4 v[20:23], v234, s[28:31], s59 offen offset:1024 sc1\n"
"v_accvgpr_read_b32 v0, a48\n"
"v_accvgpr_read_b32 v1, a49\n"
"v_accvgpr_read_b32 v2, a50\n"
"v_accvgpr_read_b32 v3, a51\n"
"v_accvgpr_read_b32 v4, a52\n"
"v_accvgpr_read_b32 v5, a53\n"
"v_accvgpr_read_b32 v6, a54\n"
"v_accvgpr_read_b32 v7, a55\n"
"v_accvgpr_read_b32 v8, a56\n"
"v_accvgpr_read_b32 v9, a57\n"
"v_accvgpr_read_b32 v10, a58\n"
"v_accvgpr_read_b32 v11, a59\n"
"v_accvgpr_read_b32 v12, a60\n"
"v_accvgpr_read_b32 v13, a61\n"
"v_accvgpr_read_b32 v14, a62\n"
"v_accvgpr_read_b32 v15, a63\n"
"v_pk_mul_f32 v[0:1], v[0:1], v[222:223]\n"
"v_pk_mul_f32 v[2:3], v[2:3], v[222:223]\n"
"v_pk_mul_f32 v[4:5], v[4:5], v[222:223]\n"
"v_pk_mul_f32 v[6:7], v[6:7], v[222:223]\n"
"v_pk_mul_f32 v[8:9], v[8:9], v[222:223]\n"
"v_pk_mul_f32 v[10:11], v[10:11], v[222:223]\n"
"v_pk_mul_f32 v[12:13], v[12:13], v[222:223]\n"
"v_pk_mul_f32 v[14:15], v[14:15], v[222:223]\n"
"v_cvt_pk_bf16_f32 v16, v0, v1\n"
"v_cvt_pk_bf16_f32 v17, v2, v3\n"
"v_cvt_pk_bf16_f32 v18, v4, v5\n"
"v_cvt_pk_bf16_f32 v19, v6, v7\n"
"v_cvt_pk_bf16_f32 v20, v8, v9\n"
"v_cvt_pk_bf16_f32 v21, v10, v11\n"
"v_cvt_pk_bf16_f32 v22, v12, v13\n"
"v_cvt_pk_bf16_f32 v23, v14, v15\n"
"buffer_store_dwordx4 v[16:19], v234, s[28:31], s59 offen offset:2048 sc1\n"
"buffer_store_dwordx4 v[20:23], v234, s[28:31], s59 offen offset:3072 sc1\n"
"s_waitcnt lgkmcnt(4)\n"
"v_mfma_f32_32x32x16_bf16 a[64:79], a[224:227], v[96:99], a[64:79]\n"
"s_branch L_amask61_%=\n"
"L_amret62_%=:\n"
"ds_read_b128 a[248:251], v215 offset:6144\n"
"v_exp_f32 v112, v128\n"
"v_mfma_f32_32x32x16_bf16 a[80:95], a[228:231], v[96:99], a[80:95]\n"
"s_waitcnt lgkmcnt(3)\n"
"ds_read_b128 a[252:255], v215 offset:7168\n"
"v_exp_f32 v113, v129\n"
"v_mfma_f32_32x32x16_bf16 a[96:111], a[232:235], v[96:99], a[96:111]\n"
"ds_read_b128 a[224:227], v215 offset:8192\n"
"v_exp_f32 v114, v130\n"
"v_exp_f32 v115, v131\n"
"v_mfma_f32_32x32x16_bf16 a[112:127], a[236:239], v[96:99], a[112:127]\n"
"s_waitcnt lgkmcnt(3)\n"
"ds_read_b128 a[228:231], v215 offset:9216\n"
"v_add_f32 v208, v112, v113\n"
"v_cvt_pk_bf16_f32 v160, v112, v113\n"
"v_mfma_f32_32x32x16_bf16 a[64:79], a[240:243], v[100:103], a[64:79]\n"
"ds_read_b128 a[232:235], v215 offset:10240\n"
"v_exp_f32 v116, v132\n"
"v_add_f32 v208, v208, v114\n"
"v_mfma_f32_32x32x16_bf16 a[80:95], a[244:247], v[100:103], a[80:95]\n"
"s_waitcnt lgkmcnt(3)\n"
"ds_read_b128 a[236:239], v215 offset:11264\n"
"v_exp_f32 v117, v133\n"
"v_add_f32 v208, v208, v115\n"
"v_mfma_f32_32x32x16_bf16 a[96:111], a[248:251], v[100:103], a[96:111]\n"
"ds_read_b128 a[240:243], v215 offset:12288\n"
"v_cvt_pk_bf16_f32 v161, v114, v115\n"
"v_exp_f32 v118, v134\n"
"v_add_f32 v208, v208, v116\n"
"v_mfma_f32_32x32x16_bf16 a[112:127], a[252:255], v[100:103], a[112:127]\n"
"s_waitcnt lgkmcnt(3)\n"
"ds_read_b128 a[244:247], v215 offset:13312\n"
"v_exp_f32 v119, v135\n"
"v_add_f32 v208, v208, v117\n"
"v_mfma_f32_32x32x16_bf16 a[64:79], a[224:227], v[104:107], a[64:79]\n"
"ds_read_b128 a[248:251], v215 offset:14336\n"
"v_cvt_pk_bf16_f32 v162, v116, v117\n"
"v_exp_f32 v120, v136\n"
"v_add_f32 v208, v208, v118\n"
"v_mfma_f32_32x32x16_bf16 a[80:95], a[228:231], v[104:107], a[80:95]\n"
"s_waitcnt lgkmcnt(3)\n"
"ds_read_b128 a[252:255], v215 offset:15360\n"
"v_exp_f32 v121, v137\n"
"v_add_f32 v208, v208, v119\n"
"v_mfma_f32_32x32x16_bf16 a[96:111], a[232:235], v[104:107], a[96:111]\n"
"v_cvt_pk_bf16_f32 v163, v118, v119\n"
"v_exp_f32 v122, v138\n"
"v_add_f32 v208, v208, v120\n"
"v_mfma_f32_32x32x16_bf16 a[112:127], a[236:239], v[104:107], a[112:127]\n"
"s_waitcnt lgkmcnt(2)\n"
"v_exp_f32 v123, v139\n"
"v_add_f32 v208, v208, v121\n"
"v_cvt_pk_bf16_f32 v164, v120, v121\n"
"v_mfma_f32_32x32x16_bf16 a[64:79], a[240:243], v[108:111], a[64:79]\n"
"s_add_u32 s59, s10, 0x4000\n"
"v_exp_f32 v124, v140\n"
"v_add_f32 v208, v208, v122\n"
"v_mfma_f32_32x32x16_bf16 a[80:95], a[244:247], v[108:111], a[80:95]\n"
"s_waitcnt lgkmcnt(0)\n"
"v_exp_f32 v125, v141\n"
"v_add_f32 v208, v208, v123\n"
"v_cvt_pk_bf16_f32 v165, v122, v123\n"
"v_mfma_f32_32x32x16_bf16 a[96:111], a[248:251], v[108:111], a[96:111]\n"
"s_sub_u32 s15, s59, 0x14000\n"
"v_exp_f32 v126, v142\n"
"v_add_f32 v208, v208, v124\n"
"v_mfma_f32_32x32x16_bf16 a[112:127], a[252:255], v[108:111], a[112:127]\n"
"s_min_u32 s10, s59, s15\n"
"v_exp_f32 v127, v143\n"
"v_add_f32 v208, v208, v125\n"
"v_cvt_pk_bf16_f32 v166, v124, v125\n"
"v_add_u32 v215, s10, v217\n"
"v_exp_f32 v112, v144\n"
"v_add_f32 v208, v208, v126\n"
"v_exp_f32 v113, v145\n"
"v_add_f32 v208, v208, v127\n"
"v_cvt_pk_bf16_f32 v167, v126, v127\n"
"v_exp_f32 v114, v146\n"
"v_exp_f32 v115, v147\n"
"v_add_f32 v209, v112, v113\n"
"v_cvt_pk_bf16_f32 v168, v112, v113\n"
"v_exp_f32 v116, v148\n"
"v_add_f32 v209, v209, v114\n"
"v_exp_f32 v117, v149\n"
"v_add_f32 v209, v209, v115\n"
"v_cvt_pk_bf16_f32 v169, v114, v115\n"
"v_exp_f32 v118, v150\n"
"v_add_f32 v209, v209, v116\n"
"v_exp_f32 v119, v151\n"
"v_add_f32 v209, v209, v117\n"
"v_cvt_pk_bf16_f32 v170, v116, v117\n"
"v_exp_f32 v120, v152\n"
"v_add_f32 v209, v209, v118\n"
"v_exp_f32 v121, v153\n"
"v_add_f32 v209, v209, v119\n"
"v_cvt_pk_bf16_f32 v171, v118, v119\n"
"v_exp_f32 v122, v154\n"
"v_add_f32 v209, v209, v120\n"
"s_waitcnt vmcnt(8)\n"
"s_barrier\n"
"v_exp_f32 v123, v155\n"
"v_add_f32 v209, v209, v121\n"
"v_cvt_pk_bf16_f32 v172, v120, v121\n"
"ds_read_b128 a[224:227], v215 offset:0\n"
"v_exp_f32 v124, v156\n"
"v_add_f32 v209, v209, v122\n"
"ds_read_b128 a[228:231], v215 offset:1024\n"
"v_exp_f32 v125, v157\n"
"v_add_f32 v209, v209, v123\n"
"v_cvt_pk_bf16_f32 v173, v122, v123\n"
"ds_read_b128 a[232:235], v215 offset:2048\n"
"v_exp_f32 v126, v158\n"
"v_add_f32 v209, v209, v124\n"
"v_exp_f32 v127, v159\n"
"ds_read_b128 a[236:239], v215 offset:3072\n"
"v_add_f32 v209, v209, v125\n"
"v_cvt_pk_bf16_f32 v174, v124, v125\n"
"v_add_f32 v209, v209, v126\n"
"v_add_f32 v209, v209, v127\n"
"ds_read_b128 a[240:243], v215 offset:4096\n"
"v_max_f32 v219, v208, v209\n"
"v_cmp_nle_f32 vcc, v219, v220\n"
"v_cvt_pk_bf16_f32 v175, v126, v127\n"
"s_waitcnt lgkmcnt(3)\n"
"ds_read_b128 a[244:247], v215 offset:5120\n"
"s_cbranch_vccnz L_aft67_%=\n"
"L_afr68_%=:\n"
"v_add_f32 v210, v210, v208\n"
"v_add_f32 v211, v211, v209\n"
"v_mfma_f32_32x32x16_bf16 a[64:79], a[224:227], v[160:163], a[64:79]\n"
"ds_read_b128 a[248:251], v215 offset:6144\n"
"v_mfma_f32_32x32x16_bf16 a[80:95], a[228:231], v[160:163], a[80:95]\n"
"s_waitcnt lgkmcnt(3)\n"
"ds_read_b128 a[252:255], v215 offset:7168\n"
"v_mfma_f32_32x32x16_bf16 a[96:111], a[232:235], v[160:163], a[96:111]\n"
"ds_read_b128 a[224:227], v215 offset:8192\n"
"v_mfma_f32_32x32x16_bf16 a[112:127], a[236:239], v[160:163], a[112:127]\n"
"s_waitcnt lgkmcnt(3)\n"
"ds_read_b128 a[228:231], v215 offset:9216\n"
"v_mfma_f32_32x32x16_bf16 a[64:79], a[240:243], v[164:167], a[64:79]\n"
"ds_read_b128 a[232:235], v215 offset:10240\n"
"v_mfma_f32_32x32x16_bf16 a[80:95], a[244:247], v[164:167], a[80:95]\n"
"s_waitcnt lgkmcnt(3)\n"
"ds_read_b128 a[236:239], v215 offset:11264\n"
"v_mfma_f32_32x32x16_bf16 a[96:111], a[248:251], v[164:167], a[96:111]\n"
"ds_read_b128 a[240:243], v215 offset:12288\n"
"v_mfma_f32_32x32x16_bf16 a[112:127], a[252:255], v[164:167], a[112:127]\n"
"s_waitcnt lgkmcnt(3)\n"
"ds_read_b128 a[244:247], v215 offset:13312\n"
"v_mfma_f32_32x32x16_bf16 a[64:79], a[224:227], v[168:171], a[64:79]\n"
"ds_read_b128 a[248:251], v215 offset:14336\n"
"v_mfma_f32_32x32x16_bf16 a[80:95], a[228:231], v[168:171], a[80:95]\n"
"s_waitcnt lgkmcnt(3)\n"
"ds_read_b128 a[252:255], v215 offset:15360\n"
"v_mfma_f32_32x32x16_bf16 a[96:111], a[232:235], v[168:171], a[96:111]\n"
"v_mfma_f32_32x32x16_bf16 a[112:127], a[236:239], v[168:171], a[112:127]\n"
"s_waitcnt lgkmcnt(2)\n"
"v_mfma_f32_32x32x16_bf16 a[64:79], a[240:243], v[172:175], a[64:79]\n"
"v_mfma_f32_32x32x16_bf16 a[80:95], a[244:247], v[172:175], a[80:95]\n"
"s_waitcnt lgkmcnt(0)\n"
"v_mfma_f32_32x32x16_bf16 a[96:111], a[248:251], v[172:175], a[96:111]\n"
"v_mfma_f32_32x32x16_bf16 a[112:127], a[252:255], v[172:175], a[112:127]\n"
"v_add_f32 v211, v210, v211\n"
"s_nop 7\n"
"s_nop 3\n"
"v_mov_b32 v222, v211\n"
"s_nop 1\n"
"v_permlane32_swap_b32 v211, v222\n"
"v_add_f32 v211, v211, v222\n"
"v_rcp_f32 v222, v211\n"
"s_add_u32 s59, s17, 4096\n"
"s_nop 0\n"
"v_mov_b32 v223, v222\n"
"v_accvgpr_read_b32 v64, a64\n"
"v_accvgpr_read_b32 v65, a65\n"
"v_accvgpr_read_b32 v66, a66\n"
"v_accvgpr_read_b32 v67, a67\n"
"v_accvgpr_read_b32 v68, a68\n"
"v_accvgpr_read_b32 v69, a69\n"
"v_accvgpr_read_b32 v70, a70\n"
"v_accvgpr_read_b32 v71, a71\n"
"v_accvgpr_read_b32 v72, a72\n"
"v_accvgpr_read_b32 v73, a73\n"
"v_accvgpr_read_b32 v74, a74\n"
"v_accvgpr_read_b32 v75, a75\n"
"v_accvgpr_read_b32 v76, a76\n"
"v_accvgpr_read_b32 v77, a77\n"
"v_accvgpr_read_b32 v78, a78\n"
"v_accvgpr_read_b32 v79, a79\n"
"v_pk_mul_f32 v[64:65], v[64:65], v[222:223]\n"
"v_pk_mul_f32 v[66:67], v[66:67], v[222:223]\n"
"v_pk_mul_f32 v[68:69], v[68:69], v[222:223]\n"
"v_pk_mul_f32 v[70:71], v[70:71], v[222:223]\n"
"v_pk_mul_f32 v[72:73], v[72:73], v[222:223]\n"
"v_pk_mul_f32 v[74:75], v[74:75], v[222:223]\n"
"v_pk_mul_f32 v[76:77], v[76:77], v[222:223]\n"
"v_pk_mul_f32 v[78:79], v[78:79], v[222:223]\n"
"v_cvt_pk_bf16_f32 v80, v64, v65\n"
"v_cvt_pk_bf16_f32 v81, v66, v67\n"
"v_cvt_pk_bf16_f32 v82, v68, v69\n"
"v_cvt_pk_bf16_f32 v83, v70, v71\n"
"v_cvt_pk_bf16_f32 v84, v72, v73\n"
"v_cvt_pk_bf16_f32 v85, v74, v75\n"
"v_cvt_pk_bf16_f32 v86, v76, v77\n"
"v_cvt_pk_bf16_f32 v87, v78, v79\n"
"buffer_store_dwordx4 v[80:83], v234, s[28:31], s17 offen offset:0 sc1\n"
"buffer_store_dwordx4 v[84:87], v234, s[28:31], s17 offen offset:1024 sc1\n"
"v_accvgpr_read_b32 v88, a80\n"
"v_accvgpr_read_b32 v89, a81\n"
"v_accvgpr_read_b32 v90, a82\n"
"v_accvgpr_read_b32 v91, a83\n"
"v_accvgpr_read_b32 v92, a84\n"
"v_accvgpr_read_b32 v93, a85\n"
"v_accvgpr_read_b32 v94, a86\n"
"v_accvgpr_read_b32 v95, a87\n"
"v_accvgpr_read_b32 v96, a88\n"
"v_accvgpr_read_b32 v97, a89\n"
"v_accvgpr_read_b32 v98, a90\n"
"v_accvgpr_read_b32 v99, a91\n"
"v_accvgpr_read_b32 v100, a92\n"
"v_accvgpr_read_b32 v101, a93\n"
"v_accvgpr_read_b32 v102, a94\n"
"v_accvgpr_read_b32 v103, a95\n"
"v_pk_mul_f32 v[88:89], v[88:89], v[222:223]\n"
"v_pk_mul_f32 v[90:91], v[90:91], v[222:223]\n"
"v_pk_mul_f32 v[92:93], v[92:93], v[222:223]\n"
"v_pk_mul_f32 v[94:95], v[94:95], v[222:223]\n"
"v_pk_mul_f32 v[96:97], v[96:97], v[222:223]\n"
"v_pk_mul_f32 v[98:99], v[98:99], v[222:223]\n"
"v_pk_mul_f32 v[100:101], v[100:101], v[222:223]\n"
"v_pk_mul_f32 v[102:103], v[102:103], v[222:223]\n"
"v_cvt_pk_bf16_f32 v104, v88, v89\n"
"v_cvt_pk_bf16_f32 v105, v90, v91\n"
"v_cvt_pk_bf16_f32 v106, v92, v93\n"
"v_cvt_pk_bf16_f32 v107, v94, v95\n"
"v_cvt_pk_bf16_f32 v108, v96, v97\n"
"v_cvt_pk_bf16_f32 v109, v98, v99\n"
"v_cvt_pk_bf16_f32 v110, v100, v101\n"
"v_cvt_pk_bf16_f32 v111, v102, v103\n"
"buffer_store_dwordx4 v[104:107], v234, s[28:31], s17 offen offset:2048 sc1\n"
"buffer_store_dwordx4 v[108:111], v234, s[28:31], s17 offen offset:3072 sc1\n"
"v_accvgpr_read_b32 v112, a96\n"
"v_accvgpr_read_b32 v113, a97\n"
"v_accvgpr_read_b32 v114, a98\n"
"v_accvgpr_read_b32 v115, a99\n"
"v_accvgpr_read_b32 v116, a100\n"
"v_accvgpr_read_b32 v117, a101\n"
"v_accvgpr_read_b32 v118, a102\n"
"v_accvgpr_read_b32 v119, a103\n"
"v_accvgpr_read_b32 v120, a104\n"
"v_accvgpr_read_b32 v121, a105\n"
"v_accvgpr_read_b32 v122, a106\n"
"v_accvgpr_read_b32 v123, a107\n"
"v_accvgpr_read_b32 v124, a108\n"
"v_accvgpr_read_b32 v125, a109\n"
"v_accvgpr_read_b32 v126, a110\n"
"v_accvgpr_read_b32 v127, a111\n"
"v_pk_mul_f32 v[112:113], v[112:113], v[222:223]\n"
"v_pk_mul_f32 v[114:115], v[114:115], v[222:223]\n"
"v_pk_mul_f32 v[116:117], v[116:117], v[222:223]\n"
"v_pk_mul_f32 v[118:119], v[118:119], v[222:223]\n"
"v_pk_mul_f32 v[120:121], v[120:121], v[222:223]\n"
"v_pk_mul_f32 v[122:123], v[122:123], v[222:223]\n"
"v_pk_mul_f32 v[124:125], v[124:125], v[222:223]\n"
"v_pk_mul_f32 v[126:127], v[126:127], v[222:223]\n"
"v_cvt_pk_bf16_f32 v128, v112, v113\n"
"v_cvt_pk_bf16_f32 v129, v114, v115\n"
"v_cvt_pk_bf16_f32 v130, v116, v117\n"
"v_cvt_pk_bf16_f32 v131, v118, v119\n"
"v_cvt_pk_bf16_f32 v132, v120, v121\n"
"v_cvt_pk_bf16_f32 v133, v122, v123\n"
"v_cvt_pk_bf16_f32 v134, v124, v125\n"
"v_cvt_pk_bf16_f32 v135, v126, v127\n"
"buffer_store_dwordx4 v[128:131], v234, s[28:31], s59 offen offset:0 sc1\n"
"buffer_store_dwordx4 v[132:135], v234, s[28:31], s59 offen offset:1024 sc1\n"
"v_accvgpr_read_b32 v136, a112\n"
"v_accvgpr_read_b32 v137, a113\n"
"v_accvgpr_read_b32 v138, a114\n"
"v_accvgpr_read_b32 v139, a115\n"
"v_accvgpr_read_b32 v140, a116\n"
"v_accvgpr_read_b32 v141, a117\n"
"v_accvgpr_read_b32 v142, a118\n"
"v_accvgpr_read_b32 v143, a119\n"
"v_accvgpr_read_b32 v144, a120\n"
"v_accvgpr_read_b32 v145, a121\n"
"v_accvgpr_read_b32 v146, a122\n"
"v_accvgpr_read_b32 v147, a123\n"
"v_accvgpr_read_b32 v148, a124\n"
"v_accvgpr_read_b32 v149, a125\n"
"v_accvgpr_read_b32 v150, a126\n"
"v_accvgpr_read_b32 v151, a127\n"
"v_pk_mul_f32 v[136:137], v[136:137], v[222:223]\n"
"v_pk_mul_f32 v[138:139], v[138:139], v[222:223]\n"
"v_pk_mul_f32 v[140:141], v[140:141], v[222:223]\n"
"v_pk_mul_f32 v[142:143], v[142:143], v[222:223]\n"
"v_pk_mul_f32 v[144:145], v[144:145], v[222:223]\n"
"v_pk_mul_f32 v[146:147], v[146:147], v[222:223]\n"
"v_pk_mul_f32 v[148:149], v[148:149], v[222:223]\n"
"v_pk_mul_f32 v[150:151], v[150:151], v[222:223]\n"
"v_cvt_pk_bf16_f32 v152, v136, v137\n"
"v_cvt_pk_bf16_f32 v153, v138, v139\n"
"v_cvt_pk_bf16_f32 v154, v140, v141\n"
"v_cvt_pk_bf16_f32 v155, v142, v143\n"
"v_cvt_pk_bf16_f32 v156, v144, v145\n"
"v_cvt_pk_bf16_f32 v157, v146, v147\n"
"v_cvt_pk_bf16_f32 v158, v148, v149\n"
"v_cvt_pk_bf16_f32 v159, v150, v151\n"
"buffer_store_dwordx4 v[152:155], v234, s[28:31], s59 offen offset:2048 sc1\n"
"buffer_store_dwordx4 v[156:159], v234, s[28:31], s59 offen offset:3072 sc1\n"
"s_branch L_end_%=\n"
"L_aspin3_%=:\n"
"s_mov_b32 s13, 0x200000\n"
"L_aspl4_%=:\n"
"s_sleep 1\n"
"s_lshl_b32 s58, s50, 7\n"
"s_add_u32 s58, s58, 20480\n"
"buffer_load_dwordx2 v[240:241], off, s[44:47], s58 sc1\n"
"s_waitcnt vmcnt(0)\n"
"v_cmp_ne_u64 vcc, s[96:97], v[240:241]\n"
"s_sub_u32 s13, s13, 1\n"
"s_cbranch_scc1 L_ac1r2_%=\n"
"s_nop 1\n"
"s_cbranch_vccnz L_aspl4_%=\n"
"s_branch L_ac1r2_%=\n"
"L_abump7_%=:\n"
"s_mov_b32 s13, 0x200000\n"
"L_abl9_%=:\n"
"v_cmp_ne_u64_e64 s[56:57], s[96:97], v[244:245]\n"
"v_cmp_ne_u64_e64 s[58:59], s[96:97], v[246:247]\n"
"s_nop 4\n"
"s_or_b64 s[56:57], s[56:57], s[58:59]\n"
"v_cmp_ne_u64_e64 s[58:59], s[96:97], v[248:249]\n"
"s_nop 4\n"
"s_or_b64 s[56:57], s[56:57], s[58:59]\n"
"v_cmp_ne_u64_e64 s[58:59], s[96:97], v[250:251]\n"
"s_nop 4\n"
"s_or_b64 s[56:57], s[56:57], s[58:59]\n"
"s_cmp_eq_u64 s[56:57], 0\n"
"s_cbranch_scc1 L_abok10_%=\n"
"s_sub_u32 s13, s13, 1\n"
"s_cbranch_scc1 L_abumpr8_%=\n"
"s_sleep 1\n"
"v_lshlrev_b32 v252, 3, v236\n"
"s_movk_i32 s59, 16384\n"
"buffer_load_dwordx2 v[244:245], v252, s[44:47], s59 offen offset:0 sc1\n"
"buffer_load_dwordx2 v[246:247], v252, s[44:47], s59 offen offset:512 sc1\n"
"buffer_load_dwordx2 v[248:249], v252, s[44:47], s59 offen offset:1024 sc1\n"
"buffer_load_dwordx2 v[250:251], v252, s[44:47], s59 offen offset:1536 sc1\n"
"s_waitcnt vmcnt(0)\n"
"s_branch L_abl9_%=\n"
"L_abok10_%=:\n"
"v_mov_b32 v244, s96\n"
"v_mov_b32 v245, s97\n"
"s_movk_i32 s59, 2048\n"
"buffer_store_dwordx2 v[244:245], off, s[44:47], s59 sc1\n"
"s_branch L_abumpr8_%=\n"
"L_aspin12_%=:\n"
"s_mov_b32 s13, 0x200000\n"
"L_aspl13_%=:\n"
"s_sleep 1\n"
"s_lshl_b32 s58, s50, 7\n"
"s_add_u32 s58, s58, 20488\n"
"buffer_load_dwordx2 v[240:241], v242, s[44:47], s58 offen sc1\n"
"s_waitcnt vmcnt(0)\n"
"v_cmp_ne_u64_e64 s[56:57], s[96:97], v[240:241]\n"
"s_sub_u32 s13, s13, 1\n"
"s_cbranch_scc1 L_ac2r11_%=\n"
"s_nop 4\n"
"s_cmp_lg_u64 s[56:57], 0\n"
"s_cbranch_scc1 L_aspl13_%=\n"
"s_branch L_ac2r11_%=\n"
"L_aft15_%=:\n"
"s_mov_b32 s13, 0\n"
"s_branch L_fix_s0_%=\n"
"L_amask17_%=:\n"
"s_nop 7\n"
"s_nop 3\n"
"v_mov_b32 v221, 0xff800000\n"
"s_add_u32 s14, s12, 0\n"
"s_cmp_lt_u32 s14, s8\n"
"s_cbranch_scc1 L_amd20_%=\n"
"s_cmp_eq_u32 s14, s8\n"
"s_cbranch_scc1 L_atri19_%=\n"
"v_mov_b32 v64, v221\n"
"v_mov_b32 v65, v221\n"
"v_mov_b32 v66, v221\n"
"v_mov_b32 v67, v221\n"
"v_mov_b32 v68, v221\n"
"v_mov_b32 v69, v221\n"
"v_mov_b32 v70, v221\n"
"v_mov_b32 v71, v221\n"
"v_mov_b32 v72, v221\n"
"v_mov_b32 v73, v221\n"
"v_mov_b32 v74, v221\n"
"v_mov_b32 v75, v221\n"
"v_mov_b32 v76, v221\n"
"v_mov_b32 v77, v221\n"
"v_mov_b32 v78, v221\n"
"v_mov_b32 v79, v221\n"
"s_branch L_amd20_%=\n"
"L_atri19_%=:\n"
"v_cndmask_b32_e64 v64, v64, v221, s[64:65]\n"
"v_cndmask_b32_e64 v65, v65, v221, s[66:67]\n"
"v_cndmask_b32_e64 v66, v66, v221, s[68:69]\n"
"v_cndmask_b32_e64 v67, v67, v221, s[70:71]\n"
"v_cndmask_b32_e64 v68, v68, v221, s[72:73]\n"
"v_cndmask_b32_e64 v69, v69, v221, s[74:75]\n"
"v_cndmask_b32_e64 v70, v70, v221, s[76:77]\n"
"v_cndmask_b32_e64 v71, v71, v221, s[78:79]\n"
"v_cndmask_b32_e64 v72, v72, v221, s[80:81]\n"
"v_cndmask_b32_e64 v73, v73, v221, s[82:83]\n"
"v_cndmask_b32_e64 v74, v74, v221, s[84:85]\n"
"v_cndmask_b32_e64 v75, v75, v221, s[86:87]\n"
"v_cndmask_b32_e64 v76, v76, v221, s[88:89]\n"
"v_cndmask_b32_e64 v77, v77, v221, s[90:91]\n"
"v_cndmask_b32_e64 v78, v78, v221, s[92:93]\n"
"v_cndmask_b32_e64 v79, v79, v221, s[94:95]\n"
"L_amd20_%=:\n"
"s_branch L_amret18_%=\n"
"L_aft21_%=:\n"
"s_mov_b32 s13, 0\n"
"s_branch L_fix_s1_%=\n"
"L_aft23_%=:\n"
"s_mov_b32 s13, 1\n"
"s_branch L_fix_s0_%=\n"
"L_amask25_%=:\n"
"s_nop 7\n"
"s_nop 3\n"
"v_mov_b32 v221, 0xff800000\n"
"s_add_u32 s14, s12, 0\n"
"s_cmp_lt_u32 s14, s8\n"
"s_cbranch_scc1 L_amd28_%=\n"
"s_cmp_eq_u32 s14, s8\n"
"s_cbranch_scc1 L_atri27_%=\n"
"v_mov_b32 v128, v221\n"
"v_mov_b32 v129, v221\n"
"v_mov_b32 v130, v221\n"
"v_mov_b32 v131, v221\n"
"v_mov_b32 v132, v221\n"
"v_mov_b32 v133, v221\n"
"v_mov_b32 v134, v221\n"
"v_mov_b32 v135, v221\n"
"v_mov_b32 v136, v221\n"
"v_mov_b32 v137, v221\n"
"v_mov_b32 v138, v221\n"
"v_mov_b32 v139, v221\n"
"v_mov_b32 v140, v221\n"
"v_mov_b32 v141, v221\n"
"v_mov_b32 v142, v221\n"
"v_mov_b32 v143, v221\n"
"s_branch L_amd28_%=\n"
"L_atri27_%=:\n"
"v_cndmask_b32_e64 v128, v128, v221, s[64:65]\n"
"v_cndmask_b32_e64 v129, v129, v221, s[66:67]\n"
"v_cndmask_b32_e64 v130, v130, v221, s[68:69]\n"
"v_cndmask_b32_e64 v131, v131, v221, s[70:71]\n"
"v_cndmask_b32_e64 v132, v132, v221, s[72:73]\n"
"v_cndmask_b32_e64 v133, v133, v221, s[74:75]\n"
"v_cndmask_b32_e64 v134, v134, v221, s[76:77]\n"
"v_cndmask_b32_e64 v135, v135, v221, s[78:79]\n"
"v_cndmask_b32_e64 v136, v136, v221, s[80:81]\n"
"v_cndmask_b32_e64 v137, v137, v221, s[82:83]\n"
"v_cndmask_b32_e64 v138, v138, v221, s[84:85]\n"
"v_cndmask_b32_e64 v139, v139, v221, s[86:87]\n"
"v_cndmask_b32_e64 v140, v140, v221, s[88:89]\n"
"v_cndmask_b32_e64 v141, v141, v221, s[90:91]\n"
"v_cndmask_b32_e64 v142, v142, v221, s[92:93]\n"
"v_cndmask_b32_e64 v143, v143, v221, s[94:95]\n"
"L_amd28_%=:\n"
"s_branch L_amret26_%=\n"
"L_amask29_%=:\n"
"s_nop 7\n"
"s_nop 3\n"
"v_mov_b32 v221, 0xff800000\n"
"s_add_u32 s14, s12, 0\n"
"s_cmp_lt_u32 s14, s8\n"
"s_cbranch_scc1 L_amd32_%=\n"
"s_cmp_eq_u32 s14, s8\n"
"s_cbranch_scc1 L_atri31_%=\n"
"v_mov_b32 v64, v221\n"
"v_mov_b32 v65, v221\n"
"v_mov_b32 v66, v221\n"
"v_mov_b32 v67, v221\n"
"v_mov_b32 v68, v221\n"
"v_mov_b32 v69, v221\n"
"v_mov_b32 v70, v221\n"
"v_mov_b32 v71, v221\n"
"v_mov_b32 v72, v221\n"
"v_mov_b32 v73, v221\n"
"v_mov_b32 v74, v221\n"
"v_mov_b32 v75, v221\n"
"v_mov_b32 v76, v221\n"
"v_mov_b32 v77, v221\n"
"v_mov_b32 v78, v221\n"
"v_mov_b32 v79, v221\n"
"s_branch L_amd32_%=\n"
"L_atri31_%=:\n"
"v_cndmask_b32_e64 v64, v64, v221, s[64:65]\n"
"v_cndmask_b32_e64 v65, v65, v221, s[66:67]\n"
"v_cndmask_b32_e64 v66, v66, v221, s[68:69]\n"
"v_cndmask_b32_e64 v67, v67, v221, s[70:71]\n"
"v_cndmask_b32_e64 v68, v68, v221, s[72:73]\n"
"v_cndmask_b32_e64 v69, v69, v221, s[74:75]\n"
"v_cndmask_b32_e64 v70, v70, v221, s[76:77]\n"
"v_cndmask_b32_e64 v71, v71, v221, s[78:79]\n"
"v_cndmask_b32_e64 v72, v72, v221, s[80:81]\n"
"v_cndmask_b32_e64 v73, v73, v221, s[82:83]\n"
"v_cndmask_b32_e64 v74, v74, v221, s[84:85]\n"
"v_cndmask_b32_e64 v75, v75, v221, s[86:87]\n"
"v_cndmask_b32_e64 v76, v76, v221, s[88:89]\n"
"v_cndmask_b32_e64 v77, v77, v221, s[90:91]\n"
"v_cndmask_b32_e64 v78, v78, v221, s[92:93]\n"
"v_cndmask_b32_e64 v79, v79, v221, s[94:95]\n"
"L_amd32_%=:\n"
"s_branch L_amret30_%=\n"
"L_aft33_%=:\n"
"s_mov_b32 s13, 1\n"
"s_branch L_fix_s1_%=\n"
"L_aft35_%=:\n"
"s_mov_b32 s13, 2\n"
"s_branch L_fix_s0_%=\n"
"L_amask37_%=:\n"
"s_nop 7\n"
"s_nop 3\n"
"v_mov_b32 v221, 0xff800000\n"
"s_add_u32 s14, s12, 0\n"
"s_cmp_lt_u32 s14, s8\n"
"s_cbranch_scc1 L_amd40_%=\n"
"s_cmp_eq_u32 s14, s8\n"
"s_cbranch_scc1 L_atri39_%=\n"
"v_mov_b32 v128, v221\n"
"v_mov_b32 v129, v221\n"
"v_mov_b32 v130, v221\n"
"v_mov_b32 v131, v221\n"
"v_mov_b32 v132, v221\n"
"v_mov_b32 v133, v221\n"
"v_mov_b32 v134, v221\n"
"v_mov_b32 v135, v221\n"
"v_mov_b32 v136, v221\n"
"v_mov_b32 v137, v221\n"
"v_mov_b32 v138, v221\n"
"v_mov_b32 v139, v221\n"
"v_mov_b32 v140, v221\n"
"v_mov_b32 v141, v221\n"
"v_mov_b32 v142, v221\n"
"v_mov_b32 v143, v221\n"
"s_branch L_amd40_%=\n"
"L_atri39_%=:\n"
"v_cndmask_b32_e64 v128, v128, v221, s[64:65]\n"
"v_cndmask_b32_e64 v129, v129, v221, s[66:67]\n"
"v_cndmask_b32_e64 v130, v130, v221, s[68:69]\n"
"v_cndmask_b32_e64 v131, v131, v221, s[70:71]\n"
"v_cndmask_b32_e64 v132, v132, v221, s[72:73]\n"
"v_cndmask_b32_e64 v133, v133, v221, s[74:75]\n"
"v_cndmask_b32_e64 v134, v134, v221, s[76:77]\n"
"v_cndmask_b32_e64 v135, v135, v221, s[78:79]\n"
"v_cndmask_b32_e64 v136, v136, v221, s[80:81]\n"
"v_cndmask_b32_e64 v137, v137, v221, s[82:83]\n"
"v_cndmask_b32_e64 v138, v138, v221, s[84:85]\n"
"v_cndmask_b32_e64 v139, v139, v221, s[86:87]\n"
"v_cndmask_b32_e64 v140, v140, v221, s[88:89]\n"
"v_cndmask_b32_e64 v141, v141, v221, s[90:91]\n"
"v_cndmask_b32_e64 v142, v142, v221, s[92:93]\n"
"v_cndmask_b32_e64 v143, v143, v221, s[94:95]\n"
"L_amd40_%=:\n"
"s_branch L_amret38_%=\n"
"L_amask41_%=:\n"
"s_nop 7\n"
"s_nop 3\n"
"v_mov_b32 v221, 0xff800000\n"
"s_add_u32 s14, s12, 0\n"
"s_cmp_lt_u32 s14, s9\n"
"s_cbranch_scc1 L_amd44_%=\n"
"s_cmp_eq_u32 s14, s9\n"
"s_cbranch_scc1 L_atri43_%=\n"
"v_mov_b32 v64, v221\n"
"v_mov_b32 v65, v221\n"
"v_mov_b32 v66, v221\n"
"v_mov_b32 v67, v221\n"
"v_mov_b32 v68, v221\n"
"v_mov_b32 v69, v221\n"
"v_mov_b32 v70, v221\n"
"v_mov_b32 v71, v221\n"
"v_mov_b32 v72, v221\n"
"v_mov_b32 v73, v221\n"
"v_mov_b32 v74, v221\n"
"v_mov_b32 v75, v221\n"
"v_mov_b32 v76, v221\n"
"v_mov_b32 v77, v221\n"
"v_mov_b32 v78, v221\n"
"v_mov_b32 v79, v221\n"
"s_branch L_amd44_%=\n"
"L_atri43_%=:\n"
"v_cndmask_b32_e64 v64, v64, v221, s[64:65]\n"
"v_cndmask_b32_e64 v65, v65, v221, s[66:67]\n"
"v_cndmask_b32_e64 v66, v66, v221, s[68:69]\n"
"v_cndmask_b32_e64 v67, v67, v221, s[70:71]\n"
"v_cndmask_b32_e64 v68, v68, v221, s[72:73]\n"
"v_cndmask_b32_e64 v69, v69, v221, s[74:75]\n"
"v_cndmask_b32_e64 v70, v70, v221, s[76:77]\n"
"v_cndmask_b32_e64 v71, v71, v221, s[78:79]\n"
"v_cndmask_b32_e64 v72, v72, v221, s[80:81]\n"
"v_cndmask_b32_e64 v73, v73, v221, s[82:83]\n"
"v_cndmask_b32_e64 v74, v74, v221, s[84:85]\n"
"v_cndmask_b32_e64 v75, v75, v221, s[86:87]\n"
"v_cndmask_b32_e64 v76, v76, v221, s[88:89]\n"
"v_cndmask_b32_e64 v77, v77, v221, s[90:91]\n"
"v_cndmask_b32_e64 v78, v78, v221, s[92:93]\n"
"v_cndmask_b32_e64 v79, v79, v221, s[94:95]\n"
"L_amd44_%=:\n"
"s_add_u32 s14, s12, 1\n"
"s_cmp_lt_u32 s14, s9\n"
"s_cbranch_scc1 L_amd46_%=\n"
"s_cmp_eq_u32 s14, s9\n"
"s_cbranch_scc1 L_atri45_%=\n"
"v_mov_b32 v80, v221\n"
"v_mov_b32 v81, v221\n"
"v_mov_b32 v82, v221\n"
"v_mov_b32 v83, v221\n"
"v_mov_b32 v84, v221\n"
"v_mov_b32 v85, v221\n"
"v_mov_b32 v86, v221\n"
"v_mov_b32 v87, v221\n"
"v_mov_b32 v88, v221\n"
"v_mov_b32 v89, v221\n"
"v_mov_b32 v90, v221\n"
"v_mov_b32 v91, v221\n"
"v_mov_b32 v92, v221\n"
"v_mov_b32 v93, v221\n"
"v_mov_b32 v94, v221\n"
"v_mov_b32 v95, v221\n"
"s_branch L_amd46_%=\n"
"L_atri45_%=:\n"
"v_cndmask_b32_e64 v80, v80, v221, s[64:65]\n"
"v_cndmask_b32_e64 v81, v81, v221, s[66:67]\n"
"v_cndmask_b32_e64 v82, v82, v221, s[68:69]\n"
"v_cndmask_b32_e64 v83, v83, v221, s[70:71]\n"
"v_cndmask_b32_e64 v84, v84, v221, s[72:73]\n"
"v_cndmask_b32_e64 v85, v85, v221, s[74:75]\n"
"v_cndmask_b32_e64 v86, v86, v221, s[76:77]\n"
"v_cndmask_b32_e64 v87, v87, v221, s[78:79]\n"
"v_cndmask_b32_e64 v88, v88, v221, s[80:81]\n"
"v_cndmask_b32_e64 v89, v89, v221, s[82:83]\n"
"v_cndmask_b32_e64 v90, v90, v221, s[84:85]\n"
"v_cndmask_b32_e64 v91, v91, v221, s[86:87]\n"
"v_cndmask_b32_e64 v92, v92, v221, s[88:89]\n"
"v_cndmask_b32_e64 v93, v93, v221, s[90:91]\n"
"v_cndmask_b32_e64 v94, v94, v221, s[92:93]\n"
"v_cndmask_b32_e64 v95, v95, v221, s[94:95]\n"
"L_amd46_%=:\n"
"s_branch L_amret42_%=\n"
"L_aft47_%=:\n"
"s_mov_b32 s13, 0\n"
"s_branch L_fix_t1_%=\n"
"L_aft49_%=:\n"
"s_mov_b32 s13, 0\n"
"s_branch L_fix_o0_%=\n"
"L_amask51_%=:\n"
"s_nop 7\n"
"s_nop 3\n"
"v_mov_b32 v221, 0xff800000\n"
"s_add_u32 s14, s12, 0\n"
"s_cmp_lt_u32 s14, s9\n"
"s_cbranch_scc1 L_amd54_%=\n"
"s_cmp_eq_u32 s14, s9\n"
"s_cbranch_scc1 L_atri53_%=\n"
"v_mov_b32 v64, v221\n"
"v_mov_b32 v65, v221\n"
"v_mov_b32 v66, v221\n"
"v_mov_b32 v67, v221\n"
"v_mov_b32 v68, v221\n"
"v_mov_b32 v69, v221\n"
"v_mov_b32 v70, v221\n"
"v_mov_b32 v71, v221\n"
"v_mov_b32 v72, v221\n"
"v_mov_b32 v73, v221\n"
"v_mov_b32 v74, v221\n"
"v_mov_b32 v75, v221\n"
"v_mov_b32 v76, v221\n"
"v_mov_b32 v77, v221\n"
"v_mov_b32 v78, v221\n"
"v_mov_b32 v79, v221\n"
"s_branch L_amd54_%=\n"
"L_atri53_%=:\n"
"v_cndmask_b32_e64 v64, v64, v221, s[64:65]\n"
"v_cndmask_b32_e64 v65, v65, v221, s[66:67]\n"
"v_cndmask_b32_e64 v66, v66, v221, s[68:69]\n"
"v_cndmask_b32_e64 v67, v67, v221, s[70:71]\n"
"v_cndmask_b32_e64 v68, v68, v221, s[72:73]\n"
"v_cndmask_b32_e64 v69, v69, v221, s[74:75]\n"
"v_cndmask_b32_e64 v70, v70, v221, s[76:77]\n"
"v_cndmask_b32_e64 v71, v71, v221, s[78:79]\n"
"v_cndmask_b32_e64 v72, v72, v221, s[80:81]\n"
"v_cndmask_b32_e64 v73, v73, v221, s[82:83]\n"
"v_cndmask_b32_e64 v74, v74, v221, s[84:85]\n"
"v_cndmask_b32_e64 v75, v75, v221, s[86:87]\n"
"v_cndmask_b32_e64 v76, v76, v221, s[88:89]\n"
"v_cndmask_b32_e64 v77, v77, v221, s[90:91]\n"
"v_cndmask_b32_e64 v78, v78, v221, s[92:93]\n"
"v_cndmask_b32_e64 v79, v79, v221, s[94:95]\n"
"L_amd54_%=:\n"
"s_add_u32 s14, s12, 1\n"
"s_cmp_lt_u32 s14, s9\n"
"s_cbranch_scc1 L_amd56_%=\n"
"s_cmp_eq_u32 s14, s9\n"
"s_cbranch_scc1 L_atri55_%=\n"
"v_mov_b32 v80, v221\n"
"v_mov_b32 v81, v221\n"
"v_mov_b32 v82, v221\n"
"v_mov_b32 v83, v221\n"
"v_mov_b32 v84, v221\n"
"v_mov_b32 v85, v221\n"
"v_mov_b32 v86, v221\n"
"v_mov_b32 v87, v221\n"
"v_mov_b32 v88, v221\n"
"v_mov_b32 v89, v221\n"
"v_mov_b32 v90, v221\n"
"v_mov_b32 v91, v221\n"
"v_mov_b32 v92, v221\n"
"v_mov_b32 v93, v221\n"
"v_mov_b32 v94, v221\n"
"v_mov_b32 v95, v221\n"
"s_branch L_amd56_%=\n"
"L_atri55_%=:\n"
"v_cndmask_b32_e64 v80, v80, v221, s[64:65]\n"
"v_cndmask_b32_e64 v81, v81, v221, s[66:67]\n"
"v_cndmask_b32_e64 v82, v82, v221, s[68:69]\n"
"v_cndmask_b32_e64 v83, v83, v221, s[70:71]\n"
"v_cndmask_b32_e64 v84, v84, v221, s[72:73]\n"
"v_cndmask_b32_e64 v85, v85, v221, s[74:75]\n"
"v_cndmask_b32_e64 v86, v86, v221, s[76:77]\n"
"v_cndmask_b32_e64 v87, v87, v221, s[78:79]\n"
"v_cndmask_b32_e64 v88, v88, v221, s[80:81]\n"
"v_cndmask_b32_e64 v89, v89, v221, s[82:83]\n"
"v_cndmask_b32_e64 v90, v90, v221, s[84:85]\n"
"v_cndmask_b32_e64 v91, v91, v221, s[86:87]\n"
"v_cndmask_b32_e64 v92, v92, v221, s[88:89]\n"
"v_cndmask_b32_e64 v93, v93, v221, s[90:91]\n"
"v_cndmask_b32_e64 v94, v94, v221, s[92:93]\n"
"v_cndmask_b32_e64 v95, v95, v221, s[94:95]\n"
"L_amd56_%=:\n"
"s_branch L_amret52_%=\n"
"L_aft57_%=:\n"
"s_mov_b32 s13, 0\n"
"s_branch L_fix_o1_%=\n"
"L_aft59_%=:\n"
"s_mov_b32 s13, 1\n"
"s_branch L_fix_o0_%=\n"
"L_amask61_%=:\n"
"s_nop 7\n"
"s_nop 3\n"
"v_mov_b32 v221, 0xff800000\n"
"s_add_u32 s14, s12, 0\n"
"s_cmp_lt_u32 s14, s9\n"
"s_cbranch_scc1 L_amd64_%=\n"
"s_cmp_eq_u32 s14, s9\n"
"s_cbranch_scc1 L_atri63_%=\n"
"v_mov_b32 v128, v221\n"
"v_mov_b32 v129, v221\n"
"v_mov_b32 v130, v221\n"
"v_mov_b32 v131, v221\n"
"v_mov_b32 v132, v221\n"
"v_mov_b32 v133, v221\n"
"v_mov_b32 v134, v221\n"
"v_mov_b32 v135, v221\n"
"v_mov_b32 v136, v221\n"
"v_mov_b32 v137, v221\n"
"v_mov_b32 v138, v221\n"
"v_mov_b32 v139, v221\n"
"v_mov_b32 v140, v221\n"
"v_mov_b32 v141, v221\n"
"v_mov_b32 v142, v221\n"
"v_mov_b32 v143, v221\n"
"s_branch L_amd64_%=\n"
"L_atri63_%=:\n"
"v_cndmask_b32_e64 v128, v128, v221, s[64:65]\n"
"v_cndmask_b32_e64 v129, v129, v221, s[66:67]\n"
"v_cndmask_b32_e64 v130, v130, v221, s[68:69]\n"
"v_cndmask_b32_e64 v131, v131, v221, s[70:71]\n"
"v_cndmask_b32_e64 v132, v132, v221, s[72:73]\n"
"v_cndmask_b32_e64 v133, v133, v221, s[74:75]\n"
"v_cndmask_b32_e64 v134, v134, v221, s[76:77]\n"
"v_cndmask_b32_e64 v135, v135, v221, s[78:79]\n"
"v_cndmask_b32_e64 v136, v136, v221, s[80:81]\n"
"v_cndmask_b32_e64 v137, v137, v221, s[82:83]\n"
"v_cndmask_b32_e64 v138, v138, v221, s[84:85]\n"
"v_cndmask_b32_e64 v139, v139, v221, s[86:87]\n"
"v_cndmask_b32_e64 v140, v140, v221, s[88:89]\n"
"v_cndmask_b32_e64 v141, v141, v221, s[90:91]\n"
"v_cndmask_b32_e64 v142, v142, v221, s[92:93]\n"
"v_cndmask_b32_e64 v143, v143, v221, s[94:95]\n"
"L_amd64_%=:\n"
"s_add_u32 s14, s12, 1\n"
"s_cmp_lt_u32 s14, s9\n"
"s_cbranch_scc1 L_amd66_%=\n"
"s_cmp_eq_u32 s14, s9\n"
"s_cbranch_scc1 L_atri65_%=\n"
"v_mov_b32 v144, v221\n"
"v_mov_b32 v145, v221\n"
"v_mov_b32 v146, v221\n"
"v_mov_b32 v147, v221\n"
"v_mov_b32 v148, v221\n"
"v_mov_b32 v149, v221\n"
"v_mov_b32 v150, v221\n"
"v_mov_b32 v151, v221\n"
"v_mov_b32 v152, v221\n"
"v_mov_b32 v153, v221\n"
"v_mov_b32 v154, v221\n"
"v_mov_b32 v155, v221\n"
"v_mov_b32 v156, v221\n"
"v_mov_b32 v157, v221\n"
"v_mov_b32 v158, v221\n"
"v_mov_b32 v159, v221\n"
"s_branch L_amd66_%=\n"
"L_atri65_%=:\n"
"v_cndmask_b32_e64 v144, v144, v221, s[64:65]\n"
"v_cndmask_b32_e64 v145, v145, v221, s[66:67]\n"
"v_cndmask_b32_e64 v146, v146, v221, s[68:69]\n"
"v_cndmask_b32_e64 v147, v147, v221, s[70:71]\n"
"v_cndmask_b32_e64 v148, v148, v221, s[72:73]\n"
"v_cndmask_b32_e64 v149, v149, v221, s[74:75]\n"
"v_cndmask_b32_e64 v150, v150, v221, s[76:77]\n"
"v_cndmask_b32_e64 v151, v151, v221, s[78:79]\n"
"v_cndmask_b32_e64 v152, v152, v221, s[80:81]\n"
"v_cndmask_b32_e64 v153, v153, v221, s[82:83]\n"
"v_cndmask_b32_e64 v154, v154, v221, s[84:85]\n"
"v_cndmask_b32_e64 v155, v155, v221, s[86:87]\n"
"v_cndmask_b32_e64 v156, v156, v221, s[88:89]\n"
"v_cndmask_b32_e64 v157, v157, v221, s[90:91]\n"
"v_cndmask_b32_e64 v158, v158, v221, s[92:93]\n"
"v_cndmask_b32_e64 v159, v159, v221, s[94:95]\n"
"L_amd66_%=:\n"
"s_branch L_amret62_%=\n"
"L_aft67_%=:\n"
"s_mov_b32 s13, 1\n"
"s_branch L_fix_o1_%=\n"
"L_fix_o0_%=:\n"
"s_nop 7\n"
"s_nop 3\n"
"v_max3_f32 v222, v64, v65, v66\n"
"v_max3_f32 v222, v222, v67, v68\n"
"v_max3_f32 v222, v222, v69, v70\n"
"v_max3_f32 v222, v222, v71, v72\n"
"v_max3_f32 v222, v222, v73, v74\n"
"v_max3_f32 v222, v222, v75, v76\n"
"v_max3_f32 v222, v222, v77, v78\n"
"v_max3_f32 v222, v222, v79, v80\n"
"v_max3_f32 v222, v222, v81, v82\n"
"v_max3_f32 v222, v222, v83, v84\n"
"v_max3_f32 v222, v222, v85, v86\n"
"v_max3_f32 v222, v222, v87, v88\n"
"v_max3_f32 v222, v222, v89, v90\n"
"v_max3_f32 v222, v222, v91, v92\n"
"v_max3_f32 v222, v222, v93, v94\n"
"v_max_f32 v222, v222, v95\n"
"v_mov_b32 v224, v222\n"
"s_nop 1\n"
"v_permlane32_swap_b32 v222, v224\n"
"v_max_f32 v222, v222, v224\n"
"v_max_f32 v222, 0, v222\n"
"v_ceil_f32 v222, v222\n"
"v_cvt_i32_f32 v223, v222\n"
"v_sub_u32 v223, 0, v223\n"
"v_sub_f32 v64, v64, v222\n"
"v_sub_f32 v65, v65, v222\n"
"v_sub_f32 v66, v66, v222\n"
"v_sub_f32 v67, v67, v222\n"
"v_sub_f32 v68, v68, v222\n"
"v_sub_f32 v69, v69, v222\n"
"v_sub_f32 v70, v70, v222\n"
"v_sub_f32 v71, v71, v222\n"
"v_sub_f32 v72, v72, v222\n"
"v_sub_f32 v73, v73, v222\n"
"v_sub_f32 v74, v74, v222\n"
"v_sub_f32 v75, v75, v222\n"
"v_sub_f32 v76, v76, v222\n"
"v_sub_f32 v77, v77, v222\n"
"v_sub_f32 v78, v78, v222\n"
"v_sub_f32 v79, v79, v222\n"
"v_sub_f32 v128, v128, v222\n"
"v_sub_f32 v129, v129, v222\n"
"v_sub_f32 v130, v130, v222\n"
"v_sub_f32 v131, v131, v222\n"
"v_sub_f32 v132, v132, v222\n"
"v_sub_f32 v133, v133, v222\n"
"v_sub_f32 v134, v134, v222\n"
"v_sub_f32 v135, v135, v222\n"
"v_sub_f32 v136, v136, v222\n"
"v_sub_f32 v137, v137, v222\n"
"v_sub_f32 v138, v138, v222\n"
"v_sub_f32 v139, v139, v222\n"
"v_sub_f32 v140, v140, v222\n"
"v_sub_f32 v141, v141, v222\n"
"v_sub_f32 v142, v142, v222\n"
"v_sub_f32 v143, v143, v222\n"
"v_sub_f32 v80, v80, v222\n"
"v_sub_f32 v81, v81, v222\n"
"v_sub_f32 v82, v82, v222\n"
"v_sub_f32 v83, v83, v222\n"
"v_sub_f32 v84, v84, v222\n"
"v_sub_f32 v85, v85, v222\n"
"v_sub_f32 v86, v86, v222\n"
"v_sub_f32 v87, v87, v222\n"
"v_sub_f32 v88, v88, v222\n"
"v_sub_f32 v89, v89, v222\n"
"v_sub_f32 v90, v90, v222\n"
"v_sub_f32 v91, v91, v222\n"
"v_sub_f32 v92, v92, v222\n"
"v_sub_f32 v93, v93, v222\n"
"v_sub_f32 v94, v94, v222\n"
"v_sub_f32 v95, v95, v222\n"
"v_sub_f32 v144, v144, v222\n"
"v_sub_f32 v145, v145, v222\n"
"v_sub_f32 v146, v146, v222\n"
"v_sub_f32 v147, v147, v222\n"
"v_sub_f32 v148, v148, v222\n"
"v_sub_f32 v149, v149, v222\n"
"v_sub_f32 v150, v150, v222\n"
"v_sub_f32 v151, v151, v222\n"
"v_sub_f32 v152, v152, v222\n"
"v_sub_f32 v153, v153, v222\n"
"v_sub_f32 v154, v154, v222\n"
"v_sub_f32 v155, v155, v222\n"
"v_sub_f32 v156, v156, v222\n"
"v_sub_f32 v157, v157, v222\n"
"v_sub_f32 v158, v158, v222\n"
"v_sub_f32 v159, v159, v222\n"
"v_sub_f32 v192, v192, v222\n"
"v_sub_f32 v193, v193, v222\n"
"v_sub_f32 v194, v194, v222\n"
"v_sub_f32 v195, v195, v222\n"
"v_sub_f32 v196, v196, v222\n"
"v_sub_f32 v197, v197, v222\n"
"v_sub_f32 v198, v198, v222\n"
"v_sub_f32 v199, v199, v222\n"
"v_sub_f32 v200, v200, v222\n"
"v_sub_f32 v201, v201, v222\n"
"v_sub_f32 v202, v202, v222\n"
"v_sub_f32 v203, v203, v222\n"
"v_sub_f32 v204, v204, v222\n"
"v_sub_f32 v205, v205, v222\n"
"v_sub_f32 v206, v206, v222\n"
"v_sub_f32 v207, v207, v222\n"
"v_accvgpr_read_b32 v227, a64\n"
"v_ldexp_f32 v227, v227, v223\n"
"v_accvgpr_write_b32 a64, v227\n"
"v_accvgpr_read_b32 v228, a65\n"
"v_ldexp_f32 v228, v228, v223\n"
"v_accvgpr_write_b32 a65, v228\n"
"v_accvgpr_read_b32 v229, a66\n"
"v_ldexp_f32 v229, v229, v223\n"
"v_accvgpr_write_b32 a66, v229\n"
"v_accvgpr_read_b32 v230, a67\n"
"v_ldexp_f32 v230, v230, v223\n"
"v_accvgpr_write_b32 a67, v230\n"
"v_accvgpr_read_b32 v227, a68\n"
"v_ldexp_f32 v227, v227, v223\n"
"v_accvgpr_write_b32 a68, v227\n"
"v_accvgpr_read_b32 v228, a69\n"
"v_ldexp_f32 v228, v228, v223\n"
"v_accvgpr_write_b32 a69, v228\n"
"v_accvgpr_read_b32 v229, a70\n"
"v_ldexp_f32 v229, v229, v223\n"
"v_accvgpr_write_b32 a70, v229\n"
"v_accvgpr_read_b32 v230, a71\n"
"v_ldexp_f32 v230, v230, v223\n"
"v_accvgpr_write_b32 a71, v230\n"
"v_accvgpr_read_b32 v227, a72\n"
"v_ldexp_f32 v227, v227, v223\n"
"v_accvgpr_write_b32 a72, v227\n"
"v_accvgpr_read_b32 v228, a73\n"
"v_ldexp_f32 v228, v228, v223\n"
"v_accvgpr_write_b32 a73, v228\n"
"v_accvgpr_read_b32 v229, a74\n"
"v_ldexp_f32 v229, v229, v223\n"
"v_accvgpr_write_b32 a74, v229\n"
"v_accvgpr_read_b32 v230, a75\n"
"v_ldexp_f32 v230, v230, v223\n"
"v_accvgpr_write_b32 a75, v230\n"
"v_accvgpr_read_b32 v227, a76\n"
"v_ldexp_f32 v227, v227, v223\n"
"v_accvgpr_write_b32 a76, v227\n"
"v_accvgpr_read_b32 v228, a77\n"
"v_ldexp_f32 v228, v228, v223\n"
"v_accvgpr_write_b32 a77, v228\n"
"v_accvgpr_read_b32 v229, a78\n"
"v_ldexp_f32 v229, v229, v223\n"
"v_accvgpr_write_b32 a78, v229\n"
"v_accvgpr_read_b32 v230, a79\n"
"v_ldexp_f32 v230, v230, v223\n"
"v_accvgpr_write_b32 a79, v230\n"
"v_accvgpr_read_b32 v227, a80\n"
"v_ldexp_f32 v227, v227, v223\n"
"v_accvgpr_write_b32 a80, v227\n"
"v_accvgpr_read_b32 v228, a81\n"
"v_ldexp_f32 v228, v228, v223\n"
"v_accvgpr_write_b32 a81, v228\n"
"v_accvgpr_read_b32 v229, a82\n"
"v_ldexp_f32 v229, v229, v223\n"
"v_accvgpr_write_b32 a82, v229\n"
"v_accvgpr_read_b32 v230, a83\n"
"v_ldexp_f32 v230, v230, v223\n"
"v_accvgpr_write_b32 a83, v230\n"
"v_accvgpr_read_b32 v227, a84\n"
"v_ldexp_f32 v227, v227, v223\n"
"v_accvgpr_write_b32 a84, v227\n"
"v_accvgpr_read_b32 v228, a85\n"
"v_ldexp_f32 v228, v228, v223\n"
"v_accvgpr_write_b32 a85, v228\n"
"v_accvgpr_read_b32 v229, a86\n"
"v_ldexp_f32 v229, v229, v223\n"
"v_accvgpr_write_b32 a86, v229\n"
"v_accvgpr_read_b32 v230, a87\n"
"v_ldexp_f32 v230, v230, v223\n"
"v_accvgpr_write_b32 a87, v230\n"
"v_accvgpr_read_b32 v227, a88\n"
"v_ldexp_f32 v227, v227, v223\n"
"v_accvgpr_write_b32 a88, v227\n"
"v_accvgpr_read_b32 v228, a89\n"
"v_ldexp_f32 v228, v228, v223\n"
"v_accvgpr_write_b32 a89, v228\n"
"v_accvgpr_read_b32 v229, a90\n"
"v_ldexp_f32 v229, v229, v223\n"
"v_accvgpr_write_b32 a90, v229\n"
"v_accvgpr_read_b32 v230, a91\n"
"v_ldexp_f32 v230, v230, v223\n"
"v_accvgpr_write_b32 a91, v230\n"
"v_accvgpr_read_b32 v227, a92\n"
"v_ldexp_f32 v227, v227, v223\n"
"v_accvgpr_write_b32 a92, v227\n"
"v_accvgpr_read_b32 v228, a93\n"
"v_ldexp_f32 v228, v228, v223\n"
"v_accvgpr_write_b32 a93, v228\n"
"v_accvgpr_read_b32 v229, a94\n"
"v_ldexp_f32 v229, v229, v223\n"
"v_accvgpr_write_b32 a94, v229\n"
"v_accvgpr_read_b32 v230, a95\n"
"v_ldexp_f32 v230, v230, v223\n"
"v_accvgpr_write_b32 a95, v230\n"
"v_accvgpr_read_b32 v227, a96\n"
"v_ldexp_f32 v227, v227, v223\n"
"v_accvgpr_write_b32 a96, v227\n"
"v_accvgpr_read_b32 v228, a97\n"
"v_ldexp_f32 v228, v228, v223\n"
"v_accvgpr_write_b32 a97, v228\n"
"v_accvgpr_read_b32 v229, a98\n"
"v_ldexp_f32 v229, v229, v223\n"
"v_accvgpr_write_b32 a98, v229\n"
"v_accvgpr_read_b32 v230, a99\n"
"v_ldexp_f32 v230, v230, v223\n"
"v_accvgpr_write_b32 a99, v230\n"
"v_accvgpr_read_b32 v227, a100\n"
"v_ldexp_f32 v227, v227, v223\n"
"v_accvgpr_write_b32 a100, v227\n"
"v_accvgpr_read_b32 v228, a101\n"
"v_ldexp_f32 v228, v228, v223\n"
"v_accvgpr_write_b32 a101, v228\n"
"v_accvgpr_read_b32 v229, a102\n"
"v_ldexp_f32 v229, v229, v223\n"
"v_accvgpr_write_b32 a102, v229\n"
"v_accvgpr_read_b32 v230, a103\n"
"v_ldexp_f32 v230, v230, v223\n"
"v_accvgpr_write_b32 a103, v230\n"
"v_accvgpr_read_b32 v227, a104\n"
"v_ldexp_f32 v227, v227, v223\n"
"v_accvgpr_write_b32 a104, v227\n"
"v_accvgpr_read_b32 v228, a105\n"
"v_ldexp_f32 v228, v228, v223\n"
"v_accvgpr_write_b32 a105, v228\n"
"v_accvgpr_read_b32 v229, a106\n"
"v_ldexp_f32 v229, v229, v223\n"
"v_accvgpr_write_b32 a106, v229\n"
"v_accvgpr_read_b32 v230, a107\n"
"v_ldexp_f32 v230, v230, v223\n"
"v_accvgpr_write_b32 a107, v230\n"
"v_accvgpr_read_b32 v227, a108\n"
"v_ldexp_f32 v227, v227, v223\n"
"v_accvgpr_write_b32 a108, v227\n"
"v_accvgpr_read_b32 v228, a109\n"
"v_ldexp_f32 v228, v228, v223\n"
"v_accvgpr_write_b32 a109, v228\n"
"v_accvgpr_read_b32 v229, a110\n"
"v_ldexp_f32 v229, v229, v223\n"
"v_accvgpr_write_b32 a110, v229\n"
"v_accvgpr_read_b32 v230, a111\n"
"v_ldexp_f32 v230, v230, v223\n"
"v_accvgpr_write_b32 a111, v230\n"
"v_accvgpr_read_b32 v227, a112\n"
"v_ldexp_f32 v227, v227, v223\n"
"v_accvgpr_write_b32 a112, v227\n"
"v_accvgpr_read_b32 v228, a113\n"
"v_ldexp_f32 v228, v228, v223\n"
"v_accvgpr_write_b32 a113, v228\n"
"v_accvgpr_read_b32 v229, a114\n"
"v_ldexp_f32 v229, v229, v223\n"
"v_accvgpr_write_b32 a114, v229\n"
"v_accvgpr_read_b32 v230, a115\n"
"v_ldexp_f32 v230, v230, v223\n"
"v_accvgpr_write_b32 a115, v230\n"
"v_accvgpr_read_b32 v227, a116\n"
"v_ldexp_f32 v227, v227, v223\n"
"v_accvgpr_write_b32 a116, v227\n"
"v_accvgpr_read_b32 v228, a117\n"
"v_ldexp_f32 v228, v228, v223\n"
"v_accvgpr_write_b32 a117, v228\n"
"v_accvgpr_read_b32 v229, a118\n"
"v_ldexp_f32 v229, v229, v223\n"
"v_accvgpr_write_b32 a118, v229\n"
"v_accvgpr_read_b32 v230, a119\n"
"v_ldexp_f32 v230, v230, v223\n"
"v_accvgpr_write_b32 a119, v230\n"
"v_accvgpr_read_b32 v227, a120\n"
"v_ldexp_f32 v227, v227, v223\n"
"v_accvgpr_write_b32 a120, v227\n"
"v_accvgpr_read_b32 v228, a121\n"
"v_ldexp_f32 v228, v228, v223\n"
"v_accvgpr_write_b32 a121, v228\n"
"v_accvgpr_read_b32 v229, a122\n"
"v_ldexp_f32 v229, v229, v223\n"
"v_accvgpr_write_b32 a122, v229\n"
"v_accvgpr_read_b32 v230, a123\n"
"v_ldexp_f32 v230, v230, v223\n"
"v_accvgpr_write_b32 a123, v230\n"
"v_accvgpr_read_b32 v227, a124\n"
"v_ldexp_f32 v227, v227, v223\n"
"v_accvgpr_write_b32 a124, v227\n"
"v_accvgpr_read_b32 v228, a125\n"
"v_ldexp_f32 v228, v228, v223\n"
"v_accvgpr_write_b32 a125, v228\n"
"v_accvgpr_read_b32 v229, a126\n"
"v_ldexp_f32 v229, v229, v223\n"
"v_accvgpr_write_b32 a126, v229\n"
"v_accvgpr_read_b32 v230, a127\n"
"v_ldexp_f32 v230, v230, v223\n"
"v_accvgpr_write_b32 a127, v230\n"
"v_ldexp_f32 v210, v210, v223\n"
"v_exp_f32 v112, v64\n"
"v_exp_f32 v113, v65\n"
"v_exp_f32 v114, v66\n"
"v_exp_f32 v115, v67\n"
"v_exp_f32 v116, v68\n"
"v_exp_f32 v117, v69\n"
"v_exp_f32 v118, v70\n"
"v_exp_f32 v119, v71\n"
"v_exp_f32 v120, v72\n"
"v_exp_f32 v121, v73\n"
"v_exp_f32 v122, v74\n"
"v_exp_f32 v123, v75\n"
"v_exp_f32 v124, v76\n"
"v_exp_f32 v125, v77\n"
"v_exp_f32 v126, v78\n"
"v_exp_f32 v127, v79\n"
"s_nop 0\n"
"v_add_f32 v208, v112, v113\n"
"v_add_f32 v208, v208, v114\n"
"v_add_f32 v208, v208, v115\n"
"v_add_f32 v208, v208, v116\n"
"v_add_f32 v208, v208, v117\n"
"v_add_f32 v208, v208, v118\n"
"v_add_f32 v208, v208, v119\n"
"v_add_f32 v208, v208, v120\n"
"v_add_f32 v208, v208, v121\n"
"v_add_f32 v208, v208, v122\n"
"v_add_f32 v208, v208, v123\n"
"v_add_f32 v208, v208, v124\n"
"v_add_f32 v208, v208, v125\n"
"v_add_f32 v208, v208, v126\n"
"v_add_f32 v208, v208, v127\n"
"v_cvt_pk_bf16_f32 v96, v112, v113\n"
"v_cvt_pk_bf16_f32 v97, v114, v115\n"
"v_cvt_pk_bf16_f32 v98, v116, v117\n"
"v_cvt_pk_bf16_f32 v99, v118, v119\n"
"v_cvt_pk_bf16_f32 v100, v120, v121\n"
"v_cvt_pk_bf16_f32 v101, v122, v123\n"
"v_cvt_pk_bf16_f32 v102, v124, v125\n"
"v_cvt_pk_bf16_f32 v103, v126, v127\n"
"v_ldexp_f32 v211, v211, v223\n"
"v_exp_f32 v112, v80\n"
"v_exp_f32 v113, v81\n"
"v_exp_f32 v114, v82\n"
"v_exp_f32 v115, v83\n"
"v_exp_f32 v116, v84\n"
"v_exp_f32 v117, v85\n"
"v_exp_f32 v118, v86\n"
"v_exp_f32 v119, v87\n"
"v_exp_f32 v120, v88\n"
"v_exp_f32 v121, v89\n"
"v_exp_f32 v122, v90\n"
"v_exp_f32 v123, v91\n"
"v_exp_f32 v124, v92\n"
"v_exp_f32 v125, v93\n"
"v_exp_f32 v126, v94\n"
"v_exp_f32 v127, v95\n"
"s_nop 0\n"
"v_add_f32 v209, v112, v113\n"
"v_add_f32 v209, v209, v114\n"
"v_add_f32 v209, v209, v115\n"
"v_add_f32 v209, v209, v116\n"
"v_add_f32 v209, v209, v117\n"
"v_add_f32 v209, v209, v118\n"
"v_add_f32 v209, v209, v119\n"
"v_add_f32 v209, v209, v120\n"
"v_add_f32 v209, v209, v121\n"
"v_add_f32 v209, v209, v122\n"
"v_add_f32 v209, v209, v123\n"
"v_add_f32 v209, v209, v124\n"
"v_add_f32 v209, v209, v125\n"
"v_add_f32 v209, v209, v126\n"
"v_add_f32 v209, v209, v127\n"
"v_cvt_pk_bf16_f32 v104, v112, v113\n"
"v_cvt_pk_bf16_f32 v105, v114, v115\n"
"v_cvt_pk_bf16_f32 v106, v116, v117\n"
"v_cvt_pk_bf16_f32 v107, v118, v119\n"
"v_cvt_pk_bf16_f32 v108, v120, v121\n"
"v_cvt_pk_bf16_f32 v109, v122, v123\n"
"v_cvt_pk_bf16_f32 v110, v124, v125\n"
"v_cvt_pk_bf16_f32 v111, v126, v127\n"
"s_nop 1\n"
"s_cmp_eq_u32 s13, 0\n"
"s_cbranch_scc1 L_afr50_%=\n"
"s_branch L_afr60_%=\n"
"L_fix_o1_%=:\n"
"s_nop 7\n"
"s_nop 3\n"
"v_max3_f32 v222, v128, v129, v130\n"
"v_max3_f32 v222, v222, v131, v132\n"
"v_max3_f32 v222, v222, v133, v134\n"
"v_max3_f32 v222, v222, v135, v136\n"
"v_max3_f32 v222, v222, v137, v138\n"
"v_max3_f32 v222, v222, v139, v140\n"
"v_max3_f32 v222, v222, v141, v142\n"
"v_max3_f32 v222, v222, v143, v144\n"
"v_max3_f32 v222, v222, v145, v146\n"
"v_max3_f32 v222, v222, v147, v148\n"
"v_max3_f32 v222, v222, v149, v150\n"
"v_max3_f32 v222, v222, v151, v152\n"
"v_max3_f32 v222, v222, v153, v154\n"
"v_max3_f32 v222, v222, v155, v156\n"
"v_max3_f32 v222, v222, v157, v158\n"
"v_max_f32 v222, v222, v159\n"
"v_mov_b32 v224, v222\n"
"s_nop 1\n"
"v_permlane32_swap_b32 v222, v224\n"
"v_max_f32 v222, v222, v224\n"
"v_max_f32 v222, 0, v222\n"
"v_ceil_f32 v222, v222\n"
"v_cvt_i32_f32 v223, v222\n"
"v_sub_u32 v223, 0, v223\n"
"v_sub_f32 v128, v128, v222\n"
"v_sub_f32 v129, v129, v222\n"
"v_sub_f32 v130, v130, v222\n"
"v_sub_f32 v131, v131, v222\n"
"v_sub_f32 v132, v132, v222\n"
"v_sub_f32 v133, v133, v222\n"
"v_sub_f32 v134, v134, v222\n"
"v_sub_f32 v135, v135, v222\n"
"v_sub_f32 v136, v136, v222\n"
"v_sub_f32 v137, v137, v222\n"
"v_sub_f32 v138, v138, v222\n"
"v_sub_f32 v139, v139, v222\n"
"v_sub_f32 v140, v140, v222\n"
"v_sub_f32 v141, v141, v222\n"
"v_sub_f32 v142, v142, v222\n"
"v_sub_f32 v143, v143, v222\n"
"v_sub_f32 v64, v64, v222\n"
"v_sub_f32 v65, v65, v222\n"
"v_sub_f32 v66, v66, v222\n"
"v_sub_f32 v67, v67, v222\n"
"v_sub_f32 v68, v68, v222\n"
"v_sub_f32 v69, v69, v222\n"
"v_sub_f32 v70, v70, v222\n"
"v_sub_f32 v71, v71, v222\n"
"v_sub_f32 v72, v72, v222\n"
"v_sub_f32 v73, v73, v222\n"
"v_sub_f32 v74, v74, v222\n"
"v_sub_f32 v75, v75, v222\n"
"v_sub_f32 v76, v76, v222\n"
"v_sub_f32 v77, v77, v222\n"
"v_sub_f32 v78, v78, v222\n"
"v_sub_f32 v79, v79, v222\n"
"v_sub_f32 v144, v144, v222\n"
"v_sub_f32 v145, v145, v222\n"
"v_sub_f32 v146, v146, v222\n"
"v_sub_f32 v147, v147, v222\n"
"v_sub_f32 v148, v148, v222\n"
"v_sub_f32 v149, v149, v222\n"
"v_sub_f32 v150, v150, v222\n"
"v_sub_f32 v151, v151, v222\n"
"v_sub_f32 v152, v152, v222\n"
"v_sub_f32 v153, v153, v222\n"
"v_sub_f32 v154, v154, v222\n"
"v_sub_f32 v155, v155, v222\n"
"v_sub_f32 v156, v156, v222\n"
"v_sub_f32 v157, v157, v222\n"
"v_sub_f32 v158, v158, v222\n"
"v_sub_f32 v159, v159, v222\n"
"v_sub_f32 v80, v80, v222\n"
"v_sub_f32 v81, v81, v222\n"
"v_sub_f32 v82, v82, v222\n"
"v_sub_f32 v83, v83, v222\n"
"v_sub_f32 v84, v84, v222\n"
"v_sub_f32 v85, v85, v222\n"
"v_sub_f32 v86, v86, v222\n"
"v_sub_f32 v87, v87, v222\n"
"v_sub_f32 v88, v88, v222\n"
"v_sub_f32 v89, v89, v222\n"
"v_sub_f32 v90, v90, v222\n"
"v_sub_f32 v91, v91, v222\n"
"v_sub_f32 v92, v92, v222\n"
"v_sub_f32 v93, v93, v222\n"
"v_sub_f32 v94, v94, v222\n"
"v_sub_f32 v95, v95, v222\n"
"v_sub_f32 v192, v192, v222\n"
"v_sub_f32 v193, v193, v222\n"
"v_sub_f32 v194, v194, v222\n"
"v_sub_f32 v195, v195, v222\n"
"v_sub_f32 v196, v196, v222\n"
"v_sub_f32 v197, v197, v222\n"
"v_sub_f32 v198, v198, v222\n"
"v_sub_f32 v199, v199, v222\n"
"v_sub_f32 v200, v200, v222\n"
"v_sub_f32 v201, v201, v222\n"
"v_sub_f32 v202, v202, v222\n"
"v_sub_f32 v203, v203, v222\n"
"v_sub_f32 v204, v204, v222\n"
"v_sub_f32 v205, v205, v222\n"
"v_sub_f32 v206, v206, v222\n"
"v_sub_f32 v207, v207, v222\n"
"v_accvgpr_read_b32 v227, a64\n"
"v_ldexp_f32 v227, v227, v223\n"
"v_accvgpr_write_b32 a64, v227\n"
"v_accvgpr_read_b32 v228, a65\n"
"v_ldexp_f32 v228, v228, v223\n"
"v_accvgpr_write_b32 a65, v228\n"
"v_accvgpr_read_b32 v229, a66\n"
"v_ldexp_f32 v229, v229, v223\n"
"v_accvgpr_write_b32 a66, v229\n"
"v_accvgpr_read_b32 v230, a67\n"
"v_ldexp_f32 v230, v230, v223\n"
"v_accvgpr_write_b32 a67, v230\n"
"v_accvgpr_read_b32 v227, a68\n"
"v_ldexp_f32 v227, v227, v223\n"
"v_accvgpr_write_b32 a68, v227\n"
"v_accvgpr_read_b32 v228, a69\n"
"v_ldexp_f32 v228, v228, v223\n"
"v_accvgpr_write_b32 a69, v228\n"
"v_accvgpr_read_b32 v229, a70\n"
"v_ldexp_f32 v229, v229, v223\n"
"v_accvgpr_write_b32 a70, v229\n"
"v_accvgpr_read_b32 v230, a71\n"
"v_ldexp_f32 v230, v230, v223\n"
"v_accvgpr_write_b32 a71, v230\n"
"v_accvgpr_read_b32 v227, a72\n"
"v_ldexp_f32 v227, v227, v223\n"
"v_accvgpr_write_b32 a72, v227\n"
"v_accvgpr_read_b32 v228, a73\n"
"v_ldexp_f32 v228, v228, v223\n"
"v_accvgpr_write_b32 a73, v228\n"
"v_accvgpr_read_b32 v229, a74\n"
"v_ldexp_f32 v229, v229, v223\n"
"v_accvgpr_write_b32 a74, v229\n"
"v_accvgpr_read_b32 v230, a75\n"
"v_ldexp_f32 v230, v230, v223\n"
"v_accvgpr_write_b32 a75, v230\n"
"v_accvgpr_read_b32 v227, a76\n"
"v_ldexp_f32 v227, v227, v223\n"
"v_accvgpr_write_b32 a76, v227\n"
"v_accvgpr_read_b32 v228, a77\n"
"v_ldexp_f32 v228, v228, v223\n"
"v_accvgpr_write_b32 a77, v228\n"
"v_accvgpr_read_b32 v229, a78\n"
"v_ldexp_f32 v229, v229, v223\n"
"v_accvgpr_write_b32 a78, v229\n"
"v_accvgpr_read_b32 v230, a79\n"
"v_ldexp_f32 v230, v230, v223\n"
"v_accvgpr_write_b32 a79, v230\n"
"v_accvgpr_read_b32 v227, a80\n"
"v_ldexp_f32 v227, v227, v223\n"
"v_accvgpr_write_b32 a80, v227\n"
"v_accvgpr_read_b32 v228, a81\n"
"v_ldexp_f32 v228, v228, v223\n"
"v_accvgpr_write_b32 a81, v228\n"
"v_accvgpr_read_b32 v229, a82\n"
"v_ldexp_f32 v229, v229, v223\n"
"v_accvgpr_write_b32 a82, v229\n"
"v_accvgpr_read_b32 v230, a83\n"
"v_ldexp_f32 v230, v230, v223\n"
"v_accvgpr_write_b32 a83, v230\n"
"v_accvgpr_read_b32 v227, a84\n"
"v_ldexp_f32 v227, v227, v223\n"
"v_accvgpr_write_b32 a84, v227\n"
"v_accvgpr_read_b32 v228, a85\n"
"v_ldexp_f32 v228, v228, v223\n"
"v_accvgpr_write_b32 a85, v228\n"
"v_accvgpr_read_b32 v229, a86\n"
"v_ldexp_f32 v229, v229, v223\n"
"v_accvgpr_write_b32 a86, v229\n"
"v_accvgpr_read_b32 v230, a87\n"
"v_ldexp_f32 v230, v230, v223\n"
"v_accvgpr_write_b32 a87, v230\n"
"v_accvgpr_read_b32 v227, a88\n"
"v_ldexp_f32 v227, v227, v223\n"
"v_accvgpr_write_b32 a88, v227\n"
"v_accvgpr_read_b32 v228, a89\n"
"v_ldexp_f32 v228, v228, v223\n"
"v_accvgpr_write_b32 a89, v228\n"
"v_accvgpr_read_b32 v229, a90\n"
"v_ldexp_f32 v229, v229, v223\n"
"v_accvgpr_write_b32 a90, v229\n"
"v_accvgpr_read_b32 v230, a91\n"
"v_ldexp_f32 v230, v230, v223\n"
"v_accvgpr_write_b32 a91, v230\n"
"v_accvgpr_read_b32 v227, a92\n"
"v_ldexp_f32 v227, v227, v223\n"
"v_accvgpr_write_b32 a92, v227\n"
"v_accvgpr_read_b32 v228, a93\n"
"v_ldexp_f32 v228, v228, v223\n"
"v_accvgpr_write_b32 a93, v228\n"
"v_accvgpr_read_b32 v229, a94\n"
"v_ldexp_f32 v229, v229, v223\n"
"v_accvgpr_write_b32 a94, v229\n"
"v_accvgpr_read_b32 v230, a95\n"
"v_ldexp_f32 v230, v230, v223\n"
"v_accvgpr_write_b32 a95, v230\n"
"v_accvgpr_read_b32 v227, a96\n"
"v_ldexp_f32 v227, v227, v223\n"
"v_accvgpr_write_b32 a96, v227\n"
"v_accvgpr_read_b32 v228, a97\n"
"v_ldexp_f32 v228, v228, v223\n"
"v_accvgpr_write_b32 a97, v228\n"
"v_accvgpr_read_b32 v229, a98\n"
"v_ldexp_f32 v229, v229, v223\n"
"v_accvgpr_write_b32 a98, v229\n"
"v_accvgpr_read_b32 v230, a99\n"
"v_ldexp_f32 v230, v230, v223\n"
"v_accvgpr_write_b32 a99, v230\n"
"v_accvgpr_read_b32 v227, a100\n"
"v_ldexp_f32 v227, v227, v223\n"
"v_accvgpr_write_b32 a100, v227\n"
"v_accvgpr_read_b32 v228, a101\n"
"v_ldexp_f32 v228, v228, v223\n"
"v_accvgpr_write_b32 a101, v228\n"
"v_accvgpr_read_b32 v229, a102\n"
"v_ldexp_f32 v229, v229, v223\n"
"v_accvgpr_write_b32 a102, v229\n"
"v_accvgpr_read_b32 v230, a103\n"
"v_ldexp_f32 v230, v230, v223\n"
"v_accvgpr_write_b32 a103, v230\n"
"v_accvgpr_read_b32 v227, a104\n"
"v_ldexp_f32 v227, v227, v223\n"
"v_accvgpr_write_b32 a104, v227\n"
"v_accvgpr_read_b32 v228, a105\n"
"v_ldexp_f32 v228, v228, v223\n"
"v_accvgpr_write_b32 a105, v228\n"
"v_accvgpr_read_b32 v229, a106\n"
"v_ldexp_f32 v229, v229, v223\n"
"v_accvgpr_write_b32 a106, v229\n"
"v_accvgpr_read_b32 v230, a107\n"
"v_ldexp_f32 v230, v230, v223\n"
"v_accvgpr_write_b32 a107, v230\n"
"v_accvgpr_read_b32 v227, a108\n"
"v_ldexp_f32 v227, v227, v223\n"
"v_accvgpr_write_b32 a108, v227\n"
"v_accvgpr_read_b32 v228, a109\n"
"v_ldexp_f32 v228, v228, v223\n"
"v_accvgpr_write_b32 a109, v228\n"
"v_accvgpr_read_b32 v229, a110\n"
"v_ldexp_f32 v229, v229, v223\n"
"v_accvgpr_write_b32 a110, v229\n"
"v_accvgpr_read_b32 v230, a111\n"
"v_ldexp_f32 v230, v230, v223\n"
"v_accvgpr_write_b32 a111, v230\n"
"v_accvgpr_read_b32 v227, a112\n"
"v_ldexp_f32 v227, v227, v223\n"
"v_accvgpr_write_b32 a112, v227\n"
"v_accvgpr_read_b32 v228, a113\n"
"v_ldexp_f32 v228, v228, v223\n"
"v_accvgpr_write_b32 a113, v228\n"
"v_accvgpr_read_b32 v229, a114\n"
"v_ldexp_f32 v229, v229, v223\n"
"v_accvgpr_write_b32 a114, v229\n"
"v_accvgpr_read_b32 v230, a115\n"
"v_ldexp_f32 v230, v230, v223\n"
"v_accvgpr_write_b32 a115, v230\n"
"v_accvgpr_read_b32 v227, a116\n"
"v_ldexp_f32 v227, v227, v223\n"
"v_accvgpr_write_b32 a116, v227\n"
"v_accvgpr_read_b32 v228, a117\n"
"v_ldexp_f32 v228, v228, v223\n"
"v_accvgpr_write_b32 a117, v228\n"
"v_accvgpr_read_b32 v229, a118\n"
"v_ldexp_f32 v229, v229, v223\n"
"v_accvgpr_write_b32 a118, v229\n"
"v_accvgpr_read_b32 v230, a119\n"
"v_ldexp_f32 v230, v230, v223\n"
"v_accvgpr_write_b32 a119, v230\n"
"v_accvgpr_read_b32 v227, a120\n"
"v_ldexp_f32 v227, v227, v223\n"
"v_accvgpr_write_b32 a120, v227\n"
"v_accvgpr_read_b32 v228, a121\n"
"v_ldexp_f32 v228, v228, v223\n"
"v_accvgpr_write_b32 a121, v228\n"
"v_accvgpr_read_b32 v229, a122\n"
"v_ldexp_f32 v229, v229, v223\n"
"v_accvgpr_write_b32 a122, v229\n"
"v_accvgpr_read_b32 v230, a123\n"
"v_ldexp_f32 v230, v230, v223\n"
"v_accvgpr_write_b32 a123, v230\n"
"v_accvgpr_read_b32 v227, a124\n"
"v_ldexp_f32 v227, v227, v223\n"
"v_accvgpr_write_b32 a124, v227\n"
"v_accvgpr_read_b32 v228, a125\n"
"v_ldexp_f32 v228, v228, v223\n"
"v_accvgpr_write_b32 a125, v228\n"
"v_accvgpr_read_b32 v229, a126\n"
"v_ldexp_f32 v229, v229, v223\n"
"v_accvgpr_write_b32 a126, v229\n"
"v_accvgpr_read_b32 v230, a127\n"
"v_ldexp_f32 v230, v230, v223\n"
"v_accvgpr_write_b32 a127, v230\n"
"v_ldexp_f32 v210, v210, v223\n"
"v_exp_f32 v112, v128\n"
"v_exp_f32 v113, v129\n"
"v_exp_f32 v114, v130\n"
"v_exp_f32 v115, v131\n"
"v_exp_f32 v116, v132\n"
"v_exp_f32 v117, v133\n"
"v_exp_f32 v118, v134\n"
"v_exp_f32 v119, v135\n"
"v_exp_f32 v120, v136\n"
"v_exp_f32 v121, v137\n"
"v_exp_f32 v122, v138\n"
"v_exp_f32 v123, v139\n"
"v_exp_f32 v124, v140\n"
"v_exp_f32 v125, v141\n"
"v_exp_f32 v126, v142\n"
"v_exp_f32 v127, v143\n"
"s_nop 0\n"
"v_add_f32 v208, v112, v113\n"
"v_add_f32 v208, v208, v114\n"
"v_add_f32 v208, v208, v115\n"
"v_add_f32 v208, v208, v116\n"
"v_add_f32 v208, v208, v117\n"
"v_add_f32 v208, v208, v118\n"
"v_add_f32 v208, v208, v119\n"
"v_add_f32 v208, v208, v120\n"
"v_add_f32 v208, v208, v121\n"
"v_add_f32 v208, v208, v122\n"
"v_add_f32 v208, v208, v123\n"
"v_add_f32 v208, v208, v124\n"
"v_add_f32 v208, v208, v125\n"
"v_add_f32 v208, v208, v126\n"
"v_add_f32 v208, v208, v127\n"
"v_cvt_pk_bf16_f32 v160, v112, v113\n"
"v_cvt_pk_bf16_f32 v161, v114, v115\n"
"v_cvt_pk_bf16_f32 v162, v116, v117\n"
"v_cvt_pk_bf16_f32 v163, v118, v119\n"
"v_cvt_pk_bf16_f32 v164, v120, v121\n"
"v_cvt_pk_bf16_f32 v165, v122, v123\n"
"v_cvt_pk_bf16_f32 v166, v124, v125\n"
"v_cvt_pk_bf16_f32 v167, v126, v127\n"
"v_ldexp_f32 v211, v211, v223\n"
"v_exp_f32 v112, v144\n"
"v_exp_f32 v113, v145\n"
"v_exp_f32 v114, v146\n"
"v_exp_f32 v115, v147\n"
"v_exp_f32 v116, v148\n"
"v_exp_f32 v117, v149\n"
"v_exp_f32 v118, v150\n"
"v_exp_f32 v119, v151\n"
"v_exp_f32 v120, v152\n"
"v_exp_f32 v121, v153\n"
"v_exp_f32 v122, v154\n"
"v_exp_f32 v123, v155\n"
"v_exp_f32 v124, v156\n"
"v_exp_f32 v125, v157\n"
"v_exp_f32 v126, v158\n"
"v_exp_f32 v127, v159\n"
"s_nop 0\n"
"v_add_f32 v209, v112, v113\n"
"v_add_f32 v209, v209, v114\n"
"v_add_f32 v209, v209, v115\n"
"v_add_f32 v209, v209, v116\n"
"v_add_f32 v209, v209, v117\n"
"v_add_f32 v209, v209, v118\n"
"v_add_f32 v209, v209, v119\n"
"v_add_f32 v209, v209, v120\n"
"v_add_f32 v209, v209, v121\n"
"v_add_f32 v209, v209, v122\n"
"v_add_f32 v209, v209, v123\n"
"v_add_f32 v209, v209, v124\n"
"v_add_f32 v209, v209, v125\n"
"v_add_f32 v209, v209, v126\n"
"v_add_f32 v209, v209, v127\n"
"v_cvt_pk_bf16_f32 v168, v112, v113\n"
"v_cvt_pk_bf16_f32 v169, v114, v115\n"
"v_cvt_pk_bf16_f32 v170, v116, v117\n"
"v_cvt_pk_bf16_f32 v171, v118, v119\n"
"v_cvt_pk_bf16_f32 v172, v120, v121\n"
"v_cvt_pk_bf16_f32 v173, v122, v123\n"
"v_cvt_pk_bf16_f32 v174, v124, v125\n"
"v_cvt_pk_bf16_f32 v175, v126, v127\n"
"s_nop 1\n"
"s_cmp_eq_u32 s13, 0\n"
"s_cbranch_scc1 L_afr58_%=\n"
"s_branch L_afr68_%=\n"
"L_fix_s0_%=:\n"
"s_nop 7\n"
"s_nop 3\n"
"v_max3_f32 v222, v64, v65, v66\n"
"v_max3_f32 v222, v222, v67, v68\n"
"v_max3_f32 v222, v222, v69, v70\n"
"v_max3_f32 v222, v222, v71, v72\n"
"v_max3_f32 v222, v222, v73, v74\n"
"v_max3_f32 v222, v222, v75, v76\n"
"v_max3_f32 v222, v222, v77, v78\n"
"v_max_f32 v222, v222, v79\n"
"v_mov_b32 v224, v222\n"
"s_nop 1\n"
"v_permlane32_swap_b32 v222, v224\n"
"v_max_f32 v222, v222, v224\n"
"v_max_f32 v222, 0, v222\n"
"v_ceil_f32 v222, v222\n"
"v_cvt_i32_f32 v223, v222\n"
"v_sub_u32 v223, 0, v223\n"
"v_sub_f32 v64, v64, v222\n"
"v_sub_f32 v65, v65, v222\n"
"v_sub_f32 v66, v66, v222\n"
"v_sub_f32 v67, v67, v222\n"
"v_sub_f32 v68, v68, v222\n"
"v_sub_f32 v69, v69, v222\n"
"v_sub_f32 v70, v70, v222\n"
"v_sub_f32 v71, v71, v222\n"
"v_sub_f32 v72, v72, v222\n"
"v_sub_f32 v73, v73, v222\n"
"v_sub_f32 v74, v74, v222\n"
"v_sub_f32 v75, v75, v222\n"
"v_sub_f32 v76, v76, v222\n"
"v_sub_f32 v77, v77, v222\n"
"v_sub_f32 v78, v78, v222\n"
"v_sub_f32 v79, v79, v222\n"
"v_sub_f32 v128, v128, v222\n"
"v_sub_f32 v129, v129, v222\n"
"v_sub_f32 v130, v130, v222\n"
"v_sub_f32 v131, v131, v222\n"
"v_sub_f32 v132, v132, v222\n"
"v_sub_f32 v133, v133, v222\n"
"v_sub_f32 v134, v134, v222\n"
"v_sub_f32 v135, v135, v222\n"
"v_sub_f32 v136, v136, v222\n"
"v_sub_f32 v137, v137, v222\n"
"v_sub_f32 v138, v138, v222\n"
"v_sub_f32 v139, v139, v222\n"
"v_sub_f32 v140, v140, v222\n"
"v_sub_f32 v141, v141, v222\n"
"v_sub_f32 v142, v142, v222\n"
"v_sub_f32 v143, v143, v222\n"
"v_sub_f32 v176, v176, v222\n"
"v_sub_f32 v177, v177, v222\n"
"v_sub_f32 v178, v178, v222\n"
"v_sub_f32 v179, v179, v222\n"
"v_sub_f32 v180, v180, v222\n"
"v_sub_f32 v181, v181, v222\n"
"v_sub_f32 v182, v182, v222\n"
"v_sub_f32 v183, v183, v222\n"
"v_sub_f32 v184, v184, v222\n"
"v_sub_f32 v185, v185, v222\n"
"v_sub_f32 v186, v186, v222\n"
"v_sub_f32 v187, v187, v222\n"
"v_sub_f32 v188, v188, v222\n"
"v_sub_f32 v189, v189, v222\n"
"v_sub_f32 v190, v190, v222\n"
"v_sub_f32 v191, v191, v222\n"
"v_accvgpr_read_b32 v227, a0\n"
"v_ldexp_f32 v227, v227, v223\n"
"v_accvgpr_write_b32 a0, v227\n"
"v_accvgpr_read_b32 v228, a1\n"
"v_ldexp_f32 v228, v228, v223\n"
"v_accvgpr_write_b32 a1, v228\n"
"v_accvgpr_read_b32 v229, a2\n"
"v_ldexp_f32 v229, v229, v223\n"
"v_accvgpr_write_b32 a2, v229\n"
"v_accvgpr_read_b32 v230, a3\n"
"v_ldexp_f32 v230, v230, v223\n"
"v_accvgpr_write_b32 a3, v230\n"
"v_accvgpr_read_b32 v227, a4\n"
"v_ldexp_f32 v227, v227, v223\n"
"v_accvgpr_write_b32 a4, v227\n"
"v_accvgpr_read_b32 v228, a5\n"
"v_ldexp_f32 v228, v228, v223\n"
"v_accvgpr_write_b32 a5, v228\n"
"v_accvgpr_read_b32 v229, a6\n"
"v_ldexp_f32 v229, v229, v223\n"
"v_accvgpr_write_b32 a6, v229\n"
"v_accvgpr_read_b32 v230, a7\n"
"v_ldexp_f32 v230, v230, v223\n"
"v_accvgpr_write_b32 a7, v230\n"
"v_accvgpr_read_b32 v227, a8\n"
"v_ldexp_f32 v227, v227, v223\n"
"v_accvgpr_write_b32 a8, v227\n"
"v_accvgpr_read_b32 v228, a9\n"
"v_ldexp_f32 v228, v228, v223\n"
"v_accvgpr_write_b32 a9, v228\n"
"v_accvgpr_read_b32 v229, a10\n"
"v_ldexp_f32 v229, v229, v223\n"
"v_accvgpr_write_b32 a10, v229\n"
"v_accvgpr_read_b32 v230, a11\n"
"v_ldexp_f32 v230, v230, v223\n"
"v_accvgpr_write_b32 a11, v230\n"
"v_accvgpr_read_b32 v227, a12\n"
"v_ldexp_f32 v227, v227, v223\n"
"v_accvgpr_write_b32 a12, v227\n"
"v_accvgpr_read_b32 v228, a13\n"
"v_ldexp_f32 v228, v228, v223\n"
"v_accvgpr_write_b32 a13, v228\n"
"v_accvgpr_read_b32 v229, a14\n"
"v_ldexp_f32 v229, v229, v223\n"
"v_accvgpr_write_b32 a14, v229\n"
"v_accvgpr_read_b32 v230, a15\n"
"v_ldexp_f32 v230, v230, v223\n"
"v_accvgpr_write_b32 a15, v230\n"
"v_accvgpr_read_b32 v227, a16\n"
"v_ldexp_f32 v227, v227, v223\n"
"v_accvgpr_write_b32 a16, v227\n"
"v_accvgpr_read_b32 v228, a17\n"
"v_ldexp_f32 v228, v228, v223\n"
"v_accvgpr_write_b32 a17, v228\n"
"v_accvgpr_read_b32 v229, a18\n"
"v_ldexp_f32 v229, v229, v223\n"
"v_accvgpr_write_b32 a18, v229\n"
"v_accvgpr_read_b32 v230, a19\n"
"v_ldexp_f32 v230, v230, v223\n"
"v_accvgpr_write_b32 a19, v230\n"
"v_accvgpr_read_b32 v227, a20\n"
"v_ldexp_f32 v227, v227, v223\n"
"v_accvgpr_write_b32 a20, v227\n"
"v_accvgpr_read_b32 v228, a21\n"
"v_ldexp_f32 v228, v228, v223\n"
"v_accvgpr_write_b32 a21, v228\n"
"v_accvgpr_read_b32 v229, a22\n"
"v_ldexp_f32 v229, v229, v223\n"
"v_accvgpr_write_b32 a22, v229\n"
"v_accvgpr_read_b32 v230, a23\n"
"v_ldexp_f32 v230, v230, v223\n"
"v_accvgpr_write_b32 a23, v230\n"
"v_accvgpr_read_b32 v227, a24\n"
"v_ldexp_f32 v227, v227, v223\n"
"v_accvgpr_write_b32 a24, v227\n"
"v_accvgpr_read_b32 v228, a25\n"
"v_ldexp_f32 v228, v228, v223\n"
"v_accvgpr_write_b32 a25, v228\n"
"v_accvgpr_read_b32 v229, a26\n"
"v_ldexp_f32 v229, v229, v223\n"
"v_accvgpr_write_b32 a26, v229\n"
"v_accvgpr_read_b32 v230, a27\n"
"v_ldexp_f32 v230, v230, v223\n"
"v_accvgpr_write_b32 a27, v230\n"
"v_accvgpr_read_b32 v227, a28\n"
"v_ldexp_f32 v227, v227, v223\n"
"v_accvgpr_write_b32 a28, v227\n"
"v_accvgpr_read_b32 v228, a29\n"
"v_ldexp_f32 v228, v228, v223\n"
"v_accvgpr_write_b32 a29, v228\n"
"v_accvgpr_read_b32 v229, a30\n"
"v_ldexp_f32 v229, v229, v223\n"
"v_accvgpr_write_b32 a30, v229\n"
"v_accvgpr_read_b32 v230, a31\n"
"v_ldexp_f32 v230, v230, v223\n"
"v_accvgpr_write_b32 a31, v230\n"
"v_accvgpr_read_b32 v227, a32\n"
"v_ldexp_f32 v227, v227, v223\n"
"v_accvgpr_write_b32 a32, v227\n"
"v_accvgpr_read_b32 v228, a33\n"
"v_ldexp_f32 v228, v228, v223\n"
"v_accvgpr_write_b32 a33, v228\n"
"v_accvgpr_read_b32 v229, a34\n"
"v_ldexp_f32 v229, v229, v223\n"
"v_accvgpr_write_b32 a34, v229\n"
"v_accvgpr_read_b32 v230, a35\n"
"v_ldexp_f32 v230, v230, v223\n"
"v_accvgpr_write_b32 a35, v230\n"
"v_accvgpr_read_b32 v227, a36\n"
"v_ldexp_f32 v227, v227, v223\n"
"v_accvgpr_write_b32 a36, v227\n"
"v_accvgpr_read_b32 v228, a37\n"
"v_ldexp_f32 v228, v228, v223\n"
"v_accvgpr_write_b32 a37, v228\n"
"v_accvgpr_read_b32 v229, a38\n"
"v_ldexp_f32 v229, v229, v223\n"
"v_accvgpr_write_b32 a38, v229\n"
"v_accvgpr_read_b32 v230, a39\n"
"v_ldexp_f32 v230, v230, v223\n"
"v_accvgpr_write_b32 a39, v230\n"
"v_accvgpr_read_b32 v227, a40\n"
"v_ldexp_f32 v227, v227, v223\n"
"v_accvgpr_write_b32 a40, v227\n"
"v_accvgpr_read_b32 v228, a41\n"
"v_ldexp_f32 v228, v228, v223\n"
"v_accvgpr_write_b32 a41, v228\n"
"v_accvgpr_read_b32 v229, a42\n"
"v_ldexp_f32 v229, v229, v223\n"
"v_accvgpr_write_b32 a42, v229\n"
"v_accvgpr_read_b32 v230, a43\n"
"v_ldexp_f32 v230, v230, v223\n"
"v_accvgpr_write_b32 a43, v230\n"
"v_accvgpr_read_b32 v227, a44\n"
"v_ldexp_f32 v227, v227, v223\n"
"v_accvgpr_write_b32 a44, v227\n"
"v_accvgpr_read_b32 v228, a45\n"
"v_ldexp_f32 v228, v228, v223\n"
"v_accvgpr_write_b32 a45, v228\n"
"v_accvgpr_read_b32 v229, a46\n"
"v_ldexp_f32 v229, v229, v223\n"
"v_accvgpr_write_b32 a46, v229\n"
"v_accvgpr_read_b32 v230, a47\n"
"v_ldexp_f32 v230, v230, v223\n"
"v_accvgpr_write_b32 a47, v230\n"
"v_accvgpr_read_b32 v227, a48\n"
"v_ldexp_f32 v227, v227, v223\n"
"v_accvgpr_write_b32 a48, v227\n"
"v_accvgpr_read_b32 v228, a49\n"
"v_ldexp_f32 v228, v228, v223\n"
"v_accvgpr_write_b32 a49, v228\n"
"v_accvgpr_read_b32 v229, a50\n"
"v_ldexp_f32 v229, v229, v223\n"
"v_accvgpr_write_b32 a50, v229\n"
"v_accvgpr_read_b32 v230, a51\n"
"v_ldexp_f32 v230, v230, v223\n"
"v_accvgpr_write_b32 a51, v230\n"
"v_accvgpr_read_b32 v227, a52\n"
"v_ldexp_f32 v227, v227, v223\n"
"v_accvgpr_write_b32 a52, v227\n"
"v_accvgpr_read_b32 v228, a53\n"
"v_ldexp_f32 v228, v228, v223\n"
"v_accvgpr_write_b32 a53, v228\n"
"v_accvgpr_read_b32 v229, a54\n"
"v_ldexp_f32 v229, v229, v223\n"
"v_accvgpr_write_b32 a54, v229\n"
"v_accvgpr_read_b32 v230, a55\n"
"v_ldexp_f32 v230, v230, v223\n"
"v_accvgpr_write_b32 a55, v230\n"
"v_accvgpr_read_b32 v227, a56\n"
"v_ldexp_f32 v227, v227, v223\n"
"v_accvgpr_write_b32 a56, v227\n"
"v_accvgpr_read_b32 v228, a57\n"
"v_ldexp_f32 v228, v228, v223\n"
"v_accvgpr_write_b32 a57, v228\n"
"v_accvgpr_read_b32 v229, a58\n"
"v_ldexp_f32 v229, v229, v223\n"
"v_accvgpr_write_b32 a58, v229\n"
"v_accvgpr_read_b32 v230, a59\n"
"v_ldexp_f32 v230, v230, v223\n"
"v_accvgpr_write_b32 a59, v230\n"
"v_accvgpr_read_b32 v227, a60\n"
"v_ldexp_f32 v227, v227, v223\n"
"v_accvgpr_write_b32 a60, v227\n"
"v_accvgpr_read_b32 v228, a61\n"
"v_ldexp_f32 v228, v228, v223\n"
"v_accvgpr_write_b32 a61, v228\n"
"v_accvgpr_read_b32 v229, a62\n"
"v_ldexp_f32 v229, v229, v223\n"
"v_accvgpr_write_b32 a62, v229\n"
"v_accvgpr_read_b32 v230, a63\n"
"v_ldexp_f32 v230, v230, v223\n"
"v_accvgpr_write_b32 a63, v230\n"
"v_ldexp_f32 v210, v210, v223\n"
"v_exp_f32 v112, v64\n"
"v_exp_f32 v113, v65\n"
"v_exp_f32 v114, v66\n"
"v_exp_f32 v115, v67\n"
"v_exp_f32 v116, v68\n"
"v_exp_f32 v117, v69\n"
"v_exp_f32 v118, v70\n"
"v_exp_f32 v119, v71\n"
"v_exp_f32 v120, v72\n"
"v_exp_f32 v121, v73\n"
"v_exp_f32 v122, v74\n"
"v_exp_f32 v123, v75\n"
"v_exp_f32 v124, v76\n"
"v_exp_f32 v125, v77\n"
"v_exp_f32 v126, v78\n"
"v_exp_f32 v127, v79\n"
"s_nop 0\n"
"v_add_f32 v208, v112, v113\n"
"v_add_f32 v208, v208, v114\n"
"v_add_f32 v208, v208, v115\n"
"v_add_f32 v208, v208, v116\n"
"v_add_f32 v208, v208, v117\n"
"v_add_f32 v208, v208, v118\n"
"v_add_f32 v208, v208, v119\n"
"v_add_f32 v208, v208, v120\n"
"v_add_f32 v208, v208, v121\n"
"v_add_f32 v208, v208, v122\n"
"v_add_f32 v208, v208, v123\n"
"v_add_f32 v208, v208, v124\n"
"v_add_f32 v208, v208, v125\n"
"v_add_f32 v208, v208, v126\n"
"v_add_f32 v208, v208, v127\n"
"v_cvt_pk_bf16_f32 v96, v112, v113\n"
"v_cvt_pk_bf16_f32 v97, v114, v115\n"
"v_cvt_pk_bf16_f32 v98, v116, v117\n"
"v_cvt_pk_bf16_f32 v99, v118, v119\n"
"v_cvt_pk_bf16_f32 v100, v120, v121\n"
"v_cvt_pk_bf16_f32 v101, v122, v123\n"
"v_cvt_pk_bf16_f32 v102, v124, v125\n"
"v_cvt_pk_bf16_f32 v103, v126, v127\n"
"v_max3_f32 v222, v80, v81, v82\n"
"v_max3_f32 v222, v222, v83, v84\n"
"v_max3_f32 v222, v222, v85, v86\n"
"v_max3_f32 v222, v222, v87, v88\n"
"v_max3_f32 v222, v222, v89, v90\n"
"v_max3_f32 v222, v222, v91, v92\n"
"v_max3_f32 v222, v222, v93, v94\n"
"v_max_f32 v222, v222, v95\n"
"v_mov_b32 v224, v222\n"
"s_nop 1\n"
"v_permlane32_swap_b32 v222, v224\n"
"v_max_f32 v222, v222, v224\n"
"v_max_f32 v222, 0, v222\n"
"v_ceil_f32 v222, v222\n"
"v_cvt_i32_f32 v223, v222\n"
"v_sub_u32 v223, 0, v223\n"
"v_sub_f32 v80, v80, v222\n"
"v_sub_f32 v81, v81, v222\n"
"v_sub_f32 v82, v82, v222\n"
"v_sub_f32 v83, v83, v222\n"
"v_sub_f32 v84, v84, v222\n"
"v_sub_f32 v85, v85, v222\n"
"v_sub_f32 v86, v86, v222\n"
"v_sub_f32 v87, v87, v222\n"
"v_sub_f32 v88, v88, v222\n"
"v_sub_f32 v89, v89, v222\n"
"v_sub_f32 v90, v90, v222\n"
"v_sub_f32 v91, v91, v222\n"
"v_sub_f32 v92, v92, v222\n"
"v_sub_f32 v93, v93, v222\n"
"v_sub_f32 v94, v94, v222\n"
"v_sub_f32 v95, v95, v222\n"
"v_sub_f32 v144, v144, v222\n"
"v_sub_f32 v145, v145, v222\n"
"v_sub_f32 v146, v146, v222\n"
"v_sub_f32 v147, v147, v222\n"
"v_sub_f32 v148, v148, v222\n"
"v_sub_f32 v149, v149, v222\n"
"v_sub_f32 v150, v150, v222\n"
"v_sub_f32 v151, v151, v222\n"
"v_sub_f32 v152, v152, v222\n"
"v_sub_f32 v153, v153, v222\n"
"v_sub_f32 v154, v154, v222\n"
"v_sub_f32 v155, v155, v222\n"
"v_sub_f32 v156, v156, v222\n"
"v_sub_f32 v157, v157, v222\n"
"v_sub_f32 v158, v158, v222\n"
"v_sub_f32 v159, v159, v222\n"
"v_sub_f32 v192, v192, v222\n"
"v_sub_f32 v193, v193, v222\n"
"v_sub_f32 v194, v194, v222\n"
"v_sub_f32 v195, v195, v222\n"
"v_sub_f32 v196, v196, v222\n"
"v_sub_f32 v197, v197, v222\n"
"v_sub_f32 v198, v198, v222\n"
"v_sub_f32 v199, v199, v222\n"
"v_sub_f32 v200, v200, v222\n"
"v_sub_f32 v201, v201, v222\n"
"v_sub_f32 v202, v202, v222\n"
"v_sub_f32 v203, v203, v222\n"
"v_sub_f32 v204, v204, v222\n"
"v_sub_f32 v205, v205, v222\n"
"v_sub_f32 v206, v206, v222\n"
"v_sub_f32 v207, v207, v222\n"
"v_accvgpr_read_b32 v227, a64\n"
"v_ldexp_f32 v227, v227, v223\n"
"v_accvgpr_write_b32 a64, v227\n"
"v_accvgpr_read_b32 v228, a65\n"
"v_ldexp_f32 v228, v228, v223\n"
"v_accvgpr_write_b32 a65, v228\n"
"v_accvgpr_read_b32 v229, a66\n"
"v_ldexp_f32 v229, v229, v223\n"
"v_accvgpr_write_b32 a66, v229\n"
"v_accvgpr_read_b32 v230, a67\n"
"v_ldexp_f32 v230, v230, v223\n"
"v_accvgpr_write_b32 a67, v230\n"
"v_accvgpr_read_b32 v227, a68\n"
"v_ldexp_f32 v227, v227, v223\n"
"v_accvgpr_write_b32 a68, v227\n"
"v_accvgpr_read_b32 v228, a69\n"
"v_ldexp_f32 v228, v228, v223\n"
"v_accvgpr_write_b32 a69, v228\n"
"v_accvgpr_read_b32 v229, a70\n"
"v_ldexp_f32 v229, v229, v223\n"
"v_accvgpr_write_b32 a70, v229\n"
"v_accvgpr_read_b32 v230, a71\n"
"v_ldexp_f32 v230, v230, v223\n"
"v_accvgpr_write_b32 a71, v230\n"
"v_accvgpr_read_b32 v227, a72\n"
"v_ldexp_f32 v227, v227, v223\n"
"v_accvgpr_write_b32 a72, v227\n"
"v_accvgpr_read_b32 v228, a73\n"
"v_ldexp_f32 v228, v228, v223\n"
"v_accvgpr_write_b32 a73, v228\n"
"v_accvgpr_read_b32 v229, a74\n"
"v_ldexp_f32 v229, v229, v223\n"
"v_accvgpr_write_b32 a74, v229\n"
"v_accvgpr_read_b32 v230, a75\n"
"v_ldexp_f32 v230, v230, v223\n"
"v_accvgpr_write_b32 a75, v230\n"
"v_accvgpr_read_b32 v227, a76\n"
"v_ldexp_f32 v227, v227, v223\n"
"v_accvgpr_write_b32 a76, v227\n"
"v_accvgpr_read_b32 v228, a77\n"
"v_ldexp_f32 v228, v228, v223\n"
"v_accvgpr_write_b32 a77, v228\n"
"v_accvgpr_read_b32 v229, a78\n"
"v_ldexp_f32 v229, v229, v223\n"
"v_accvgpr_write_b32 a78, v229\n"
"v_accvgpr_read_b32 v230, a79\n"
"v_ldexp_f32 v230, v230, v223\n"
"v_accvgpr_write_b32 a79, v230\n"
"v_accvgpr_read_b32 v227, a80\n"
"v_ldexp_f32 v227, v227, v223\n"
"v_accvgpr_write_b32 a80, v227\n"
"v_accvgpr_read_b32 v228, a81\n"
"v_ldexp_f32 v228, v228, v223\n"
"v_accvgpr_write_b32 a81, v228\n"
"v_accvgpr_read_b32 v229, a82\n"
"v_ldexp_f32 v229, v229, v223\n"
"v_accvgpr_write_b32 a82, v229\n"
"v_accvgpr_read_b32 v230, a83\n"
"v_ldexp_f32 v230, v230, v223\n"
"v_accvgpr_write_b32 a83, v230\n"
"v_accvgpr_read_b32 v227, a84\n"
"v_ldexp_f32 v227, v227, v223\n"
"v_accvgpr_write_b32 a84, v227\n"
"v_accvgpr_read_b32 v228, a85\n"
"v_ldexp_f32 v228, v228, v223\n"
"v_accvgpr_write_b32 a85, v228\n"
"v_accvgpr_read_b32 v229, a86\n"
"v_ldexp_f32 v229, v229, v223\n"
"v_accvgpr_write_b32 a86, v229\n"
"v_accvgpr_read_b32 v230, a87\n"
"v_ldexp_f32 v230, v230, v223\n"
"v_accvgpr_write_b32 a87, v230\n"
"v_accvgpr_read_b32 v227, a88\n"
"v_ldexp_f32 v227, v227, v223\n"
"v_accvgpr_write_b32 a88, v227\n"
"v_accvgpr_read_b32 v228, a89\n"
"v_ldexp_f32 v228, v228, v223\n"
"v_accvgpr_write_b32 a89, v228\n"
"v_accvgpr_read_b32 v229, a90\n"
"v_ldexp_f32 v229, v229, v223\n"
"v_accvgpr_write_b32 a90, v229\n"
"v_accvgpr_read_b32 v230, a91\n"
"v_ldexp_f32 v230, v230, v223\n"
"v_accvgpr_write_b32 a91, v230\n"
"v_accvgpr_read_b32 v227, a92\n"
"v_ldexp_f32 v227, v227, v223\n"
"v_accvgpr_write_b32 a92, v227\n"
"v_accvgpr_read_b32 v228, a93\n"
"v_ldexp_f32 v228, v228, v223\n"
"v_accvgpr_write_b32 a93, v228\n"
"v_accvgpr_read_b32 v229, a94\n"
"v_ldexp_f32 v229, v229, v223\n"
"v_accvgpr_write_b32 a94, v229\n"
"v_accvgpr_read_b32 v230, a95\n"
"v_ldexp_f32 v230, v230, v223\n"
"v_accvgpr_write_b32 a95, v230\n"
"v_accvgpr_read_b32 v227, a96\n"
"v_ldexp_f32 v227, v227, v223\n"
"v_accvgpr_write_b32 a96, v227\n"
"v_accvgpr_read_b32 v228, a97\n"
"v_ldexp_f32 v228, v228, v223\n"
"v_accvgpr_write_b32 a97, v228\n"
"v_accvgpr_read_b32 v229, a98\n"
"v_ldexp_f32 v229, v229, v223\n"
"v_accvgpr_write_b32 a98, v229\n"
"v_accvgpr_read_b32 v230, a99\n"
"v_ldexp_f32 v230, v230, v223\n"
"v_accvgpr_write_b32 a99, v230\n"
"v_accvgpr_read_b32 v227, a100\n"
"v_ldexp_f32 v227, v227, v223\n"
"v_accvgpr_write_b32 a100, v227\n"
"v_accvgpr_read_b32 v228, a101\n"
"v_ldexp_f32 v228, v228, v223\n"
"v_accvgpr_write_b32 a101, v228\n"
"v_accvgpr_read_b32 v229, a102\n"
"v_ldexp_f32 v229, v229, v223\n"
"v_accvgpr_write_b32 a102, v229\n"
"v_accvgpr_read_b32 v230, a103\n"
"v_ldexp_f32 v230, v230, v223\n"
"v_accvgpr_write_b32 a103, v230\n"
"v_accvgpr_read_b32 v227, a104\n"
"v_ldexp_f32 v227, v227, v223\n"
"v_accvgpr_write_b32 a104, v227\n"
"v_accvgpr_read_b32 v228, a105\n"
"v_ldexp_f32 v228, v228, v223\n"
"v_accvgpr_write_b32 a105, v228\n"
"v_accvgpr_read_b32 v229, a106\n"
"v_ldexp_f32 v229, v229, v223\n"
"v_accvgpr_write_b32 a106, v229\n"
"v_accvgpr_read_b32 v230, a107\n"
"v_ldexp_f32 v230, v230, v223\n"
"v_accvgpr_write_b32 a107, v230\n"
"v_accvgpr_read_b32 v227, a108\n"
"v_ldexp_f32 v227, v227, v223\n"
"v_accvgpr_write_b32 a108, v227\n"
"v_accvgpr_read_b32 v228, a109\n"
"v_ldexp_f32 v228, v228, v223\n"
"v_accvgpr_write_b32 a109, v228\n"
"v_accvgpr_read_b32 v229, a110\n"
"v_ldexp_f32 v229, v229, v223\n"
"v_accvgpr_write_b32 a110, v229\n"
"v_accvgpr_read_b32 v230, a111\n"
"v_ldexp_f32 v230, v230, v223\n"
"v_accvgpr_write_b32 a111, v230\n"
"v_accvgpr_read_b32 v227, a112\n"
"v_ldexp_f32 v227, v227, v223\n"
"v_accvgpr_write_b32 a112, v227\n"
"v_accvgpr_read_b32 v228, a113\n"
"v_ldexp_f32 v228, v228, v223\n"
"v_accvgpr_write_b32 a113, v228\n"
"v_accvgpr_read_b32 v229, a114\n"
"v_ldexp_f32 v229, v229, v223\n"
"v_accvgpr_write_b32 a114, v229\n"
"v_accvgpr_read_b32 v230, a115\n"
"v_ldexp_f32 v230, v230, v223\n"
"v_accvgpr_write_b32 a115, v230\n"
"v_accvgpr_read_b32 v227, a116\n"
"v_ldexp_f32 v227, v227, v223\n"
"v_accvgpr_write_b32 a116, v227\n"
"v_accvgpr_read_b32 v228, a117\n"
"v_ldexp_f32 v228, v228, v223\n"
"v_accvgpr_write_b32 a117, v228\n"
"v_accvgpr_read_b32 v229, a118\n"
"v_ldexp_f32 v229, v229, v223\n"
"v_accvgpr_write_b32 a118, v229\n"
"v_accvgpr_read_b32 v230, a119\n"
"v_ldexp_f32 v230, v230, v223\n"
"v_accvgpr_write_b32 a119, v230\n"
"v_accvgpr_read_b32 v227, a120\n"
"v_ldexp_f32 v227, v227, v223\n"
"v_accvgpr_write_b32 a120, v227\n"
"v_accvgpr_read_b32 v228, a121\n"
"v_ldexp_f32 v228, v228, v223\n"
"v_accvgpr_write_b32 a121, v228\n"
"v_accvgpr_read_b32 v229, a122\n"
"v_ldexp_f32 v229, v229, v223\n"
"v_accvgpr_write_b32 a122, v229\n"
"v_accvgpr_read_b32 v230, a123\n"
"v_ldexp_f32 v230, v230, v223\n"
"v_accvgpr_write_b32 a123, v230\n"
"v_accvgpr_read_b32 v227, a124\n"
"v_ldexp_f32 v227, v227, v223\n"
"v_accvgpr_write_b32 a124, v227\n"
"v_accvgpr_read_b32 v228, a125\n"
"v_ldexp_f32 v228, v228, v223\n"
"v_accvgpr_write_b32 a125, v228\n"
"v_accvgpr_read_b32 v229, a126\n"
"v_ldexp_f32 v229, v229, v223\n"
"v_accvgpr_write_b32 a126, v229\n"
"v_accvgpr_read_b32 v230, a127\n"
"v_ldexp_f32 v230, v230, v223\n"
"v_accvgpr_write_b32 a127, v230\n"
"v_ldexp_f32 v211, v211, v223\n"
"v_exp_f32 v112, v80\n"
"v_exp_f32 v113, v81\n"
"v_exp_f32 v114, v82\n"
"v_exp_f32 v115, v83\n"
"v_exp_f32 v116, v84\n"
"v_exp_f32 v117, v85\n"
"v_exp_f32 v118, v86\n"
"v_exp_f32 v119, v87\n"
"v_exp_f32 v120, v88\n"
"v_exp_f32 v121, v89\n"
"v_exp_f32 v122, v90\n"
"v_exp_f32 v123, v91\n"
"v_exp_f32 v124, v92\n"
"v_exp_f32 v125, v93\n"
"v_exp_f32 v126, v94\n"
"v_exp_f32 v127, v95\n"
"s_nop 0\n"
"v_add_f32 v209, v112, v113\n"
"v_add_f32 v209, v209, v114\n"
"v_add_f32 v209, v209, v115\n"
"v_add_f32 v209, v209, v116\n"
"v_add_f32 v209, v209, v117\n"
"v_add_f32 v209, v209, v118\n"
"v_add_f32 v209, v209, v119\n"
"v_add_f32 v209, v209, v120\n"
"v_add_f32 v209, v209, v121\n"
"v_add_f32 v209, v209, v122\n"
"v_add_f32 v209, v209, v123\n"
"v_add_f32 v209, v209, v124\n"
"v_add_f32 v209, v209, v125\n"
"v_add_f32 v209, v209, v126\n"
"v_add_f32 v209, v209, v127\n"
"v_cvt_pk_bf16_f32 v104, v112, v113\n"
"v_cvt_pk_bf16_f32 v105, v114, v115\n"
"v_cvt_pk_bf16_f32 v106, v116, v117\n"
"v_cvt_pk_bf16_f32 v107, v118, v119\n"
"v_cvt_pk_bf16_f32 v108, v120, v121\n"
"v_cvt_pk_bf16_f32 v109, v122, v123\n"
"v_cvt_pk_bf16_f32 v110, v124, v125\n"
"v_cvt_pk_bf16_f32 v111, v126, v127\n"
"s_nop 1\n"
"s_cmp_eq_u32 s13, 0\n"
"s_cbranch_scc1 L_afr16_%=\n"
"s_cmp_eq_u32 s13, 1\n"
"s_cbranch_scc1 L_afr24_%=\n"
"s_branch L_afr36_%=\n"
"L_fix_s1_%=:\n"
"s_nop 7\n"
"s_nop 3\n"
"v_max3_f32 v222, v128, v129, v130\n"
"v_max3_f32 v222, v222, v131, v132\n"
"v_max3_f32 v222, v222, v133, v134\n"
"v_max3_f32 v222, v222, v135, v136\n"
"v_max3_f32 v222, v222, v137, v138\n"
"v_max3_f32 v222, v222, v139, v140\n"
"v_max3_f32 v222, v222, v141, v142\n"
"v_max_f32 v222, v222, v143\n"
"v_mov_b32 v224, v222\n"
"s_nop 1\n"
"v_permlane32_swap_b32 v222, v224\n"
"v_max_f32 v222, v222, v224\n"
"v_max_f32 v222, 0, v222\n"
"v_ceil_f32 v222, v222\n"
"v_cvt_i32_f32 v223, v222\n"
"v_sub_u32 v223, 0, v223\n"
"v_sub_f32 v128, v128, v222\n"
"v_sub_f32 v129, v129, v222\n"
"v_sub_f32 v130, v130, v222\n"
"v_sub_f32 v131, v131, v222\n"
"v_sub_f32 v132, v132, v222\n"
"v_sub_f32 v133, v133, v222\n"
"v_sub_f32 v134, v134, v222\n"
"v_sub_f32 v135, v135, v222\n"
"v_sub_f32 v136, v136, v222\n"
"v_sub_f32 v137, v137, v222\n"
"v_sub_f32 v138, v138, v222\n"
"v_sub_f32 v139, v139, v222\n"
"v_sub_f32 v140, v140, v222\n"
"v_sub_f32 v141, v141, v222\n"
"v_sub_f32 v142, v142, v222\n"
"v_sub_f32 v143, v143, v222\n"
"v_sub_f32 v64, v64, v222\n"
"v_sub_f32 v65, v65, v222\n"
"v_sub_f32 v66, v66, v222\n"
"v_sub_f32 v67, v67, v222\n"
"v_sub_f32 v68, v68, v222\n"
"v_sub_f32 v69, v69, v222\n"
"v_sub_f32 v70, v70, v222\n"
"v_sub_f32 v71, v71, v222\n"
"v_sub_f32 v72, v72, v222\n"
"v_sub_f32 v73, v73, v222\n"
"v_sub_f32 v74, v74, v222\n"
"v_sub_f32 v75, v75, v222\n"
"v_sub_f32 v76, v76, v222\n"
"v_sub_f32 v77, v77, v222\n"
"v_sub_f32 v78, v78, v222\n"
"v_sub_f32 v79, v79, v222\n"
"v_sub_f32 v176, v176, v222\n"
"v_sub_f32 v177, v177, v222\n"
"v_sub_f32 v178, v178, v222\n"
"v_sub_f32 v179, v179, v222\n"
"v_sub_f32 v180, v180, v222\n"
"v_sub_f32 v181, v181, v222\n"
"v_sub_f32 v182, v182, v222\n"
"v_sub_f32 v183, v183, v222\n"
"v_sub_f32 v184, v184, v222\n"
"v_sub_f32 v185, v185, v222\n"
"v_sub_f32 v186, v186, v222\n"
"v_sub_f32 v187, v187, v222\n"
"v_sub_f32 v188, v188, v222\n"
"v_sub_f32 v189, v189, v222\n"
"v_sub_f32 v190, v190, v222\n"
"v_sub_f32 v191, v191, v222\n"
"v_accvgpr_read_b32 v227, a0\n"
"v_ldexp_f32 v227, v227, v223\n"
"v_accvgpr_write_b32 a0, v227\n"
"v_accvgpr_read_b32 v228, a1\n"
"v_ldexp_f32 v228, v228, v223\n"
"v_accvgpr_write_b32 a1, v228\n"
"v_accvgpr_read_b32 v229, a2\n"
"v_ldexp_f32 v229, v229, v223\n"
"v_accvgpr_write_b32 a2, v229\n"
"v_accvgpr_read_b32 v230, a3\n"
"v_ldexp_f32 v230, v230, v223\n"
"v_accvgpr_write_b32 a3, v230\n"
"v_accvgpr_read_b32 v227, a4\n"
"v_ldexp_f32 v227, v227, v223\n"
"v_accvgpr_write_b32 a4, v227\n"
"v_accvgpr_read_b32 v228, a5\n"
"v_ldexp_f32 v228, v228, v223\n"
"v_accvgpr_write_b32 a5, v228\n"
"v_accvgpr_read_b32 v229, a6\n"
"v_ldexp_f32 v229, v229, v223\n"
"v_accvgpr_write_b32 a6, v229\n"
"v_accvgpr_read_b32 v230, a7\n"
"v_ldexp_f32 v230, v230, v223\n"
"v_accvgpr_write_b32 a7, v230\n"
"v_accvgpr_read_b32 v227, a8\n"
"v_ldexp_f32 v227, v227, v223\n"
"v_accvgpr_write_b32 a8, v227\n"
"v_accvgpr_read_b32 v228, a9\n"
"v_ldexp_f32 v228, v228, v223\n"
"v_accvgpr_write_b32 a9, v228\n"
"v_accvgpr_read_b32 v229, a10\n"
"v_ldexp_f32 v229, v229, v223\n"
"v_accvgpr_write_b32 a10, v229\n"
"v_accvgpr_read_b32 v230, a11\n"
"v_ldexp_f32 v230, v230, v223\n"
"v_accvgpr_write_b32 a11, v230\n"
"v_accvgpr_read_b32 v227, a12\n"
"v_ldexp_f32 v227, v227, v223\n"
"v_accvgpr_write_b32 a12, v227\n"
"v_accvgpr_read_b32 v228, a13\n"
"v_ldexp_f32 v228, v228, v223\n"
"v_accvgpr_write_b32 a13, v228\n"
"v_accvgpr_read_b32 v229, a14\n"
"v_ldexp_f32 v229, v229, v223\n"
"v_accvgpr_write_b32 a14, v229\n"
"v_accvgpr_read_b32 v230, a15\n"
"v_ldexp_f32 v230, v230, v223\n"
"v_accvgpr_write_b32 a15, v230\n"
"v_accvgpr_read_b32 v227, a16\n"
"v_ldexp_f32 v227, v227, v223\n"
"v_accvgpr_write_b32 a16, v227\n"
"v_accvgpr_read_b32 v228, a17\n"
"v_ldexp_f32 v228, v228, v223\n"
"v_accvgpr_write_b32 a17, v228\n"
"v_accvgpr_read_b32 v229, a18\n"
"v_ldexp_f32 v229, v229, v223\n"
"v_accvgpr_write_b32 a18, v229\n"
"v_accvgpr_read_b32 v230, a19\n"
"v_ldexp_f32 v230, v230, v223\n"
"v_accvgpr_write_b32 a19, v230\n"
"v_accvgpr_read_b32 v227, a20\n"
"v_ldexp_f32 v227, v227, v223\n"
"v_accvgpr_write_b32 a20, v227\n"
"v_accvgpr_read_b32 v228, a21\n"
"v_ldexp_f32 v228, v228, v223\n"
"v_accvgpr_write_b32 a21, v228\n"
"v_accvgpr_read_b32 v229, a22\n"
"v_ldexp_f32 v229, v229, v223\n"
"v_accvgpr_write_b32 a22, v229\n"
"v_accvgpr_read_b32 v230, a23\n"
"v_ldexp_f32 v230, v230, v223\n"
"v_accvgpr_write_b32 a23, v230\n"
"v_accvgpr_read_b32 v227, a24\n"
"v_ldexp_f32 v227, v227, v223\n"
"v_accvgpr_write_b32 a24, v227\n"
"v_accvgpr_read_b32 v228, a25\n"
"v_ldexp_f32 v228, v228, v223\n"
"v_accvgpr_write_b32 a25, v228\n"
"v_accvgpr_read_b32 v229, a26\n"
"v_ldexp_f32 v229, v229, v223\n"
"v_accvgpr_write_b32 a26, v229\n"
"v_accvgpr_read_b32 v230, a27\n"
"v_ldexp_f32 v230, v230, v223\n"
"v_accvgpr_write_b32 a27, v230\n"
"v_accvgpr_read_b32 v227, a28\n"
"v_ldexp_f32 v227, v227, v223\n"
"v_accvgpr_write_b32 a28, v227\n"
"v_accvgpr_read_b32 v228, a29\n"
"v_ldexp_f32 v228, v228, v223\n"
"v_accvgpr_write_b32 a29, v228\n"
"v_accvgpr_read_b32 v229, a30\n"
"v_ldexp_f32 v229, v229, v223\n"
"v_accvgpr_write_b32 a30, v229\n"
"v_accvgpr_read_b32 v230, a31\n"
"v_ldexp_f32 v230, v230, v223\n"
"v_accvgpr_write_b32 a31, v230\n"
"v_accvgpr_read_b32 v227, a32\n"
"v_ldexp_f32 v227, v227, v223\n"
"v_accvgpr_write_b32 a32, v227\n"
"v_accvgpr_read_b32 v228, a33\n"
"v_ldexp_f32 v228, v228, v223\n"
"v_accvgpr_write_b32 a33, v228\n"
"v_accvgpr_read_b32 v229, a34\n"
"v_ldexp_f32 v229, v229, v223\n"
"v_accvgpr_write_b32 a34, v229\n"
"v_accvgpr_read_b32 v230, a35\n"
"v_ldexp_f32 v230, v230, v223\n"
"v_accvgpr_write_b32 a35, v230\n"
"v_accvgpr_read_b32 v227, a36\n"
"v_ldexp_f32 v227, v227, v223\n"
"v_accvgpr_write_b32 a36, v227\n"
"v_accvgpr_read_b32 v228, a37\n"
"v_ldexp_f32 v228, v228, v223\n"
"v_accvgpr_write_b32 a37, v228\n"
"v_accvgpr_read_b32 v229, a38\n"
"v_ldexp_f32 v229, v229, v223\n"
"v_accvgpr_write_b32 a38, v229\n"
"v_accvgpr_read_b32 v230, a39\n"
"v_ldexp_f32 v230, v230, v223\n"
"v_accvgpr_write_b32 a39, v230\n"
"v_accvgpr_read_b32 v227, a40\n"
"v_ldexp_f32 v227, v227, v223\n"
"v_accvgpr_write_b32 a40, v227\n"
"v_accvgpr_read_b32 v228, a41\n"
"v_ldexp_f32 v228, v228, v223\n"
"v_accvgpr_write_b32 a41, v228\n"
"v_accvgpr_read_b32 v229, a42\n"
"v_ldexp_f32 v229, v229, v223\n"
"v_accvgpr_write_b32 a42, v229\n"
"v_accvgpr_read_b32 v230, a43\n"
"v_ldexp_f32 v230, v230, v223\n"
"v_accvgpr_write_b32 a43, v230\n"
"v_accvgpr_read_b32 v227, a44\n"
"v_ldexp_f32 v227, v227, v223\n"
"v_accvgpr_write_b32 a44, v227\n"
"v_accvgpr_read_b32 v228, a45\n"
"v_ldexp_f32 v228, v228, v223\n"
"v_accvgpr_write_b32 a45, v228\n"
"v_accvgpr_read_b32 v229, a46\n"
"v_ldexp_f32 v229, v229, v223\n"
"v_accvgpr_write_b32 a46, v229\n"
"v_accvgpr_read_b32 v230, a47\n"
"v_ldexp_f32 v230, v230, v223\n"
"v_accvgpr_write_b32 a47, v230\n"
"v_accvgpr_read_b32 v227, a48\n"
"v_ldexp_f32 v227, v227, v223\n"
"v_accvgpr_write_b32 a48, v227\n"
"v_accvgpr_read_b32 v228, a49\n"
"v_ldexp_f32 v228, v228, v223\n"
"v_accvgpr_write_b32 a49, v228\n"
"v_accvgpr_read_b32 v229, a50\n"
"v_ldexp_f32 v229, v229, v223\n"
"v_accvgpr_write_b32 a50, v229\n"
"v_accvgpr_read_b32 v230, a51\n"
"v_ldexp_f32 v230, v230, v223\n"
"v_accvgpr_write_b32 a51, v230\n"
"v_accvgpr_read_b32 v227, a52\n"
"v_ldexp_f32 v227, v227, v223\n"
"v_accvgpr_write_b32 a52, v227\n"
"v_accvgpr_read_b32 v228, a53\n"
"v_ldexp_f32 v228, v228, v223\n"
"v_accvgpr_write_b32 a53, v228\n"
"v_accvgpr_read_b32 v229, a54\n"
"v_ldexp_f32 v229, v229, v223\n"
"v_accvgpr_write_b32 a54, v229\n"
"v_accvgpr_read_b32 v230, a55\n"
"v_ldexp_f32 v230, v230, v223\n"
"v_accvgpr_write_b32 a55, v230\n"
"v_accvgpr_read_b32 v227, a56\n"
"v_ldexp_f32 v227, v227, v223\n"
"v_accvgpr_write_b32 a56, v227\n"
"v_accvgpr_read_b32 v228, a57\n"
"v_ldexp_f32 v228, v228, v223\n"
"v_accvgpr_write_b32 a57, v228\n"
"v_accvgpr_read_b32 v229, a58\n"
"v_ldexp_f32 v229, v229, v223\n"
"v_accvgpr_write_b32 a58, v229\n"
"v_accvgpr_read_b32 v230, a59\n"
"v_ldexp_f32 v230, v230, v223\n"
"v_accvgpr_write_b32 a59, v230\n"
"v_accvgpr_read_b32 v227, a60\n"
"v_ldexp_f32 v227, v227, v223\n"
"v_accvgpr_write_b32 a60, v227\n"
"v_accvgpr_read_b32 v228, a61\n"
"v_ldexp_f32 v228, v228, v223\n"
"v_accvgpr_write_b32 a61, v228\n"
"v_accvgpr_read_b32 v229, a62\n"
"v_ldexp_f32 v229, v229, v223\n"
"v_accvgpr_write_b32 a62, v229\n"
"v_accvgpr_read_b32 v230, a63\n"
"v_ldexp_f32 v230, v230, v223\n"
"v_accvgpr_write_b32 a63, v230\n"
"v_ldexp_f32 v210, v210, v223\n"
"v_exp_f32 v112, v128\n"
"v_exp_f32 v113, v129\n"
"v_exp_f32 v114, v130\n"
"v_exp_f32 v115, v131\n"
"v_exp_f32 v116, v132\n"
"v_exp_f32 v117, v133\n"
"v_exp_f32 v118, v134\n"
"v_exp_f32 v119, v135\n"
"v_exp_f32 v120, v136\n"
"v_exp_f32 v121, v137\n"
"v_exp_f32 v122, v138\n"
"v_exp_f32 v123, v139\n"
"v_exp_f32 v124, v140\n"
"v_exp_f32 v125, v141\n"
"v_exp_f32 v126, v142\n"
"v_exp_f32 v127, v143\n"
"s_nop 0\n"
"v_add_f32 v208, v112, v113\n"
"v_add_f32 v208, v208, v114\n"
"v_add_f32 v208, v208, v115\n"
"v_add_f32 v208, v208, v116\n"
"v_add_f32 v208, v208, v117\n"
"v_add_f32 v208, v208, v118\n"
"v_add_f32 v208, v208, v119\n"
"v_add_f32 v208, v208, v120\n"
"v_add_f32 v208, v208, v121\n"
"v_add_f32 v208, v208, v122\n"
"v_add_f32 v208, v208, v123\n"
"v_add_f32 v208, v208, v124\n"
"v_add_f32 v208, v208, v125\n"
"v_add_f32 v208, v208, v126\n"
"v_add_f32 v208, v208, v127\n"
"v_cvt_pk_bf16_f32 v160, v112, v113\n"
"v_cvt_pk_bf16_f32 v161, v114, v115\n"
"v_cvt_pk_bf16_f32 v162, v116, v117\n"
"v_cvt_pk_bf16_f32 v163, v118, v119\n"
"v_cvt_pk_bf16_f32 v164, v120, v121\n"
"v_cvt_pk_bf16_f32 v165, v122, v123\n"
"v_cvt_pk_bf16_f32 v166, v124, v125\n"
"v_cvt_pk_bf16_f32 v167, v126, v127\n"
"v_max3_f32 v222, v144, v145, v146\n"
"v_max3_f32 v222, v222, v147, v148\n"
"v_max3_f32 v222, v222, v149, v150\n"
"v_max3_f32 v222, v222, v151, v152\n"
"v_max3_f32 v222, v222, v153, v154\n"
"v_max3_f32 v222, v222, v155, v156\n"
"v_max3_f32 v222, v222, v157, v158\n"
"v_max_f32 v222, v222, v159\n"
"v_mov_b32 v224, v222\n"
"s_nop 1\n"
"v_permlane32_swap_b32 v222, v224\n"
"v_max_f32 v222, v222, v224\n"
"v_max_f32 v222, 0, v222\n"
"v_ceil_f32 v222, v222\n"
"v_cvt_i32_f32 v223, v222\n"
"v_sub_u32 v223, 0, v223\n"
"v_sub_f32 v144, v144, v222\n"
"v_sub_f32 v145, v145, v222\n"
"v_sub_f32 v146, v146, v222\n"
"v_sub_f32 v147, v147, v222\n"
"v_sub_f32 v148, v148, v222\n"
"v_sub_f32 v149, v149, v222\n"
"v_sub_f32 v150, v150, v222\n"
"v_sub_f32 v151, v151, v222\n"
"v_sub_f32 v152, v152, v222\n"
"v_sub_f32 v153, v153, v222\n"
"v_sub_f32 v154, v154, v222\n"
"v_sub_f32 v155, v155, v222\n"
"v_sub_f32 v156, v156, v222\n"
"v_sub_f32 v157, v157, v222\n"
"v_sub_f32 v158, v158, v222\n"
"v_sub_f32 v159, v159, v222\n"
"v_sub_f32 v80, v80, v222\n"
"v_sub_f32 v81, v81, v222\n"
"v_sub_f32 v82, v82, v222\n"
"v_sub_f32 v83, v83, v222\n"
"v_sub_f32 v84, v84, v222\n"
"v_sub_f32 v85, v85, v222\n"
"v_sub_f32 v86, v86, v222\n"
"v_sub_f32 v87, v87, v222\n"
"v_sub_f32 v88, v88, v222\n"
"v_sub_f32 v89, v89, v222\n"
"v_sub_f32 v90, v90, v222\n"
"v_sub_f32 v91, v91, v222\n"
"v_sub_f32 v92, v92, v222\n"
"v_sub_f32 v93, v93, v222\n"
"v_sub_f32 v94, v94, v222\n"
"v_sub_f32 v95, v95, v222\n"
"v_sub_f32 v192, v192, v222\n"
"v_sub_f32 v193, v193, v222\n"
"v_sub_f32 v194, v194, v222\n"
"v_sub_f32 v195, v195, v222\n"
"v_sub_f32 v196, v196, v222\n"
"v_sub_f32 v197, v197, v222\n"
"v_sub_f32 v198, v198, v222\n"
"v_sub_f32 v199, v199, v222\n"
"v_sub_f32 v200, v200, v222\n"
"v_sub_f32 v201, v201, v222\n"
"v_sub_f32 v202, v202, v222\n"
"v_sub_f32 v203, v203, v222\n"
"v_sub_f32 v204, v204, v222\n"
"v_sub_f32 v205, v205, v222\n"
"v_sub_f32 v206, v206, v222\n"
"v_sub_f32 v207, v207, v222\n"
"v_accvgpr_read_b32 v227, a64\n"
"v_ldexp_f32 v227, v227, v223\n"
"v_accvgpr_write_b32 a64, v227\n"
"v_accvgpr_read_b32 v228, a65\n"
"v_ldexp_f32 v228, v228, v223\n"
"v_accvgpr_write_b32 a65, v228\n"
"v_accvgpr_read_b32 v229, a66\n"
"v_ldexp_f32 v229, v229, v223\n"
"v_accvgpr_write_b32 a66, v229\n"
"v_accvgpr_read_b32 v230, a67\n"
"v_ldexp_f32 v230, v230, v223\n"
"v_accvgpr_write_b32 a67, v230\n"
"v_accvgpr_read_b32 v227, a68\n"
"v_ldexp_f32 v227, v227, v223\n"
"v_accvgpr_write_b32 a68, v227\n"
"v_accvgpr_read_b32 v228, a69\n"
"v_ldexp_f32 v228, v228, v223\n"
"v_accvgpr_write_b32 a69, v228\n"
"v_accvgpr_read_b32 v229, a70\n"
"v_ldexp_f32 v229, v229, v223\n"
"v_accvgpr_write_b32 a70, v229\n"
"v_accvgpr_read_b32 v230, a71\n"
"v_ldexp_f32 v230, v230, v223\n"
"v_accvgpr_write_b32 a71, v230\n"
"v_accvgpr_read_b32 v227, a72\n"
"v_ldexp_f32 v227, v227, v223\n"
"v_accvgpr_write_b32 a72, v227\n"
"v_accvgpr_read_b32 v228, a73\n"
"v_ldexp_f32 v228, v228, v223\n"
"v_accvgpr_write_b32 a73, v228\n"
"v_accvgpr_read_b32 v229, a74\n"
"v_ldexp_f32 v229, v229, v223\n"
"v_accvgpr_write_b32 a74, v229\n"
"v_accvgpr_read_b32 v230, a75\n"
"v_ldexp_f32 v230, v230, v223\n"
"v_accvgpr_write_b32 a75, v230\n"
"v_accvgpr_read_b32 v227, a76\n"
"v_ldexp_f32 v227, v227, v223\n"
"v_accvgpr_write_b32 a76, v227\n"
"v_accvgpr_read_b32 v228, a77\n"
"v_ldexp_f32 v228, v228, v223\n"
"v_accvgpr_write_b32 a77, v228\n"
"v_accvgpr_read_b32 v229, a78\n"
"v_ldexp_f32 v229, v229, v223\n"
"v_accvgpr_write_b32 a78, v229\n"
"v_accvgpr_read_b32 v230, a79\n"
"v_ldexp_f32 v230, v230, v223\n"
"v_accvgpr_write_b32 a79, v230\n"
"v_accvgpr_read_b32 v227, a80\n"
"v_ldexp_f32 v227, v227, v223\n"
"v_accvgpr_write_b32 a80, v227\n"
"v_accvgpr_read_b32 v228, a81\n"
"v_ldexp_f32 v228, v228, v223\n"
"v_accvgpr_write_b32 a81, v228\n"
"v_accvgpr_read_b32 v229, a82\n"
"v_ldexp_f32 v229, v229, v223\n"
"v_accvgpr_write_b32 a82, v229\n"
"v_accvgpr_read_b32 v230, a83\n"
"v_ldexp_f32 v230, v230, v223\n"
"v_accvgpr_write_b32 a83, v230\n"
"v_accvgpr_read_b32 v227, a84\n"
"v_ldexp_f32 v227, v227, v223\n"
"v_accvgpr_write_b32 a84, v227\n"
"v_accvgpr_read_b32 v228, a85\n"
"v_ldexp_f32 v228, v228, v223\n"
"v_accvgpr_write_b32 a85, v228\n"
"v_accvgpr_read_b32 v229, a86\n"
"v_ldexp_f32 v229, v229, v223\n"
"v_accvgpr_write_b32 a86, v229\n"
"v_accvgpr_read_b32 v230, a87\n"
"v_ldexp_f32 v230, v230, v223\n"
"v_accvgpr_write_b32 a87, v230\n"
"v_accvgpr_read_b32 v227, a88\n"
"v_ldexp_f32 v227, v227, v223\n"
"v_accvgpr_write_b32 a88, v227\n"
"v_accvgpr_read_b32 v228, a89\n"
"v_ldexp_f32 v228, v228, v223\n"
"v_accvgpr_write_b32 a89, v228\n"
"v_accvgpr_read_b32 v229, a90\n"
"v_ldexp_f32 v229, v229, v223\n"
"v_accvgpr_write_b32 a90, v229\n"
"v_accvgpr_read_b32 v230, a91\n"
"v_ldexp_f32 v230, v230, v223\n"
"v_accvgpr_write_b32 a91, v230\n"
"v_accvgpr_read_b32 v227, a92\n"
"v_ldexp_f32 v227, v227, v223\n"
"v_accvgpr_write_b32 a92, v227\n"
"v_accvgpr_read_b32 v228, a93\n"
"v_ldexp_f32 v228, v228, v223\n"
"v_accvgpr_write_b32 a93, v228\n"
"v_accvgpr_read_b32 v229, a94\n"
"v_ldexp_f32 v229, v229, v223\n"
"v_accvgpr_write_b32 a94, v229\n"
"v_accvgpr_read_b32 v230, a95\n"
"v_ldexp_f32 v230, v230, v223\n"
"v_accvgpr_write_b32 a95, v230\n"
"v_accvgpr_read_b32 v227, a96\n"
"v_ldexp_f32 v227, v227, v223\n"
"v_accvgpr_write_b32 a96, v227\n"
"v_accvgpr_read_b32 v228, a97\n"
"v_ldexp_f32 v228, v228, v223\n"
"v_accvgpr_write_b32 a97, v228\n"
"v_accvgpr_read_b32 v229, a98\n"
"v_ldexp_f32 v229, v229, v223\n"
"v_accvgpr_write_b32 a98, v229\n"
"v_accvgpr_read_b32 v230, a99\n"
"v_ldexp_f32 v230, v230, v223\n"
"v_accvgpr_write_b32 a99, v230\n"
"v_accvgpr_read_b32 v227, a100\n"
"v_ldexp_f32 v227, v227, v223\n"
"v_accvgpr_write_b32 a100, v227\n"
"v_accvgpr_read_b32 v228, a101\n"
"v_ldexp_f32 v228, v228, v223\n"
"v_accvgpr_write_b32 a101, v228\n"
"v_accvgpr_read_b32 v229, a102\n"
"v_ldexp_f32 v229, v229, v223\n"
"v_accvgpr_write_b32 a102, v229\n"
"v_accvgpr_read_b32 v230, a103\n"
"v_ldexp_f32 v230, v230, v223\n"
"v_accvgpr_write_b32 a103, v230\n"
"v_accvgpr_read_b32 v227, a104\n"
"v_ldexp_f32 v227, v227, v223\n"
"v_accvgpr_write_b32 a104, v227\n"
"v_accvgpr_read_b32 v228, a105\n"
"v_ldexp_f32 v228, v228, v223\n"
"v_accvgpr_write_b32 a105, v228\n"
"v_accvgpr_read_b32 v229, a106\n"
"v_ldexp_f32 v229, v229, v223\n"
"v_accvgpr_write_b32 a106, v229\n"
"v_accvgpr_read_b32 v230, a107\n"
"v_ldexp_f32 v230, v230, v223\n"
"v_accvgpr_write_b32 a107, v230\n"
"v_accvgpr_read_b32 v227, a108\n"
"v_ldexp_f32 v227, v227, v223\n"
"v_accvgpr_write_b32 a108, v227\n"
"v_accvgpr_read_b32 v228, a109\n"
"v_ldexp_f32 v228, v228, v223\n"
"v_accvgpr_write_b32 a109, v228\n"
"v_accvgpr_read_b32 v229, a110\n"
"v_ldexp_f32 v229, v229, v223\n"
"v_accvgpr_write_b32 a110, v229\n"
"v_accvgpr_read_b32 v230, a111\n"
"v_ldexp_f32 v230, v230, v223\n"
"v_accvgpr_write_b32 a111, v230\n"
"v_accvgpr_read_b32 v227, a112\n"
"v_ldexp_f32 v227, v227, v223\n"
"v_accvgpr_write_b32 a112, v227\n"
"v_accvgpr_read_b32 v228, a113\n"
"v_ldexp_f32 v228, v228, v223\n"
"v_accvgpr_write_b32 a113, v228\n"
"v_accvgpr_read_b32 v229, a114\n"
"v_ldexp_f32 v229, v229, v223\n"
"v_accvgpr_write_b32 a114, v229\n"
"v_accvgpr_read_b32 v230, a115\n"
"v_ldexp_f32 v230, v230, v223\n"
"v_accvgpr_write_b32 a115, v230\n"
"v_accvgpr_read_b32 v227, a116\n"
"v_ldexp_f32 v227, v227, v223\n"
"v_accvgpr_write_b32 a116, v227\n"
"v_accvgpr_read_b32 v228, a117\n"
"v_ldexp_f32 v228, v228, v223\n"
"v_accvgpr_write_b32 a117, v228\n"
"v_accvgpr_read_b32 v229, a118\n"
"v_ldexp_f32 v229, v229, v223\n"
"v_accvgpr_write_b32 a118, v229\n"
"v_accvgpr_read_b32 v230, a119\n"
"v_ldexp_f32 v230, v230, v223\n"
"v_accvgpr_write_b32 a119, v230\n"
"v_accvgpr_read_b32 v227, a120\n"
"v_ldexp_f32 v227, v227, v223\n"
"v_accvgpr_write_b32 a120, v227\n"
"v_accvgpr_read_b32 v228, a121\n"
"v_ldexp_f32 v228, v228, v223\n"
"v_accvgpr_write_b32 a121, v228\n"
"v_accvgpr_read_b32 v229, a122\n"
"v_ldexp_f32 v229, v229, v223\n"
"v_accvgpr_write_b32 a122, v229\n"
"v_accvgpr_read_b32 v230, a123\n"
"v_ldexp_f32 v230, v230, v223\n"
"v_accvgpr_write_b32 a123, v230\n"
"v_accvgpr_read_b32 v227, a124\n"
"v_ldexp_f32 v227, v227, v223\n"
"v_accvgpr_write_b32 a124, v227\n"
"v_accvgpr_read_b32 v228, a125\n"
"v_ldexp_f32 v228, v228, v223\n"
"v_accvgpr_write_b32 a125, v228\n"
"v_accvgpr_read_b32 v229, a126\n"
"v_ldexp_f32 v229, v229, v223\n"
"v_accvgpr_write_b32 a126, v229\n"
"v_accvgpr_read_b32 v230, a127\n"
"v_ldexp_f32 v230, v230, v223\n"
"v_accvgpr_write_b32 a127, v230\n"
"v_ldexp_f32 v211, v211, v223\n"
"v_exp_f32 v112, v144\n"
"v_exp_f32 v113, v145\n"
"v_exp_f32 v114, v146\n"
"v_exp_f32 v115, v147\n"
"v_exp_f32 v116, v148\n"
"v_exp_f32 v117, v149\n"
"v_exp_f32 v118, v150\n"
"v_exp_f32 v119, v151\n"
"v_exp_f32 v120, v152\n"
"v_exp_f32 v121, v153\n"
"v_exp_f32 v122, v154\n"
"v_exp_f32 v123, v155\n"
"v_exp_f32 v124, v156\n"
"v_exp_f32 v125, v157\n"
"v_exp_f32 v126, v158\n"
"v_exp_f32 v127, v159\n"
"s_nop 0\n"
"v_add_f32 v209, v112, v113\n"
"v_add_f32 v209, v209, v114\n"
"v_add_f32 v209, v209, v115\n"
"v_add_f32 v209, v209, v116\n"
"v_add_f32 v209, v209, v117\n"
"v_add_f32 v209, v209, v118\n"
"v_add_f32 v209, v209, v119\n"
"v_add_f32 v209, v209, v120\n"
"v_add_f32 v209, v209, v121\n"
"v_add_f32 v209, v209, v122\n"
"v_add_f32 v209, v209, v123\n"
"v_add_f32 v209, v209, v124\n"
"v_add_f32 v209, v209, v125\n"
"v_add_f32 v209, v209, v126\n"
"v_add_f32 v209, v209, v127\n"
"v_cvt_pk_bf16_f32 v168, v112, v113\n"
"v_cvt_pk_bf16_f32 v169, v114, v115\n"
"v_cvt_pk_bf16_f32 v170, v116, v117\n"
"v_cvt_pk_bf16_f32 v171, v118, v119\n"
"v_cvt_pk_bf16_f32 v172, v120, v121\n"
"v_cvt_pk_bf16_f32 v173, v122, v123\n"
"v_cvt_pk_bf16_f32 v174, v124, v125\n"
"v_cvt_pk_bf16_f32 v175, v126, v127\n"
"s_nop 1\n"
"s_cmp_eq_u32 s13, 0\n"
"s_cbranch_scc1 L_afr22_%=\n"
"s_branch L_afr34_%=\n"
"L_fix_t1_%=:\n"
"s_nop 7\n"
"s_nop 3\n"
"v_max3_f32 v222, v128, v129, v130\n"
"v_max3_f32 v222, v222, v131, v132\n"
"v_max3_f32 v222, v222, v133, v134\n"
"v_max3_f32 v222, v222, v135, v136\n"
"v_max3_f32 v222, v222, v137, v138\n"
"v_max3_f32 v222, v222, v139, v140\n"
"v_max3_f32 v222, v222, v141, v142\n"
"v_max_f32 v222, v222, v143\n"
"v_mov_b32 v224, v222\n"
"s_nop 1\n"
"v_permlane32_swap_b32 v222, v224\n"
"v_max_f32 v222, v222, v224\n"
"v_max_f32 v222, 0, v222\n"
"v_ceil_f32 v222, v222\n"
"v_cvt_i32_f32 v223, v222\n"
"v_sub_u32 v223, 0, v223\n"
"v_sub_f32 v128, v128, v222\n"
"v_sub_f32 v129, v129, v222\n"
"v_sub_f32 v130, v130, v222\n"
"v_sub_f32 v131, v131, v222\n"
"v_sub_f32 v132, v132, v222\n"
"v_sub_f32 v133, v133, v222\n"
"v_sub_f32 v134, v134, v222\n"
"v_sub_f32 v135, v135, v222\n"
"v_sub_f32 v136, v136, v222\n"
"v_sub_f32 v137, v137, v222\n"
"v_sub_f32 v138, v138, v222\n"
"v_sub_f32 v139, v139, v222\n"
"v_sub_f32 v140, v140, v222\n"
"v_sub_f32 v141, v141, v222\n"
"v_sub_f32 v142, v142, v222\n"
"v_sub_f32 v143, v143, v222\n"
"v_accvgpr_read_b32 v227, a0\n"
"v_ldexp_f32 v227, v227, v223\n"
"v_accvgpr_write_b32 a0, v227\n"
"v_accvgpr_read_b32 v228, a1\n"
"v_ldexp_f32 v228, v228, v223\n"
"v_accvgpr_write_b32 a1, v228\n"
"v_accvgpr_read_b32 v229, a2\n"
"v_ldexp_f32 v229, v229, v223\n"
"v_accvgpr_write_b32 a2, v229\n"
"v_accvgpr_read_b32 v230, a3\n"
"v_ldexp_f32 v230, v230, v223\n"
"v_accvgpr_write_b32 a3, v230\n"
"v_accvgpr_read_b32 v227, a4\n"
"v_ldexp_f32 v227, v227, v223\n"
"v_accvgpr_write_b32 a4, v227\n"
"v_accvgpr_read_b32 v228, a5\n"
"v_ldexp_f32 v228, v228, v223\n"
"v_accvgpr_write_b32 a5, v228\n"
"v_accvgpr_read_b32 v229, a6\n"
"v_ldexp_f32 v229, v229, v223\n"
"v_accvgpr_write_b32 a6, v229\n"
"v_accvgpr_read_b32 v230, a7\n"
"v_ldexp_f32 v230, v230, v223\n"
"v_accvgpr_write_b32 a7, v230\n"
"v_accvgpr_read_b32 v227, a8\n"
"v_ldexp_f32 v227, v227, v223\n"
"v_accvgpr_write_b32 a8, v227\n"
"v_accvgpr_read_b32 v228, a9\n"
"v_ldexp_f32 v228, v228, v223\n"
"v_accvgpr_write_b32 a9, v228\n"
"v_accvgpr_read_b32 v229, a10\n"
"v_ldexp_f32 v229, v229, v223\n"
"v_accvgpr_write_b32 a10, v229\n"
"v_accvgpr_read_b32 v230, a11\n"
"v_ldexp_f32 v230, v230, v223\n"
"v_accvgpr_write_b32 a11, v230\n"
"v_accvgpr_read_b32 v227, a12\n"
"v_ldexp_f32 v227, v227, v223\n"
"v_accvgpr_write_b32 a12, v227\n"
"v_accvgpr_read_b32 v228, a13\n"
"v_ldexp_f32 v228, v228, v223\n"
"v_accvgpr_write_b32 a13, v228\n"
"v_accvgpr_read_b32 v229, a14\n"
"v_ldexp_f32 v229, v229, v223\n"
"v_accvgpr_write_b32 a14, v229\n"
"v_accvgpr_read_b32 v230, a15\n"
"v_ldexp_f32 v230, v230, v223\n"
"v_accvgpr_write_b32 a15, v230\n"
"v_accvgpr_read_b32 v227, a16\n"
"v_ldexp_f32 v227, v227, v223\n"
"v_accvgpr_write_b32 a16, v227\n"
"v_accvgpr_read_b32 v228, a17\n"
"v_ldexp_f32 v228, v228, v223\n"
"v_accvgpr_write_b32 a17, v228\n"
"v_accvgpr_read_b32 v229, a18\n"
"v_ldexp_f32 v229, v229, v223\n"
"v_accvgpr_write_b32 a18, v229\n"
"v_accvgpr_read_b32 v230, a19\n"
"v_ldexp_f32 v230, v230, v223\n"
"v_accvgpr_write_b32 a19, v230\n"
"v_accvgpr_read_b32 v227, a20\n"
"v_ldexp_f32 v227, v227, v223\n"
"v_accvgpr_write_b32 a20, v227\n"
"v_accvgpr_read_b32 v228, a21\n"
"v_ldexp_f32 v228, v228, v223\n"
"v_accvgpr_write_b32 a21, v228\n"
"v_accvgpr_read_b32 v229, a22\n"
"v_ldexp_f32 v229, v229, v223\n"
"v_accvgpr_write_b32 a22, v229\n"
"v_accvgpr_read_b32 v230, a23\n"
"v_ldexp_f32 v230, v230, v223\n"
"v_accvgpr_write_b32 a23, v230\n"
"v_accvgpr_read_b32 v227, a24\n"
"v_ldexp_f32 v227, v227, v223\n"
"v_accvgpr_write_b32 a24, v227\n"
"v_accvgpr_read_b32 v228, a25\n"
"v_ldexp_f32 v228, v228, v223\n"
"v_accvgpr_write_b32 a25, v228\n"
"v_accvgpr_read_b32 v229, a26\n"
"v_ldexp_f32 v229, v229, v223\n"
"v_accvgpr_write_b32 a26, v229\n"
"v_accvgpr_read_b32 v230, a27\n"
"v_ldexp_f32 v230, v230, v223\n"
"v_accvgpr_write_b32 a27, v230\n"
"v_accvgpr_read_b32 v227, a28\n"
"v_ldexp_f32 v227, v227, v223\n"
"v_accvgpr_write_b32 a28, v227\n"
"v_accvgpr_read_b32 v228, a29\n"
"v_ldexp_f32 v228, v228, v223\n"
"v_accvgpr_write_b32 a29, v228\n"
"v_accvgpr_read_b32 v229, a30\n"
"v_ldexp_f32 v229, v229, v223\n"
"v_accvgpr_write_b32 a30, v229\n"
"v_accvgpr_read_b32 v230, a31\n"
"v_ldexp_f32 v230, v230, v223\n"
"v_accvgpr_write_b32 a31, v230\n"
"v_accvgpr_read_b32 v227, a32\n"
"v_ldexp_f32 v227, v227, v223\n"
"v_accvgpr_write_b32 a32, v227\n"
"v_accvgpr_read_b32 v228, a33\n"
"v_ldexp_f32 v228, v228, v223\n"
"v_accvgpr_write_b32 a33, v228\n"
"v_accvgpr_read_b32 v229, a34\n"
"v_ldexp_f32 v229, v229, v223\n"
"v_accvgpr_write_b32 a34, v229\n"
"v_accvgpr_read_b32 v230, a35\n"
"v_ldexp_f32 v230, v230, v223\n"
"v_accvgpr_write_b32 a35, v230\n"
"v_accvgpr_read_b32 v227, a36\n"
"v_ldexp_f32 v227, v227, v223\n"
"v_accvgpr_write_b32 a36, v227\n"
"v_accvgpr_read_b32 v228, a37\n"
"v_ldexp_f32 v228, v228, v223\n"
"v_accvgpr_write_b32 a37, v228\n"
"v_accvgpr_read_b32 v229, a38\n"
"v_ldexp_f32 v229, v229, v223\n"
"v_accvgpr_write_b32 a38, v229\n"
"v_accvgpr_read_b32 v230, a39\n"
"v_ldexp_f32 v230, v230, v223\n"
"v_accvgpr_write_b32 a39, v230\n"
"v_accvgpr_read_b32 v227, a40\n"
"v_ldexp_f32 v227, v227, v223\n"
"v_accvgpr_write_b32 a40, v227\n"
"v_accvgpr_read_b32 v228, a41\n"
"v_ldexp_f32 v228, v228, v223\n"
"v_accvgpr_write_b32 a41, v228\n"
"v_accvgpr_read_b32 v229, a42\n"
"v_ldexp_f32 v229, v229, v223\n"
"v_accvgpr_write_b32 a42, v229\n"
"v_accvgpr_read_b32 v230, a43\n"
"v_ldexp_f32 v230, v230, v223\n"
"v_accvgpr_write_b32 a43, v230\n"
"v_accvgpr_read_b32 v227, a44\n"
"v_ldexp_f32 v227, v227, v223\n"
"v_accvgpr_write_b32 a44, v227\n"
"v_accvgpr_read_b32 v228, a45\n"
"v_ldexp_f32 v228, v228, v223\n"
"v_accvgpr_write_b32 a45, v228\n"
"v_accvgpr_read_b32 v229, a46\n"
"v_ldexp_f32 v229, v229, v223\n"
"v_accvgpr_write_b32 a46, v229\n"
"v_accvgpr_read_b32 v230, a47\n"
"v_ldexp_f32 v230, v230, v223\n"
"v_accvgpr_write_b32 a47, v230\n"
"v_accvgpr_read_b32 v227, a48\n"
"v_ldexp_f32 v227, v227, v223\n"
"v_accvgpr_write_b32 a48, v227\n"
"v_accvgpr_read_b32 v228, a49\n"
"v_ldexp_f32 v228, v228, v223\n"
"v_accvgpr_write_b32 a49, v228\n"
"v_accvgpr_read_b32 v229, a50\n"
"v_ldexp_f32 v229, v229, v223\n"
"v_accvgpr_write_b32 a50, v229\n"
"v_accvgpr_read_b32 v230, a51\n"
"v_ldexp_f32 v230, v230, v223\n"
"v_accvgpr_write_b32 a51, v230\n"
"v_accvgpr_read_b32 v227, a52\n"
"v_ldexp_f32 v227, v227, v223\n"
"v_accvgpr_write_b32 a52, v227\n"
"v_accvgpr_read_b32 v228, a53\n"
"v_ldexp_f32 v228, v228, v223\n"
"v_accvgpr_write_b32 a53, v228\n"
"v_accvgpr_read_b32 v229, a54\n"
"v_ldexp_f32 v229, v229, v223\n"
"v_accvgpr_write_b32 a54, v229\n"
"v_accvgpr_read_b32 v230, a55\n"
"v_ldexp_f32 v230, v230, v223\n"
"v_accvgpr_write_b32 a55, v230\n"
"v_accvgpr_read_b32 v227, a56\n"
"v_ldexp_f32 v227, v227, v223\n"
"v_accvgpr_write_b32 a56, v227\n"
"v_accvgpr_read_b32 v228, a57\n"
"v_ldexp_f32 v228, v228, v223\n"
"v_accvgpr_write_b32 a57, v228\n"
"v_accvgpr_read_b32 v229, a58\n"
"v_ldexp_f32 v229, v229, v223\n"
"v_accvgpr_write_b32 a58, v229\n"
"v_accvgpr_read_b32 v230, a59\n"
"v_ldexp_f32 v230, v230, v223\n"
"v_accvgpr_write_b32 a59, v230\n"
"v_accvgpr_read_b32 v227, a60\n"
"v_ldexp_f32 v227, v227, v223\n"
"v_accvgpr_write_b32 a60, v227\n"
"v_accvgpr_read_b32 v228, a61\n"
"v_ldexp_f32 v228, v228, v223\n"
"v_accvgpr_write_b32 a61, v228\n"
"v_accvgpr_read_b32 v229, a62\n"
"v_ldexp_f32 v229, v229, v223\n"
"v_accvgpr_write_b32 a62, v229\n"
"v_accvgpr_read_b32 v230, a63\n"
"v_ldexp_f32 v230, v230, v223\n"
"v_accvgpr_write_b32 a63, v230\n"
"v_ldexp_f32 v210, v210, v223\n"
"v_exp_f32 v112, v128\n"
"v_exp_f32 v113, v129\n"
"v_exp_f32 v114, v130\n"
"v_exp_f32 v115, v131\n"
"v_exp_f32 v116, v132\n"
"v_exp_f32 v117, v133\n"
"v_exp_f32 v118, v134\n"
"v_exp_f32 v119, v135\n"
"v_exp_f32 v120, v136\n"
"v_exp_f32 v121, v137\n"
"v_exp_f32 v122, v138\n"
"v_exp_f32 v123, v139\n"
"v_exp_f32 v124, v140\n"
"v_exp_f32 v125, v141\n"
"v_exp_f32 v126, v142\n"
"v_exp_f32 v127, v143\n"
"s_nop 0\n"
"v_add_f32 v208, v112, v113\n"
"v_add_f32 v208, v208, v114\n"
"v_add_f32 v208, v208, v115\n"
"v_add_f32 v208, v208, v116\n"
"v_add_f32 v208, v208, v117\n"
"v_add_f32 v208, v208, v118\n"
"v_add_f32 v208, v208, v119\n"
"v_add_f32 v208, v208, v120\n"
"v_add_f32 v208, v208, v121\n"
"v_add_f32 v208, v208, v122\n"
"v_add_f32 v208, v208, v123\n"
"v_add_f32 v208, v208, v124\n"
"v_add_f32 v208, v208, v125\n"
"v_add_f32 v208, v208, v126\n"
"v_add_f32 v208, v208, v127\n"
"v_cvt_pk_bf16_f32 v160, v112, v113\n"
"v_cvt_pk_bf16_f32 v161, v114, v115\n"
"v_cvt_pk_bf16_f32 v162, v116, v117\n"
"v_cvt_pk_bf16_f32 v163, v118, v119\n"
"v_cvt_pk_bf16_f32 v164, v120, v121\n"
"v_cvt_pk_bf16_f32 v165, v122, v123\n"
"v_cvt_pk_bf16_f32 v166, v124, v125\n"
"v_cvt_pk_bf16_f32 v167, v126, v127\n"
"v_max3_f32 v222, v144, v145, v146\n"
"v_max3_f32 v222, v222, v147, v148\n"
"v_max3_f32 v222, v222, v149, v150\n"
"v_max3_f32 v222, v222, v151, v152\n"
"v_max3_f32 v222, v222, v153, v154\n"
"v_max3_f32 v222, v222, v155, v156\n"
"v_max3_f32 v222, v222, v157, v158\n"
"v_max_f32 v222, v222, v159\n"
"v_mov_b32 v224, v222\n"
"s_nop 1\n"
"v_permlane32_swap_b32 v222, v224\n"
"v_max_f32 v222, v222, v224\n"
"v_max_f32 v222, 0, v222\n"
"v_ceil_f32 v222, v222\n"
"v_cvt_i32_f32 v223, v222\n"
"v_sub_u32 v223, 0, v223\n"
"v_sub_f32 v144, v144, v222\n"
"v_sub_f32 v145, v145, v222\n"
"v_sub_f32 v146, v146, v222\n"
"v_sub_f32 v147, v147, v222\n"
"v_sub_f32 v148, v148, v222\n"
"v_sub_f32 v149, v149, v222\n"
"v_sub_f32 v150, v150, v222\n"
"v_sub_f32 v151, v151, v222\n"
"v_sub_f32 v152, v152, v222\n"
"v_sub_f32 v153, v153, v222\n"
"v_sub_f32 v154, v154, v222\n"
"v_sub_f32 v155, v155, v222\n"
"v_sub_f32 v156, v156, v222\n"
"v_sub_f32 v157, v157, v222\n"
"v_sub_f32 v158, v158, v222\n"
"v_sub_f32 v159, v159, v222\n"
"v_sub_f32 v64, v64, v222\n"
"v_sub_f32 v65, v65, v222\n"
"v_sub_f32 v66, v66, v222\n"
"v_sub_f32 v67, v67, v222\n"
"v_sub_f32 v68, v68, v222\n"
"v_sub_f32 v69, v69, v222\n"
"v_sub_f32 v70, v70, v222\n"
"v_sub_f32 v71, v71, v222\n"
"v_sub_f32 v72, v72, v222\n"
"v_sub_f32 v73, v73, v222\n"
"v_sub_f32 v74, v74, v222\n"
"v_sub_f32 v75, v75, v222\n"
"v_sub_f32 v76, v76, v222\n"
"v_sub_f32 v77, v77, v222\n"
"v_sub_f32 v78, v78, v222\n"
"v_sub_f32 v79, v79, v222\n"
"v_sub_f32 v80, v80, v222\n"
"v_sub_f32 v81, v81, v222\n"
"v_sub_f32 v82, v82, v222\n"
"v_sub_f32 v83, v83, v222\n"
"v_sub_f32 v84, v84, v222\n"
"v_sub_f32 v85, v85, v222\n"
"v_sub_f32 v86, v86, v222\n"
"v_sub_f32 v87, v87, v222\n"
"v_sub_f32 v88, v88, v222\n"
"v_sub_f32 v89, v89, v222\n"
"v_sub_f32 v90, v90, v222\n"
"v_sub_f32 v91, v91, v222\n"
"v_sub_f32 v92, v92, v222\n"
"v_sub_f32 v93, v93, v222\n"
"v_sub_f32 v94, v94, v222\n"
"v_sub_f32 v95, v95, v222\n"
"v_sub_f32 v192, v192, v222\n"
"v_sub_f32 v193, v193, v222\n"
"v_sub_f32 v194, v194, v222\n"
"v_sub_f32 v195, v195, v222\n"
"v_sub_f32 v196, v196, v222\n"
"v_sub_f32 v197, v197, v222\n"
"v_sub_f32 v198, v198, v222\n"
"v_sub_f32 v199, v199, v222\n"
"v_sub_f32 v200, v200, v222\n"
"v_sub_f32 v201, v201, v222\n"
"v_sub_f32 v202, v202, v222\n"
"v_sub_f32 v203, v203, v222\n"
"v_sub_f32 v204, v204, v222\n"
"v_sub_f32 v205, v205, v222\n"
"v_sub_f32 v206, v206, v222\n"
"v_sub_f32 v207, v207, v222\n"
"v_accvgpr_read_b32 v227, a64\n"
"v_ldexp_f32 v227, v227, v223\n"
"v_accvgpr_write_b32 a64, v227\n"
"v_accvgpr_read_b32 v228, a65\n"
"v_ldexp_f32 v228, v228, v223\n"
"v_accvgpr_write_b32 a65, v228\n"
"v_accvgpr_read_b32 v229, a66\n"
"v_ldexp_f32 v229, v229, v223\n"
"v_accvgpr_write_b32 a66, v229\n"
"v_accvgpr_read_b32 v230, a67\n"
"v_ldexp_f32 v230, v230, v223\n"
"v_accvgpr_write_b32 a67, v230\n"
"v_accvgpr_read_b32 v227, a68\n"
"v_ldexp_f32 v227, v227, v223\n"
"v_accvgpr_write_b32 a68, v227\n"
"v_accvgpr_read_b32 v228, a69\n"
"v_ldexp_f32 v228, v228, v223\n"
"v_accvgpr_write_b32 a69, v228\n"
"v_accvgpr_read_b32 v229, a70\n"
"v_ldexp_f32 v229, v229, v223\n"
"v_accvgpr_write_b32 a70, v229\n"
"v_accvgpr_read_b32 v230, a71\n"
"v_ldexp_f32 v230, v230, v223\n"
"v_accvgpr_write_b32 a71, v230\n"
"v_accvgpr_read_b32 v227, a72\n"
"v_ldexp_f32 v227, v227, v223\n"
"v_accvgpr_write_b32 a72, v227\n"
"v_accvgpr_read_b32 v228, a73\n"
"v_ldexp_f32 v228, v228, v223\n"
"v_accvgpr_write_b32 a73, v228\n"
"v_accvgpr_read_b32 v229, a74\n"
"v_ldexp_f32 v229, v229, v223\n"
"v_accvgpr_write_b32 a74, v229\n"
"v_accvgpr_read_b32 v230, a75\n"
"v_ldexp_f32 v230, v230, v223\n"
"v_accvgpr_write_b32 a75, v230\n"
"v_accvgpr_read_b32 v227, a76\n"
"v_ldexp_f32 v227, v227, v223\n"
"v_accvgpr_write_b32 a76, v227\n"
"v_accvgpr_read_b32 v228, a77\n"
"v_ldexp_f32 v228, v228, v223\n"
"v_accvgpr_write_b32 a77, v228\n"
"v_accvgpr_read_b32 v229, a78\n"
"v_ldexp_f32 v229, v229, v223\n"
"v_accvgpr_write_b32 a78, v229\n"
"v_accvgpr_read_b32 v230, a79\n"
"v_ldexp_f32 v230, v230, v223\n"
"v_accvgpr_write_b32 a79, v230\n"
"v_accvgpr_read_b32 v227, a80\n"
"v_ldexp_f32 v227, v227, v223\n"
"v_accvgpr_write_b32 a80, v227\n"
"v_accvgpr_read_b32 v228, a81\n"
"v_ldexp_f32 v228, v228, v223\n"
"v_accvgpr_write_b32 a81, v228\n"
"v_accvgpr_read_b32 v229, a82\n"
"v_ldexp_f32 v229, v229, v223\n"
"v_accvgpr_write_b32 a82, v229\n"
"v_accvgpr_read_b32 v230, a83\n"
"v_ldexp_f32 v230, v230, v223\n"
"v_accvgpr_write_b32 a83, v230\n"
"v_accvgpr_read_b32 v227, a84\n"
"v_ldexp_f32 v227, v227, v223\n"
"v_accvgpr_write_b32 a84, v227\n"
"v_accvgpr_read_b32 v228, a85\n"
"v_ldexp_f32 v228, v228, v223\n"
"v_accvgpr_write_b32 a85, v228\n"
"v_accvgpr_read_b32 v229, a86\n"
"v_ldexp_f32 v229, v229, v223\n"
"v_accvgpr_write_b32 a86, v229\n"
"v_accvgpr_read_b32 v230, a87\n"
"v_ldexp_f32 v230, v230, v223\n"
"v_accvgpr_write_b32 a87, v230\n"
"v_accvgpr_read_b32 v227, a88\n"
"v_ldexp_f32 v227, v227, v223\n"
"v_accvgpr_write_b32 a88, v227\n"
"v_accvgpr_read_b32 v228, a89\n"
"v_ldexp_f32 v228, v228, v223\n"
"v_accvgpr_write_b32 a89, v228\n"
"v_accvgpr_read_b32 v229, a90\n"
"v_ldexp_f32 v229, v229, v223\n"
"v_accvgpr_write_b32 a90, v229\n"
"v_accvgpr_read_b32 v230, a91\n"
"v_ldexp_f32 v230, v230, v223\n"
"v_accvgpr_write_b32 a91, v230\n"
"v_accvgpr_read_b32 v227, a92\n"
"v_ldexp_f32 v227, v227, v223\n"
"v_accvgpr_write_b32 a92, v227\n"
"v_accvgpr_read_b32 v228, a93\n"
"v_ldexp_f32 v228, v228, v223\n"
"v_accvgpr_write_b32 a93, v228\n"
"v_accvgpr_read_b32 v229, a94\n"
"v_ldexp_f32 v229, v229, v223\n"
"v_accvgpr_write_b32 a94, v229\n"
"v_accvgpr_read_b32 v230, a95\n"
"v_ldexp_f32 v230, v230, v223\n"
"v_accvgpr_write_b32 a95, v230\n"
"v_accvgpr_read_b32 v227, a96\n"
"v_ldexp_f32 v227, v227, v223\n"
"v_accvgpr_write_b32 a96, v227\n"
"v_accvgpr_read_b32 v228, a97\n"
"v_ldexp_f32 v228, v228, v223\n"
"v_accvgpr_write_b32 a97, v228\n"
"v_accvgpr_read_b32 v229, a98\n"
"v_ldexp_f32 v229, v229, v223\n"
"v_accvgpr_write_b32 a98, v229\n"
"v_accvgpr_read_b32 v230, a99\n"
"v_ldexp_f32 v230, v230, v223\n"
"v_accvgpr_write_b32 a99, v230\n"
"v_accvgpr_read_b32 v227, a100\n"
"v_ldexp_f32 v227, v227, v223\n"
"v_accvgpr_write_b32 a100, v227\n"
"v_accvgpr_read_b32 v228, a101\n"
"v_ldexp_f32 v228, v228, v223\n"
"v_accvgpr_write_b32 a101, v228\n"
"v_accvgpr_read_b32 v229, a102\n"
"v_ldexp_f32 v229, v229, v223\n"
"v_accvgpr_write_b32 a102, v229\n"
"v_accvgpr_read_b32 v230, a103\n"
"v_ldexp_f32 v230, v230, v223\n"
"v_accvgpr_write_b32 a103, v230\n"
"v_accvgpr_read_b32 v227, a104\n"
"v_ldexp_f32 v227, v227, v223\n"
"v_accvgpr_write_b32 a104, v227\n"
"v_accvgpr_read_b32 v228, a105\n"
"v_ldexp_f32 v228, v228, v223\n"
"v_accvgpr_write_b32 a105, v228\n"
"v_accvgpr_read_b32 v229, a106\n"
"v_ldexp_f32 v229, v229, v223\n"
"v_accvgpr_write_b32 a106, v229\n"
"v_accvgpr_read_b32 v230, a107\n"
"v_ldexp_f32 v230, v230, v223\n"
"v_accvgpr_write_b32 a107, v230\n"
"v_accvgpr_read_b32 v227, a108\n"
"v_ldexp_f32 v227, v227, v223\n"
"v_accvgpr_write_b32 a108, v227\n"
"v_accvgpr_read_b32 v228, a109\n"
"v_ldexp_f32 v228, v228, v223\n"
"v_accvgpr_write_b32 a109, v228\n"
"v_accvgpr_read_b32 v229, a110\n"
"v_ldexp_f32 v229, v229, v223\n"
"v_accvgpr_write_b32 a110, v229\n"
"v_accvgpr_read_b32 v230, a111\n"
"v_ldexp_f32 v230, v230, v223\n"
"v_accvgpr_write_b32 a111, v230\n"
"v_accvgpr_read_b32 v227, a112\n"
"v_ldexp_f32 v227, v227, v223\n"
"v_accvgpr_write_b32 a112, v227\n"
"v_accvgpr_read_b32 v228, a113\n"
"v_ldexp_f32 v228, v228, v223\n"
"v_accvgpr_write_b32 a113, v228\n"
"v_accvgpr_read_b32 v229, a114\n"
"v_ldexp_f32 v229, v229, v223\n"
"v_accvgpr_write_b32 a114, v229\n"
"v_accvgpr_read_b32 v230, a115\n"
"v_ldexp_f32 v230, v230, v223\n"
"v_accvgpr_write_b32 a115, v230\n"
"v_accvgpr_read_b32 v227, a116\n"
"v_ldexp_f32 v227, v227, v223\n"
"v_accvgpr_write_b32 a116, v227\n"
"v_accvgpr_read_b32 v228, a117\n"
"v_ldexp_f32 v228, v228, v223\n"
"v_accvgpr_write_b32 a117, v228\n"
"v_accvgpr_read_b32 v229, a118\n"
"v_ldexp_f32 v229, v229, v223\n"
"v_accvgpr_write_b32 a118, v229\n"
"v_accvgpr_read_b32 v230, a119\n"
"v_ldexp_f32 v230, v230, v223\n"
"v_accvgpr_write_b32 a119, v230\n"
"v_accvgpr_read_b32 v227, a120\n"
"v_ldexp_f32 v227, v227, v223\n"
"v_accvgpr_write_b32 a120, v227\n"
"v_accvgpr_read_b32 v228, a121\n"
"v_ldexp_f32 v228, v228, v223\n"
"v_accvgpr_write_b32 a121, v228\n"
"v_accvgpr_read_b32 v229, a122\n"
"v_ldexp_f32 v229, v229, v223\n"
"v_accvgpr_write_b32 a122, v229\n"
"v_accvgpr_read_b32 v230, a123\n"
"v_ldexp_f32 v230, v230, v223\n"
"v_accvgpr_write_b32 a123, v230\n"
"v_accvgpr_read_b32 v227, a124\n"
"v_ldexp_f32 v227, v227, v223\n"
"v_accvgpr_write_b32 a124, v227\n"
"v_accvgpr_read_b32 v228, a125\n"
"v_ldexp_f32 v228, v228, v223\n"
"v_accvgpr_write_b32 a125, v228\n"
"v_accvgpr_read_b32 v229, a126\n"
"v_ldexp_f32 v229, v229, v223\n"
"v_accvgpr_write_b32 a126, v229\n"
"v_accvgpr_read_b32 v230, a127\n"
"v_ldexp_f32 v230, v230, v223\n"
"v_accvgpr_write_b32 a127, v230\n"
"v_ldexp_f32 v211, v211, v223\n"
"v_exp_f32 v112, v144\n"
"v_exp_f32 v113, v145\n"
"v_exp_f32 v114, v146\n"
"v_exp_f32 v115, v147\n"
"v_exp_f32 v116, v148\n"
"v_exp_f32 v117, v149\n"
"v_exp_f32 v118, v150\n"
"v_exp_f32 v119, v151\n"
"v_exp_f32 v120, v152\n"
"v_exp_f32 v121, v153\n"
"v_exp_f32 v122, v154\n"
"v_exp_f32 v123, v155\n"
"v_exp_f32 v124, v156\n"
"v_exp_f32 v125, v157\n"
"v_exp_f32 v126, v158\n"
"v_exp_f32 v127, v159\n"
"s_nop 0\n"
"v_add_f32 v209, v112, v113\n"
"v_add_f32 v209, v209, v114\n"
"v_add_f32 v209, v209, v115\n"
"v_add_f32 v209, v209, v116\n"
"v_add_f32 v209, v209, v117\n"
"v_add_f32 v209, v209, v118\n"
"v_add_f32 v209, v209, v119\n"
"v_add_f32 v209, v209, v120\n"
"v_add_f32 v209, v209, v121\n"
"v_add_f32 v209, v209, v122\n"
"v_add_f32 v209, v209, v123\n"
"v_add_f32 v209, v209, v124\n"
"v_add_f32 v209, v209, v125\n"
"v_add_f32 v209, v209, v126\n"
"v_add_f32 v209, v209, v127\n"
"v_cvt_pk_bf16_f32 v168, v112, v113\n"
"v_cvt_pk_bf16_f32 v169, v114, v115\n"
"v_cvt_pk_bf16_f32 v170, v116, v117\n"
"v_cvt_pk_bf16_f32 v171, v118, v119\n"
"v_cvt_pk_bf16_f32 v172, v120, v121\n"
"v_cvt_pk_bf16_f32 v173, v122, v123\n"
"v_cvt_pk_bf16_f32 v174, v124, v125\n"
"v_cvt_pk_bf16_f32 v175, v126, v127\n"
"s_nop 1\n"
"s_branch L_afr48_%=\n"
"L_spin3_%=:\n"
"s_mov_b32 s61, 0x200000\n"
"L_spl5_%=:\n"
"s_sleep 1\n"
"s_movk_i32 s58, 0\n"
"buffer_load_dwordx2 v[242:243], v244, s[44:47], s58 offen sc1\n"
"s_waitcnt vmcnt(0)\n"
"v_cmp_ne_u64 vcc, s[96:97], v[242:243]\n"
"s_sub_u32 s61, s61, 1\n"
"s_cbranch_scc1 L_spinr4_%=\n"
"s_nop 1\n"
"s_cbranch_vccnz L_spl5_%=\n"
"s_branch L_spinr4_%=\n"
"L_hspin7_%=:\n"
"s_mov_b32 s61, 0x200000\n"
"L_hspl9_%=:\n"
"s_sleep 1\n"
"s_movk_i32 s58, 16384\n"
"buffer_load_dwordx2 v[248:249], v250, s[44:47], s58 offen sc1\n"
"s_waitcnt vmcnt(0)\n"
"v_cmp_ne_u64 vcc, s[96:97], v[248:249]\n"
"s_sub_u32 s61, s61, 1\n"
"s_cbranch_scc1 L_hspinr8_%=\n"
"s_nop 1\n"
"s_cbranch_vccnz L_hspl9_%=\n"
"s_branch L_hspinr8_%=\n"
"L_spin11_%=:\n"
"s_mov_b32 s61, 0x200000\n"
"L_spl13_%=:\n"
"s_sleep 1\n"
"s_movk_i32 s58, 0\n"
"buffer_load_dwordx2 v[242:243], v244, s[44:47], s58 offen sc1\n"
"s_waitcnt vmcnt(0)\n"
"v_cmp_ne_u64 vcc, s[96:97], v[242:243]\n"
"s_sub_u32 s61, s61, 1\n"
"s_cbranch_scc1 L_spinr12_%=\n"
"s_nop 1\n"
"s_cbranch_vccnz L_spl13_%=\n"
"s_branch L_spinr12_%=\n"
"L_hspin14_%=:\n"
"s_mov_b32 s61, 0x200000\n"
"L_hspl16_%=:\n"
"s_sleep 1\n"
"s_movk_i32 s58, 16384\n"
"buffer_load_dwordx2 v[248:249], v250, s[44:47], s58 offen sc1\n"
"s_waitcnt vmcnt(0)\n"
"v_cmp_ne_u64 vcc, s[96:97], v[248:249]\n"
"s_sub_u32 s61, s61, 1\n"
"s_cbranch_scc1 L_hspinr15_%=\n"
"s_nop 1\n"
"s_cbranch_vccnz L_hspl16_%=\n"
"s_branch L_hspinr15_%=\n"
"L_end_%=:\n"
  :: "s"(ka), "s"(wave), "s"(blk) : "v0", "v1", "v2", "v3", "v4", "v5", "v6", "v7", "v8", "v9", "v10", "v11", "v12", "v13", "v14", "v15", "v16", "v17", "v18", "v19", "v20", "v21", "v22", "v23", "v24", "v25", "v26", "v27", "v28", "v29", "v30", "v31", "v32", "v33", "v34", "v35", "v36", "v37", "v38", "v39", "v40", "v41", "v42", "v43", "v44", "v45", "v46", "v47", "v48", "v49", "v50", "v51", "v52", "v53", "v54", "v55", "v56", "v57", "v58", "v59", "v60", "v61", "v62", "v63", "v64", "v65", "v66", "v67", "v68", "v69", "v70", "v71", "v72", "v73", "v74", "v75", "v76", "v77", "v78", "v79", "v80", "v81", "v82", "v83", "v84", "v85", "v86", "v87", "v88", "v89", "v90", "v91", "v92", "v93", "v94", "v95", "v96", "v97", "v98", "v99", "v100", "v101", "v102", "v103", "v104", "v105", "v106", "v107", "v108", "v109", "v110", "v111", "v112", "v113", "v114", "v115", "v116", "v117", "v118", "v119", "v120", "v121", "v122", "v123", "v124", "v125", "v126", "v127", "v128", "v129", "v130", "v131", "v132", "v133", "v134", "v135", "v136", "v137", "v138", "v139", "v140", "v141", "v142", "v143", "v144", "v145", "v146", "v147", "v148", "v149", "v150", "v151", "v152", "v153", "v154", "v155", "v156", "v157", "v158", "v159", "v160", "v161", "v162", "v163", "v164", "v165", "v166", "v167", "v168", "v169", "v170", "v171", "v172", "v173", "v174", "v175", "v176", "v177", "v178", "v179", "v180", "v181", "v182", "v183", "v184", "v185", "v186", "v187", "v188", "v189", "v190", "v191", "v192", "v193", "v194", "v195", "v196", "v197", "v198", "v199", "v200", "v201", "v202", "v203", "v204", "v205", "v206", "v207", "v208", "v209", "v210", "v211", "v212", "v213", "v214", "v215", "v216", "v217", "v218", "v219", "v220", "v221", "v222", "v223", "v224", "v225", "v226", "v227", "v228", "v229", "v230", "v231", "v232", "v233", "v234", "v235", "v236", "v237", "v238", "v239", "v240", "v241", "v242", "v243", "v244", "v245", "v246", "v247", "v248", "v249", "v250", "v251", "v252", "v253", "v254", "v255", "a0", "a1", "a2", "a3", "a4", "a5", "a6", "a7", "a8", "a9", "a10", "a11", "a12", "a13", "a14", "a15", "a16", "a17", "a18", "a19", "a20", "a21", "a22", "a23", "a24", "a25", "a26", "a27", "a28", "a29", "a30", "a31", "a32", "a33", "a34", "a35", "a36", "a37", "a38", "a39", "a40", "a41", "a42", "a43", "a44", "a45", "a46", "a47", "a48", "a49", "a50", "a51", "a52", "a53", "a54", "a55", "a56", "a57", "a58", "a59", "a60", "a61", "a62", "a63", "a64", "a65", "a66", "a67", "a68", "a69", "a70", "a71", "a72", "a73", "a74", "a75", "a76", "a77", "a78", "a79", "a80", "a81", "a82", "a83", "a84", "a85", "a86", "a87", "a88", "a89", "a90", "a91", "a92", "a93", "a94", "a95", "a96", "a97", "a98", "a99", "a100", "a101", "a102", "a103", "a104", "a105", "a106", "a107", "a108", "a109", "a110", "a111", "a112", "a113", "a114", "a115", "a116", "a117", "a118", "a119", "a120", "a121", "a122", "a123", "a124", "a125", "a126", "a127", "a128", "a129", "a130", "a131", "a132", "a133", "a134", "a135", "a136", "a137", "a138", "a139", "a140", "a141", "a142", "a143", "a144", "a145", "a146", "a147", "a148", "a149", "a150", "a151", "a152", "a153", "a154", "a155", "a156", "a157", "a158", "a159", "a160", "a161", "a162", "a163", "a164", "a165", "a166", "a167", "a168", "a169", "a170", "a171", "a172", "a173", "a174", "a175", "a176", "a177", "a178", "a179", "a180", "a181", "a182", "a183", "a184", "a185", "a186", "a187", "a188", "a189", "a190", "a191", "a192", "a193", "a194", "a195", "a196", "a197", "a198", "a199", "a200", "a201", "a202", "a203", "a204", "a205", "a206", "a207", "a208", "a209", "a210", "a211", "a212", "a213", "a214", "a215", "a216", "a217", "a218", "a219", "a220", "a221", "a222", "a223", "a224", "a225", "a226", "a227", "a228", "a229", "a230", "a231", "a232", "a233", "a234", "a235", "a236", "a237", "a238", "a239", "a240", "a241", "a242", "a243", "a244", "a245", "a246", "a247", "a248", "a249", "a250", "a251", "a252", "a253", "a254", "a255", "s8", "s9", "s10", "s11", "s12", "s13", "s14", "s15", "s16", "s17", "s18", "s19", "s20", "s21", "s22", "s23", "s24", "s25", "s26", "s27", "s28", "s29", "s30", "s31", "s32", "s33", "s34", "s35", "s36", "s37", "s38", "s39", "s40", "s41", "s42", "s43", "s44", "s45", "s46", "s47", "s48", "s49", "s50", "s51", "s52", "s53", "s54", "s55", "s56", "s57", "s58", "s59", "s60", "s61", "s62", "s63", "s64", "s65", "s66", "s67", "s68", "s69", "s70", "s71", "s72", "s73", "s74", "s75", "s76", "s77", "s78", "s79", "s80", "s81", "s82", "s83", "s84", "s85", "s86", "s87", "s88", "s89", "s90", "s91", "s92", "s93", "s94", "s95", "s96", "s97", "s98", "s99", "s100", "s101", "vcc", "memory", "scc");
}
__global__ __launch_bounds__(256, 1) void k3_oproj(const u16* wo, char* ws) {
  unsigned wave = __builtin_amdgcn_readfirstlane(threadIdx.x >> 6); unsigned blk = blockIdx.x;
  asm volatile(
"s_getpc_b64 s[64:65]\n"
"L_kpc_%=:\n"
"s_and_b32 s65, s65, 0xffff\n"
"s_mov_b32 s66, L_kend_%=-L_kpc_%=\n"
"s_mov_b32 s67, 0x20000\n"
"s_lshr_b32 s68, %3, 3\n"
"s_lshl_b32 s68, s68, 2\n"
"s_add_u32 s68, s68, %2\n"
"s_lshl_b32 s68, s68, 12\n"
"v_mbcnt_lo_u32_b32 v255, -1, 0\n"
"v_mbcnt_hi_u32_b32 v255, -1, v255\n"
"v_lshlrev_b32 v255, 6, v255\n"
"buffer_load_dword a255, v255, s[64:67], s68 offen\n"
"s_mov_b32 s48, %2\n"
"s_mov_b32 s49, %3\n"
"s_bfe_u32 s56, s49, 0x20001\n"
"s_bfe_u32 s55, s49, 0x20003\n"
"s_and_b32 s50, s49, 1\n"
"s_lshl_b32 s50, s50, 3\n"
"s_lshr_b32 s51, s49, 5\n"
"s_add_u32 s54, s50, s51\n"
"s_and_b32 s57, s48, 1\n"
"s_lshr_b32 s58, s48, 1\n"
"s_lshl_b32 s50, s56, 20\n"
"s_lshl_b32 s51, s58, 19\n"
"s_add_u32 s50, s50, s51\n"
"s_lshl_b32 s51, s55, 8\n"
"s_add_u32 s50, s50, s51\n"
"s_mov_b64 s[24:25], %0\n"
"s_add_u32 s24, s24, s50\n"
"s_addc_u32 s25, s25, 0\n"
"s_and_b32 s25, s25, 0xffff\n"
"s_mov_b32 s26, 0x80000\n"
"s_mov_b32 s27, 0x20000\n"
"v_mbcnt_lo_u32_b32 v220, -1, 0\n"
"v_mbcnt_hi_u32_b32 v220, -1, v220\n"
"v_lshrrev_b32 v221, 4, v220\n"
"v_and_b32 v224, 15, v220\n"
"v_lshrrev_b32 v222, 2, v224\n"
"v_and_b32 v223, 3, v220\n"
"s_lshl_b32 s52, s57, 4\n"
"v_add_u32 v225, s52, v221\n"
"v_lshlrev_b32 v225, 10, v225\n"
"v_lshlrev_b32 v226, 1, v221\n"
"v_xor_b32 v226, v226, v224\n"
"v_lshlrev_b32 v226, 4, v226\n"
"v_add_u32 v226, v226, v225\n"
"v_mov_b32 v208, v226\n"
"v_lshlrev_b32 v226, 1, v221\n"
"v_xor_b32 v226, v226, v224\n"
"v_xor_b32 v226, 8, v226\n"
"v_lshlrev_b32 v226, 4, v226\n"
"v_add_u32 v226, v226, v225\n"
"v_add_u32 v209, 3072, v226\n"
"v_lshlrev_b32 v226, 1, v221\n"
"v_xor_b32 v226, v226, v224\n"
"v_lshlrev_b32 v226, 4, v226\n"
"v_add_u32 v226, v226, v225\n"
"v_add_u32 v210, 6144, v226\n"
"v_lshlrev_b32 v226, 1, v221\n"
"v_xor_b32 v226, v226, v224\n"
"v_xor_b32 v226, 8, v226\n"
"v_lshlrev_b32 v226, 4, v226\n"
"v_add_u32 v226, v226, v225\n"
"v_add_u32 v211, 9216, v226\n"
"s_lshl_b32 s62, s58, 13\n"
"s_lshl_b32 s50, s57, 12\n"
"s_add_u32 s62, s62, s50\n"
"s_lshl_b32 s50, s54, 20\n"
"s_lshl_b32 s51, s57, 19\n"
"s_add_u32 s50, s50, s51\n"
"s_lshl_b32 s51, s56, 16\n"
"s_add_u32 s50, s50, s51\n"
"s_lshl_b32 s51, s58, 15\n"
"s_add_u32 s50, s50, s51\n"
"s_mov_b64 s[28:29], %1\n"
"s_add_u32 s28, s28, s50\n"
"s_addc_u32 s29, s29, 0\n"
"s_add_u32 s28, s28, 39845888\n"
"s_addc_u32 s29, s29, 0\n"
"s_and_b32 s29, s29, 0xffff\n"
"s_mov_b32 s30, 0x80000\n"
"s_mov_b32 s31, 0x20000\n"
"v_lshrrev_b32 v225, 1, v221\n"
"v_lshlrev_b32 v225, 10, v225\n"
"v_and_b32 v226, 1, v221\n"
"v_lshlrev_b32 v227, 9, v226\n"
"v_add_u32 v225, v225, v227\n"
"v_lshlrev_b32 v227, 4, v224\n"
"v_add_u32 v212, v225, v227\n"
"v_add_u32 v213, 0x40000, v212\n"
"s_add_u32 m0, s62, 0\n"
"s_mov_b32 s61, 0\n"
"s_nop 0\n"
"buffer_load_dwordx4 v208, s[24:27], s61 offen offset:0 lds\n"
"buffer_load_dwordx4 v209, s[24:27], s61 offen offset:1024 lds\n"
"buffer_load_dwordx4 v210, s[24:27], s61 offen offset:2048 lds\n"
"buffer_load_dwordx4 v211, s[24:27], s61 offen offset:3072 lds\n"
"s_movk_i32 s60, 0\n"
"buffer_load_dwordx4 a[0:3], v212, s[28:31], s60 offen offset:0\n"
"buffer_load_dwordx4 a[4:7], v212, s[28:31], s60 offen offset:256\n"
"buffer_load_dwordx4 a[8:11], v213, s[28:31], s60 offen offset:0\n"
"buffer_load_dwordx4 a[12:15], v213, s[28:31], s60 offen offset:256\n"
"s_add_u32 m0, s62, 16384\n"
"s_mov_b32 s61, 32768\n"
"s_nop 0\n"
"buffer_load_dwordx4 v208, s[24:27], s61 offen offset:0 lds\n"
"buffer_load_dwordx4 v209, s[24:27], s61 offen offset:1024 lds\n"
"buffer_load_dwordx4 v210, s[24:27], s61 offen offset:2048 lds\n"
"buffer_load_dwordx4 v211, s[24:27], s61 offen offset:3072 lds\n"
"s_add_u32 m0, s62, 32768\n"
"s_mov_b32 s61, 65536\n"
"s_nop 0\n"
"buffer_load_dwordx4 v208, s[24:27], s61 offen offset:0 lds\n"
"buffer_load_dwordx4 v209, s[24:27], s61 offen offset:1024 lds\n"
"buffer_load_dwordx4 v210, s[24:27], s61 offen offset:2048 lds\n"
"buffer_load_dwordx4 v211, s[24:27], s61 offen offset:3072 lds\n"
"s_movk_i32 s60, 2048\n"
"buffer_load_dwordx4 a[16:19], v212, s[28:31], s60 offen offset:0\n"
"buffer_load_dwordx4 a[20:23], v212, s[28:31], s60 offen offset:256\n"
"buffer_load_dwordx4 a[24:27], v213, s[28:31], s60 offen offset:0\n"
"buffer_load_dwordx4 a[28:31], v213, s[28:31], s60 offen offset:256\n"
"v_lshrrev_b32 v225, 1, v221\n"
"v_lshlrev_b32 v225, 4, v225\n"
"v_and_b32 v226, 1, v221\n"
"v_lshlrev_b32 v226, 2, v226\n"
"v_add_u32 v226, v226, v222\n"
"v_add_u32 v225, v225, v226\n"
"v_lshlrev_b32 v225, 8, v225\n"
"v_lshlrev_b32 v226, 5, v226\n"
"v_or_b32 v225, v225, v226\n"
"v_lshlrev_b32 v226, 3, v223\n"
"v_or_b32 v225, v225, v226\n"
"s_xor_b32 s50, s58, 1\n"
"s_lshl_b32 s50, s50, 7\n"
"s_lshl_b32 s51, s58, 13\n"
"s_or_b32 s50, s50, s51\n"
"v_xor_b32 v192, s50, v225\n"
"v_xor_b32 v193, 32, v192\n"
"v_xor_b32 v194, 64, v192\n"
"v_xor_b32 v195, 96, v192\n"
"v_xor_b32 v196, 128, v192\n"
"v_xor_b32 v197, 160, v192\n"
"v_xor_b32 v198, 192, v192\n"
"v_xor_b32 v199, 224, v192\n"
"v_add_u32 v200, 0x10000, v192\n"
"v_add_u32 v201, 0x10000, v193\n"
"v_add_u32 v202, 0x10000, v194\n"
"v_add_u32 v203, 0x10000, v195\n"
"v_add_u32 v204, 0x10000, v196\n"
"v_add_u32 v205, 0x10000, v197\n"
"v_add_u32 v206, 0x10000, v198\n"
"v_add_u32 v207, 0x10000, v199\n"
"v_lshlrev_b32 v216, 4, v220\n"
"s_lshl_b32 s50, s48, 14\n"
"v_add_u32 v214, s50, v216\n"
"v_xor_b32 v215, 0x8000, v214\n"
"s_lshl_b32 s50, s56, 21\n"
"s_lshl_b32 s51, s54, 17\n"
"s_add_u32 s50, s50, s51\n"
"s_lshl_b32 s51, s57, 16\n"
"s_add_u32 s50, s50, s51\n"
"s_lshl_b32 s51, s55, 12\n"
"s_add_u32 s50, s50, s51\n"
"s_lshl_b32 s51, s58, 11\n"
"s_add_u32 s50, s50, s51\n"
"s_mov_b64 s[32:33], %1\n"
"s_add_u32 s32, s32, s50\n"
"s_addc_u32 s33, s33, 0\n"
"s_add_u32 s32, s32, 57671680\n"
"s_addc_u32 s33, s33, 0\n"
"s_and_b32 s33, s33, 0xffff\n"
"s_mov_b32 s34, 0x10000\n"
"s_mov_b32 s35, 0x20000\n"
"s_mov_b32 s64, 0\n"
"s_mov_b32 s65, 16384\n"
"s_mov_b32 s66, 32768\n"
"s_mov_b32 s67, 49152\n"
"s_waitcnt vmcnt(16)\n"
"s_barrier\n"
"ds_read_b64_tr_b16 v[128:129], v192 offset:0\n"
"ds_read_b64_tr_b16 v[130:131], v192 offset:2048\n"
"ds_read_b64_tr_b16 v[132:133], v193 offset:0\n"
"ds_read_b64_tr_b16 v[134:135], v193 offset:2048\n"
"ds_read_b64_tr_b16 v[136:137], v194 offset:0\n"
"ds_read_b64_tr_b16 v[138:139], v194 offset:2048\n"
"ds_read_b64_tr_b16 v[140:141], v195 offset:0\n"
"ds_read_b64_tr_b16 v[142:143], v195 offset:2048\n"
"ds_read_b64_tr_b16 v[144:145], v196 offset:0\n"
"ds_read_b64_tr_b16 v[146:147], v196 offset:2048\n"
"ds_read_b64_tr_b16 v[148:149], v197 offset:0\n"
"ds_read_b64_tr_b16 v[150:151], v197 offset:2048\n"
"ds_read_b64_tr_b16 v[152:153], v198 offset:0\n"
"ds_read_b64_tr_b16 v[154:155], v198 offset:2048\n"
"ds_read_b64_tr_b16 v[156:157], v199 offset:0\n"
"ds_read_b64_tr_b16 v[158:159], v199 offset:2048\n"
"s_waitcnt vmcnt(8) lgkmcnt(0)\n"
"s_barrier\n"
"v_mfma_f32_16x16x32_bf16 v[0:3], v[128:131], a[0:3], 0\n"
"ds_read_b64_tr_b16 v[160:161], v192 offset:16384\n"
"s_add_u32 m0, s62, 49152\n"
"s_mov_b32 s61, 98304\n"
"v_mfma_f32_16x16x32_bf16 v[4:7], v[128:131], a[4:7], 0\n"
"buffer_load_dwordx4 v208, s[24:27], s61 offen offset:0 lds\n"
"v_mfma_f32_16x16x32_bf16 v[8:11], v[128:131], a[8:11], 0\n"
"ds_read_b64_tr_b16 v[162:163], v192 offset:18432\n"
"v_mfma_f32_16x16x32_bf16 v[12:15], v[128:131], a[12:15], 0\n"
"v_mfma_f32_16x16x32_bf16 v[16:19], v[132:135], a[0:3], 0\n"
"ds_read_b64_tr_b16 v[164:165], v193 offset:16384\n"
"v_mfma_f32_16x16x32_bf16 v[20:23], v[132:135], a[4:7], 0\n"
"buffer_load_dwordx4 v209, s[24:27], s61 offen offset:1024 lds\n"
"v_mfma_f32_16x16x32_bf16 v[24:27], v[132:135], a[8:11], 0\n"
"ds_read_b64_tr_b16 v[166:167], v193 offset:18432\n"
"v_mfma_f32_16x16x32_bf16 v[28:31], v[132:135], a[12:15], 0\n"
"v_mfma_f32_16x16x32_bf16 v[32:35], v[136:139], a[0:3], 0\n"
"ds_read_b64_tr_b16 v[168:169], v194 offset:16384\n"
"v_mfma_f32_16x16x32_bf16 v[36:39], v[136:139], a[4:7], 0\n"
"buffer_load_dwordx4 v210, s[24:27], s61 offen offset:2048 lds\n"
"v_mfma_f32_16x16x32_bf16 v[40:43], v[136:139], a[8:11], 0\n"
"ds_read_b64_tr_b16 v[170:171], v194 offset:18432\n"
"v_mfma_f32_16x16x32_bf16 v[44:47], v[136:139], a[12:15], 0\n"
"v_mfma_f32_16x16x32_bf16 v[48:51], v[140:143], a[0:3], 0\n"
"ds_read_b64_tr_b16 v[172:173], v195 offset:16384\n"
"v_mfma_f32_16x16x32_bf16 v[52:55], v[140:143], a[4:7], 0\n"
"buffer_load_dwordx4 v211, s[24:27], s61 offen offset:3072 lds\n"
"v_mfma_f32_16x16x32_bf16 v[56:59], v[140:143], a[8:11], 0\n"
"ds_read_b64_tr_b16 v[174:175], v195 offset:18432\n"
"v_mfma_f32_16x16x32_bf16 v[60:63], v[140:143], a[12:15], 0\n"
"v_mfma_f32_16x16x32_bf16 v[64:67], v[144:147], a[0:3], 0\n"
"ds_read_b64_tr_b16 v[176:177], v196 offset:16384\n"
"s_movk_i32 s60, 4096\n"
"v_mfma_f32_16x16x32_bf16 v[68:71], v[144:147], a[4:7], 0\n"
"buffer_load_dwordx4 a[32:35], v212, s[28:31], s60 offen offset:0\n"
"v_mfma_f32_16x16x32_bf16 v[72:75], v[144:147], a[8:11], 0\n"
"ds_read_b64_tr_b16 v[178:179], v196 offset:18432\n"
"v_mfma_f32_16x16x32_bf16 v[76:79], v[144:147], a[12:15], 0\n"
"v_mfma_f32_16x16x32_bf16 v[80:83], v[148:151], a[0:3], 0\n"
"ds_read_b64_tr_b16 v[180:181], v197 offset:16384\n"
"v_mfma_f32_16x16x32_bf16 v[84:87], v[148:151], a[4:7], 0\n"
"buffer_load_dwordx4 a[36:39], v212, s[28:31], s60 offen offset:256\n"
"v_mfma_f32_16x16x32_bf16 v[88:91], v[148:151], a[8:11], 0\n"
"ds_read_b64_tr_b16 v[182:183], v197 offset:18432\n"
"v_mfma_f32_16x16x32_bf16 v[92:95], v[148:151], a[12:15], 0\n"
"v_mfma_f32_16x16x32_bf16 v[96:99], v[152:155], a[0:3], 0\n"
"ds_read_b64_tr_b16 v[184:185], v198 offset:16384\n"
"v_mfma_f32_16x16x32_bf16 v[100:103], v[152:155], a[4:7], 0\n"
"buffer_load_dwordx4 a[40:43], v213, s[28:31], s60 offen offset:0\n"
"v_mfma_f32_16x16x32_bf16 v[104:107], v[152:155], a[8:11], 0\n"
"ds_read_b64_tr_b16 v[186:187], v198 offset:18432\n"
"v_mfma_f32_16x16x32_bf16 v[108:111], v[152:155], a[12:15], 0\n"
"v_mfma_f32_16x16x32_bf16 v[112:115], v[156:159], a[0:3], 0\n"
"ds_read_b64_tr_b16 v[188:189], v199 offset:16384\n"
"v_mfma_f32_16x16x32_bf16 v[116:119], v[156:159], a[4:7], 0\n"
"buffer_load_dwordx4 a[44:47], v213, s[28:31], s60 offen offset:256\n"
"v_mfma_f32_16x16x32_bf16 v[120:123], v[156:159], a[8:11], 0\n"
"ds_read_b64_tr_b16 v[190:191], v199 offset:18432\n"
"v_mfma_f32_16x16x32_bf16 v[124:127], v[156:159], a[12:15], 0\n"
"s_waitcnt lgkmcnt(8)\n"
"s_waitcnt vmcnt(8)\n"
"s_barrier\n"
"v_mfma_f32_16x16x32_bf16 v[0:3], v[160:163], a[16:19], v[0:3]\n"
"ds_read_b64_tr_b16 v[128:129], v192 offset:32768\n"
"s_add_u32 m0, s62, 65536\n"
"s_mov_b32 s61, 131072\n"
"v_mfma_f32_16x16x32_bf16 v[4:7], v[160:163], a[20:23], v[4:7]\n"
"buffer_load_dwordx4 v208, s[24:27], s61 offen offset:0 lds\n"
"v_mfma_f32_16x16x32_bf16 v[8:11], v[160:163], a[24:27], v[8:11]\n"
"ds_read_b64_tr_b16 v[130:131], v192 offset:34816\n"
"v_mfma_f32_16x16x32_bf16 v[12:15], v[160:163], a[28:31], v[12:15]\n"
"v_mfma_f32_16x16x32_bf16 v[16:19], v[164:167], a[16:19], v[16:19]\n"
"ds_read_b64_tr_b16 v[132:133], v193 offset:32768\n"
"v_mfma_f32_16x16x32_bf16 v[20:23], v[164:167], a[20:23], v[20:23]\n"
"buffer_load_dwordx4 v209, s[24:27], s61 offen offset:1024 lds\n"
"v_mfma_f32_16x16x32_bf16 v[24:27], v[164:167], a[24:27], v[24:27]\n"
"ds_read_b64_tr_b16 v[134:135], v193 offset:34816\n"
"v_mfma_f32_16x16x32_bf16 v[28:31], v[164:167], a[28:31], v[28:31]\n"
"v_mfma_f32_16x16x32_bf16 v[32:35], v[168:171], a[16:19], v[32:35]\n"
"ds_read_b64_tr_b16 v[136:137], v194 offset:32768\n"
"v_mfma_f32_16x16x32_bf16 v[36:39], v[168:171], a[20:23], v[36:39]\n"
"buffer_load_dwordx4 v210, s[24:27], s61 offen offset:2048 lds\n"
"v_mfma_f32_16x16x32_bf16 v[40:43], v[168:171], a[24:27], v[40:43]\n"
"ds_read_b64_tr_b16 v[138:139], v194 offset:34816\n"
"v_mfma_f32_16x16x32_bf16 v[44:47], v[168:171], a[28:31], v[44:47]\n"
"v_mfma_f32_16x16x32_bf16 v[48:51], v[172:175], a[16:19], v[48:51]\n"
"ds_read_b64_tr_b16 v[140:141], v195 offset:32768\n"
"v_mfma_f32_16x16x32_bf16 v[52:55], v[172:175], a[20:23], v[52:55]\n"
"buffer_load_dwordx4 v211, s[24:27], s61 offen offset:3072 lds\n"
"v_mfma_f32_16x16x32_bf16 v[56:59], v[172:175], a[24:27], v[56:59]\n"
"ds_read_b64_tr_b16 v[142:143], v195 offset:34816\n"
"v_mfma_f32_16x16x32_bf16 v[60:63], v[172:175], a[28:31], v[60:63]\n"
"s_waitcnt lgkmcnt(8)\n"
"v_mfma_f32_16x16x32_bf16 v[64:67], v[176:179], a[16:19], v[64:67]\n"
"ds_read_b64_tr_b16 v[144:145], v196 offset:32768\n"
"s_movk_i32 s60, 6144\n"
"v_mfma_f32_16x16x32_bf16 v[68:71], v[176:179], a[20:23], v[68:71]\n"
"buffer_load_dwordx4 a[48:51], v212, s[28:31], s60 offen offset:0\n"
"v_mfma_f32_16x16x32_bf16 v[72:75], v[176:179], a[24:27], v[72:75]\n"
"ds_read_b64_tr_b16 v[146:147], v196 offset:34816\n"
"v_mfma_f32_16x16x32_bf16 v[76:79], v[176:179], a[28:31], v[76:79]\n"
"v_mfma_f32_16x16x32_bf16 v[80:83], v[180:183], a[16:19], v[80:83]\n"
"ds_read_b64_tr_b16 v[148:149], v197 offset:32768\n"
"v_mfma_f32_16x16x32_bf16 v[84:87], v[180:183], a[20:23], v[84:87]\n"
"buffer_load_dwordx4 a[52:55], v212, s[28:31], s60 offen offset:256\n"
"v_mfma_f32_16x16x32_bf16 v[88:91], v[180:183], a[24:27], v[88:91]\n"
"ds_read_b64_tr_b16 v[150:151], v197 offset:34816\n"
"v_mfma_f32_16x16x32_bf16 v[92:95], v[180:183], a[28:31], v[92:95]\n"
"v_mfma_f32_16x16x32_bf16 v[96:99], v[184:187], a[16:19], v[96:99]\n"
"ds_read_b64_tr_b16 v[152:153], v198 offset:32768\n"
"v_mfma_f32_16x16x32_bf16 v[100:103], v[184:187], a[20:23], v[100:103]\n"
"buffer_load_dwordx4 a[56:59], v213, s[28:31], s60 offen offset:0\n"
"v_mfma_f32_16x16x32_bf16 v[104:107], v[184:187], a[24:27], v[104:107]\n"
"ds_read_b64_tr_b16 v[154:155], v198 offset:34816\n"
"v_mfma_f32_16x16x32_bf16 v[108:111], v[184:187], a[28:31], v[108:111]\n"
"v_mfma_f32_16x16x32_bf16 v[112:115], v[188:191], a[16:19], v[112:115]\n"
"ds_read_b64_tr_b16 v[156:157], v199 offset:32768\n"
"v_mfma_f32_16x16x32_bf16 v[116:119], v[188:191], a[20:23], v[116:119]\n"
"buffer_load_dwordx4 a[60:63], v213, s[28:31], s60 offen offset:256\n"
"v_mfma_f32_16x16x32_bf16 v[120:123], v[188:191], a[24:27], v[120:123]\n"
"ds_read_b64_tr_b16 v[158:159], v199 offset:34816\n"
"v_mfma_f32_16x16x32_bf16 v[124:127], v[188:191], a[28:31], v[124:127]\n"
"s_waitcnt lgkmcnt(8)\n"
"s_waitcnt vmcnt(8)\n"
"s_barrier\n"
"v_mfma_f32_16x16x32_bf16 v[0:3], v[128:131], a[32:35], v[0:3]\n"
"ds_read_b64_tr_b16 v[160:161], v192 offset:49152\n"
"s_add_u32 m0, s62, 81920\n"
"s_mov_b32 s61, 163840\n"
"v_mfma_f32_16x16x32_bf16 v[4:7], v[128:131], a[36:39], v[4:7]\n"
"buffer_load_dwordx4 v208, s[24:27], s61 offen offset:0 lds\n"
"v_mfma_f32_16x16x32_bf16 v[8:11], v[128:131], a[40:43], v[8:11]\n"
"ds_read_b64_tr_b16 v[162:163], v192 offset:51200\n"
"v_mfma_f32_16x16x32_bf16 v[12:15], v[128:131], a[44:47], v[12:15]\n"
"v_mfma_f32_16x16x32_bf16 v[16:19], v[132:135], a[32:35], v[16:19]\n"
"ds_read_b64_tr_b16 v[164:165], v193 offset:49152\n"
"v_mfma_f32_16x16x32_bf16 v[20:23], v[132:135], a[36:39], v[20:23]\n"
"buffer_load_dwordx4 v209, s[24:27], s61 offen offset:1024 lds\n"
"v_mfma_f32_16x16x32_bf16 v[24:27], v[132:135], a[40:43], v[24:27]\n"
"ds_read_b64_tr_b16 v[166:167], v193 offset:51200\n"
"v_mfma_f32_16x16x32_bf16 v[28:31], v[132:135], a[44:47], v[28:31]\n"
"v_mfma_f32_16x16x32_bf16 v[32:35], v[136:139], a[32:35], v[32:35]\n"
"ds_read_b64_tr_b16 v[168:169], v194 offset:49152\n"
"v_mfma_f32_16x16x32_bf16 v[36:39], v[136:139], a[36:39], v[36:39]\n"
"buffer_load_dwordx4 v210, s[24:27], s61 offen offset:2048 lds\n"
"v_mfma_f32_16x16x32_bf16 v[40:43], v[136:139], a[40:43], v[40:43]\n"
"ds_read_b64_tr_b16 v[170:171], v194 offset:51200\n"
"v_mfma_f32_16x16x32_bf16 v[44:47], v[136:139], a[44:47], v[44:47]\n"
"v_mfma_f32_16x16x32_bf16 v[48:51], v[140:143], a[32:35], v[48:51]\n"
"ds_read_b64_tr_b16 v[172:173], v195 offset:49152\n"
"v_mfma_f32_16x16x32_bf16 v[52:55], v[140:143], a[36:39], v[52:55]\n"
"buffer_load_dwordx4 v211, s[24:27], s61 offen offset:3072 lds\n"
"v_mfma_f32_16x16x32_bf16 v[56:59], v[140:143], a[40:43], v[56:59]\n"
"ds_read_b64_tr_b16 v[174:175], v195 offset:51200\n"
"v_mfma_f32_16x16x32_bf16 v[60:63], v[140:143], a[44:47], v[60:63]\n"
"s_waitcnt lgkmcnt(8)\n"
"v_mfma_f32_16x16x32_bf16 v[64:67], v[144:147], a[32:35], v[64:67]\n"
"ds_read_b64_tr_b16 v[176:177], v196 offset:49152\n"
"s_movk_i32 s60, 8192\n"
"v_mfma_f32_16x16x32_bf16 v[68:71], v[144:147], a[36:39], v[68:71]\n"
"buffer_load_dwordx4 a[64:67], v212, s[28:31], s60 offen offset:0\n"
"v_mfma_f32_16x16x32_bf16 v[72:75], v[144:147], a[40:43], v[72:75]\n"
"ds_read_b64_tr_b16 v[178:179], v196 offset:51200\n"
"v_mfma_f32_16x16x32_bf16 v[76:79], v[144:147], a[44:47], v[76:79]\n"
"v_mfma_f32_16x16x32_bf16 v[80:83], v[148:151], a[32:35], v[80:83]\n"
"ds_read_b64_tr_b16 v[180:181], v197 offset:49152\n"
"v_mfma_f32_16x16x32_bf16 v[84:87], v[148:151], a[36:39], v[84:87]\n"
"buffer_load_dwordx4 a[68:71], v212, s[28:31], s60 offen offset:256\n"
"v_mfma_f32_16x16x32_bf16 v[88:91], v[148:151], a[40:43], v[88:91]\n"
"ds_read_b64_tr_b16 v[182:183], v197 offset:51200\n"
"v_mfma_f32_16x16x32_bf16 v[92:95], v[148:151], a[44:47], v[92:95]\n"
"v_mfma_f32_16x16x32_bf16 v[96:99], v[152:155], a[32:35], v[96:99]\n"
"ds_read_b64_tr_b16 v[184:185], v198 offset:49152\n"
"v_mfma_f32_16x16x32_bf16 v[100:103], v[152:155], a[36:39], v[100:103]\n"
"buffer_load_dwordx4 a[72:75], v213, s[28:31], s60 offen offset:0\n"
"v_mfma_f32_16x16x32_bf16 v[104:107], v[152:155], a[40:43], v[104:107]\n"
"ds_read_b64_tr_b16 v[186:187], v198 offset:51200\n"
"v_mfma_f32_16x16x32_bf16 v[108:111], v[152:155], a[44:47], v[108:111]\n"
"v_mfma_f32_16x16x32_bf16 v[112:115], v[156:159], a[32:35], v[112:115]\n"
"ds_read_b64_tr_b16 v[188:189], v199 offset:49152\n"
"v_mfma_f32_16x16x32_bf16 v[116:119], v[156:159], a[36:39], v[116:119]\n"
"buffer_load_dwordx4 a[76:79], v213, s[28:31], s60 offen offset:256\n"
"v_mfma_f32_16x16x32_bf16 v[120:123], v[156:159], a[40:43], v[120:123]\n"
"ds_read_b64_tr_b16 v[190:191], v199 offset:51200\n"
"v_mfma_f32_16x16x32_bf16 v[124:127], v[156:159], a[44:47], v[124:127]\n"
"s_waitcnt lgkmcnt(8)\n"
"s_waitcnt vmcnt(8)\n"
"s_barrier\n"
"v_mfma_f32_16x16x32_bf16 v[0:3], v[160:163], a[48:51], v[0:3]\n"
"ds_read_b64_tr_b16 v[128:129], v200 offset:0\n"
"s_add_u32 m0, s62, 98304\n"
"s_mov_b32 s61, 196608\n"
"v_mfma_f32_16x16x32_bf16 v[4:7], v[160:163], a[52:55], v[4:7]\n"
"buffer_load_dwordx4 v208, s[24:27], s61 offen offset:0 lds\n"
"v_mfma_f32_16x16x32_bf16 v[8:11], v[160:163], a[56:59], v[8:11]\n"
"ds_read_b64_tr_b16 v[130:131], v200 offset:2048\n"
"v_mfma_f32_16x16x32_bf16 v[12:15], v[160:163], a[60:63], v[12:15]\n"
"v_mfma_f32_16x16x32_bf16 v[16:19], v[164:167], a[48:51], v[16:19]\n"
"ds_read_b64_tr_b16 v[132:133], v201 offset:0\n"
"v_mfma_f32_16x16x32_bf16 v[20:23], v[164:167], a[52:55], v[20:23]\n"
"buffer_load_dwordx4 v209, s[24:27], s61 offen offset:1024 lds\n"
"v_mfma_f32_16x16x32_bf16 v[24:27], v[164:167], a[56:59], v[24:27]\n"
"ds_read_b64_tr_b16 v[134:135], v201 offset:2048\n"
"v_mfma_f32_16x16x32_bf16 v[28:31], v[164:167], a[60:63], v[28:31]\n"
"v_mfma_f32_16x16x32_bf16 v[32:35], v[168:171], a[48:51], v[32:35]\n"
"ds_read_b64_tr_b16 v[136:137], v202 offset:0\n"
"v_mfma_f32_16x16x32_bf16 v[36:39], v[168:171], a[52:55], v[36:39]\n"
"buffer_load_dwordx4 v210, s[24:27], s61 offen offset:2048 lds\n"
"v_mfma_f32_16x16x32_bf16 v[40:43], v[168:171], a[56:59], v[40:43]\n"
"ds_read_b64_tr_b16 v[138:139], v202 offset:2048\n"
"v_mfma_f32_16x16x32_bf16 v[44:47], v[168:171], a[60:63], v[44:47]\n"
"v_mfma_f32_16x16x32_bf16 v[48:51], v[172:175], a[48:51], v[48:51]\n"
"ds_read_b64_tr_b16 v[140:141], v203 offset:0\n"
"v_mfma_f32_16x16x32_bf16 v[52:55], v[172:175], a[52:55], v[52:55]\n"
"buffer_load_dwordx4 v211, s[24:27], s61 offen offset:3072 lds\n"
"v_mfma_f32_16x16x32_bf16 v[56:59], v[172:175], a[56:59], v[56:59]\n"
"ds_read_b64_tr_b16 v[142:143], v203 offset:2048\n"
"v_mfma_f32_16x16x32_bf16 v[60:63], v[172:175], a[60:63], v[60:63]\n"
"s_waitcnt lgkmcnt(8)\n"
"v_mfma_f32_16x16x32_bf16 v[64:67], v[176:179], a[48:51], v[64:67]\n"
"ds_read_b64_tr_b16 v[144:145], v204 offset:0\n"
"s_movk_i32 s60, 10240\n"
"v_mfma_f32_16x16x32_bf16 v[68:71], v[176:179], a[52:55], v[68:71]\n"
"buffer_load_dwordx4 a[80:83], v212, s[28:31], s60 offen offset:0\n"
"v_mfma_f32_16x16x32_bf16 v[72:75], v[176:179], a[56:59], v[72:75]\n"
"ds_read_b64_tr_b16 v[146:147], v204 offset:2048\n"
"v_mfma_f32_16x16x32_bf16 v[76:79], v[176:179], a[60:63], v[76:79]\n"
"v_mfma_f32_16x16x32_bf16 v[80:83], v[180:183], a[48:51], v[80:83]\n"
"ds_read_b64_tr_b16 v[148:149], v205 offset:0\n"
"v_mfma_f32_16x16x32_bf16 v[84:87], v[180:183], a[52:55], v[84:87]\n"
"buffer_load_dwordx4 a[84:87], v212, s[28:31], s60 offen offset:256\n"
"v_mfma_f32_16x16x32_bf16 v[88:91], v[180:183], a[56:59], v[88:91]\n"
"ds_read_b64_tr_b16 v[150:151], v205 offset:2048\n"
"v_mfma_f32_16x16x32_bf16 v[92:95], v[180:183], a[60:63], v[92:95]\n"
"v_mfma_f32_16x16x32_bf16 v[96:99], v[184:187], a[48:51], v[96:99]\n"
"ds_read_b64_tr_b16 v[152:153], v206 offset:0\n"
"v_mfma_f32_16x16x32_bf16 v[100:103], v[184:187], a[52:55], v[100:103]\n"
"buffer_load_dwordx4 a[88:91], v213, s[28:31], s60 offen offset:0\n"
"v_mfma_f32_16x16x32_bf16 v[104:107], v[184:187], a[56:59], v[104:107]\n"
"ds_read_b64_tr_b16 v[154:155], v206 offset:2048\n"
"v_mfma_f32_16x16x32_bf16 v[108:111], v[184:187], a[60:63], v[108:111]\n"
"v_mfma_f32_16x16x32_bf16 v[112:115], v[188:191], a[48:51], v[112:115]\n"
"ds_read_b64_tr_b16 v[156:157], v207 offset:0\n"
"v_mfma_f32_16x16x32_bf16 v[116:119], v[188:191], a[52:55], v[116:119]\n"
"buffer_load_dwordx4 a[92:95], v213, s[28:31], s60 offen offset:256\n"
"v_mfma_f32_16x16x32_bf16 v[120:123], v[188:191], a[56:59], v[120:123]\n"
"ds_read_b64_tr_b16 v[158:159], v207 offset:2048\n"
"v_mfma_f32_16x16x32_bf16 v[124:127], v[188:191], a[60:63], v[124:127]\n"
"s_waitcnt lgkmcnt(8)\n"
"s_waitcnt vmcnt(8)\n"
"s_barrier\n"
"v_mfma_f32_16x16x32_bf16 v[0:3], v[128:131], a[64:67], v[0:3]\n"
"ds_read_b64_tr_b16 v[160:161], v200 offset:16384\n"
"s_add_u32 m0, s62, 114688\n"
"s_mov_b32 s61, 229376\n"
"v_mfma_f32_16x16x32_bf16 v[4:7], v[128:131], a[68:71], v[4:7]\n"
"buffer_load_dwordx4 v208, s[24:27], s61 offen offset:0 lds\n"
"v_mfma_f32_16x16x32_bf16 v[8:11], v[128:131], a[72:75], v[8:11]\n"
"ds_read_b64_tr_b16 v[162:163], v200 offset:18432\n"
"v_mfma_f32_16x16x32_bf16 v[12:15], v[128:131], a[76:79], v[12:15]\n"
"v_mfma_f32_16x16x32_bf16 v[16:19], v[132:135], a[64:67], v[16:19]\n"
"ds_read_b64_tr_b16 v[164:165], v201 offset:16384\n"
"v_mfma_f32_16x16x32_bf16 v[20:23], v[132:135], a[68:71], v[20:23]\n"
"buffer_load_dwordx4 v209, s[24:27], s61 offen offset:1024 lds\n"
"v_mfma_f32_16x16x32_bf16 v[24:27], v[132:135], a[72:75], v[24:27]\n"
"ds_read_b64_tr_b16 v[166:167], v201 offset:18432\n"
"v_mfma_f32_16x16x32_bf16 v[28:31], v[132:135], a[76:79], v[28:31]\n"
"v_mfma_f32_16x16x32_bf16 v[32:35], v[136:139], a[64:67], v[32:35]\n"
"ds_read_b64_tr_b16 v[168:169], v202 offset:16384\n"
"v_mfma_f32_16x16x32_bf16 v[36:39], v[136:139], a[68:71], v[36:39]\n"
"buffer_load_dwordx4 v210, s[24:27], s61 offen offset:2048 lds\n"
"v_mfma_f32_16x16x32_bf16 v[40:43], v[136:139], a[72:75], v[40:43]\n"
"ds_read_b64_tr_b16 v[170:171], v202 offset:18432\n"
"v_mfma_f32_16x16x32_bf16 v[44:47], v[136:139], a[76:79], v[44:47]\n"
"v_mfma_f32_16x16x32_bf16 v[48:51], v[140:143], a[64:67], v[48:51]\n"
"ds_read_b64_tr_b16 v[172:173], v203 offset:16384\n"
"v_mfma_f32_16x16x32_bf16 v[52:55], v[140:143], a[68:71], v[52:55]\n"
"buffer_load_dwordx4 v211, s[24:27], s61 offen offset:3072 lds\n"
"v_mfma_f32_16x16x32_bf16 v[56:59], v[140:143], a[72:75], v[56:59]\n"
"ds_read_b64_tr_b16 v[174:175], v203 offset:18432\n"
"v_mfma_f32_16x16x32_bf16 v[60:63], v[140:143], a[76:79], v[60:63]\n"
"s_waitcnt lgkmcnt(8)\n"
"v_mfma_f32_16x16x32_bf16 v[64:67], v[144:147], a[64:67], v[64:67]\n"
"ds_read_b64_tr_b16 v[176:177], v204 offset:16384\n"
"s_movk_i32 s60, 12288\n"
"v_mfma_f32_16x16x32_bf16 v[68:71], v[144:147], a[68:71], v[68:71]\n"
"buffer_load_dwordx4 a[96:99], v212, s[28:31], s60 offen offset:0\n"
"v_mfma_f32_16x16x32_bf16 v[72:75], v[144:147], a[72:75], v[72:75]\n"
"ds_read_b64_tr_b16 v[178:179], v204 offset:18432\n"
"v_mfma_f32_16x16x32_bf16 v[76:79], v[144:147], a[76:79], v[76:79]\n"
"v_mfma_f32_16x16x32_bf16 v[80:83], v[148:151], a[64:67], v[80:83]\n"
"ds_read_b64_tr_b16 v[180:181], v205 offset:16384\n"
"v_mfma_f32_16x16x32_bf16 v[84:87], v[148:151], a[68:71], v[84:87]\n"
"buffer_load_dwordx4 a[100:103], v212, s[28:31], s60 offen offset:256\n"
"v_mfma_f32_16x16x32_bf16 v[88:91], v[148:151], a[72:75], v[88:91]\n"
"ds_read_b64_tr_b16 v[182:183], v205 offset:18432\n"
"v_mfma_f32_16x16x32_bf16 v[92:95], v[148:151], a[76:79], v[92:95]\n"
"v_mfma_f32_16x16x32_bf16 v[96:99], v[152:155], a[64:67], v[96:99]\n"
"ds_read_b64_tr_b16 v[184:185], v206 offset:16384\n"
"v_mfma_f32_16x16x32_bf16 v[100:103], v[152:155], a[68:71], v[100:103]\n"
"buffer_load_dwordx4 a[104:107], v213, s[28:31], s60 offen offset:0\n"
"v_mfma_f32_16x16x32_bf16 v[104:107], v[152:155], a[72:75], v[104:107]\n"
"ds_read_b64_tr_b16 v[186:187], v206 offset:18432\n"
"v_mfma_f32_16x16x32_bf16 v[108:111], v[152:155], a[76:79], v[108:111]\n"
"v_mfma_f32_16x16x32_bf16 v[112:115], v[156:159], a[64:67], v[112:115]\n"
"ds_read_b64_tr_b16 v[188:189], v207 offset:16384\n"
"v_mfma_f32_16x16x32_bf16 v[116:119], v[156:159], a[68:71], v[116:119]\n"
"buffer_load_dwordx4 a[108:111], v213, s[28:31], s60 offen offset:256\n"
"v_mfma_f32_16x16x32_bf16 v[120:123], v[156:159], a[72:75], v[120:123]\n"
"ds_read_b64_tr_b16 v[190:191], v207 offset:18432\n"
"v_mfma_f32_16x16x32_bf16 v[124:127], v[156:159], a[76:79], v[124:127]\n"
"s_waitcnt lgkmcnt(8)\n"
"s_waitcnt vmcnt(8)\n"
"s_barrier\n"
"v_mfma_f32_16x16x32_bf16 v[0:3], v[160:163], a[80:83], v[0:3]\n"
"ds_read_b64_tr_b16 v[128:129], v200 offset:32768\n"
"s_add_u32 m0, s62, 0\n"
"s_mov_b32 s61, 262144\n"
"v_mfma_f32_16x16x32_bf16 v[4:7], v[160:163], a[84:87], v[4:7]\n"
"buffer_load_dwordx4 v208, s[24:27], s61 offen offset:0 lds\n"
"v_mfma_f32_16x16x32_bf16 v[8:11], v[160:163], a[88:91], v[8:11]\n"
"ds_read_b64_tr_b16 v[130:131], v200 offset:34816\n"
"v_mfma_f32_16x16x32_bf16 v[12:15], v[160:163], a[92:95], v[12:15]\n"
"v_mfma_f32_16x16x32_bf16 v[16:19], v[164:167], a[80:83], v[16:19]\n"
"ds_read_b64_tr_b16 v[132:133], v201 offset:32768\n"
"v_mfma_f32_16x16x32_bf16 v[20:23], v[164:167], a[84:87], v[20:23]\n"
"buffer_load_dwordx4 v209, s[24:27], s61 offen offset:1024 lds\n"
"v_mfma_f32_16x16x32_bf16 v[24:27], v[164:167], a[88:91], v[24:27]\n"
"ds_read_b64_tr_b16 v[134:135], v201 offset:34816\n"
"v_mfma_f32_16x16x32_bf16 v[28:31], v[164:167], a[92:95], v[28:31]\n"
"v_mfma_f32_16x16x32_bf16 v[32:35], v[168:171], a[80:83], v[32:35]\n"
"ds_read_b64_tr_b16 v[136:137], v202 offset:32768\n"
"v_mfma_f32_16x16x32_bf16 v[36:39], v[168:171], a[84:87], v[36:39]\n"
"buffer_load_dwordx4 v210, s[24:27], s61 offen offset:2048 lds\n"
"v_mfma_f32_16x16x32_bf16 v[40:43], v[168:171], a[88:91], v[40:43]\n"
"ds_read_b64_tr_b16 v[138:139], v202 offset:34816\n"
"v_mfma_f32_16x16x32_bf16 v[44:47], v[168:171], a[92:95], v[44:47]\n"
"v_mfma_f32_16x16x32_bf16 v[48:51], v[172:175], a[80:83], v[48:51]\n"
"ds_read_b64_tr_b16 v[140:141], v203 offset:32768\n"
"v_mfma_f32_16x16x32_bf16 v[52:55], v[172:175], a[84:87], v[52:55]\n"
"buffer_load_dwordx4 v211, s[24:27], s61 offen offset:3072 lds\n"
"v_mfma_f32_16x16x32_bf16 v[56:59], v[172:175], a[88:91], v[56:59]\n"
"ds_read_b64_tr_b16 v[142:143], v203 offset:34816\n"
"v_mfma_f32_16x16x32_bf16 v[60:63], v[172:175], a[92:95], v[60:63]\n"
"s_waitcnt lgkmcnt(8)\n"
"v_mfma_f32_16x16x32_bf16 v[64:67], v[176:179], a[80:83], v[64:67]\n"
"ds_read_b64_tr_b16 v[144:145], v204 offset:32768\n"
"s_movk_i32 s60, 14336\n"
"v_mfma_f32_16x16x32_bf16 v[68:71], v[176:179], a[84:87], v[68:71]\n"
"buffer_load_dwordx4 a[112:115], v212, s[28:31], s60 offen offset:0\n"
"v_mfma_f32_16x16x32_bf16 v[72:75], v[176:179], a[88:91], v[72:75]\n"
"ds_read_b64_tr_b16 v[146:147], v204 offset:34816\n"
"v_mfma_f32_16x16x32_bf16 v[76:79], v[176:179], a[92:95], v[76:79]\n"
"v_mfma_f32_16x16x32_bf16 v[80:83], v[180:183], a[80:83], v[80:83]\n"
"ds_read_b64_tr_b16 v[148:149], v205 offset:32768\n"
"v_mfma_f32_16x16x32_bf16 v[84:87], v[180:183], a[84:87], v[84:87]\n"
"buffer_load_dwordx4 a[116:119], v212, s[28:31], s60 offen offset:256\n"
"v_mfma_f32_16x16x32_bf16 v[88:91], v[180:183], a[88:91], v[88:91]\n"
"ds_read_b64_tr_b16 v[150:151], v205 offset:34816\n"
"v_mfma_f32_16x16x32_bf16 v[92:95], v[180:183], a[92:95], v[92:95]\n"
"v_mfma_f32_16x16x32_bf16 v[96:99], v[184:187], a[80:83], v[96:99]\n"
"ds_read_b64_tr_b16 v[152:153], v206 offset:32768\n"
"v_mfma_f32_16x16x32_bf16 v[100:103], v[184:187], a[84:87], v[100:103]\n"
"buffer_load_dwordx4 a[120:123], v213, s[28:31], s60 offen offset:0\n"
"v_mfma_f32_16x16x32_bf16 v[104:107], v[184:187], a[88:91], v[104:107]\n"
"ds_read_b64_tr_b16 v[154:155], v206 offset:34816\n"
"v_mfma_f32_16x16x32_bf16 v[108:111], v[184:187], a[92:95], v[108:111]\n"
"v_mfma_f32_16x16x32_bf16 v[112:115], v[188:191], a[80:83], v[112:115]\n"
"ds_read_b64_tr_b16 v[156:157], v207 offset:32768\n"
"v_mfma_f32_16x16x32_bf16 v[116:119], v[188:191], a[84:87], v[116:119]\n"
"buffer_load_dwordx4 a[124:127], v213, s[28:31], s60 offen offset:256\n"
"v_mfma_f32_16x16x32_bf16 v[120:123], v[188:191], a[88:91], v[120:123]\n"
"ds_read_b64_tr_b16 v[158:159], v207 offset:34816\n"
"v_mfma_f32_16x16x32_bf16 v[124:127], v[188:191], a[92:95], v[124:127]\n"
"s_waitcnt lgkmcnt(8)\n"
"s_waitcnt vmcnt(8)\n"
"s_barrier\n"
"v_mfma_f32_16x16x32_bf16 v[0:3], v[128:131], a[96:99], v[0:3]\n"
"ds_read_b64_tr_b16 v[160:161], v200 offset:49152\n"
"s_add_u32 m0, s62, 16384\n"
"s_mov_b32 s61, 294912\n"
"v_mfma_f32_16x16x32_bf16 v[4:7], v[128:131], a[100:103], v[4:7]\n"
"buffer_load_dwordx4 v208, s[24:27], s61 offen offset:0 lds\n"
"v_mfma_f32_16x16x32_bf16 v[8:11], v[128:131], a[104:107], v[8:11]\n"
"ds_read_b64_tr_b16 v[162:163], v200 offset:51200\n"
"v_mfma_f32_16x16x32_bf16 v[12:15], v[128:131], a[108:111], v[12:15]\n"
"v_mfma_f32_16x16x32_bf16 v[16:19], v[132:135], a[96:99], v[16:19]\n"
"ds_read_b64_tr_b16 v[164:165], v201 offset:49152\n"
"v_mfma_f32_16x16x32_bf16 v[20:23], v[132:135], a[100:103], v[20:23]\n"
"buffer_load_dwordx4 v209, s[24:27], s61 offen offset:1024 lds\n"
"v_mfma_f32_16x16x32_bf16 v[24:27], v[132:135], a[104:107], v[24:27]\n"
"ds_read_b64_tr_b16 v[166:167], v201 offset:51200\n"
"v_mfma_f32_16x16x32_bf16 v[28:31], v[132:135], a[108:111], v[28:31]\n"
"v_mfma_f32_16x16x32_bf16 v[32:35], v[136:139], a[96:99], v[32:35]\n"
"ds_read_b64_tr_b16 v[168:169], v202 offset:49152\n"
"v_mfma_f32_16x16x32_bf16 v[36:39], v[136:139], a[100:103], v[36:39]\n"
"buffer_load_dwordx4 v210, s[24:27], s61 offen offset:2048 lds\n"
"v_mfma_f32_16x16x32_bf16 v[40:43], v[136:139], a[104:107], v[40:43]\n"
"ds_read_b64_tr_b16 v[170:171], v202 offset:51200\n"
"v_mfma_f32_16x16x32_bf16 v[44:47], v[136:139], a[108:111], v[44:47]\n"
"v_mfma_f32_16x16x32_bf16 v[48:51], v[140:143], a[96:99], v[48:51]\n"
"ds_read_b64_tr_b16 v[172:173], v203 offset:49152\n"
"v_mfma_f32_16x16x32_bf16 v[52:55], v[140:143], a[100:103], v[52:55]\n"
"buffer_load_dwordx4 v211, s[24:27], s61 offen offset:3072 lds\n"
"v_mfma_f32_16x16x32_bf16 v[56:59], v[140:143], a[104:107], v[56:59]\n"
"ds_read_b64_tr_b16 v[174:175], v203 offset:51200\n"
"v_mfma_f32_16x16x32_bf16 v[60:63], v[140:143], a[108:111], v[60:63]\n"
"s_waitcnt lgkmcnt(8)\n"
"v_mfma_f32_16x16x32_bf16 v[64:67], v[144:147], a[96:99], v[64:67]\n"
"ds_read_b64_tr_b16 v[176:177], v204 offset:49152\n"
"s_movk_i32 s60, 16384\n"
"v_mfma_f32_16x16x32_bf16 v[68:71], v[144:147], a[100:103], v[68:71]\n"
"buffer_load_dwordx4 a[128:131], v212, s[28:31], s60 offen offset:0\n"
"v_mfma_f32_16x16x32_bf16 v[72:75], v[144:147], a[104:107], v[72:75]\n"
"ds_read_b64_tr_b16 v[178:179], v204 offset:51200\n"
"v_mfma_f32_16x16x32_bf16 v[76:79], v[144:147], a[108:111], v[76:79]\n"
"v_mfma_f32_16x16x32_bf16 v[80:83], v[148:151], a[96:99], v[80:83]\n"
"ds_read_b64_tr_b16 v[180:181], v205 offset:49152\n"
"v_mfma_f32_16x16x32_bf16 v[84:87], v[148:151], a[100:103], v[84:87]\n"
"buffer_load_dwordx4 a[132:135], v212, s[28:31], s60 offen offset:256\n"
"v_mfma_f32_16x16x32_bf16 v[88:91], v[148:151], a[104:107], v[88:91]\n"
"ds_read_b64_tr_b16 v[182:183], v205 offset:51200\n"
"v_mfma_f32_16x16x32_bf16 v[92:95], v[148:151], a[108:111], v[92:95]\n"
"v_mfma_f32_16x16x32_bf16 v[96:99], v[152:155], a[96:99], v[96:99]\n"
"ds_read_b64_tr_b16 v[184:185], v206 offset:49152\n"
"v_mfma_f32_16x16x32_bf16 v[100:103], v[152:155], a[100:103], v[100:103]\n"
"buffer_load_dwordx4 a[136:139], v213, s[28:31], s60 offen offset:0\n"
"v_mfma_f32_16x16x32_bf16 v[104:107], v[152:155], a[104:107], v[104:107]\n"
"ds_read_b64_tr_b16 v[186:187], v206 offset:51200\n"
"v_mfma_f32_16x16x32_bf16 v[108:111], v[152:155], a[108:111], v[108:111]\n"
"v_mfma_f32_16x16x32_bf16 v[112:115], v[156:159], a[96:99], v[112:115]\n"
"ds_read_b64_tr_b16 v[188:189], v207 offset:49152\n"
"v_mfma_f32_16x16x32_bf16 v[116:119], v[156:159], a[100:103], v[116:119]\n"
"buffer_load_dwordx4 a[140:143], v213, s[28:31], s60 offen offset:256\n"
"v_mfma_f32_16x16x32_bf16 v[120:123], v[156:159], a[104:107], v[120:123]\n"
"ds_read_b64_tr_b16 v[190:191], v207 offset:51200\n"
"v_mfma_f32_16x16x32_bf16 v[124:127], v[156:159], a[108:111], v[124:127]\n"
"s_waitcnt lgkmcnt(8)\n"
"s_waitcnt vmcnt(8)\n"
"s_barrier\n"
"v_mfma_f32_16x16x32_bf16 v[0:3], v[160:163], a[112:115], v[0:3]\n"
"ds_read_b64_tr_b16 v[128:129], v192 offset:0\n"
"s_add_u32 m0, s62, 32768\n"
"s_mov_b32 s61, 327680\n"
"v_mfma_f32_16x16x32_bf16 v[4:7], v[160:163], a[116:119], v[4:7]\n"
"buffer_load_dwordx4 v208, s[24:27], s61 offen offset:0 lds\n"
"v_mfma_f32_16x16x32_bf16 v[8:11], v[160:163], a[120:123], v[8:11]\n"
"ds_read_b64_tr_b16 v[130:131], v192 offset:2048\n"
"v_mfma_f32_16x16x32_bf16 v[12:15], v[160:163], a[124:127], v[12:15]\n"
"v_mfma_f32_16x16x32_bf16 v[16:19], v[164:167], a[112:115], v[16:19]\n"
"ds_read_b64_tr_b16 v[132:133], v193 offset:0\n"
"v_mfma_f32_16x16x32_bf16 v[20:23], v[164:167], a[116:119], v[20:23]\n"
"buffer_load_dwordx4 v209, s[24:27], s61 offen offset:1024 lds\n"
"v_mfma_f32_16x16x32_bf16 v[24:27], v[164:167], a[120:123], v[24:27]\n"
"ds_read_b64_tr_b16 v[134:135], v193 offset:2048\n"
"v_mfma_f32_16x16x32_bf16 v[28:31], v[164:167], a[124:127], v[28:31]\n"
"v_mfma_f32_16x16x32_bf16 v[32:35], v[168:171], a[112:115], v[32:35]\n"
"ds_read_b64_tr_b16 v[136:137], v194 offset:0\n"
"v_mfma_f32_16x16x32_bf16 v[36:39], v[168:171], a[116:119], v[36:39]\n"
"buffer_load_dwordx4 v210, s[24:27], s61 offen offset:2048 lds\n"
"v_mfma_f32_16x16x32_bf16 v[40:43], v[168:171], a[120:123], v[40:43]\n"
"ds_read_b64_tr_b16 v[138:139], v194 offset:2048\n"
"v_mfma_f32_16x16x32_bf16 v[44:47], v[168:171], a[124:127], v[44:47]\n"
"v_mfma_f32_16x16x32_bf16 v[48:51], v[172:175], a[112:115], v[48:51]\n"
"ds_read_b64_tr_b16 v[140:141], v195 offset:0\n"
"v_mfma_f32_16x16x32_bf16 v[52:55], v[172:175], a[116:119], v[52:55]\n"
"buffer_load_dwordx4 v211, s[24:27], s61 offen offset:3072 lds\n"
"v_mfma_f32_16x16x32_bf16 v[56:59], v[172:175], a[120:123], v[56:59]\n"
"ds_read_b64_tr_b16 v[142:143], v195 offset:2048\n"
"v_mfma_f32_16x16x32_bf16 v[60:63], v[172:175], a[124:127], v[60:63]\n"
"s_waitcnt lgkmcnt(8)\n"
"v_mfma_f32_16x16x32_bf16 v[64:67], v[176:179], a[112:115], v[64:67]\n"
"ds_read_b64_tr_b16 v[144:145], v196 offset:0\n"
"s_movk_i32 s60, 18432\n"
"v_mfma_f32_16x16x32_bf16 v[68:71], v[176:179], a[116:119], v[68:71]\n"
"buffer_load_dwordx4 a[144:147], v212, s[28:31], s60 offen offset:0\n"
"v_mfma_f32_16x16x32_bf16 v[72:75], v[176:179], a[120:123], v[72:75]\n"
"ds_read_b64_tr_b16 v[146:147], v196 offset:2048\n"
"v_mfma_f32_16x16x32_bf16 v[76:79], v[176:179], a[124:127], v[76:79]\n"
"v_mfma_f32_16x16x32_bf16 v[80:83], v[180:183], a[112:115], v[80:83]\n"
"ds_read_b64_tr_b16 v[148:149], v197 offset:0\n"
"v_mfma_f32_16x16x32_bf16 v[84:87], v[180:183], a[116:119], v[84:87]\n"
"buffer_load_dwordx4 a[148:151], v212, s[28:31], s60 offen offset:256\n"
"v_mfma_f32_16x16x32_bf16 v[88:91], v[180:183], a[120:123], v[88:91]\n"
"ds_read_b64_tr_b16 v[150:151], v197 offset:2048\n"
"v_mfma_f32_16x16x32_bf16 v[92:95], v[180:183], a[124:127], v[92:95]\n"
"v_mfma_f32_16x16x32_bf16 v[96:99], v[184:187], a[112:115], v[96:99]\n"
"ds_read_b64_tr_b16 v[152:153], v198 offset:0\n"
"v_mfma_f32_16x16x32_bf16 v[100:103], v[184:187], a[116:119], v[100:103]\n"
"buffer_load_dwordx4 a[152:155], v213, s[28:31], s60 offen offset:0\n"
"v_mfma_f32_16x16x32_bf16 v[104:107], v[184:187], a[120:123], v[104:107]\n"
"ds_read_b64_tr_b16 v[154:155], v198 offset:2048\n"
"v_mfma_f32_16x16x32_bf16 v[108:111], v[184:187], a[124:127], v[108:111]\n"
"v_mfma_f32_16x16x32_bf16 v[112:115], v[188:191], a[112:115], v[112:115]\n"
"ds_read_b64_tr_b16 v[156:157], v199 offset:0\n"
"v_mfma_f32_16x16x32_bf16 v[116:119], v[188:191], a[116:119], v[116:119]\n"
"buffer_load_dwordx4 a[156:159], v213, s[28:31], s60 offen offset:256\n"
"v_mfma_f32_16x16x32_bf16 v[120:123], v[188:191], a[120:123], v[120:123]\n"
"ds_read_b64_tr_b16 v[158:159], v199 offset:2048\n"
"v_mfma_f32_16x16x32_bf16 v[124:127], v[188:191], a[124:127], v[124:127]\n"
"s_waitcnt lgkmcnt(8)\n"
"s_waitcnt vmcnt(8)\n"
"s_barrier\n"
"v_mfma_f32_16x16x32_bf16 v[0:3], v[128:131], a[128:131], v[0:3]\n"
"ds_read_b64_tr_b16 v[160:161], v192 offset:16384\n"
"s_add_u32 m0, s62, 49152\n"
"s_mov_b32 s61, 360448\n"
"v_mfma_f32_16x16x32_bf16 v[4:7], v[128:131], a[132:135], v[4:7]\n"
"buffer_load_dwordx4 v208, s[24:27], s61 offen offset:0 lds\n"
"v_mfma_f32_16x16x32_bf16 v[8:11], v[128:131], a[136:139], v[8:11]\n"
"ds_read_b64_tr_b16 v[162:163], v192 offset:18432\n"
"v_mfma_f32_16x16x32_bf16 v[12:15], v[128:131], a[140:143], v[12:15]\n"
"v_mfma_f32_16x16x32_bf16 v[16:19], v[132:135], a[128:131], v[16:19]\n"
"ds_read_b64_tr_b16 v[164:165], v193 offset:16384\n"
"v_mfma_f32_16x16x32_bf16 v[20:23], v[132:135], a[132:135], v[20:23]\n"
"buffer_load_dwordx4 v209, s[24:27], s61 offen offset:1024 lds\n"
"v_mfma_f32_16x16x32_bf16 v[24:27], v[132:135], a[136:139], v[24:27]\n"
"ds_read_b64_tr_b16 v[166:167], v193 offset:18432\n"
"v_mfma_f32_16x16x32_bf16 v[28:31], v[132:135], a[140:143], v[28:31]\n"
"v_mfma_f32_16x16x32_bf16 v[32:35], v[136:139], a[128:131], v[32:35]\n"
"ds_read_b64_tr_b16 v[168:169], v194 offset:16384\n"
"v_mfma_f32_16x16x32_bf16 v[36:39], v[136:139], a[132:135], v[36:39]\n"
"buffer_load_dwordx4 v210, s[24:27], s61 offen offset:2048 lds\n"
"v_mfma_f32_16x16x32_bf16 v[40:43], v[136:139], a[136:139], v[40:43]\n"
"ds_read_b64_tr_b16 v[170:171], v194 offset:18432\n"
"v_mfma_f32_16x16x32_bf16 v[44:47], v[136:139], a[140:143], v[44:47]\n"
"v_mfma_f32_16x16x32_bf16 v[48:51], v[140:143], a[128:131], v[48:51]\n"
"ds_read_b64_tr_b16 v[172:173], v195 offset:16384\n"
"v_mfma_f32_16x16x32_bf16 v[52:55], v[140:143], a[132:135], v[52:55]\n"
"buffer_load_dwordx4 v211, s[24:27], s61 offen offset:3072 lds\n"
"v_mfma_f32_16x16x32_bf16 v[56:59], v[140:143], a[136:139], v[56:59]\n"
"ds_read_b64_tr_b16 v[174:175], v195 offset:18432\n"
"v_mfma_f32_16x16x32_bf16 v[60:63], v[140:143], a[140:143], v[60:63]\n"
"s_waitcnt lgkmcnt(8)\n"
"v_mfma_f32_16x16x32_bf16 v[64:67], v[144:147], a[128:131], v[64:67]\n"
"ds_read_b64_tr_b16 v[176:177], v196 offset:16384\n"
"s_movk_i32 s60, 20480\n"
"v_mfma_f32_16x16x32_bf16 v[68:71], v[144:147], a[132:135], v[68:71]\n"
"buffer_load_dwordx4 a[160:163], v212, s[28:31], s60 offen offset:0\n"
"v_mfma_f32_16x16x32_bf16 v[72:75], v[144:147], a[136:139], v[72:75]\n"
"ds_read_b64_tr_b16 v[178:179], v196 offset:18432\n"
"v_mfma_f32_16x16x32_bf16 v[76:79], v[144:147], a[140:143], v[76:79]\n"
"v_mfma_f32_16x16x32_bf16 v[80:83], v[148:151], a[128:131], v[80:83]\n"
"ds_read_b64_tr_b16 v[180:181], v197 offset:16384\n"
"v_mfma_f32_16x16x32_bf16 v[84:87], v[148:151], a[132:135], v[84:87]\n"
"buffer_load_dwordx4 a[164:167], v212, s[28:31], s60 offen offset:256\n"
"v_mfma_f32_16x16x32_bf16 v[88:91], v[148:151], a[136:139], v[88:91]\n"
"ds_read_b64_tr_b16 v[182:183], v197 offset:18432\n"
"v_mfma_f32_16x16x32_bf16 v[92:95], v[148:151], a[140:143], v[92:95]\n"
"v_mfma_f32_16x16x32_bf16 v[96:99], v[152:155], a[128:131], v[96:99]\n"
"ds_read_b64_tr_b16 v[184:185], v198 offset:16384\n"
"v_mfma_f32_16x16x32_bf16 v[100:103], v[152:155], a[132:135], v[100:103]\n"
"buffer_load_dwordx4 a[168:171], v213, s[28:31], s60 offen offset:0\n"
"v_mfma_f32_16x16x32_bf16 v[104:107], v[152:155], a[136:139], v[104:107]\n"
"ds_read_b64_tr_b16 v[186:187], v198 offset:18432\n"
"v_mfma_f32_16x16x32_bf16 v[108:111], v[152:155], a[140:143], v[108:111]\n"
"v_mfma_f32_16x16x32_bf16 v[112:115], v[156:159], a[128:131], v[112:115]\n"
"ds_read_b64_tr_b16 v[188:189], v199 offset:16384\n"
"v_mfma_f32_16x16x32_bf16 v[116:119], v[156:159], a[132:135], v[116:119]\n"
"buffer_load_dwordx4 a[172:175], v213, s[28:31], s60 offen offset:256\n"
"v_mfma_f32_16x16x32_bf16 v[120:123], v[156:159], a[136:139], v[120:123]\n"
"ds_read_b64_tr_b16 v[190:191], v199 offset:18432\n"
"v_mfma_f32_16x16x32_bf16 v[124:127], v[156:159], a[140:143], v[124:127]\n"
"s_waitcnt lgkmcnt(8)\n"
"s_waitcnt vmcnt(8)\n"
"s_barrier\n"
"v_mfma_f32_16x16x32_bf16 v[0:3], v[160:163], a[144:147], v[0:3]\n"
"ds_read_b64_tr_b16 v[128:129], v192 offset:32768\n"
"s_add_u32 m0, s62, 65536\n"
"s_mov_b32 s61, 393216\n"
"v_mfma_f32_16x16x32_bf16 v[4:7], v[160:163], a[148:151], v[4:7]\n"
"buffer_load_dwordx4 v208, s[24:27], s61 offen offset:0 lds\n"
"v_mfma_f32_16x16x32_bf16 v[8:11], v[160:163], a[152:155], v[8:11]\n"
"ds_read_b64_tr_b16 v[130:131], v192 offset:34816\n"
"v_mfma_f32_16x16x32_bf16 v[12:15], v[160:163], a[156:159], v[12:15]\n"
"v_mfma_f32_16x16x32_bf16 v[16:19], v[164:167], a[144:147], v[16:19]\n"
"ds_read_b64_tr_b16 v[132:133], v193 offset:32768\n"
"v_mfma_f32_16x16x32_bf16 v[20:23], v[164:167], a[148:151], v[20:23]\n"
"buffer_load_dwordx4 v209, s[24:27], s61 offen offset:1024 lds\n"
"v_mfma_f32_16x16x32_bf16 v[24:27], v[164:167], a[152:155], v[24:27]\n"
"ds_read_b64_tr_b16 v[134:135], v193 offset:34816\n"
"v_mfma_f32_16x16x32_bf16 v[28:31], v[164:167], a[156:159], v[28:31]\n"
"v_mfma_f32_16x16x32_bf16 v[32:35], v[168:171], a[144:147], v[32:35]\n"
"ds_read_b64_tr_b16 v[136:137], v194 offset:32768\n"
"v_mfma_f32_16x16x32_bf16 v[36:39], v[168:171], a[148:151], v[36:39]\n"
"buffer_load_dwordx4 v210, s[24:27], s61 offen offset:2048 lds\n"
"v_mfma_f32_16x16x32_bf16 v[40:43], v[168:171], a[152:155], v[40:43]\n"
"ds_read_b64_tr_b16 v[138:139], v194 offset:34816\n"
"v_mfma_f32_16x16x32_bf16 v[44:47], v[168:171], a[156:159], v[44:47]\n"
"v_mfma_f32_16x16x32_bf16 v[48:51], v[172:175], a[144:147], v[48:51]\n"
"ds_read_b64_tr_b16 v[140:141], v195 offset:32768\n"
"v_mfma_f32_16x16x32_bf16 v[52:55], v[172:175], a[148:151], v[52:55]\n"
"buffer_load_dwordx4 v211, s[24:27], s61 offen offset:3072 lds\n"
"v_mfma_f32_16x16x32_bf16 v[56:59], v[172:175], a[152:155], v[56:59]\n"
"ds_read_b64_tr_b16 v[142:143], v195 offset:34816\n"
"v_mfma_f32_16x16x32_bf16 v[60:63], v[172:175], a[156:159], v[60:63]\n"
"s_waitcnt lgkmcnt(8)\n"
"v_mfma_f32_16x16x32_bf16 v[64:67], v[176:179], a[144:147], v[64:67]\n"
"ds_read_b64_tr_b16 v[144:145], v196 offset:32768\n"
"s_movk_i32 s60, 22528\n"
"v_mfma_f32_16x16x32_bf16 v[68:71], v[176:179], a[148:151], v[68:71]\n"
"buffer_load_dwordx4 a[176:179], v212, s[28:31], s60 offen offset:0\n"
"v_mfma_f32_16x16x32_bf16 v[72:75], v[176:179], a[152:155], v[72:75]\n"
"ds_read_b64_tr_b16 v[146:147], v196 offset:34816\n"
"v_mfma_f32_16x16x32_bf16 v[76:79], v[176:179], a[156:159], v[76:79]\n"
"v_mfma_f32_16x16x32_bf16 v[80:83], v[180:183], a[144:147], v[80:83]\n"
"ds_read_b64_tr_b16 v[148:149], v197 offset:32768\n"
"v_mfma_f32_16x16x32_bf16 v[84:87], v[180:183], a[148:151], v[84:87]\n"
"buffer_load_dwordx4 a[180:183], v212, s[28:31], s60 offen offset:256\n"
"v_mfma_f32_16x16x32_bf16 v[88:91], v[180:183], a[152:155], v[88:91]\n"
"ds_read_b64_tr_b16 v[150:151], v197 offset:34816\n"
"v_mfma_f32_16x16x32_bf16 v[92:95], v[180:183], a[156:159], v[92:95]\n"
"v_mfma_f32_16x16x32_bf16 v[96:99], v[184:187], a[144:147], v[96:99]\n"
"ds_read_b64_tr_b16 v[152:153], v198 offset:32768\n"
"v_mfma_f32_16x16x32_bf16 v[100:103], v[184:187], a[148:151], v[100:103]\n"
"buffer_load_dwordx4 a[184:187], v213, s[28:31], s60 offen offset:0\n"
"v_mfma_f32_16x16x32_bf16 v[104:107], v[184:187], a[152:155], v[104:107]\n"
"ds_read_b64_tr_b16 v[154:155], v198 offset:34816\n"
"v_mfma_f32_16x16x32_bf16 v[108:111], v[184:187], a[156:159], v[108:111]\n"
"v_mfma_f32_16x16x32_bf16 v[112:115], v[188:191], a[144:147], v[112:115]\n"
"ds_read_b64_tr_b16 v[156:157], v199 offset:32768\n"
"v_mfma_f32_16x16x32_bf16 v[116:119], v[188:191], a[148:151], v[116:119]\n"
"buffer_load_dwordx4 a[188:191], v213, s[28:31], s60 offen offset:256\n"
"v_mfma_f32_16x16x32_bf16 v[120:123], v[188:191], a[152:155], v[120:123]\n"
"ds_read_b64_tr_b16 v[158:159], v199 offset:34816\n"
"v_mfma_f32_16x16x32_bf16 v[124:127], v[188:191], a[156:159], v[124:127]\n"
"s_waitcnt lgkmcnt(8)\n"
"s_waitcnt vmcnt(8)\n"
"s_barrier\n"
"v_mfma_f32_16x16x32_bf16 v[0:3], v[128:131], a[160:163], v[0:3]\n"
"ds_read_b64_tr_b16 v[160:161], v192 offset:49152\n"
"s_add_u32 m0, s62, 81920\n"
"s_mov_b32 s61, 425984\n"
"v_mfma_f32_16x16x32_bf16 v[4:7], v[128:131], a[164:167], v[4:7]\n"
"buffer_load_dwordx4 v208, s[24:27], s61 offen offset:0 lds\n"
"v_mfma_f32_16x16x32_bf16 v[8:11], v[128:131], a[168:171], v[8:11]\n"
"ds_read_b64_tr_b16 v[162:163], v192 offset:51200\n"
"v_mfma_f32_16x16x32_bf16 v[12:15], v[128:131], a[172:175], v[12:15]\n"
"v_mfma_f32_16x16x32_bf16 v[16:19], v[132:135], a[160:163], v[16:19]\n"
"ds_read_b64_tr_b16 v[164:165], v193 offset:49152\n"
"v_mfma_f32_16x16x32_bf16 v[20:23], v[132:135], a[164:167], v[20:23]\n"
"buffer_load_dwordx4 v209, s[24:27], s61 offen offset:1024 lds\n"
"v_mfma_f32_16x16x32_bf16 v[24:27], v[132:135], a[168:171], v[24:27]\n"
"ds_read_b64_tr_b16 v[166:167], v193 offset:51200\n"
"v_mfma_f32_16x16x32_bf16 v[28:31], v[132:135], a[172:175], v[28:31]\n"
"v_mfma_f32_16x16x32_bf16 v[32:35], v[136:139], a[160:163], v[32:35]\n"
"ds_read_b64_tr_b16 v[168:169], v194 offset:49152\n"
"v_mfma_f32_16x16x32_bf16 v[36:39], v[136:139], a[164:167], v[36:39]\n"
"buffer_load_dwordx4 v210, s[24:27], s61 offen offset:2048 lds\n"
"v_mfma_f32_16x16x32_bf16 v[40:43], v[136:139], a[168:171], v[40:43]\n"
"ds_read_b64_tr_b16 v[170:171], v194 offset:51200\n"
"v_mfma_f32_16x16x32_bf16 v[44:47], v[136:139], a[172:175], v[44:47]\n"
"v_mfma_f32_16x16x32_bf16 v[48:51], v[140:143], a[160:163], v[48:51]\n"
"ds_read_b64_tr_b16 v[172:173], v195 offset:49152\n"
"v_mfma_f32_16x16x32_bf16 v[52:55], v[140:143], a[164:167], v[52:55]\n"
"buffer_load_dwordx4 v211, s[24:27], s61 offen offset:3072 lds\n"
"v_mfma_f32_16x16x32_bf16 v[56:59], v[140:143], a[168:171], v[56:59]\n"
"ds_read_b64_tr_b16 v[174:175], v195 offset:51200\n"
"v_mfma_f32_16x16x32_bf16 v[60:63], v[140:143], a[172:175], v[60:63]\n"
"s_waitcnt lgkmcnt(8)\n"
"v_mfma_f32_16x16x32_bf16 v[64:67], v[144:147], a[160:163], v[64:67]\n"
"ds_read_b64_tr_b16 v[176:177], v196 offset:49152\n"
"s_movk_i32 s60, 24576\n"
"v_mfma_f32_16x16x32_bf16 v[68:71], v[144:147], a[164:167], v[68:71]\n"
"buffer_load_dwordx4 a[192:195], v212, s[28:31], s60 offen offset:0\n"
"v_mfma_f32_16x16x32_bf16 v[72:75], v[144:147], a[168:171], v[72:75]\n"
"ds_read_b64_tr_b16 v[178:179], v196 offset:51200\n"
"v_mfma_f32_16x16x32_bf16 v[76:79], v[144:147], a[172:175], v[76:79]\n"
"v_mfma_f32_16x16x32_bf16 v[80:83], v[148:151], a[160:163], v[80:83]\n"
"ds_read_b64_tr_b16 v[180:181], v197 offset:49152\n"
"v_mfma_f32_16x16x32_bf16 v[84:87], v[148:151], a[164:167], v[84:87]\n"
"buffer_load_dwordx4 a[196:199], v212, s[28:31], s60 offen offset:256\n"
"v_mfma_f32_16x16x32_bf16 v[88:91], v[148:151], a[168:171], v[88:91]\n"
"ds_read_b64_tr_b16 v[182:183], v197 offset:51200\n"
"v_mfma_f32_16x16x32_bf16 v[92:95], v[148:151], a[172:175], v[92:95]\n"
"v_mfma_f32_16x16x32_bf16 v[96:99], v[152:155], a[160:163], v[96:99]\n"
"ds_read_b64_tr_b16 v[184:185], v198 offset:49152\n"
"v_mfma_f32_16x16x32_bf16 v[100:103], v[152:155], a[164:167], v[100:103]\n"
"buffer_load_dwordx4 a[200:203], v213, s[28:31], s60 offen offset:0\n"
"v_mfma_f32_16x16x32_bf16 v[104:107], v[152:155], a[168:171], v[104:107]\n"
"ds_read_b64_tr_b16 v[186:187], v198 offset:51200\n"
"v_mfma_f32_16x16x32_bf16 v[108:111], v[152:155], a[172:175], v[108:111]\n"
"v_mfma_f32_16x16x32_bf16 v[112:115], v[156:159], a[160:163], v[112:115]\n"
"ds_read_b64_tr_b16 v[188:189], v199 offset:49152\n"
"v_mfma_f32_16x16x32_bf16 v[116:119], v[156:159], a[164:167], v[116:119]\n"
"buffer_load_dwordx4 a[204:207], v213, s[28:31], s60 offen offset:256\n"
"v_mfma_f32_16x16x32_bf16 v[120:123], v[156:159], a[168:171], v[120:123]\n"
"ds_read_b64_tr_b16 v[190:191], v199 offset:51200\n"
"v_mfma_f32_16x16x32_bf16 v[124:127], v[156:159], a[172:175], v[124:127]\n"
"s_waitcnt lgkmcnt(8)\n"
"s_waitcnt vmcnt(8)\n"
"s_barrier\n"
"v_mfma_f32_16x16x32_bf16 v[0:3], v[160:163], a[176:179], v[0:3]\n"
"ds_read_b64_tr_b16 v[128:129], v200 offset:0\n"
"s_add_u32 m0, s62, 98304\n"
"s_mov_b32 s61, 458752\n"
"v_mfma_f32_16x16x32_bf16 v[4:7], v[160:163], a[180:183], v[4:7]\n"
"buffer_load_dwordx4 v208, s[24:27], s61 offen offset:0 lds\n"
"v_mfma_f32_16x16x32_bf16 v[8:11], v[160:163], a[184:187], v[8:11]\n"
"ds_read_b64_tr_b16 v[130:131], v200 offset:2048\n"
"v_mfma_f32_16x16x32_bf16 v[12:15], v[160:163], a[188:191], v[12:15]\n"
"v_mfma_f32_16x16x32_bf16 v[16:19], v[164:167], a[176:179], v[16:19]\n"
"ds_read_b64_tr_b16 v[132:133], v201 offset:0\n"
"v_mfma_f32_16x16x32_bf16 v[20:23], v[164:167], a[180:183], v[20:23]\n"
"buffer_load_dwordx4 v209, s[24:27], s61 offen offset:1024 lds\n"
"v_mfma_f32_16x16x32_bf16 v[24:27], v[164:167], a[184:187], v[24:27]\n"
"ds_read_b64_tr_b16 v[134:135], v201 offset:2048\n"
"v_mfma_f32_16x16x32_bf16 v[28:31], v[164:167], a[188:191], v[28:31]\n"
"v_mfma_f32_16x16x32_bf16 v[32:35], v[168:171], a[176:179], v[32:35]\n"
"ds_read_b64_tr_b16 v[136:137], v202 offset:0\n"
"v_mfma_f32_16x16x32_bf16 v[36:39], v[168:171], a[180:183], v[36:39]\n"
"buffer_load_dwordx4 v210, s[24:27], s61 offen offset:2048 lds\n"
"v_mfma_f32_16x16x32_bf16 v[40:43], v[168:171], a[184:187], v[40:43]\n"
"ds_read_b64_tr_b16 v[138:139], v202 offset:2048\n"
"v_mfma_f32_16x16x32_bf16 v[44:47], v[168:171], a[188:191], v[44:47]\n"
"v_mfma_f32_16x16x32_bf16 v[48:51], v[172:175], a[176:179], v[48:51]\n"
"ds_read_b64_tr_b16 v[140:141], v203 offset:0\n"
"v_mfma_f32_16x16x32_bf16 v[52:55], v[172:175], a[180:183], v[52:55]\n"
"buffer_load_dwordx4 v211, s[24:27], s61 offen offset:3072 lds\n"
"v_mfma_f32_16x16x32_bf16 v[56:59], v[172:175], a[184:187], v[56:59]\n"
"ds_read_b64_tr_b16 v[142:143], v203 offset:2048\n"
"v_mfma_f32_16x16x32_bf16 v[60:63], v[172:175], a[188:191], v[60:63]\n"
"s_waitcnt lgkmcnt(8)\n"
"v_mfma_f32_16x16x32_bf16 v[64:67], v[176:179], a[176:179], v[64:67]\n"
"ds_read_b64_tr_b16 v[144:145], v204 offset:0\n"
"s_movk_i32 s60, 26624\n"
"v_mfma_f32_16x16x32_bf16 v[68:71], v[176:179], a[180:183], v[68:71]\n"
"buffer_load_dwordx4 a[208:211], v212, s[28:31], s60 offen offset:0\n"
"v_mfma_f32_16x16x32_bf16 v[72:75], v[176:179], a[184:187], v[72:75]\n"
"ds_read_b64_tr_b16 v[146:147], v204 offset:2048\n"
"v_mfma_f32_16x16x32_bf16 v[76:79], v[176:179], a[188:191], v[76:79]\n"
"v_mfma_f32_16x16x32_bf16 v[80:83], v[180:183], a[176:179], v[80:83]\n"
"ds_read_b64_tr_b16 v[148:149], v205 offset:0\n"
"v_mfma_f32_16x16x32_bf16 v[84:87], v[180:183], a[180:183], v[84:87]\n"
"buffer_load_dwordx4 a[212:215], v212, s[28:31], s60 offen offset:256\n"
"v_mfma_f32_16x16x32_bf16 v[88:91], v[180:183], a[184:187], v[88:91]\n"
"ds_read_b64_tr_b16 v[150:151], v205 offset:2048\n"
"v_mfma_f32_16x16x32_bf16 v[92:95], v[180:183], a[188:191], v[92:95]\n"
"v_mfma_f32_16x16x32_bf16 v[96:99], v[184:187], a[176:179], v[96:99]\n"
"ds_read_b64_tr_b16 v[152:153], v206 offset:0\n"
"v_mfma_f32_16x16x32_bf16 v[100:103], v[184:187], a[180:183], v[100:103]\n"
"buffer_load_dwordx4 a[216:219], v213, s[28:31], s60 offen offset:0\n"
"v_mfma_f32_16x16x32_bf16 v[104:107], v[184:187], a[184:187], v[104:107]\n"
"ds_read_b64_tr_b16 v[154:155], v206 offset:2048\n"
"v_mfma_f32_16x16x32_bf16 v[108:111], v[184:187], a[188:191], v[108:111]\n"
"v_mfma_f32_16x16x32_bf16 v[112:115], v[188:191], a[176:179], v[112:115]\n"
"ds_read_b64_tr_b16 v[156:157], v207 offset:0\n"
"v_mfma_f32_16x16x32_bf16 v[116:119], v[188:191], a[180:183], v[116:119]\n"
"buffer_load_dwordx4 a[220:223], v213, s[28:31], s60 offen offset:256\n"
"v_mfma_f32_16x16x32_bf16 v[120:123], v[188:191], a[184:187], v[120:123]\n"
"ds_read_b64_tr_b16 v[158:159], v207 offset:2048\n"
"v_mfma_f32_16x16x32_bf16 v[124:127], v[188:191], a[188:191], v[124:127]\n"
"s_waitcnt lgkmcnt(8)\n"
"s_waitcnt vmcnt(8)\n"
"s_barrier\n"
"v_mfma_f32_16x16x32_bf16 v[0:3], v[128:131], a[192:195], v[0:3]\n"
"ds_read_b64_tr_b16 v[160:161], v200 offset:16384\n"
"s_add_u32 m0, s62, 114688\n"
"s_mov_b32 s61, 491520\n"
"v_mfma_f32_16x16x32_bf16 v[4:7], v[128:131], a[196:199], v[4:7]\n"
"buffer_load_dwordx4 v208, s[24:27], s61 offen offset:0 lds\n"
"v_mfma_f32_16x16x32_bf16 v[8:11], v[128:131], a[200:203], v[8:11]\n"
"ds_read_b64_tr_b16 v[162:163], v200 offset:18432\n"
"v_mfma_f32_16x16x32_bf16 v[12:15], v[128:131], a[204:207], v[12:15]\n"
"v_mfma_f32_16x16x32_bf16 v[16:19], v[132:135], a[192:195], v[16:19]\n"
"ds_read_b64_tr_b16 v[164:165], v201 offset:16384\n"
"v_mfma_f32_16x16x32_bf16 v[20:23], v[132:135], a[196:199], v[20:23]\n"
"buffer_load_dwordx4 v209, s[24:27], s61 offen offset:1024 lds\n"
"v_mfma_f32_16x16x32_bf16 v[24:27], v[132:135], a[200:203], v[24:27]\n"
"ds_read_b64_tr_b16 v[166:167], v201 offset:18432\n"
"v_mfma_f32_16x16x32_bf16 v[28:31], v[132:135], a[204:207], v[28:31]\n"
"v_mfma_f32_16x16x32_bf16 v[32:35], v[136:139], a[192:195], v[32:35]\n"
"ds_read_b64_tr_b16 v[168:169], v202 offset:16384\n"
"v_mfma_f32_16x16x32_bf16 v[36:39], v[136:139], a[196:199], v[36:39]\n"
"buffer_load_dwordx4 v210, s[24:27], s61 offen offset:2048 lds\n"
"v_mfma_f32_16x16x32_bf16 v[40:43], v[136:139], a[200:203], v[40:43]\n"
"ds_read_b64_tr_b16 v[170:171], v202 offset:18432\n"
"v_mfma_f32_16x16x32_bf16 v[44:47], v[136:139], a[204:207], v[44:47]\n"
"v_mfma_f32_16x16x32_bf16 v[48:51], v[140:143], a[192:195], v[48:51]\n"
"ds_read_b64_tr_b16 v[172:173], v203 offset:16384\n"
"v_mfma_f32_16x16x32_bf16 v[52:55], v[140:143], a[196:199], v[52:55]\n"
"buffer_load_dwordx4 v211, s[24:27], s61 offen offset:3072 lds\n"
"v_mfma_f32_16x16x32_bf16 v[56:59], v[140:143], a[200:203], v[56:59]\n"
"ds_read_b64_tr_b16 v[174:175], v203 offset:18432\n"
"v_mfma_f32_16x16x32_bf16 v[60:63], v[140:143], a[204:207], v[60:63]\n"
"s_waitcnt lgkmcnt(8)\n"
"v_mfma_f32_16x16x32_bf16 v[64:67], v[144:147], a[192:195], v[64:67]\n"
"ds_read_b64_tr_b16 v[176:177], v204 offset:16384\n"
"s_movk_i32 s60, 28672\n"
"v_mfma_f32_16x16x32_bf16 v[68:71], v[144:147], a[196:199], v[68:71]\n"
"buffer_load_dwordx4 a[224:227], v212, s[28:31], s60 offen offset:0\n"
"v_mfma_f32_16x16x32_bf16 v[72:75], v[144:147], a[200:203], v[72:75]\n"
"ds_read_b64_tr_b16 v[178:179], v204 offset:18432\n"
"v_mfma_f32_16x16x32_bf16 v[76:79], v[144:147], a[204:207], v[76:79]\n"
"v_mfma_f32_16x16x32_bf16 v[80:83], v[148:151], a[192:195], v[80:83]\n"
"ds_read_b64_tr_b16 v[180:181], v205 offset:16384\n"
"v_mfma_f32_16x16x32_bf16 v[84:87], v[148:151], a[196:199], v[84:87]\n"
"buffer_load_dwordx4 a[228:231], v212, s[28:31], s60 offen offset:256\n"
"v_mfma_f32_16x16x32_bf16 v[88:91], v[148:151], a[200:203], v[88:91]\n"
"ds_read_b64_tr_b16 v[182:183], v205 offset:18432\n"
"v_mfma_f32_16x16x32_bf16 v[92:95], v[148:151], a[204:207], v[92:95]\n"
"v_mfma_f32_16x16x32_bf16 v[96:99], v[152:155], a[192:195], v[96:99]\n"
"ds_read_b64_tr_b16 v[184:185], v206 offset:16384\n"
"v_mfma_f32_16x16x32_bf16 v[100:103], v[152:155], a[196:199], v[100:103]\n"
"buffer_load_dwordx4 a[232:235], v213, s[28:31], s60 offen offset:0\n"
"v_mfma_f32_16x16x32_bf16 v[104:107], v[152:155], a[200:203], v[104:107]\n"
"ds_read_b64_tr_b16 v[186:187], v206 offset:18432\n"
"v_mfma_f32_16x16x32_bf16 v[108:111], v[152:155], a[204:207], v[108:111]\n"
"v_mfma_f32_16x16x32_bf16 v[112:115], v[156:159], a[192:195], v[112:115]\n"
"ds_read_b64_tr_b16 v[188:189], v207 offset:16384\n"
"v_mfma_f32_16x16x32_bf16 v[116:119], v[156:159], a[196:199], v[116:119]\n"
"buffer_load_dwordx4 a[236:239], v213, s[28:31], s60 offen offset:256\n"
"v_mfma_f32_16x16x32_bf16 v[120:123], v[156:159], a[200:203], v[120:123]\n"
"ds_read_b64_tr_b16 v[190:191], v207 offset:18432\n"
"v_mfma_f32_16x16x32_bf16 v[124:127], v[156:159], a[204:207], v[124:127]\n"
"s_waitcnt lgkmcnt(8)\n"
"s_waitcnt vmcnt(8)\n"
"s_barrier\n"
"v_mfma_f32_16x16x32_bf16 v[0:3], v[160:163], a[208:211], v[0:3]\n"
"ds_read_b64_tr_b16 v[128:129], v200 offset:32768\n"
"s_movk_i32 s60, 30720\n"
"v_mfma_f32_16x16x32_bf16 v[4:7], v[160:163], a[212:215], v[4:7]\n"
"buffer_load_dwordx4 a[240:243], v212, s[28:31], s60 offen offset:0\n"
"v_mfma_f32_16x16x32_bf16 v[8:11], v[160:163], a[216:219], v[8:11]\n"
"ds_read_b64_tr_b16 v[130:131], v200 offset:34816\n"
"v_mfma_f32_16x16x32_bf16 v[12:15], v[160:163], a[220:223], v[12:15]\n"
"v_mfma_f32_16x16x32_bf16 v[16:19], v[164:167], a[208:211], v[16:19]\n"
"ds_read_b64_tr_b16 v[132:133], v201 offset:32768\n"
"v_mfma_f32_16x16x32_bf16 v[20:23], v[164:167], a[212:215], v[20:23]\n"
"v_mfma_f32_16x16x32_bf16 v[24:27], v[164:167], a[216:219], v[24:27]\n"
"ds_read_b64_tr_b16 v[134:135], v201 offset:34816\n"
"v_mfma_f32_16x16x32_bf16 v[28:31], v[164:167], a[220:223], v[28:31]\n"
"v_mfma_f32_16x16x32_bf16 v[32:35], v[168:171], a[208:211], v[32:35]\n"
"ds_read_b64_tr_b16 v[136:137], v202 offset:32768\n"
"v_mfma_f32_16x16x32_bf16 v[36:39], v[168:171], a[212:215], v[36:39]\n"
"buffer_load_dwordx4 a[244:247], v212, s[28:31], s60 offen offset:256\n"
"v_mfma_f32_16x16x32_bf16 v[40:43], v[168:171], a[216:219], v[40:43]\n"
"ds_read_b64_tr_b16 v[138:139], v202 offset:34816\n"
"v_mfma_f32_16x16x32_bf16 v[44:47], v[168:171], a[220:223], v[44:47]\n"
"v_mfma_f32_16x16x32_bf16 v[48:51], v[172:175], a[208:211], v[48:51]\n"
"ds_read_b64_tr_b16 v[140:141], v203 offset:32768\n"
"v_mfma_f32_16x16x32_bf16 v[52:55], v[172:175], a[212:215], v[52:55]\n"
"v_mfma_f32_16x16x32_bf16 v[56:59], v[172:175], a[216:219], v[56:59]\n"
"ds_read_b64_tr_b16 v[142:143], v203 offset:34816\n"
"v_mfma_f32_16x16x32_bf16 v[60:63], v[172:175], a[220:223], v[60:63]\n"
"s_waitcnt lgkmcnt(8)\n"
"v_mfma_f32_16x16x32_bf16 v[64:67], v[176:179], a[208:211], v[64:67]\n"
"ds_read_b64_tr_b16 v[144:145], v204 offset:32768\n"
"v_mfma_f32_16x16x32_bf16 v[68:71], v[176:179], a[212:215], v[68:71]\n"
"buffer_load_dwordx4 a[248:251], v213, s[28:31], s60 offen offset:0\n"
"v_mfma_f32_16x16x32_bf16 v[72:75], v[176:179], a[216:219], v[72:75]\n"
"ds_read_b64_tr_b16 v[146:147], v204 offset:34816\n"
"v_mfma_f32_16x16x32_bf16 v[76:79], v[176:179], a[220:223], v[76:79]\n"
"v_mfma_f32_16x16x32_bf16 v[80:83], v[180:183], a[208:211], v[80:83]\n"
"ds_read_b64_tr_b16 v[148:149], v205 offset:32768\n"
"v_mfma_f32_16x16x32_bf16 v[84:87], v[180:183], a[212:215], v[84:87]\n"
"v_mfma_f32_16x16x32_bf16 v[88:91], v[180:183], a[216:219], v[88:91]\n"
"ds_read_b64_tr_b16 v[150:151], v205 offset:34816\n"
"v_mfma_f32_16x16x32_bf16 v[92:95], v[180:183], a[220:223], v[92:95]\n"
"v_mfma_f32_16x16x32_bf16 v[96:99], v[184:187], a[208:211], v[96:99]\n"
"ds_read_b64_tr_b16 v[152:153], v206 offset:32768\n"
"v_mfma_f32_16x16x32_bf16 v[100:103], v[184:187], a[212:215], v[100:103]\n"
"buffer_load_dwordx4 a[252:255], v213, s[28:31], s60 offen offset:256\n"
"v_mfma_f32_16x16x32_bf16 v[104:107], v[184:187], a[216:219], v[104:107]\n"
"ds_read_b64_tr_b16 v[154:155], v206 offset:34816\n"
"v_mfma_f32_16x16x32_bf16 v[108:111], v[184:187], a[220:223], v[108:111]\n"
"v_mfma_f32_16x16x32_bf16 v[112:115], v[188:191], a[208:211], v[112:115]\n"
"ds_read_b64_tr_b16 v[156:157], v207 offset:32768\n"
"v_mfma_f32_16x16x32_bf16 v[116:119], v[188:191], a[212:215], v[116:119]\n"
"v_mfma_f32_16x16x32_bf16 v[120:123], v[188:191], a[216:219], v[120:123]\n"
"ds_read_b64_tr_b16 v[158:159], v207 offset:34816\n"
"v_mfma_f32_16x16x32_bf16 v[124:127], v[188:191], a[220:223], v[124:127]\n"
"s_waitcnt lgkmcnt(8)\n"
"s_waitcnt vmcnt(4)\n"
"s_barrier\n"
"v_mfma_f32_16x16x32_bf16 v[0:3], v[128:131], a[224:227], v[0:3]\n"
"ds_read_b64_tr_b16 v[160:161], v200 offset:49152\n"
"v_mfma_f32_16x16x32_bf16 v[4:7], v[128:131], a[228:231], v[4:7]\n"
"v_mfma_f32_16x16x32_bf16 v[8:11], v[128:131], a[232:235], v[8:11]\n"
"ds_read_b64_tr_b16 v[162:163], v200 offset:51200\n"
"v_mfma_f32_16x16x32_bf16 v[12:15], v[128:131], a[236:239], v[12:15]\n"
"v_mfma_f32_16x16x32_bf16 v[16:19], v[132:135], a[224:227], v[16:19]\n"
"ds_read_b64_tr_b16 v[164:165], v201 offset:49152\n"
"v_mfma_f32_16x16x32_bf16 v[20:23], v[132:135], a[228:231], v[20:23]\n"
"v_mfma_f32_16x16x32_bf16 v[24:27], v[132:135], a[232:235], v[24:27]\n"
"ds_read_b64_tr_b16 v[166:167], v201 offset:51200\n"
"v_mfma_f32_16x16x32_bf16 v[28:31], v[132:135], a[236:239], v[28:31]\n"
"v_mfma_f32_16x16x32_bf16 v[32:35], v[136:139], a[224:227], v[32:35]\n"
"ds_read_b64_tr_b16 v[168:169], v202 offset:49152\n"
"v_mfma_f32_16x16x32_bf16 v[36:39], v[136:139], a[228:231], v[36:39]\n"
"v_mfma_f32_16x16x32_bf16 v[40:43], v[136:139], a[232:235], v[40:43]\n"
"ds_read_b64_tr_b16 v[170:171], v202 offset:51200\n"
"v_mfma_f32_16x16x32_bf16 v[44:47], v[136:139], a[236:239], v[44:47]\n"
"v_mfma_f32_16x16x32_bf16 v[48:51], v[140:143], a[224:227], v[48:51]\n"
"ds_read_b64_tr_b16 v[172:173], v203 offset:49152\n"
"v_mfma_f32_16x16x32_bf16 v[52:55], v[140:143], a[228:231], v[52:55]\n"
"v_mfma_f32_16x16x32_bf16 v[56:59], v[140:143], a[232:235], v[56:59]\n"
"ds_read_b64_tr_b16 v[174:175], v203 offset:51200\n"
"v_mfma_f32_16x16x32_bf16 v[60:63], v[140:143], a[236:239], v[60:63]\n"
"s_waitcnt lgkmcnt(8)\n"
"v_mfma_f32_16x16x32_bf16 v[64:67], v[144:147], a[224:227], v[64:67]\n"
"ds_read_b64_tr_b16 v[176:177], v204 offset:49152\n"
"v_mfma_f32_16x16x32_bf16 v[68:71], v[144:147], a[228:231], v[68:71]\n"
"v_mfma_f32_16x16x32_bf16 v[72:75], v[144:147], a[232:235], v[72:75]\n"
"ds_read_b64_tr_b16 v[178:179], v204 offset:51200\n"
"v_mfma_f32_16x16x32_bf16 v[76:79], v[144:147], a[236:239], v[76:79]\n"
"v_mfma_f32_16x16x32_bf16 v[80:83], v[148:151], a[224:227], v[80:83]\n"
"ds_read_b64_tr_b16 v[180:181], v205 offset:49152\n"
"v_mfma_f32_16x16x32_bf16 v[84:87], v[148:151], a[228:231], v[84:87]\n"
"v_mfma_f32_16x16x32_bf16 v[88:91], v[148:151], a[232:235], v[88:91]\n"
"ds_read_b64_tr_b16 v[182:183], v205 offset:51200\n"
"v_mfma_f32_16x16x32_bf16 v[92:95], v[148:151], a[236:239], v[92:95]\n"
"v_mfma_f32_16x16x32_bf16 v[96:99], v[152:155], a[224:227], v[96:99]\n"
"ds_read_b64_tr_b16 v[184:185], v206 offset:49152\n"
"v_mfma_f32_16x16x32_bf16 v[100:103], v[152:155], a[228:231], v[100:103]\n"
"v_mfma_f32_16x16x32_bf16 v[104:107], v[152:155], a[232:235], v[104:107]\n"
"ds_read_b64_tr_b16 v[186:187], v206 offset:51200\n"
"v_mfma_f32_16x16x32_bf16 v[108:111], v[152:155], a[236:239], v[108:111]\n"
"v_mfma_f32_16x16x32_bf16 v[112:115], v[156:159], a[224:227], v[112:115]\n"
"ds_read_b64_tr_b16 v[188:189], v207 offset:49152\n"
"v_mfma_f32_16x16x32_bf16 v[116:119], v[156:159], a[228:231], v[116:119]\n"
"v_mfma_f32_16x16x32_bf16 v[120:123], v[156:159], a[232:235], v[120:123]\n"
"ds_read_b64_tr_b16 v[190:191], v207 offset:51200\n"
"v_mfma_f32_16x16x32_bf16 v[124:127], v[156:159], a[236:239], v[124:127]\n"
"s_waitcnt lgkmcnt(8)\n"
"s_waitcnt vmcnt(0)\n"
"v_mfma_f32_16x16x32_bf16 v[0:3], v[160:163], a[240:243], v[0:3]\n"
"v_mfma_f32_16x16x32_bf16 v[4:7], v[160:163], a[244:247], v[4:7]\n"
"v_mfma_f32_16x16x32_bf16 v[8:11], v[160:163], a[248:251], v[8:11]\n"
"v_mfma_f32_16x16x32_bf16 v[12:15], v[160:163], a[252:255], v[12:15]\n"
"v_mfma_f32_16x16x32_bf16 v[16:19], v[164:167], a[240:243], v[16:19]\n"
"v_mfma_f32_16x16x32_bf16 v[20:23], v[164:167], a[244:247], v[20:23]\n"
"s_nop 2\n"
"ds_write_b128 v214, v[0:3] offset:0\n"
"v_mfma_f32_16x16x32_bf16 v[24:27], v[164:167], a[248:251], v[24:27]\n"
"ds_write_b128 v214, v[4:7] offset:1024\n"
"v_mfma_f32_16x16x32_bf16 v[28:31], v[164:167], a[252:255], v[28:31]\n"
"ds_write_b128 v214, v[8:11] offset:2048\n"
"v_mfma_f32_16x16x32_bf16 v[32:35], v[168:171], a[240:243], v[32:35]\n"
"ds_write_b128 v214, v[12:15] offset:3072\n"
"v_mfma_f32_16x16x32_bf16 v[36:39], v[168:171], a[244:247], v[36:39]\n"
"ds_write_b128 v214, v[16:19] offset:4096\n"
"v_mfma_f32_16x16x32_bf16 v[40:43], v[168:171], a[248:251], v[40:43]\n"
"ds_write_b128 v214, v[20:23] offset:5120\n"
"v_mfma_f32_16x16x32_bf16 v[44:47], v[168:171], a[252:255], v[44:47]\n"
"ds_write_b128 v214, v[24:27] offset:6144\n"
"v_mfma_f32_16x16x32_bf16 v[48:51], v[172:175], a[240:243], v[48:51]\n"
"ds_write_b128 v214, v[28:31] offset:7168\n"
"v_mfma_f32_16x16x32_bf16 v[52:55], v[172:175], a[244:247], v[52:55]\n"
"ds_write_b128 v214, v[32:35] offset:8192\n"
"v_mfma_f32_16x16x32_bf16 v[56:59], v[172:175], a[248:251], v[56:59]\n"
"ds_write_b128 v214, v[36:39] offset:9216\n"
"v_mfma_f32_16x16x32_bf16 v[60:63], v[172:175], a[252:255], v[60:63]\n"
"s_waitcnt lgkmcnt(0)\n"
"ds_write_b128 v214, v[40:43] offset:10240\n"
"v_mfma_f32_16x16x32_bf16 v[64:67], v[176:179], a[240:243], v[64:67]\n"
"ds_write_b128 v214, v[44:47] offset:11264\n"
"v_mfma_f32_16x16x32_bf16 v[68:71], v[176:179], a[244:247], v[68:71]\n"
"ds_write_b128 v214, v[48:51] offset:12288\n"
"v_mfma_f32_16x16x32_bf16 v[72:75], v[176:179], a[248:251], v[72:75]\n"
"ds_write_b128 v214, v[52:55] offset:13312\n"
"v_mfma_f32_16x16x32_bf16 v[76:79], v[176:179], a[252:255], v[76:79]\n"
"ds_write_b128 v214, v[56:59] offset:14336\n"
"v_mfma_f32_16x16x32_bf16 v[80:83], v[180:183], a[240:243], v[80:83]\n"
"ds_write_b128 v214, v[60:63] offset:15360\n"
"v_mfma_f32_16x16x32_bf16 v[84:87], v[180:183], a[244:247], v[84:87]\n"
"v_mfma_f32_16x16x32_bf16 v[88:91], v[180:183], a[248:251], v[88:91]\n"
"v_mfma_f32_16x16x32_bf16 v[92:95], v[180:183], a[252:255], v[92:95]\n"
"v_mfma_f32_16x16x32_bf16 v[96:99], v[184:187], a[240:243], v[96:99]\n"
"v_mfma_f32_16x16x32_bf16 v[100:103], v[184:187], a[244:247], v[100:103]\n"
"v_mfma_f32_16x16x32_bf16 v[104:107], v[184:187], a[248:251], v[104:107]\n"
"v_mfma_f32_16x16x32_bf16 v[108:111], v[184:187], a[252:255], v[108:111]\n"
"v_mfma_f32_16x16x32_bf16 v[112:115], v[188:191], a[240:243], v[112:115]\n"
"v_mfma_f32_16x16x32_bf16 v[116:119], v[188:191], a[244:247], v[116:119]\n"
"v_mfma_f32_16x16x32_bf16 v[120:123], v[188:191], a[248:251], v[120:123]\n"
"v_mfma_f32_16x16x32_bf16 v[124:127], v[188:191], a[252:255], v[124:127]\n"
"s_waitcnt lgkmcnt(0)\n"
"s_barrier\n"
"ds_read_b128 v[128:131], v215 offset:0\n"
"ds_read_b128 v[132:135], v215 offset:1024\n"
"ds_read_b128 v[136:139], v215 offset:2048\n"
"ds_read_b128 v[140:143], v215 offset:3072\n"
"ds_read_b128 v[144:147], v215 offset:4096\n"
"ds_read_b128 v[148:151], v215 offset:5120\n"
"ds_read_b128 v[152:155], v215 offset:6144\n"
"ds_read_b128 v[156:159], v215 offset:7168\n"
"ds_read_b128 v[160:163], v215 offset:8192\n"
"ds_read_b128 v[164:167], v215 offset:9216\n"
"ds_read_b128 v[168:171], v215 offset:10240\n"
"ds_read_b128 v[172:175], v215 offset:11264\n"
"ds_read_b128 v[176:179], v215 offset:12288\n"
"ds_read_b128 v[180:183], v215 offset:13312\n"
"ds_read_b128 v[184:187], v215 offset:14336\n"
"ds_read_b128 v[188:191], v215 offset:15360\n"
"s_waitcnt lgkmcnt(8)\n"
"v_pk_add_f32 v[64:65], v[64:65], v[128:129]\n"
"v_pk_add_f32 v[66:67], v[66:67], v[130:131]\n"
"v_pk_add_f32 v[80:81], v[80:81], v[144:145]\n"
"v_pk_add_f32 v[82:83], v[82:83], v[146:147]\n"
"v_cvt_pk_bf16_f32 v0, v64, v65\n"
"v_cvt_pk_bf16_f32 v1, v66, v67\n"
"v_cvt_pk_bf16_f32 v2, v80, v81\n"
"v_cvt_pk_bf16_f32 v3, v82, v83\n"
"buffer_store_dwordx4 v[0:3], v216, s[32:35], s64 offen offset:0 sc1\n"
"v_pk_add_f32 v[68:69], v[68:69], v[132:133]\n"
"v_pk_add_f32 v[70:71], v[70:71], v[134:135]\n"
"v_pk_add_f32 v[84:85], v[84:85], v[148:149]\n"
"v_pk_add_f32 v[86:87], v[86:87], v[150:151]\n"
"v_cvt_pk_bf16_f32 v8, v68, v69\n"
"v_cvt_pk_bf16_f32 v9, v70, v71\n"
"v_cvt_pk_bf16_f32 v10, v84, v85\n"
"v_cvt_pk_bf16_f32 v11, v86, v87\n"
"buffer_store_dwordx4 v[8:11], v216, s[32:35], s65 offen offset:0 sc1\n"
"v_pk_add_f32 v[72:73], v[72:73], v[136:137]\n"
"v_pk_add_f32 v[74:75], v[74:75], v[138:139]\n"
"v_pk_add_f32 v[88:89], v[88:89], v[152:153]\n"
"v_pk_add_f32 v[90:91], v[90:91], v[154:155]\n"
"v_cvt_pk_bf16_f32 v16, v72, v73\n"
"v_cvt_pk_bf16_f32 v17, v74, v75\n"
"v_cvt_pk_bf16_f32 v18, v88, v89\n"
"v_cvt_pk_bf16_f32 v19, v90, v91\n"
"buffer_store_dwordx4 v[16:19], v216, s[32:35], s66 offen offset:0 sc1\n"
"v_pk_add_f32 v[76:77], v[76:77], v[140:141]\n"
"v_pk_add_f32 v[78:79], v[78:79], v[142:143]\n"
"v_pk_add_f32 v[92:93], v[92:93], v[156:157]\n"
"v_pk_add_f32 v[94:95], v[94:95], v[158:159]\n"
"v_cvt_pk_bf16_f32 v24, v76, v77\n"
"v_cvt_pk_bf16_f32 v25, v78, v79\n"
"v_cvt_pk_bf16_f32 v26, v92, v93\n"
"v_cvt_pk_bf16_f32 v27, v94, v95\n"
"buffer_store_dwordx4 v[24:27], v216, s[32:35], s67 offen offset:0 sc1\n"
"s_waitcnt lgkmcnt(0)\n"
"v_pk_add_f32 v[96:97], v[96:97], v[160:161]\n"
"v_pk_add_f32 v[98:99], v[98:99], v[162:163]\n"
"v_pk_add_f32 v[112:113], v[112:113], v[176:177]\n"
"v_pk_add_f32 v[114:115], v[114:115], v[178:179]\n"
"v_cvt_pk_bf16_f32 v4, v96, v97\n"
"v_cvt_pk_bf16_f32 v5, v98, v99\n"
"v_cvt_pk_bf16_f32 v6, v112, v113\n"
"v_cvt_pk_bf16_f32 v7, v114, v115\n"
"buffer_store_dwordx4 v[4:7], v216, s[32:35], s64 offen offset:1024 sc1\n"
"v_pk_add_f32 v[100:101], v[100:101], v[164:165]\n"
"v_pk_add_f32 v[102:103], v[102:103], v[166:167]\n"
"v_pk_add_f32 v[116:117], v[116:117], v[180:181]\n"
"v_pk_add_f32 v[118:119], v[118:119], v[182:183]\n"
"v_cvt_pk_bf16_f32 v12, v100, v101\n"
"v_cvt_pk_bf16_f32 v13, v102, v103\n"
"v_cvt_pk_bf16_f32 v14, v116, v117\n"
"v_cvt_pk_bf16_f32 v15, v118, v119\n"
"buffer_store_dwordx4 v[12:15], v216, s[32:35], s65 offen offset:1024 sc1\n"
"v_pk_add_f32 v[104:105], v[104:105], v[168:169]\n"
"v_pk_add_f32 v[106:107], v[106:107], v[170:171]\n"
"v_pk_add_f32 v[120:121], v[120:121], v[184:185]\n"
"v_pk_add_f32 v[122:123], v[122:123], v[186:187]\n"
"v_cvt_pk_bf16_f32 v20, v104, v105\n"
"v_cvt_pk_bf16_f32 v21, v106, v107\n"
"v_cvt_pk_bf16_f32 v22, v120, v121\n"
"v_cvt_pk_bf16_f32 v23, v122, v123\n"
"buffer_store_dwordx4 v[20:23], v216, s[32:35], s66 offen offset:1024 sc1\n"
"v_pk_add_f32 v[108:109], v[108:109], v[172:173]\n"
"v_pk_add_f32 v[110:111], v[110:111], v[174:175]\n"
"v_pk_add_f32 v[124:125], v[124:125], v[188:189]\n"
"v_pk_add_f32 v[126:127], v[126:127], v[190:191]\n"
"v_cvt_pk_bf16_f32 v28, v108, v109\n"
"v_cvt_pk_bf16_f32 v29, v110, v111\n"
"v_cvt_pk_bf16_f32 v30, v124, v125\n"
"v_cvt_pk_bf16_f32 v31, v126, v127\n"
"buffer_store_dwordx4 v[28:31], v216, s[32:35], s67 offen offset:1024 sc1\n"
"L_kend_%=:\n"
  :: "s"(wo), "s"(ws), "s"(wave), "s"(blk) : "v0", "v1", "v2", "v3", "v4", "v5", "v6", "v7", "v8", "v9", "v10", "v11", "v12", "v13", "v14", "v15", "v16", "v17", "v18", "v19", "v20", "v21", "v22", "v23", "v24", "v25", "v26", "v27", "v28", "v29", "v30", "v31", "v32", "v33", "v34", "v35", "v36", "v37", "v38", "v39", "v40", "v41", "v42", "v43", "v44", "v45", "v46", "v47", "v48", "v49", "v50", "v51", "v52", "v53", "v54", "v55", "v56", "v57", "v58", "v59", "v60", "v61", "v62", "v63", "v64", "v65", "v66", "v67", "v68", "v69", "v70", "v71", "v72", "v73", "v74", "v75", "v76", "v77", "v78", "v79", "v80", "v81", "v82", "v83", "v84", "v85", "v86", "v87", "v88", "v89", "v90", "v91", "v92", "v93", "v94", "v95", "v96", "v97", "v98", "v99", "v100", "v101", "v102", "v103", "v104", "v105", "v106", "v107", "v108", "v109", "v110", "v111", "v112", "v113", "v114", "v115", "v116", "v117", "v118", "v119", "v120", "v121", "v122", "v123", "v124", "v125", "v126", "v127", "v128", "v129", "v130", "v131", "v132", "v133", "v134", "v135", "v136", "v137", "v138", "v139", "v140", "v141", "v142", "v143", "v144", "v145", "v146", "v147", "v148", "v149", "v150", "v151", "v152", "v153", "v154", "v155", "v156", "v157", "v158", "v159", "v160", "v161", "v162", "v163", "v164", "v165", "v166", "v167", "v168", "v169", "v170", "v171", "v172", "v173", "v174", "v175", "v176", "v177", "v178", "v179", "v180", "v181", "v182", "v183", "v184", "v185", "v186", "v187", "v188", "v189", "v190", "v191", "v192", "v193", "v194", "v195", "v196", "v197", "v198", "v199", "v200", "v201", "v202", "v203", "v204", "v205", "v206", "v207", "v208", "v209", "v210", "v211", "v212", "v213", "v214", "v215", "v216", "v217", "v218", "v219", "v220", "v221", "v222", "v223", "v224", "v225", "v226", "v227", "v228", "v229", "v230", "v231", "v232", "v233", "v234", "v235", "v236", "v237", "v238", "v239", "v240", "v241", "v242", "v243", "v244", "v245", "v246", "v247", "v248", "v249", "v250", "v251", "v252", "v253", "v254", "v255", "a0", "a1", "a2", "a3", "a4", "a5", "a6", "a7", "a8", "a9", "a10", "a11", "a12", "a13", "a14", "a15", "a16", "a17", "a18", "a19", "a20", "a21", "a22", "a23", "a24", "a25", "a26", "a27", "a28", "a29", "a30", "a31", "a32", "a33", "a34", "a35", "a36", "a37", "a38", "a39", "a40", "a41", "a42", "a43", "a44", "a45", "a46", "a47", "a48", "a49", "a50", "a51", "a52", "a53", "a54", "a55", "a56", "a57", "a58", "a59", "a60", "a61", "a62", "a63", "a64", "a65", "a66", "a67", "a68", "a69", "a70", "a71", "a72", "a73", "a74", "a75", "a76", "a77", "a78", "a79", "a80", "a81", "a82", "a83", "a84", "a85", "a86", "a87", "a88", "a89", "a90", "a91", "a92", "a93", "a94", "a95", "a96", "a97", "a98", "a99", "a100", "a101", "a102", "a103", "a104", "a105", "a106", "a107", "a108", "a109", "a110", "a111", "a112", "a113", "a114", "a115", "a116", "a117", "a118", "a119", "a120", "a121", "a122", "a123", "a124", "a125", "a126", "a127", "a128", "a129", "a130", "a131", "a132", "a133", "a134", "a135", "a136", "a137", "a138", "a139", "a140", "a141", "a142", "a143", "a144", "a145", "a146", "a147", "a148", "a149", "a150", "a151", "a152", "a153", "a154", "a155", "a156", "a157", "a158", "a159", "a160", "a161", "a162", "a163", "a164", "a165", "a166", "a167", "a168", "a169", "a170", "a171", "a172", "a173", "a174", "a175", "a176", "a177", "a178", "a179", "a180", "a181", "a182", "a183", "a184", "a185", "a186", "a187", "a188", "a189", "a190", "a191", "a192", "a193", "a194", "a195", "a196", "a197", "a198", "a199", "a200", "a201", "a202", "a203", "a204", "a205", "a206", "a207", "a208", "a209", "a210", "a211", "a212", "a213", "a214", "a215", "a216", "a217", "a218", "a219", "a220", "a221", "a222", "a223", "a224", "a225", "a226", "a227", "a228", "a229", "a230", "a231", "a232", "a233", "a234", "a235", "a236", "a237", "a238", "a239", "a240", "a241", "a242", "a243", "a244", "a245", "a246", "a247", "a248", "a249", "a250", "a251", "a252", "a253", "a254", "a255", "s24", "s25", "s26", "s27", "s28", "s29", "s30", "s31", "s32", "s33", "s34", "s35", "s36", "s37", "s38", "s39", "s40", "s41", "s42", "s43", "s44", "s45", "s46", "s47", "s48", "s49", "s50", "s51", "s52", "s53", "s54", "s55", "s56", "s57", "s58", "s59", "s60", "s61", "s62", "s63", "s64", "s65", "s66", "s67", "s68", "s69", "s70", "s71", "s72", "s73", "s74", "s75", "s76", "s77", "s78", "s79", "s80", "s81", "s82", "s83", "s84", "s85", "s86", "s87", "s88", "s89", "s90", "s91", "s92", "s93", "s94", "s95", "s96", "s97", "s98", "s99", "s100", "s101", "vcc", "memory", "scc");
}
__global__ __launch_bounds__(256) void k4_ln2(const u16* __restrict__ x, char* __restrict__ ws) {
  __shared__ float red[2][4][8];
  const int t = threadIdx.x, m8 = t & 7, g = (t >> 3) & 3, c = t >> 5, blk = blockIdx.x, w = t >> 6;
  const int rb16 = blk >> 1, pos = (g * 16 + (blk & 1) * 8 + m8) * 16, row = blk * 8 + m8;
  const float* mod = (const float*)(ws + WS_MOD);
  u32x4 p[2][4]; u32x2 xv[2][2]; f32x4 ga[2][2], sb[2][2], cb[2][2];
#pragma unroll
  for (int z = 0; z < 2; ++z) {
    const int cc = c + 8 * z;
#pragma unroll
    for (int s = 0; s < 4; ++s) p[z][s] = *(const u32x4*)(ws + WS_PART + ((size_t)((s * 128 + rb16) * 16 + cc) << 10) + pos);
#pragma unroll
    for (int h = 0; h < 2; ++h) { const int n = cc * 32 + h * 16 + g * 4;
      xv[z][h] = *(const u32x2*)(x + row * 512 + n);
      ga[z][h] = *(const f32x4*)(mod + 1024 + n); sb[z][h] = *(const f32x4*)(mod + 1536 + n); cb[z][h] = *(const f32x4*)(mod + 2048 + n); }
  }
  float v[2][8]; float sum = 0.f; u32x4 xo[2];
#pragma unroll
  for (int z = 0; z < 2; ++z) {
#pragma unroll
    for (int e = 0; e < 4; ++e) {
      float lo = 0.f, hi = 0.f;
#pragma unroll
      for (int s = 0; s < 4; ++s) { lo += __uint_as_float(p[z][s][e] << 16); hi += __uint_as_float(p[z][s][e] & 0xffff0000u); }
      const unsigned xr = xv[z][e >> 1][e & 1];
      lo = __uint_as_float(xr << 16) + ga[z][e >> 1][(e & 1) * 2] * lo; hi = __uint_as_float(xr & 0xffff0000u) + ga[z][e >> 1][(e & 1) * 2 + 1] * hi;
      const unsigned pk = cvtpk(lo, hi); xo[z][e] = pk;
      v[z][2 * e] = __uint_as_float(pk << 16); v[z][2 * e + 1] = __uint_as_float(pk & 0xffff0000u);
      sum += v[z][2 * e] + v[z][2 * e + 1];
    }
    char* q = ws + WS_X1 + ((size_t)(rb16 * 16 + c + 8 * z) << 10) + pos;
    asm volatile("global_store_dwordx4 %0, %1, off sc1\n s_nop 1" :: "v"(q), "v"(xo[z]) : "memory");
  }
  sum += __shfl_xor(sum, 8); sum += __shfl_xor(sum, 16); sum += __shfl_xor(sum, 32);
  if ((t & 63) < 8) red[0][w][m8] = sum;
  __syncthreads();
  const float mean = (red[0][0][m8] + red[0][1][m8] + red[0][2][m8] + red[0][3][m8]) * (1.f / 512.f);
  float q2 = 0.f;
#pragma unroll
  for (int z = 0; z < 2; ++z)
#pragma unroll
    for (int e = 0; e < 8; ++e) { v[z][e] -= mean; q2 += v[z][e] * v[z][e]; }
  q2 += __shfl_xor(q2, 8); q2 += __shfl_xor(q2, 16); q2 += __shfl_xor(q2, 32);
  if ((t & 63) < 8) red[1][w][m8] = q2;
  __syncthreads();
  const float rs = rsqrtf((red[1][0][m8] + red[1][1][m8] + red[1][2][m8] + red[1][3][m8]) * (1.f / 512.f) + 1e-5f);
#pragma unroll
  for (int z = 0; z < 2; ++z) { u32x4 o;
#pragma unroll
    for (int e = 0; e < 4; ++e) { const int h = e >> 1, i = (e & 1) * 2;
      o[e] = cvtpk(v[z][2 * e] * rs * (1.f + cb[z][h][i]) + sb[z][h][i], v[z][2 * e + 1] * rs * (1.f + cb[z][h][i + 1]) + sb[z][h][i + 1]); }
    char* q = ws + WS_H2 + ((size_t)(rb16 * 16 + c + 8 * z) << 10) + pos;
    asm volatile("global_store_dwordx4 %0, %1, off sc1\n s_nop 1" :: "v"(q), "v"(o) : "memory");
  }
}
__global__ __launch_bounds__(256, 1) void k5_ffn1(const u16* w1, char* ws) {
  unsigned wave = __builtin_amdgcn_readfirstlane(threadIdx.x >> 6); unsigned blk = blockIdx.x;
  asm volatile(
"s_getpc_b64 s[64:65]\n"
"L_kpc_%=:\n"
"s_and_b32 s65, s65, 0xffff\n"
"s_mov_b32 s66, L_kend_%=-L_kpc_%=\n"
"s_mov_b32 s67, 0x20000\n"
"s_lshr_b32 s68, %3, 3\n"
"s_lshl_b32 s68, s68, 2\n"
"s_add_u32 s68, s68, %2\n"
"s_lshl_b32 s68, s68, 12\n"
"v_mbcnt_lo_u32_b32 v255, -1, 0\n"
"v_mbcnt_hi_u32_b32 v255, -1, v255\n"
"v_lshlrev_b32 v255, 6, v255\n"
"buffer_load_dword a255, v255, s[64:67], s68 offen\n"
"s_mov_b32 s48, %2\n"
"s_mov_b32 s49, %3\n"
"s_bfe_u32 s50, s49, 0x20001\n"
"s_lshl_b32 s50, s50, 2\n"
"s_bfe_u32 s51, s49, 0x20003\n"
"s_add_u32 s54, s50, s51\n"
"s_and_b32 s50, s49, 1\n"
"s_mul_i32 s50, s50, 7\n"
"s_lshr_b32 s51, s49, 5\n"
"s_add_u32 s55, s50, s51\n"
"s_lshl_b32 s50, s55, 8\n"
"s_mov_b64 s[24:25], %0\n"
"s_add_u32 s24, s24, s50\n"
"s_addc_u32 s25, s25, 0\n"
"s_and_b32 s25, s25, 0xffff\n"
"s_mov_b32 s26, 0x1c0000\n"
"s_mov_b32 s27, 0x20000\n"
"v_mbcnt_lo_u32_b32 v156, -1, 0\n"
"v_mbcnt_hi_u32_b32 v156, -1, v156\n"
"v_lshrrev_b32 v157, 4, v156\n"
"v_and_b32 v160, 15, v156\n"
"v_lshrrev_b32 v158, 2, v160\n"
"v_and_b32 v159, 3, v156\n"
"s_lshl_b32 s52, s48, 3\n"
"v_add_u32 v161, s52, v157\n"
"v_mul_u32_u24 v161, 3584, v161\n"
"v_lshlrev_b32 v162, 1, v157\n"
"v_xor_b32 v162, v162, v160\n"
"v_lshlrev_b32 v162, 4, v162\n"
"v_add_u32 v162, v162, v161\n"
"v_mov_b32 v144, v162\n"
"v_lshlrev_b32 v162, 1, v157\n"
"v_xor_b32 v162, v162, v160\n"
"v_xor_b32 v162, 8, v162\n"
"v_lshlrev_b32 v162, 4, v162\n"
"v_add_u32 v162, v162, v161\n"
"v_add_u32 v145, 13312, v162\n"
"s_lshl_b32 s62, s48, 11\n"
"s_lshl_b32 s50, s54, 17\n"
"s_lshl_b32 s51, s48, 15\n"
"s_add_u32 s50, s50, s51\n"
"s_mov_b64 s[28:29], %1\n"
"s_add_u32 s28, s28, s50\n"
"s_addc_u32 s29, s29, 0\n"
"s_add_u32 s28, s28, 80740352\n"
"s_addc_u32 s29, s29, 0\n"
"s_and_b32 s29, s29, 0xffff\n"
"s_mov_b32 s30, 0x8000\n"
"s_mov_b32 s31, 0x20000\n"
"v_lshlrev_b32 v146, 4, v156\n"
"v_add_u32 v147, 0x4000, v146\n"
"s_add_u32 m0, s62, 0\n"
"s_mov_b32 s61, 0\n"
"s_nop 0\n"
"buffer_load_dwordx4 v144, s[24:27], s61 offen offset:0 lds\n"
"buffer_load_dwordx4 v145, s[24:27], s61 offen offset:1024 lds\n"
"s_movk_i32 s60, 0\n"
"buffer_load_dwordx4 a[0:3], v146, s[28:31], s60 offen offset:0\n"
"buffer_load_dwordx4 a[4:7], v147, s[28:31], s60 offen offset:0\n"
"s_add_u32 m0, s62, 8192\n"
"s_mov_b32 s61, 114688\n"
"s_nop 0\n"
"buffer_load_dwordx4 v144, s[24:27], s61 offen offset:0 lds\n"
"buffer_load_dwordx4 v145, s[24:27], s61 offen offset:1024 lds\n"
"s_add_u32 m0, s62, 16384\n"
"s_mov_b32 s61, 229376\n"
"s_nop 0\n"
"buffer_load_dwordx4 v144, s[24:27], s61 offen offset:0 lds\n"
"buffer_load_dwordx4 v145, s[24:27], s61 offen offset:1024 lds\n"
"buffer_load_dwordx4 a[8:11], v146, s[28:31], s60 offen offset:1024\n"
"buffer_load_dwordx4 a[12:15], v147, s[28:31], s60 offen offset:1024\n"
"s_add_u32 m0, s62, 24576\n"
"s_mov_b32 s61, 344064\n"
"s_nop 0\n"
"buffer_load_dwordx4 v144, s[24:27], s61 offen offset:0 lds\n"
"buffer_load_dwordx4 v145, s[24:27], s61 offen offset:1024 lds\n"
"buffer_load_dwordx4 a[16:19], v146, s[28:31], s60 offen offset:2048\n"
"buffer_load_dwordx4 a[20:23], v147, s[28:31], s60 offen offset:2048\n"
"v_lshlrev_b32 v161, 2, v157\n"
"v_add_u32 v161, v161, v158\n"
"v_and_b32 v162, 7, v161\n"
"v_lshlrev_b32 v161, 8, v161\n"
"v_lshlrev_b32 v162, 5, v162\n"
"v_or_b32 v161, v161, v162\n"
"v_lshlrev_b32 v162, 3, v159\n"
"v_or_b32 v128, v161, v162\n"
"v_xor_b32 v129, 32, v128\n"
"v_xor_b32 v130, 64, v128\n"
"v_xor_b32 v131, 96, v128\n"
"v_xor_b32 v132, 128, v128\n"
"v_xor_b32 v133, 160, v128\n"
"v_xor_b32 v134, 192, v128\n"
"v_xor_b32 v135, 224, v128\n"
"v_add_u32 v136, 0x10000, v128\n"
"v_add_u32 v137, 0x10000, v129\n"
"v_add_u32 v138, 0x10000, v130\n"
"v_add_u32 v139, 0x10000, v131\n"
"v_add_u32 v140, 0x10000, v132\n"
"v_add_u32 v141, 0x10000, v133\n"
"v_add_u32 v142, 0x10000, v134\n"
"v_add_u32 v143, 0x10000, v135\n"
"v_lshlrev_b32 v148, 4, v156\n"
"s_lshl_b32 s50, s54, 3\n"
"s_lshl_b32 s51, s48, 1\n"
"s_add_u32 s50, s50, s51\n"
"s_mul_i32 s50, s50, 57344\n"
"s_lshl_b32 s51, s55, 12\n"
"s_add_u32 s50, s50, s51\n"
"s_mov_b64 s[32:33], %1\n"
"s_add_u32 s32, s32, s50\n"
"s_addc_u32 s33, s33, 0\n"
"s_add_u32 s32, s32, 83886080\n"
"s_addc_u32 s33, s33, 0\n"
"s_and_b32 s33, s33, 0xffff\n"
"s_mov_b32 s34, 0x20000\n"
"s_mov_b32 s35, 0x20000\n"
"s_mov_b32 s64, 0\n"
"s_mov_b32 s65, 57344\n"
"v_mov_b32 v150, 0xbfb8aa3b\n"
"v_mov_b32 v151, 0xbfb8aa3b\n"
"v_mov_b32 v152, 1.0\n"
"v_mov_b32 v153, 1.0\n"
"s_waitcnt vmcnt(12)\n"
"s_barrier\n"
"ds_read_b64_tr_b16 v[64:65], v128 offset:0\n"
"ds_read_b64_tr_b16 v[66:67], v128 offset:4096\n"
"ds_read_b64_tr_b16 v[68:69], v129 offset:0\n"
"ds_read_b64_tr_b16 v[70:71], v129 offset:4096\n"
"ds_read_b64_tr_b16 v[72:73], v130 offset:0\n"
"ds_read_b64_tr_b16 v[74:75], v130 offset:4096\n"
"ds_read_b64_tr_b16 v[76:77], v131 offset:0\n"
"ds_read_b64_tr_b16 v[78:79], v131 offset:4096\n"
"ds_read_b64_tr_b16 v[80:81], v132 offset:0\n"
"ds_read_b64_tr_b16 v[82:83], v132 offset:4096\n"
"ds_read_b64_tr_b16 v[84:85], v133 offset:0\n"
"ds_read_b64_tr_b16 v[86:87], v133 offset:4096\n"
"ds_read_b64_tr_b16 v[88:89], v134 offset:0\n"
"ds_read_b64_tr_b16 v[90:91], v134 offset:4096\n"
"ds_read_b64_tr_b16 v[92:93], v135 offset:0\n"
"ds_read_b64_tr_b16 v[94:95], v135 offset:4096\n"
"s_waitcnt vmcnt(8) lgkmcnt(0)\n"
"s_barrier\n"
"v_mfma_f32_16x16x32_bf16 v[0:3], v[64:67], a[0:3], 0\n"
"ds_read_b64_tr_b16 v[96:97], v128 offset:8192\n"
"s_add_u32 m0, s62, 32768\n"
"s_mov_b32 s61, 458752\n"
"v_mfma_f32_16x16x32_bf16 v[4:7], v[64:67], a[4:7], 0\n"
"ds_read_b64_tr_b16 v[98:99], v128 offset:12288\n"
"buffer_load_dwordx4 v144, s[24:27], s61 offen offset:0 lds\n"
"v_mfma_f32_16x16x32_bf16 v[8:11], v[68:71], a[0:3], 0\n"
"ds_read_b64_tr_b16 v[100:101], v129 offset:8192\n"
"v_mfma_f32_16x16x32_bf16 v[12:15], v[68:71], a[4:7], 0\n"
"ds_read_b64_tr_b16 v[102:103], v129 offset:12288\n"
"buffer_load_dwordx4 v145, s[24:27], s61 offen offset:1024 lds\n"
"v_mfma_f32_16x16x32_bf16 v[16:19], v[72:75], a[0:3], 0\n"
"ds_read_b64_tr_b16 v[104:105], v130 offset:8192\n"
"v_mfma_f32_16x16x32_bf16 v[20:23], v[72:75], a[4:7], 0\n"
"ds_read_b64_tr_b16 v[106:107], v130 offset:12288\n"
"buffer_load_dwordx4 a[24:27], v146, s[28:31], s60 offen offset:3072\n"
"v_mfma_f32_16x16x32_bf16 v[24:27], v[76:79], a[0:3], 0\n"
"ds_read_b64_tr_b16 v[108:109], v131 offset:8192\n"
"v_mfma_f32_16x16x32_bf16 v[28:31], v[76:79], a[4:7], 0\n"
"ds_read_b64_tr_b16 v[110:111], v131 offset:12288\n"
"buffer_load_dwordx4 a[28:31], v147, s[28:31], s60 offen offset:3072\n"
"v_mfma_f32_16x16x32_bf16 v[32:35], v[80:83], a[0:3], 0\n"
"ds_read_b64_tr_b16 v[112:113], v132 offset:8192\n"
"s_add_u32 m0, s62, 40960\n"
"s_mov_b32 s61, 573440\n"
"v_mfma_f32_16x16x32_bf16 v[36:39], v[80:83], a[4:7], 0\n"
"ds_read_b64_tr_b16 v[114:115], v132 offset:12288\n"
"buffer_load_dwordx4 v144, s[24:27], s61 offen offset:0 lds\n"
"v_mfma_f32_16x16x32_bf16 v[40:43], v[84:87], a[0:3], 0\n"
"ds_read_b64_tr_b16 v[116:117], v133 offset:8192\n"
"v_mfma_f32_16x16x32_bf16 v[44:47], v[84:87], a[4:7], 0\n"
"ds_read_b64_tr_b16 v[118:119], v133 offset:12288\n"
"buffer_load_dwordx4 v145, s[24:27], s61 offen offset:1024 lds\n"
"v_mfma_f32_16x16x32_bf16 v[48:51], v[88:91], a[0:3], 0\n"
"ds_read_b64_tr_b16 v[120:121], v134 offset:8192\n"
"s_movk_i32 s60, 4096\n"
"v_mfma_f32_16x16x32_bf16 v[52:55], v[88:91], a[4:7], 0\n"
"ds_read_b64_tr_b16 v[122:123], v134 offset:12288\n"
"buffer_load_dwordx4 a[32:35], v146, s[28:31], s60 offen offset:0\n"
"v_mfma_f32_16x16x32_bf16 v[56:59], v[92:95], a[0:3], 0\n"
"ds_read_b64_tr_b16 v[124:125], v135 offset:8192\n"
"v_mfma_f32_16x16x32_bf16 v[60:63], v[92:95], a[4:7], 0\n"
"s_waitcnt lgkmcnt(7)\n"
"ds_read_b64_tr_b16 v[126:127], v135 offset:12288\n"
"buffer_load_dwordx4 a[36:39], v147, s[28:31], s60 offen offset:0\n"
"s_waitcnt vmcnt(12)\n"
"s_barrier\n"
"v_mfma_f32_16x16x32_bf16 v[0:3], v[96:99], a[8:11], v[0:3]\n"
"ds_read_b64_tr_b16 v[64:65], v128 offset:16384\n"
"s_add_u32 m0, s62, 49152\n"
"s_mov_b32 s61, 688128\n"
"v_mfma_f32_16x16x32_bf16 v[4:7], v[96:99], a[12:15], v[4:7]\n"
"ds_read_b64_tr_b16 v[66:67], v128 offset:20480\n"
"buffer_load_dwordx4 v144, s[24:27], s61 offen offset:0 lds\n"
"v_mfma_f32_16x16x32_bf16 v[8:11], v[100:103], a[8:11], v[8:11]\n"
"ds_read_b64_tr_b16 v[68:69], v129 offset:16384\n"
"v_mfma_f32_16x16x32_bf16 v[12:15], v[100:103], a[12:15], v[12:15]\n"
"ds_read_b64_tr_b16 v[70:71], v129 offset:20480\n"
"v_mfma_f32_16x16x32_bf16 v[16:19], v[104:107], a[8:11], v[16:19]\n"
"ds_read_b64_tr_b16 v[72:73], v130 offset:16384\n"
"v_mfma_f32_16x16x32_bf16 v[20:23], v[104:107], a[12:15], v[20:23]\n"
"ds_read_b64_tr_b16 v[74:75], v130 offset:20480\n"
"buffer_load_dwordx4 v145, s[24:27], s61 offen offset:1024 lds\n"
"v_mfma_f32_16x16x32_bf16 v[24:27], v[108:111], a[8:11], v[24:27]\n"
"ds_read_b64_tr_b16 v[76:77], v131 offset:16384\n"
"v_mfma_f32_16x16x32_bf16 v[28:31], v[108:111], a[12:15], v[28:31]\n"
"s_waitcnt lgkmcnt(7)\n"
"ds_read_b64_tr_b16 v[78:79], v131 offset:20480\n"
"v_mfma_f32_16x16x32_bf16 v[32:35], v[112:115], a[8:11], v[32:35]\n"
"ds_read_b64_tr_b16 v[80:81], v132 offset:16384\n"
"v_mfma_f32_16x16x32_bf16 v[36:39], v[112:115], a[12:15], v[36:39]\n"
"ds_read_b64_tr_b16 v[82:83], v132 offset:20480\n"
"buffer_load_dwordx4 a[40:43], v146, s[28:31], s60 offen offset:1024\n"
"v_mfma_f32_16x16x32_bf16 v[40:43], v[116:119], a[8:11], v[40:43]\n"
"ds_read_b64_tr_b16 v[84:85], v133 offset:16384\n"
"v_mfma_f32_16x16x32_bf16 v[44:47], v[116:119], a[12:15], v[44:47]\n"
"ds_read_b64_tr_b16 v[86:87], v133 offset:20480\n"
"v_mfma_f32_16x16x32_bf16 v[48:51], v[120:123], a[8:11], v[48:51]\n"
"ds_read_b64_tr_b16 v[88:89], v134 offset:16384\n"
"v_mfma_f32_16x16x32_bf16 v[52:55], v[120:123], a[12:15], v[52:55]\n"
"ds_read_b64_tr_b16 v[90:91], v134 offset:20480\n"
"buffer_load_dwordx4 a[44:47], v147, s[28:31], s60 offen offset:1024\n"
"v_mfma_f32_16x16x32_bf16 v[56:59], v[124:127], a[8:11], v[56:59]\n"
"ds_read_b64_tr_b16 v[92:93], v135 offset:16384\n"
"v_mfma_f32_16x16x32_bf16 v[60:63], v[124:127], a[12:15], v[60:63]\n"
"s_waitcnt lgkmcnt(7)\n"
"ds_read_b64_tr_b16 v[94:95], v135 offset:20480\n"
"s_waitcnt vmcnt(12)\n"
"s_barrier\n"
"v_mfma_f32_16x16x32_bf16 v[0:3], v[64:67], a[16:19], v[0:3]\n"
"ds_read_b64_tr_b16 v[96:97], v128 offset:24576\n"
"s_add_u32 m0, s62, 57344\n"
"s_mov_b32 s61, 802816\n"
"v_mfma_f32_16x16x32_bf16 v[4:7], v[64:67], a[20:23], v[4:7]\n"
"ds_read_b64_tr_b16 v[98:99], v128 offset:28672\n"
"buffer_load_dwordx4 v144, s[24:27], s61 offen offset:0 lds\n"
"v_mfma_f32_16x16x32_bf16 v[8:11], v[68:71], a[16:19], v[8:11]\n"
"ds_read_b64_tr_b16 v[100:101], v129 offset:24576\n"
"v_mfma_f32_16x16x32_bf16 v[12:15], v[68:71], a[20:23], v[12:15]\n"
"ds_read_b64_tr_b16 v[102:103], v129 offset:28672\n"
"v_mfma_f32_16x16x32_bf16 v[16:19], v[72:75], a[16:19], v[16:19]\n"
"ds_read_b64_tr_b16 v[104:105], v130 offset:24576\n"
"v_mfma_f32_16x16x32_bf16 v[20:23], v[72:75], a[20:23], v[20:23]\n"
"ds_read_b64_tr_b16 v[106:107], v130 offset:28672\n"
"buffer_load_dwordx4 v145, s[24:27], s61 offen offset:1024 lds\n"
"v_mfma_f32_16x16x32_bf16 v[24:27], v[76:79], a[16:19], v[24:27]\n"
"ds_read_b64_tr_b16 v[108:109], v131 offset:24576\n"
"v_mfma_f32_16x16x32_bf16 v[28:31], v[76:79], a[20:23], v[28:31]\n"
"s_waitcnt lgkmcnt(7)\n"
"ds_read_b64_tr_b16 v[110:111], v131 offset:28672\n"
"v_mfma_f32_16x16x32_bf16 v[32:35], v[80:83], a[16:19], v[32:35]\n"
"ds_read_b64_tr_b16 v[112:113], v132 offset:24576\n"
"v_mfma_f32_16x16x32_bf16 v[36:39], v[80:83], a[20:23], v[36:39]\n"
"ds_read_b64_tr_b16 v[114:115], v132 offset:28672\n"
"buffer_load_dwordx4 a[48:51], v146, s[28:31], s60 offen offset:2048\n"
"v_mfma_f32_16x16x32_bf16 v[40:43], v[84:87], a[16:19], v[40:43]\n"
"ds_read_b64_tr_b16 v[116:117], v133 offset:24576\n"
"v_mfma_f32_16x16x32_bf16 v[44:47], v[84:87], a[20:23], v[44:47]\n"
"ds_read_b64_tr_b16 v[118:119], v133 offset:28672\n"
"v_mfma_f32_16x16x32_bf16 v[48:51], v[88:91], a[16:19], v[48:51]\n"
"ds_read_b64_tr_b16 v[120:121], v134 offset:24576\n"
"v_mfma_f32_16x16x32_bf16 v[52:55], v[88:91], a[20:23], v[52:55]\n"
"ds_read_b64_tr_b16 v[122:123], v134 offset:28672\n"
"buffer_load_dwordx4 a[52:55], v147, s[28:31], s60 offen offset:2048\n"
"v_mfma_f32_16x16x32_bf16 v[56:59], v[92:95], a[16:19], v[56:59]\n"
"ds_read_b64_tr_b16 v[124:125], v135 offset:24576\n"
"v_mfma_f32_16x16x32_bf16 v[60:63], v[92:95], a[20:23], v[60:63]\n"
"s_waitcnt lgkmcnt(7)\n"
"ds_read_b64_tr_b16 v[126:127], v135 offset:28672\n"
"s_waitcnt vmcnt(12)\n"
"s_barrier\n"
"v_mfma_f32_16x16x32_bf16 v[0:3], v[96:99], a[24:27], v[0:3]\n"
"ds_read_b64_tr_b16 v[64:65], v128 offset:32768\n"
"s_add_u32 m0, s62, 65536\n"
"s_mov_b32 s61, 917504\n"
"v_mfma_f32_16x16x32_bf16 v[4:7], v[96:99], a[28:31], v[4:7]\n"
"ds_read_b64_tr_b16 v[66:67], v128 offset:36864\n"
"buffer_load_dwordx4 v144, s[24:27], s61 offen offset:0 lds\n"
"v_mfma_f32_16x16x32_bf16 v[8:11], v[100:103], a[24:27], v[8:11]\n"
"ds_read_b64_tr_b16 v[68:69], v129 offset:32768\n"
"v_mfma_f32_16x16x32_bf16 v[12:15], v[100:103], a[28:31], v[12:15]\n"
"ds_read_b64_tr_b16 v[70:71], v129 offset:36864\n"
"v_mfma_f32_16x16x32_bf16 v[16:19], v[104:107], a[24:27], v[16:19]\n"
"ds_read_b64_tr_b16 v[72:73], v130 offset:32768\n"
"v_mfma_f32_16x16x32_bf16 v[20:23], v[104:107], a[28:31], v[20:23]\n"
"ds_read_b64_tr_b16 v[74:75], v130 offset:36864\n"
"buffer_load_dwordx4 v145, s[24:27], s61 offen offset:1024 lds\n"
"v_mfma_f32_16x16x32_bf16 v[24:27], v[108:111], a[24:27], v[24:27]\n"
"ds_read_b64_tr_b16 v[76:77], v131 offset:32768\n"
"v_mfma_f32_16x16x32_bf16 v[28:31], v[108:111], a[28:31], v[28:31]\n"
"s_waitcnt lgkmcnt(7)\n"
"ds_read_b64_tr_b16 v[78:79], v131 offset:36864\n"
"v_mfma_f32_16x16x32_bf16 v[32:35], v[112:115], a[24:27], v[32:35]\n"
"ds_read_b64_tr_b16 v[80:81], v132 offset:32768\n"
"v_mfma_f32_16x16x32_bf16 v[36:39], v[112:115], a[28:31], v[36:39]\n"
"ds_read_b64_tr_b16 v[82:83], v132 offset:36864\n"
"buffer_load_dwordx4 a[56:59], v146, s[28:31], s60 offen offset:3072\n"
"v_mfma_f32_16x16x32_bf16 v[40:43], v[116:119], a[24:27], v[40:43]\n"
"ds_read_b64_tr_b16 v[84:85], v133 offset:32768\n"
"v_mfma_f32_16x16x32_bf16 v[44:47], v[116:119], a[28:31], v[44:47]\n"
"ds_read_b64_tr_b16 v[86:87], v133 offset:36864\n"
"v_mfma_f32_16x16x32_bf16 v[48:51], v[120:123], a[24:27], v[48:51]\n"
"ds_read_b64_tr_b16 v[88:89], v134 offset:32768\n"
"v_mfma_f32_16x16x32_bf16 v[52:55], v[120:123], a[28:31], v[52:55]\n"
"ds_read_b64_tr_b16 v[90:91], v134 offset:36864\n"
"buffer_load_dwordx4 a[60:63], v147, s[28:31], s60 offen offset:3072\n"
"v_mfma_f32_16x16x32_bf16 v[56:59], v[124:127], a[24:27], v[56:59]\n"
"ds_read_b64_tr_b16 v[92:93], v135 offset:32768\n"
"v_mfma_f32_16x16x32_bf16 v[60:63], v[124:127], a[28:31], v[60:63]\n"
"s_waitcnt lgkmcnt(7)\n"
"ds_read_b64_tr_b16 v[94:95], v135 offset:36864\n"
"s_waitcnt vmcnt(12)\n"
"s_barrier\n"
"v_mfma_f32_16x16x32_bf16 v[0:3], v[64:67], a[32:35], v[0:3]\n"
"ds_read_b64_tr_b16 v[96:97], v128 offset:40960\n"
"s_add_u32 m0, s62, 73728\n"
"s_mov_b32 s61, 1032192\n"
"v_mfma_f32_16x16x32_bf16 v[4:7], v[64:67], a[36:39], v[4:7]\n"
"ds_read_b64_tr_b16 v[98:99], v128 offset:45056\n"
"buffer_load_dwordx4 v144, s[24:27], s61 offen offset:0 lds\n"
"v_mfma_f32_16x16x32_bf16 v[8:11], v[68:71], a[32:35], v[8:11]\n"
"ds_read_b64_tr_b16 v[100:101], v129 offset:40960\n"
"v_mfma_f32_16x16x32_bf16 v[12:15], v[68:71], a[36:39], v[12:15]\n"
"ds_read_b64_tr_b16 v[102:103], v129 offset:45056\n"
"v_mfma_f32_16x16x32_bf16 v[16:19], v[72:75], a[32:35], v[16:19]\n"
"ds_read_b64_tr_b16 v[104:105], v130 offset:40960\n"
"v_mfma_f32_16x16x32_bf16 v[20:23], v[72:75], a[36:39], v[20:23]\n"
"ds_read_b64_tr_b16 v[106:107], v130 offset:45056\n"
"buffer_load_dwordx4 v145, s[24:27], s61 offen offset:1024 lds\n"
"v_mfma_f32_16x16x32_bf16 v[24:27], v[76:79], a[32:35], v[24:27]\n"
"ds_read_b64_tr_b16 v[108:109], v131 offset:40960\n"
"v_mfma_f32_16x16x32_bf16 v[28:31], v[76:79], a[36:39], v[28:31]\n"
"s_waitcnt lgkmcnt(7)\n"
"ds_read_b64_tr_b16 v[110:111], v131 offset:45056\n"
"v_mfma_f32_16x16x32_bf16 v[32:35], v[80:83], a[32:35], v[32:35]\n"
"ds_read_b64_tr_b16 v[112:113], v132 offset:40960\n"
"s_movk_i32 s60, 8192\n"
"v_mfma_f32_16x16x32_bf16 v[36:39], v[80:83], a[36:39], v[36:39]\n"
"ds_read_b64_tr_b16 v[114:115], v132 offset:45056\n"
"buffer_load_dwordx4 a[64:67], v146, s[28:31], s60 offen offset:0\n"
"v_mfma_f32_16x16x32_bf16 v[40:43], v[84:87], a[32:35], v[40:43]\n"
"ds_read_b64_tr_b16 v[116:117], v133 offset:40960\n"
"v_mfma_f32_16x16x32_bf16 v[44:47], v[84:87], a[36:39], v[44:47]\n"
"ds_read_b64_tr_b16 v[118:119], v133 offset:45056\n"
"v_mfma_f32_16x16x32_bf16 v[48:51], v[88:91], a[32:35], v[48:51]\n"
"ds_read_b64_tr_b16 v[120:121], v134 offset:40960\n"
"v_mfma_f32_16x16x32_bf16 v[52:55], v[88:91], a[36:39], v[52:55]\n"
"ds_read_b64_tr_b16 v[122:123], v134 offset:45056\n"
"buffer_load_dwordx4 a[68:71], v147, s[28:31], s60 offen offset:0\n"
"v_mfma_f32_16x16x32_bf16 v[56:59], v[92:95], a[32:35], v[56:59]\n"
"ds_read_b64_tr_b16 v[124:125], v135 offset:40960\n"
"v_mfma_f32_16x16x32_bf16 v[60:63], v[92:95], a[36:39], v[60:63]\n"
"s_waitcnt lgkmcnt(7)\n"
"ds_read_b64_tr_b16 v[126:127], v135 offset:45056\n"
"s_waitcnt vmcnt(12)\n"
"s_barrier\n"
"v_mfma_f32_16x16x32_bf16 v[0:3], v[96:99], a[40:43], v[0:3]\n"
"ds_read_b64_tr_b16 v[64:65], v128 offset:49152\n"
"s_add_u32 m0, s62, 81920\n"
"s_mov_b32 s61, 1146880\n"
"v_mfma_f32_16x16x32_bf16 v[4:7], v[96:99], a[44:47], v[4:7]\n"
"ds_read_b64_tr_b16 v[66:67], v128 offset:53248\n"
"buffer_load_dwordx4 v144, s[24:27], s61 offen offset:0 lds\n"
"v_mfma_f32_16x16x32_bf16 v[8:11], v[100:103], a[40:43], v[8:11]\n"
"ds_read_b64_tr_b16 v[68:69], v129 offset:49152\n"
"v_mfma_f32_16x16x32_bf16 v[12:15], v[100:103], a[44:47], v[12:15]\n"
"ds_read_b64_tr_b16 v[70:71], v129 offset:53248\n"
"v_mfma_f32_16x16x32_bf16 v[16:19], v[104:107], a[40:43], v[16:19]\n"
"ds_read_b64_tr_b16 v[72:73], v130 offset:49152\n"
"v_mfma_f32_16x16x32_bf16 v[20:23], v[104:107], a[44:47], v[20:23]\n"
"ds_read_b64_tr_b16 v[74:75], v130 offset:53248\n"
"buffer_load_dwordx4 v145, s[24:27], s61 offen offset:1024 lds\n"
"v_mfma_f32_16x16x32_bf16 v[24:27], v[108:111], a[40:43], v[24:27]\n"
"ds_read_b64_tr_b16 v[76:77], v131 offset:49152\n"
"v_mfma_f32_16x16x32_bf16 v[28:31], v[108:111], a[44:47], v[28:31]\n"
"s_waitcnt lgkmcnt(7)\n"
"ds_read_b64_tr_b16 v[78:79], v131 offset:53248\n"
"v_mfma_f32_16x16x32_bf16 v[32:35], v[112:115], a[40:43], v[32:35]\n"
"ds_read_b64_tr_b16 v[80:81], v132 offset:49152\n"
"v_mfma_f32_16x16x32_bf16 v[36:39], v[112:115], a[44:47], v[36:39]\n"
"ds_read_b64_tr_b16 v[82:83], v132 offset:53248\n"
"buffer_load_dwordx4 a[72:75], v146, s[28:31], s60 offen offset:1024\n"
"v_mfma_f32_16x16x32_bf16 v[40:43], v[116:119], a[40:43], v[40:43]\n"
"ds_read_b64_tr_b16 v[84:85], v133 offset:49152\n"
"v_mfma_f32_16x16x32_bf16 v[44:47], v[116:119], a[44:47], v[44:47]\n"
"ds_read_b64_tr_b16 v[86:87], v133 offset:53248\n"
"v_mfma_f32_16x16x32_bf16 v[48:51], v[120:123], a[40:43], v[48:51]\n"
"ds_read_b64_tr_b16 v[88:89], v134 offset:49152\n"
"v_mfma_f32_16x16x32_bf16 v[52:55], v[120:123], a[44:47], v[52:55]\n"
"ds_read_b64_tr_b16 v[90:91], v134 offset:53248\n"
"buffer_load_dwordx4 a[76:79], v147, s[28:31], s60 offen offset:1024\n"
"v_mfma_f32_16x16x32_bf16 v[56:59], v[124:127], a[40:43], v[56:59]\n"
"ds_read_b64_tr_b16 v[92:93], v135 offset:49152\n"
"v_mfma_f32_16x16x32_bf16 v[60:63], v[124:127], a[44:47], v[60:63]\n"
"s_waitcnt lgkmcnt(7)\n"
"ds_read_b64_tr_b16 v[94:95], v135 offset:53248\n"
"s_waitcnt vmcnt(12)\n"
"s_barrier\n"
"v_mfma_f32_16x16x32_bf16 v[0:3], v[64:67], a[48:51], v[0:3]\n"
"ds_read_b64_tr_b16 v[96:97], v128 offset:57344\n"
"s_add_u32 m0, s62, 90112\n"
"s_mov_b32 s61, 1261568\n"
"v_mfma_f32_16x16x32_bf16 v[4:7], v[64:67], a[52:55], v[4:7]\n"
"ds_read_b64_tr_b16 v[98:99], v128 offset:61440\n"
"buffer_load_dwordx4 v144, s[24:27], s61 offen offset:0 lds\n"
"v_mfma_f32_16x16x32_bf16 v[8:11], v[68:71], a[48:51], v[8:11]\n"
"ds_read_b64_tr_b16 v[100:101], v129 offset:57344\n"
"v_mfma_f32_16x16x32_bf16 v[12:15], v[68:71], a[52:55], v[12:15]\n"
"ds_read_b64_tr_b16 v[102:103], v129 offset:61440\n"
"v_mfma_f32_16x16x32_bf16 v[16:19], v[72:75], a[48:51], v[16:19]\n"
"ds_read_b64_tr_b16 v[104:105], v130 offset:57344\n"
"v_mfma_f32_16x16x32_bf16 v[20:23], v[72:75], a[52:55], v[20:23]\n"
"ds_read_b64_tr_b16 v[106:107], v130 offset:61440\n"
"buffer_load_dwordx4 v145, s[24:27], s61 offen offset:1024 lds\n"
"v_mfma_f32_16x16x32_bf16 v[24:27], v[76:79], a[48:51], v[24:27]\n"
"ds_read_b64_tr_b16 v[108:109], v131 offset:57344\n"
"v_mfma_f32_16x16x32_bf16 v[28:31], v[76:79], a[52:55], v[28:31]\n"
"s_waitcnt lgkmcnt(7)\n"
"ds_read_b64_tr_b16 v[110:111], v131 offset:61440\n"
"v_mfma_f32_16x16x32_bf16 v[32:35], v[80:83], a[48:51], v[32:35]\n"
"ds_read_b64_tr_b16 v[112:113], v132 offset:57344\n"
"v_mfma_f32_16x16x32_bf16 v[36:39], v[80:83], a[52:55], v[36:39]\n"
"ds_read_b64_tr_b16 v[114:115], v132 offset:61440\n"
"buffer_load_dwordx4 a[80:83], v146, s[28:31], s60 offen offset:2048\n"
"v_mfma_f32_16x16x32_bf16 v[40:43], v[84:87], a[48:51], v[40:43]\n"
"ds_read_b64_tr_b16 v[116:117], v133 offset:57344\n"
"v_mfma_f32_16x16x32_bf16 v[44:47], v[84:87], a[52:55], v[44:47]\n"
"ds_read_b64_tr_b16 v[118:119], v133 offset:61440\n"
"v_mfma_f32_16x16x32_bf16 v[48:51], v[88:91], a[48:51], v[48:51]\n"
"ds_read_b64_tr_b16 v[120:121], v134 offset:57344\n"
"v_mfma_f32_16x16x32_bf16 v[52:55], v[88:91], a[52:55], v[52:55]\n"
"ds_read_b64_tr_b16 v[122:123], v134 offset:61440\n"
"buffer_load_dwordx4 a[84:87], v147, s[28:31], s60 offen offset:2048\n"
"v_mfma_f32_16x16x32_bf16 v[56:59], v[92:95], a[48:51], v[56:59]\n"
"ds_read_b64_tr_b16 v[124:125], v135 offset:57344\n"
"v_mfma_f32_16x16x32_bf16 v[60:63], v[92:95], a[52:55], v[60:63]\n"
"s_waitcnt lgkmcnt(7)\n"
"ds_read_b64_tr_b16 v[126:127], v135 offset:61440\n"
"s_waitcnt vmcnt(12)\n"
"s_barrier\n"
"v_mfma_f32_16x16x32_bf16 v[0:3], v[96:99], a[56:59], v[0:3]\n"
"ds_read_b64_tr_b16 v[64:65], v136 offset:0\n"
"s_add_u32 m0, s62, 98304\n"
"s_mov_b32 s61, 1376256\n"
"v_mfma_f32_16x16x32_bf16 v[4:7], v[96:99], a[60:63], v[4:7]\n"
"ds_read_b64_tr_b16 v[66:67], v136 offset:4096\n"
"buffer_load_dwordx4 v144, s[24:27], s61 offen offset:0 lds\n"
"v_mfma_f32_16x16x32_bf16 v[8:11], v[100:103], a[56:59], v[8:11]\n"
"ds_read_b64_tr_b16 v[68:69], v137 offset:0\n"
"v_mfma_f32_16x16x32_bf16 v[12:15], v[100:103], a[60:63], v[12:15]\n"
"ds_read_b64_tr_b16 v[70:71], v137 offset:4096\n"
"v_mfma_f32_16x16x32_bf16 v[16:19], v[104:107], a[56:59], v[16:19]\n"
"ds_read_b64_tr_b16 v[72:73], v138 offset:0\n"
"v_mfma_f32_16x16x32_bf16 v[20:23], v[104:107], a[60:63], v[20:23]\n"
"ds_read_b64_tr_b16 v[74:75], v138 offset:4096\n"
"buffer_load_dwordx4 v145, s[24:27], s61 offen offset:1024 lds\n"
"v_mfma_f32_16x16x32_bf16 v[24:27], v[108:111], a[56:59], v[24:27]\n"
"ds_read_b64_tr_b16 v[76:77], v139 offset:0\n"
"v_mfma_f32_16x16x32_bf16 v[28:31], v[108:111], a[60:63], v[28:31]\n"
"s_waitcnt lgkmcnt(7)\n"
"ds_read_b64_tr_b16 v[78:79], v139 offset:4096\n"
"v_mfma_f32_16x16x32_bf16 v[32:35], v[112:115], a[56:59], v[32:35]\n"
"ds_read_b64_tr_b16 v[80:81], v140 offset:0\n"
"v_mfma_f32_16x16x32_bf16 v[36:39], v[112:115], a[60:63], v[36:39]\n"
"ds_read_b64_tr_b16 v[82:83], v140 offset:4096\n"
"buffer_load_dwordx4 a[88:91], v146, s[28:31], s60 offen offset:3072\n"
"v_mfma_f32_16x16x32_bf16 v[40:43], v[116:119], a[56:59], v[40:43]\n"
"ds_read_b64_tr_b16 v[84:85], v141 offset:0\n"
"v_mfma_f32_16x16x32_bf16 v[44:47], v[116:119], a[60:63], v[44:47]\n"
"ds_read_b64_tr_b16 v[86:87], v141 offset:4096\n"
"v_mfma_f32_16x16x32_bf16 v[48:51], v[120:123], a[56:59], v[48:51]\n"
"ds_read_b64_tr_b16 v[88:89], v142 offset:0\n"
"v_mfma_f32_16x16x32_bf16 v[52:55], v[120:123], a[60:63], v[52:55]\n"
"ds_read_b64_tr_b16 v[90:91], v142 offset:4096\n"
"buffer_load_dwordx4 a[92:95], v147, s[28:31], s60 offen offset:3072\n"
"v_mfma_f32_16x16x32_bf16 v[56:59], v[124:127], a[56:59], v[56:59]\n"
"ds_read_b64_tr_b16 v[92:93], v143 offset:0\n"
"v_mfma_f32_16x16x32_bf16 v[60:63], v[124:127], a[60:63], v[60:63]\n"
"s_waitcnt lgkmcnt(7)\n"
"ds_read_b64_tr_b16 v[94:95], v143 offset:4096\n"
"s_waitcnt vmcnt(12)\n"
"s_barrier\n"
"v_mfma_f32_16x16x32_bf16 v[0:3], v[64:67], a[64:67], v[0:3]\n"
"ds_read_b64_tr_b16 v[96:97], v136 offset:8192\n"
"s_add_u32 m0, s62, 106496\n"
"s_mov_b32 s61, 1490944\n"
"v_mfma_f32_16x16x32_bf16 v[4:7], v[64:67], a[68:71], v[4:7]\n"
"ds_read_b64_tr_b16 v[98:99], v136 offset:12288\n"
"buffer_load_dwordx4 v144, s[24:27], s61 offen offset:0 lds\n"
"v_mfma_f32_16x16x32_bf16 v[8:11], v[68:71], a[64:67], v[8:11]\n"
"ds_read_b64_tr_b16 v[100:101], v137 offset:8192\n"
"v_mfma_f32_16x16x32_bf16 v[12:15], v[68:71], a[68:71], v[12:15]\n"
"ds_read_b64_tr_b16 v[102:103], v137 offset:12288\n"
"v_mfma_f32_16x16x32_bf16 v[16:19], v[72:75], a[64:67], v[16:19]\n"
"ds_read_b64_tr_b16 v[104:105], v138 offset:8192\n"
"v_mfma_f32_16x16x32_bf16 v[20:23], v[72:75], a[68:71], v[20:23]\n"
"ds_read_b64_tr_b16 v[106:107], v138 offset:12288\n"
"buffer_load_dwordx4 v145, s[24:27], s61 offen offset:1024 lds\n"
"v_mfma_f32_16x16x32_bf16 v[24:27], v[76:79], a[64:67], v[24:27]\n"
"ds_read_b64_tr_b16 v[108:109], v139 offset:8192\n"
"v_mfma_f32_16x16x32_bf16 v[28:31], v[76:79], a[68:71], v[28:31]\n"
"s_waitcnt lgkmcnt(7)\n"
"ds_read_b64_tr_b16 v[110:111], v139 offset:12288\n"
"v_mfma_f32_16x16x32_bf16 v[32:35], v[80:83], a[64:67], v[32:35]\n"
"ds_read_b64_tr_b16 v[112:113], v140 offset:8192\n"
"s_movk_i32 s60, 12288\n"
"v_mfma_f32_16x16x32_bf16 v[36:39], v[80:83], a[68:71], v[36:39]\n"
"ds_read_b64_tr_b16 v[114:115], v140 offset:12288\n"
"buffer_load_dwordx4 a[96:99], v146, s[28:31], s60 offen offset:0\n"
"v_mfma_f32_16x16x32_bf16 v[40:43], v[84:87], a[64:67], v[40:43]\n"
"ds_read_b64_tr_b16 v[116:117], v141 offset:8192\n"
"v_mfma_f32_16x16x32_bf16 v[44:47], v[84:87], a[68:71], v[44:47]\n"
"ds_read_b64_tr_b16 v[118:119], v141 offset:12288\n"
"v_mfma_f32_16x16x32_bf16 v[48:51], v[88:91], a[64:67], v[48:51]\n"
"ds_read_b64_tr_b16 v[120:121], v142 offset:8192\n"
"v_mfma_f32_16x16x32_bf16 v[52:55], v[88:91], a[68:71], v[52:55]\n"
"ds_read_b64_tr_b16 v[122:123], v142 offset:12288\n"
"buffer_load_dwordx4 a[100:103], v147, s[28:31], s60 offen offset:0\n"
"v_mfma_f32_16x16x32_bf16 v[56:59], v[92:95], a[64:67], v[56:59]\n"
"ds_read_b64_tr_b16 v[124:125], v143 offset:8192\n"
"v_mfma_f32_16x16x32_bf16 v[60:63], v[92:95], a[68:71], v[60:63]\n"
"s_waitcnt lgkmcnt(7)\n"
"ds_read_b64_tr_b16 v[126:127], v143 offset:12288\n"
"s_waitcnt vmcnt(12)\n"
"s_barrier\n"
"v_mfma_f32_16x16x32_bf16 v[0:3], v[96:99], a[72:75], v[0:3]\n"
"ds_read_b64_tr_b16 v[64:65], v136 offset:16384\n"
"s_add_u32 m0, s62, 114688\n"
"s_mov_b32 s61, 1605632\n"
"v_mfma_f32_16x16x32_bf16 v[4:7], v[96:99], a[76:79], v[4:7]\n"
"ds_read_b64_tr_b16 v[66:67], v136 offset:20480\n"
"buffer_load_dwordx4 v144, s[24:27], s61 offen offset:0 lds\n"
"v_mfma_f32_16x16x32_bf16 v[8:11], v[100:103], a[72:75], v[8:11]\n"
"ds_read_b64_tr_b16 v[68:69], v137 offset:16384\n"
"v_mfma_f32_16x16x32_bf16 v[12:15], v[100:103], a[76:79], v[12:15]\n"
"ds_read_b64_tr_b16 v[70:71], v137 offset:20480\n"
"v_mfma_f32_16x16x32_bf16 v[16:19], v[104:107], a[72:75], v[16:19]\n"
"ds_read_b64_tr_b16 v[72:73], v138 offset:16384\n"
"v_mfma_f32_16x16x32_bf16 v[20:23], v[104:107], a[76:79], v[20:23]\n"
"ds_read_b64_tr_b16 v[74:75], v138 offset:20480\n"
"buffer_load_dwordx4 v145, s[24:27], s61 offen offset:1024 lds\n"
"v_mfma_f32_16x16x32_bf16 v[24:27], v[108:111], a[72:75], v[24:27]\n"
"ds_read_b64_tr_b16 v[76:77], v139 offset:16384\n"
"v_mfma_f32_16x16x32_bf16 v[28:31], v[108:111], a[76:79], v[28:31]\n"
"s_waitcnt lgkmcnt(7)\n"
"ds_read_b64_tr_b16 v[78:79], v139 offset:20480\n"
"v_mfma_f32_16x16x32_bf16 v[32:35], v[112:115], a[72:75], v[32:35]\n"
"ds_read_b64_tr_b16 v[80:81], v140 offset:16384\n"
"v_mfma_f32_16x16x32_bf16 v[36:39], v[112:115], a[76:79], v[36:39]\n"
"ds_read_b64_tr_b16 v[82:83], v140 offset:20480\n"
"buffer_load_dwordx4 a[104:107], v146, s[28:31], s60 offen offset:1024\n"
"v_mfma_f32_16x16x32_bf16 v[40:43], v[116:119], a[72:75], v[40:43]\n"
"ds_read_b64_tr_b16 v[84:85], v141 offset:16384\n"
"v_mfma_f32_16x16x32_bf16 v[44:47], v[116:119], a[76:79], v[44:47]\n"
"ds_read_b64_tr_b16 v[86:87], v141 offset:20480\n"
"v_mfma_f32_16x16x32_bf16 v[48:51], v[120:123], a[72:75], v[48:51]\n"
"ds_read_b64_tr_b16 v[88:89], v142 offset:16384\n"
"v_mfma_f32_16x16x32_bf16 v[52:55], v[120:123], a[76:79], v[52:55]\n"
"ds_read_b64_tr_b16 v[90:91], v142 offset:20480\n"
"buffer_load_dwordx4 a[108:111], v147, s[28:31], s60 offen offset:1024\n"
"v_mfma_f32_16x16x32_bf16 v[56:59], v[124:127], a[72:75], v[56:59]\n"
"ds_read_b64_tr_b16 v[92:93], v143 offset:16384\n"
"v_mfma_f32_16x16x32_bf16 v[60:63], v[124:127], a[76:79], v[60:63]\n"
"s_waitcnt lgkmcnt(7)\n"
"ds_read_b64_tr_b16 v[94:95], v143 offset:20480\n"
"s_waitcnt vmcnt(12)\n"
"s_barrier\n"
"v_mfma_f32_16x16x32_bf16 v[0:3], v[64:67], a[80:83], v[0:3]\n"
"ds_read_b64_tr_b16 v[96:97], v136 offset:24576\n"
"s_add_u32 m0, s62, 122880\n"
"s_mov_b32 s61, 1720320\n"
"v_mfma_f32_16x16x32_bf16 v[4:7], v[64:67], a[84:87], v[4:7]\n"
"ds_read_b64_tr_b16 v[98:99], v136 offset:28672\n"
"buffer_load_dwordx4 v144, s[24:27], s61 offen offset:0 lds\n"
"v_mfma_f32_16x16x32_bf16 v[8:11], v[68:71], a[80:83], v[8:11]\n"
"ds_read_b64_tr_b16 v[100:101], v137 offset:24576\n"
"v_mfma_f32_16x16x32_bf16 v[12:15], v[68:71], a[84:87], v[12:15]\n"
"ds_read_b64_tr_b16 v[102:103], v137 offset:28672\n"
"v_mfma_f32_16x16x32_bf16 v[16:19], v[72:75], a[80:83], v[16:19]\n"
"ds_read_b64_tr_b16 v[104:105], v138 offset:24576\n"
"v_mfma_f32_16x16x32_bf16 v[20:23], v[72:75], a[84:87], v[20:23]\n"
"ds_read_b64_tr_b16 v[106:107], v138 offset:28672\n"
"buffer_load_dwordx4 v145, s[24:27], s61 offen offset:1024 lds\n"
"v_mfma_f32_16x16x32_bf16 v[24:27], v[76:79], a[80:83], v[24:27]\n"
"ds_read_b64_tr_b16 v[108:109], v139 offset:24576\n"
"v_mfma_f32_16x16x32_bf16 v[28:31], v[76:79], a[84:87], v[28:31]\n"
"s_waitcnt lgkmcnt(7)\n"
"ds_read_b64_tr_b16 v[110:111], v139 offset:28672\n"
"v_mfma_f32_16x16x32_bf16 v[32:35], v[80:83], a[80:83], v[32:35]\n"
"ds_read_b64_tr_b16 v[112:113], v140 offset:24576\n"
"v_mfma_f32_16x16x32_bf16 v[36:39], v[80:83], a[84:87], v[36:39]\n"
"ds_read_b64_tr_b16 v[114:115], v140 offset:28672\n"
"buffer_load_dwordx4 a[112:115], v146, s[28:31], s60 offen offset:2048\n"
"v_mfma_f32_16x16x32_bf16 v[40:43], v[84:87], a[80:83], v[40:43]\n"
"ds_read_b64_tr_b16 v[116:117], v141 offset:24576\n"
"v_mfma_f32_16x16x32_bf16 v[44:47], v[84:87], a[84:87], v[44:47]\n"
"ds_read_b64_tr_b16 v[118:119], v141 offset:28672\n"
"v_mfma_f32_16x16x32_bf16 v[48:51], v[88:91], a[80:83], v[48:51]\n"
"ds_read_b64_tr_b16 v[120:121], v142 offset:24576\n"
"v_mfma_f32_16x16x32_bf16 v[52:55], v[88:91], a[84:87], v[52:55]\n"
"ds_read_b64_tr_b16 v[122:123], v142 offset:28672\n"
"buffer_load_dwordx4 a[116:119], v147, s[28:31], s60 offen offset:2048\n"
"v_mfma_f32_16x16x32_bf16 v[56:59], v[92:95], a[80:83], v[56:59]\n"
"ds_read_b64_tr_b16 v[124:125], v143 offset:24576\n"
"v_mfma_f32_16x16x32_bf16 v[60:63], v[92:95], a[84:87], v[60:63]\n"
"s_waitcnt lgkmcnt(7)\n"
"ds_read_b64_tr_b16 v[126:127], v143 offset:28672\n"
"s_waitcnt vmcnt(12)\n"
"s_barrier\n"
"v_mfma_f32_16x16x32_bf16 v[0:3], v[96:99], a[88:91], v[0:3]\n"
"ds_read_b64_tr_b16 v[64:65], v136 offset:32768\n"
"v_mfma_f32_16x16x32_bf16 v[4:7], v[96:99], a[92:95], v[4:7]\n"
"ds_read_b64_tr_b16 v[66:67], v136 offset:36864\n"
"buffer_load_dwordx4 a[120:123], v146, s[28:31], s60 offen offset:3072\n"
"v_mfma_f32_16x16x32_bf16 v[8:11], v[100:103], a[88:91], v[8:11]\n"
"ds_read_b64_tr_b16 v[68:69], v137 offset:32768\n"
"v_mfma_f32_16x16x32_bf16 v[12:15], v[100:103], a[92:95], v[12:15]\n"
"ds_read_b64_tr_b16 v[70:71], v137 offset:36864\n"
"v_mfma_f32_16x16x32_bf16 v[16:19], v[104:107], a[88:91], v[16:19]\n"
"ds_read_b64_tr_b16 v[72:73], v138 offset:32768\n"
"v_mfma_f32_16x16x32_bf16 v[20:23], v[104:107], a[92:95], v[20:23]\n"
"ds_read_b64_tr_b16 v[74:75], v138 offset:36864\n"
"v_mfma_f32_16x16x32_bf16 v[24:27], v[108:111], a[88:91], v[24:27]\n"
"ds_read_b64_tr_b16 v[76:77], v139 offset:32768\n"
"v_mfma_f32_16x16x32_bf16 v[28:31], v[108:111], a[92:95], v[28:31]\n"
"s_waitcnt lgkmcnt(7)\n"
"ds_read_b64_tr_b16 v[78:79], v139 offset:36864\n"
"v_mfma_f32_16x16x32_bf16 v[32:35], v[112:115], a[88:91], v[32:35]\n"
"ds_read_b64_tr_b16 v[80:81], v140 offset:32768\n"
"v_mfma_f32_16x16x32_bf16 v[36:39], v[112:115], a[92:95], v[36:39]\n"
"ds_read_b64_tr_b16 v[82:83], v140 offset:36864\n"
"buffer_load_dwordx4 a[124:127], v147, s[28:31], s60 offen offset:3072\n"
"v_mfma_f32_16x16x32_bf16 v[40:43], v[116:119], a[88:91], v[40:43]\n"
"ds_read_b64_tr_b16 v[84:85], v141 offset:32768\n"
"v_mfma_f32_16x16x32_bf16 v[44:47], v[116:119], a[92:95], v[44:47]\n"
"ds_read_b64_tr_b16 v[86:87], v141 offset:36864\n"
"v_mfma_f32_16x16x32_bf16 v[48:51], v[120:123], a[88:91], v[48:51]\n"
"ds_read_b64_tr_b16 v[88:89], v142 offset:32768\n"
"v_mfma_f32_16x16x32_bf16 v[52:55], v[120:123], a[92:95], v[52:55]\n"
"ds_read_b64_tr_b16 v[90:91], v142 offset:36864\n"
"v_mfma_f32_16x16x32_bf16 v[56:59], v[124:127], a[88:91], v[56:59]\n"
"ds_read_b64_tr_b16 v[92:93], v143 offset:32768\n"
"v_mfma_f32_16x16x32_bf16 v[60:63], v[124:127], a[92:95], v[60:63]\n"
"s_waitcnt lgkmcnt(7)\n"
"ds_read_b64_tr_b16 v[94:95], v143 offset:36864\n"
"s_waitcnt vmcnt(10)\n"
"s_barrier\n"
"v_mfma_f32_16x16x32_bf16 v[0:3], v[64:67], a[96:99], v[0:3]\n"
"ds_read_b64_tr_b16 v[96:97], v136 offset:40960\n"
"v_mfma_f32_16x16x32_bf16 v[4:7], v[64:67], a[100:103], v[4:7]\n"
"ds_read_b64_tr_b16 v[98:99], v136 offset:45056\n"
"v_mfma_f32_16x16x32_bf16 v[8:11], v[68:71], a[96:99], v[8:11]\n"
"ds_read_b64_tr_b16 v[100:101], v137 offset:40960\n"
"v_mfma_f32_16x16x32_bf16 v[12:15], v[68:71], a[100:103], v[12:15]\n"
"ds_read_b64_tr_b16 v[102:103], v137 offset:45056\n"
"v_mfma_f32_16x16x32_bf16 v[16:19], v[72:75], a[96:99], v[16:19]\n"
"ds_read_b64_tr_b16 v[104:105], v138 offset:40960\n"
"v_mfma_f32_16x16x32_bf16 v[20:23], v[72:75], a[100:103], v[20:23]\n"
"ds_read_b64_tr_b16 v[106:107], v138 offset:45056\n"
"v_mfma_f32_16x16x32_bf16 v[24:27], v[76:79], a[96:99], v[24:27]\n"
"ds_read_b64_tr_b16 v[108:109], v139 offset:40960\n"
"v_mfma_f32_16x16x32_bf16 v[28:31], v[76:79], a[100:103], v[28:31]\n"
"s_waitcnt lgkmcnt(7)\n"
"ds_read_b64_tr_b16 v[110:111], v139 offset:45056\n"
"v_mfma_f32_16x16x32_bf16 v[32:35], v[80:83], a[96:99], v[32:35]\n"
"ds_read_b64_tr_b16 v[112:113], v140 offset:40960\n"
"v_mfma_f32_16x16x32_bf16 v[36:39], v[80:83], a[100:103], v[36:39]\n"
"ds_read_b64_tr_b16 v[114:115], v140 offset:45056\n"
"v_mfma_f32_16x16x32_bf16 v[40:43], v[84:87], a[96:99], v[40:43]\n"
"ds_read_b64_tr_b16 v[116:117], v141 offset:40960\n"
"v_mfma_f32_16x16x32_bf16 v[44:47], v[84:87], a[100:103], v[44:47]\n"
"ds_read_b64_tr_b16 v[118:119], v141 offset:45056\n"
"v_mfma_f32_16x16x32_bf16 v[48:51], v[88:91], a[96:99], v[48:51]\n"
"ds_read_b64_tr_b16 v[120:121], v142 offset:40960\n"
"v_mfma_f32_16x16x32_bf16 v[52:55], v[88:91], a[100:103], v[52:55]\n"
"ds_read_b64_tr_b16 v[122:123], v142 offset:45056\n"
"v_mfma_f32_16x16x32_bf16 v[56:59], v[92:95], a[96:99], v[56:59]\n"
"ds_read_b64_tr_b16 v[124:125], v143 offset:40960\n"
"v_mfma_f32_16x16x32_bf16 v[60:63], v[92:95], a[100:103], v[60:63]\n"
"s_waitcnt lgkmcnt(7)\n"
"ds_read_b64_tr_b16 v[126:127], v143 offset:45056\n"
"s_waitcnt vmcnt(6)\n"
"s_barrier\n"
"v_mfma_f32_16x16x32_bf16 v[0:3], v[96:99], a[104:107], v[0:3]\n"
"ds_read_b64_tr_b16 v[64:65], v136 offset:49152\n"
"v_mfma_f32_16x16x32_bf16 v[4:7], v[96:99], a[108:111], v[4:7]\n"
"ds_read_b64_tr_b16 v[66:67], v136 offset:53248\n"
"v_mfma_f32_16x16x32_bf16 v[8:11], v[100:103], a[104:107], v[8:11]\n"
"ds_read_b64_tr_b16 v[68:69], v137 offset:49152\n"
"v_mfma_f32_16x16x32_bf16 v[12:15], v[100:103], a[108:111], v[12:15]\n"
"ds_read_b64_tr_b16 v[70:71], v137 offset:53248\n"
"v_mfma_f32_16x16x32_bf16 v[16:19], v[104:107], a[104:107], v[16:19]\n"
"ds_read_b64_tr_b16 v[72:73], v138 offset:49152\n"
"v_mfma_f32_16x16x32_bf16 v[20:23], v[104:107], a[108:111], v[20:23]\n"
"ds_read_b64_tr_b16 v[74:75], v138 offset:53248\n"
"v_mfma_f32_16x16x32_bf16 v[24:27], v[108:111], a[104:107], v[24:27]\n"
"ds_read_b64_tr_b16 v[76:77], v139 offset:49152\n"
"v_mfma_f32_16x16x32_bf16 v[28:31], v[108:111], a[108:111], v[28:31]\n"
"s_waitcnt lgkmcnt(7)\n"
"ds_read_b64_tr_b16 v[78:79], v139 offset:53248\n"
"v_mfma_f32_16x16x32_bf16 v[32:35], v[112:115], a[104:107], v[32:35]\n"
"ds_read_b64_tr_b16 v[80:81], v140 offset:49152\n"
"v_mfma_f32_16x16x32_bf16 v[36:39], v[112:115], a[108:111], v[36:39]\n"
"ds_read_b64_tr_b16 v[82:83], v140 offset:53248\n"
"v_mfma_f32_16x16x32_bf16 v[40:43], v[116:119], a[104:107], v[40:43]\n"
"ds_read_b64_tr_b16 v[84:85], v141 offset:49152\n"
"v_mfma_f32_16x16x32_bf16 v[44:47], v[116:119], a[108:111], v[44:47]\n"
"ds_read_b64_tr_b16 v[86:87], v141 offset:53248\n"
"v_mfma_f32_16x16x32_bf16 v[48:51], v[120:123], a[104:107], v[48:51]\n"
"ds_read_b64_tr_b16 v[88:89], v142 offset:49152\n"
"v_mfma_f32_16x16x32_bf16 v[52:55], v[120:123], a[108:111], v[52:55]\n"
"ds_read_b64_tr_b16 v[90:91], v142 offset:53248\n"
"v_mfma_f32_16x16x32_bf16 v[56:59], v[124:127], a[104:107], v[56:59]\n"
"ds_read_b64_tr_b16 v[92:93], v143 offset:49152\n"
"v_mfma_f32_16x16x32_bf16 v[60:63], v[124:127], a[108:111], v[60:63]\n"
"s_waitcnt lgkmcnt(7)\n"
"ds_read_b64_tr_b16 v[94:95], v143 offset:53248\n"
"s_waitcnt vmcnt(2)\n"
"s_barrier\n"
"v_mfma_f32_16x16x32_bf16 v[0:3], v[64:67], a[112:115], v[0:3]\n"
"ds_read_b64_tr_b16 v[96:97], v136 offset:57344\n"
"v_mfma_f32_16x16x32_bf16 v[4:7], v[64:67], a[116:119], v[4:7]\n"
"ds_read_b64_tr_b16 v[98:99], v136 offset:61440\n"
"v_mfma_f32_16x16x32_bf16 v[8:11], v[68:71], a[112:115], v[8:11]\n"
"ds_read_b64_tr_b16 v[100:101], v137 offset:57344\n"
"v_mfma_f32_16x16x32_bf16 v[12:15], v[68:71], a[116:119], v[12:15]\n"
"ds_read_b64_tr_b16 v[102:103], v137 offset:61440\n"
"v_mfma_f32_16x16x32_bf16 v[16:19], v[72:75], a[112:115], v[16:19]\n"
"ds_read_b64_tr_b16 v[104:105], v138 offset:57344\n"
"v_mfma_f32_16x16x32_bf16 v[20:23], v[72:75], a[116:119], v[20:23]\n"
"ds_read_b64_tr_b16 v[106:107], v138 offset:61440\n"
"v_mfma_f32_16x16x32_bf16 v[24:27], v[76:79], a[112:115], v[24:27]\n"
"ds_read_b64_tr_b16 v[108:109], v139 offset:57344\n"
"v_mfma_f32_16x16x32_bf16 v[28:31], v[76:79], a[116:119], v[28:31]\n"
"s_waitcnt lgkmcnt(7)\n"
"ds_read_b64_tr_b16 v[110:111], v139 offset:61440\n"
"v_mfma_f32_16x16x32_bf16 v[32:35], v[80:83], a[112:115], v[32:35]\n"
"ds_read_b64_tr_b16 v[112:113], v140 offset:57344\n"
"v_mfma_f32_16x16x32_bf16 v[36:39], v[80:83], a[116:119], v[36:39]\n"
"ds_read_b64_tr_b16 v[114:115], v140 offset:61440\n"
"v_mfma_f32_16x16x32_bf16 v[40:43], v[84:87], a[112:115], v[40:43]\n"
"ds_read_b64_tr_b16 v[116:117], v141 offset:57344\n"
"v_mfma_f32_16x16x32_bf16 v[44:47], v[84:87], a[116:119], v[44:47]\n"
"ds_read_b64_tr_b16 v[118:119], v141 offset:61440\n"
"v_mfma_f32_16x16x32_bf16 v[48:51], v[88:91], a[112:115], v[48:51]\n"
"ds_read_b64_tr_b16 v[120:121], v142 offset:57344\n"
"v_mfma_f32_16x16x32_bf16 v[52:55], v[88:91], a[116:119], v[52:55]\n"
"ds_read_b64_tr_b16 v[122:123], v142 offset:61440\n"
"v_mfma_f32_16x16x32_bf16 v[56:59], v[92:95], a[112:115], v[56:59]\n"
"ds_read_b64_tr_b16 v[124:125], v143 offset:57344\n"
"v_mfma_f32_16x16x32_bf16 v[60:63], v[92:95], a[116:119], v[60:63]\n"
"s_waitcnt lgkmcnt(7)\n"
"ds_read_b64_tr_b16 v[126:127], v143 offset:61440\n"
"s_waitcnt vmcnt(0)\n"
"v_mfma_f32_16x16x32_bf16 v[0:3], v[96:99], a[120:123], v[0:3]\n"
"v_mfma_f32_16x16x32_bf16 v[4:7], v[96:99], a[124:127], v[4:7]\n"
"v_mfma_f32_16x16x32_bf16 v[8:11], v[100:103], a[120:123], v[8:11]\n"
"v_mfma_f32_16x16x32_bf16 v[12:15], v[100:103], a[124:127], v[12:15]\n"
"v_mfma_f32_16x16x32_bf16 v[16:19], v[104:107], a[120:123], v[16:19]\n"
"v_mfma_f32_16x16x32_bf16 v[20:23], v[104:107], a[124:127], v[20:23]\n"
"v_mfma_f32_16x16x32_bf16 v[24:27], v[108:111], a[120:123], v[24:27]\n"
"v_mfma_f32_16x16x32_bf16 v[28:31], v[108:111], a[124:127], v[28:31]\n"
"s_waitcnt lgkmcnt(0)\n"
"v_mfma_f32_16x16x32_bf16 v[32:35], v[112:115], a[120:123], v[32:35]\n"
"v_mfma_f32_16x16x32_bf16 v[36:39], v[112:115], a[124:127], v[36:39]\n"
"v_mfma_f32_16x16x32_bf16 v[40:43], v[116:119], a[120:123], v[40:43]\n"
"v_mfma_f32_16x16x32_bf16 v[44:47], v[116:119], a[124:127], v[44:47]\n"
"v_mfma_f32_16x16x32_bf16 v[48:51], v[120:123], a[120:123], v[48:51]\n"
"v_mfma_f32_16x16x32_bf16 v[52:55], v[120:123], a[124:127], v[52:55]\n"
"v_mfma_f32_16x16x32_bf16 v[56:59], v[124:127], a[120:123], v[56:59]\n"
"v_mfma_f32_16x16x32_bf16 v[60:63], v[124:127], a[124:127], v[60:63]\n"
"s_nop 7\n"
"v_pk_mul_f32 v[164:165], v[0:1], v[150:151]\n"
"v_pk_mul_f32 v[166:167], v[2:3], v[150:151]\n"
"v_pk_mul_f32 v[168:169], v[8:9], v[150:151]\n"
"v_pk_mul_f32 v[170:171], v[10:11], v[150:151]\n"
"v_exp_f32 v164, v164\n"
"v_exp_f32 v165, v165\n"
"v_exp_f32 v166, v166\n"
"v_exp_f32 v167, v167\n"
"v_exp_f32 v168, v168\n"
"v_exp_f32 v169, v169\n"
"v_exp_f32 v170, v170\n"
"v_exp_f32 v171, v171\n"
"v_pk_add_f32 v[164:165], v[164:165], v[152:153]\n"
"v_pk_add_f32 v[166:167], v[166:167], v[152:153]\n"
"v_pk_add_f32 v[168:169], v[168:169], v[152:153]\n"
"v_pk_add_f32 v[170:171], v[170:171], v[152:153]\n"
"v_rcp_f32 v164, v164\n"
"v_rcp_f32 v165, v165\n"
"v_rcp_f32 v166, v166\n"
"v_rcp_f32 v167, v167\n"
"v_rcp_f32 v168, v168\n"
"v_rcp_f32 v169, v169\n"
"v_rcp_f32 v170, v170\n"
"v_rcp_f32 v171, v171\n"
"v_pk_mul_f32 v[164:165], v[164:165], v[0:1]\n"
"v_pk_mul_f32 v[166:167], v[166:167], v[2:3]\n"
"v_pk_mul_f32 v[168:169], v[168:169], v[8:9]\n"
"v_pk_mul_f32 v[170:171], v[170:171], v[10:11]\n"
"v_cvt_pk_bf16_f32 v164, v164, v165\n"
"v_cvt_pk_bf16_f32 v165, v166, v167\n"
"v_cvt_pk_bf16_f32 v166, v168, v169\n"
"v_cvt_pk_bf16_f32 v167, v170, v171\n"
"buffer_store_dwordx4 v[164:167], v148, s[32:35], s64 offen offset:0 sc1\n"
"v_pk_mul_f32 v[172:173], v[4:5], v[150:151]\n"
"v_pk_mul_f32 v[174:175], v[6:7], v[150:151]\n"
"v_pk_mul_f32 v[176:177], v[12:13], v[150:151]\n"
"v_pk_mul_f32 v[178:179], v[14:15], v[150:151]\n"
"v_exp_f32 v172, v172\n"
"v_exp_f32 v173, v173\n"
"v_exp_f32 v174, v174\n"
"v_exp_f32 v175, v175\n"
"v_exp_f32 v176, v176\n"
"v_exp_f32 v177, v177\n"
"v_exp_f32 v178, v178\n"
"v_exp_f32 v179, v179\n"
"v_pk_add_f32 v[172:173], v[172:173], v[152:153]\n"
"v_pk_add_f32 v[174:175], v[174:175], v[152:153]\n"
"v_pk_add_f32 v[176:177], v[176:177], v[152:153]\n"
"v_pk_add_f32 v[178:179], v[178:179], v[152:153]\n"
"v_rcp_f32 v172, v172\n"
"v_rcp_f32 v173, v173\n"
"v_rcp_f32 v174, v174\n"
"v_rcp_f32 v175, v175\n"
"v_rcp_f32 v176, v176\n"
"v_rcp_f32 v177, v177\n"
"v_rcp_f32 v178, v178\n"
"v_rcp_f32 v179, v179\n"
"v_pk_mul_f32 v[172:173], v[172:173], v[4:5]\n"
"v_pk_mul_f32 v[174:175], v[174:175], v[6:7]\n"
"v_pk_mul_f32 v[176:177], v[176:177], v[12:13]\n"
"v_pk_mul_f32 v[178:179], v[178:179], v[14:15]\n"
"v_cvt_pk_bf16_f32 v172, v172, v173\n"
"v_cvt_pk_bf16_f32 v173, v174, v175\n"
"v_cvt_pk_bf16_f32 v174, v176, v177\n"
"v_cvt_pk_bf16_f32 v175, v178, v179\n"
"buffer_store_dwordx4 v[172:175], v148, s[32:35], s65 offen offset:0 sc1\n"
"v_pk_mul_f32 v[180:181], v[16:17], v[150:151]\n"
"v_pk_mul_f32 v[182:183], v[18:19], v[150:151]\n"
"v_pk_mul_f32 v[184:185], v[24:25], v[150:151]\n"
"v_pk_mul_f32 v[186:187], v[26:27], v[150:151]\n"
"v_exp_f32 v180, v180\n"
"v_exp_f32 v181, v181\n"
"v_exp_f32 v182, v182\n"
"v_exp_f32 v183, v183\n"
"v_exp_f32 v184, v184\n"
"v_exp_f32 v185, v185\n"
"v_exp_f32 v186, v186\n"
"v_exp_f32 v187, v187\n"
"v_pk_add_f32 v[180:181], v[180:181], v[152:153]\n"
"v_pk_add_f32 v[182:183], v[182:183], v[152:153]\n"
"v_pk_add_f32 v[184:185], v[184:185], v[152:153]\n"
"v_pk_add_f32 v[186:187], v[186:187], v[152:153]\n"
"v_rcp_f32 v180, v180\n"
"v_rcp_f32 v181, v181\n"
"v_rcp_f32 v182, v182\n"
"v_rcp_f32 v183, v183\n"
"v_rcp_f32 v184, v184\n"
"v_rcp_f32 v185, v185\n"
"v_rcp_f32 v186, v186\n"
"v_rcp_f32 v187, v187\n"
"v_pk_mul_f32 v[180:181], v[180:181], v[16:17]\n"
"v_pk_mul_f32 v[182:183], v[182:183], v[18:19]\n"
"v_pk_mul_f32 v[184:185], v[184:185], v[24:25]\n"
"v_pk_mul_f32 v[186:187], v[186:187], v[26:27]\n"
"v_cvt_pk_bf16_f32 v180, v180, v181\n"
"v_cvt_pk_bf16_f32 v181, v182, v183\n"
"v_cvt_pk_bf16_f32 v182, v184, v185\n"
"v_cvt_pk_bf16_f32 v183, v186, v187\n"
"buffer_store_dwordx4 v[180:183], v148, s[32:35], s64 offen offset:1024 sc1\n"
"v_pk_mul_f32 v[188:189], v[20:21], v[150:151]\n"
"v_pk_mul_f32 v[190:191], v[22:23], v[150:151]\n"
"v_pk_mul_f32 v[192:193], v[28:29], v[150:151]\n"
"v_pk_mul_f32 v[194:195], v[30:31], v[150:151]\n"
"v_exp_f32 v188, v188\n"
"v_exp_f32 v189, v189\n"
"v_exp_f32 v190, v190\n"
"v_exp_f32 v191, v191\n"
"v_exp_f32 v192, v192\n"
"v_exp_f32 v193, v193\n"
"v_exp_f32 v194, v194\n"
"v_exp_f32 v195, v195\n"
"v_pk_add_f32 v[188:189], v[188:189], v[152:153]\n"
"v_pk_add_f32 v[190:191], v[190:191], v[152:153]\n"
"v_pk_add_f32 v[192:193], v[192:193], v[152:153]\n"
"v_pk_add_f32 v[194:195], v[194:195], v[152:153]\n"
"v_rcp_f32 v188, v188\n"
"v_rcp_f32 v189, v189\n"
"v_rcp_f32 v190, v190\n"
"v_rcp_f32 v191, v191\n"
"v_rcp_f32 v192, v192\n"
"v_rcp_f32 v193, v193\n"
"v_rcp_f32 v194, v194\n"
"v_rcp_f32 v195, v195\n"
"v_pk_mul_f32 v[188:189], v[188:189], v[20:21]\n"
"v_pk_mul_f32 v[190:191], v[190:191], v[22:23]\n"
"v_pk_mul_f32 v[192:193], v[192:193], v[28:29]\n"
"v_pk_mul_f32 v[194:195], v[194:195], v[30:31]\n"
"v_cvt_pk_bf16_f32 v188, v188, v189\n"
"v_cvt_pk_bf16_f32 v189, v190, v191\n"
"v_cvt_pk_bf16_f32 v190, v192, v193\n"
"v_cvt_pk_bf16_f32 v191, v194, v195\n"
"buffer_store_dwordx4 v[188:191], v148, s[32:35], s65 offen offset:1024 sc1\n"
"v_pk_mul_f32 v[196:197], v[32:33], v[150:151]\n"
"v_pk_mul_f32 v[198:199], v[34:35], v[150:151]\n"
"v_pk_mul_f32 v[200:201], v[40:41], v[150:151]\n"
"v_pk_mul_f32 v[202:203], v[42:43], v[150:151]\n"
"v_exp_f32 v196, v196\n"
"v_exp_f32 v197, v197\n"
"v_exp_f32 v198, v198\n"
"v_exp_f32 v199, v199\n"
"v_exp_f32 v200, v200\n"
"v_exp_f32 v201, v201\n"
"v_exp_f32 v202, v202\n"
"v_exp_f32 v203, v203\n"
"v_pk_add_f32 v[196:197], v[196:197], v[152:153]\n"
"v_pk_add_f32 v[198:199], v[198:199], v[152:153]\n"
"v_pk_add_f32 v[200:201], v[200:201], v[152:153]\n"
"v_pk_add_f32 v[202:203], v[202:203], v[152:153]\n"
"v_rcp_f32 v196, v196\n"
"v_rcp_f32 v197, v197\n"
"v_rcp_f32 v198, v198\n"
"v_rcp_f32 v199, v199\n"
"v_rcp_f32 v200, v200\n"
"v_rcp_f32 v201, v201\n"
"v_rcp_f32 v202, v202\n"
"v_rcp_f32 v203, v203\n"
"v_pk_mul_f32 v[196:197], v[196:197], v[32:33]\n"
"v_pk_mul_f32 v[198:199], v[198:199], v[34:35]\n"
"v_pk_mul_f32 v[200:201], v[200:201], v[40:41]\n"
"v_pk_mul_f32 v[202:203], v[202:203], v[42:43]\n"
"v_cvt_pk_bf16_f32 v196, v196, v197\n"
"v_cvt_pk_bf16_f32 v197, v198, v199\n"
"v_cvt_pk_bf16_f32 v198, v200, v201\n"
"v_cvt_pk_bf16_f32 v199, v202, v203\n"
"buffer_store_dwordx4 v[196:199], v148, s[32:35], s64 offen offset:2048 sc1\n"
"v_pk_mul_f32 v[204:205], v[36:37], v[150:151]\n"
"v_pk_mul_f32 v[206:207], v[38:39], v[150:151]\n"
"v_pk_mul_f32 v[208:209], v[44:45], v[150:151]\n"
"v_pk_mul_f32 v[210:211], v[46:47], v[150:151]\n"
"v_exp_f32 v204, v204\n"
"v_exp_f32 v205, v205\n"
"v_exp_f32 v206, v206\n"
"v_exp_f32 v207, v207\n"
"v_exp_f32 v208, v208\n"
"v_exp_f32 v209, v209\n"
"v_exp_f32 v210, v210\n"
"v_exp_f32 v211, v211\n"
"v_pk_add_f32 v[204:205], v[204:205], v[152:153]\n"
"v_pk_add_f32 v[206:207], v[206:207], v[152:153]\n"
"v_pk_add_f32 v[208:209], v[208:209], v[152:153]\n"
"v_pk_add_f32 v[210:211], v[210:211], v[152:153]\n"
"v_rcp_f32 v204, v204\n"
"v_rcp_f32 v205, v205\n"
"v_rcp_f32 v206, v206\n"
"v_rcp_f32 v207, v207\n"
"v_rcp_f32 v208, v208\n"
"v_rcp_f32 v209, v209\n"
"v_rcp_f32 v210, v210\n"
"v_rcp_f32 v211, v211\n"
"v_pk_mul_f32 v[204:205], v[204:205], v[36:37]\n"
"v_pk_mul_f32 v[206:207], v[206:207], v[38:39]\n"
"v_pk_mul_f32 v[208:209], v[208:209], v[44:45]\n"
"v_pk_mul_f32 v[210:211], v[210:211], v[46:47]\n"
"v_cvt_pk_bf16_f32 v204, v204, v205\n"
"v_cvt_pk_bf16_f32 v205, v206, v207\n"
"v_cvt_pk_bf16_f32 v206, v208, v209\n"
"v_cvt_pk_bf16_f32 v207, v210, v211\n"
"buffer_store_dwordx4 v[204:207], v148, s[32:35], s65 offen offset:2048 sc1\n"
"v_pk_mul_f32 v[212:213], v[48:49], v[150:151]\n"
"v_pk_mul_f32 v[214:215], v[50:51], v[150:151]\n"
"v_pk_mul_f32 v[216:217], v[56:57], v[150:151]\n"
"v_pk_mul_f32 v[218:219], v[58:59], v[150:151]\n"
"v_exp_f32 v212, v212\n"
"v_exp_f32 v213, v213\n"
"v_exp_f32 v214, v214\n"
"v_exp_f32 v215, v215\n"
"v_exp_f32 v216, v216\n"
"v_exp_f32 v217, v217\n"
"v_exp_f32 v218, v218\n"
"v_exp_f32 v219, v219\n"
"v_pk_add_f32 v[212:213], v[212:213], v[152:153]\n"
"v_pk_add_f32 v[214:215], v[214:215], v[152:153]\n"
"v_pk_add_f32 v[216:217], v[216:217], v[152:153]\n"
"v_pk_add_f32 v[218:219], v[218:219], v[152:153]\n"
"v_rcp_f32 v212, v212\n"
"v_rcp_f32 v213, v213\n"
"v_rcp_f32 v214, v214\n"
"v_rcp_f32 v215, v215\n"
"v_rcp_f32 v216, v216\n"
"v_rcp_f32 v217, v217\n"
"v_rcp_f32 v218, v218\n"
"v_rcp_f32 v219, v219\n"
"v_pk_mul_f32 v[212:213], v[212:213], v[48:49]\n"
"v_pk_mul_f32 v[214:215], v[214:215], v[50:51]\n"
"v_pk_mul_f32 v[216:217], v[216:217], v[56:57]\n"
"v_pk_mul_f32 v[218:219], v[218:219], v[58:59]\n"
"v_cvt_pk_bf16_f32 v212, v212, v213\n"
"v_cvt_pk_bf16_f32 v213, v214, v215\n"
"v_cvt_pk_bf16_f32 v214, v216, v217\n"
"v_cvt_pk_bf16_f32 v215, v218, v219\n"
"buffer_store_dwordx4 v[212:215], v148, s[32:35], s64 offen offset:3072 sc1\n"
"v_pk_mul_f32 v[220:221], v[52:53], v[150:151]\n"
"v_pk_mul_f32 v[222:223], v[54:55], v[150:151]\n"
"v_pk_mul_f32 v[224:225], v[60:61], v[150:151]\n"
"v_pk_mul_f32 v[226:227], v[62:63], v[150:151]\n"
"v_exp_f32 v220, v220\n"
"v_exp_f32 v221, v221\n"
"v_exp_f32 v222, v222\n"
"v_exp_f32 v223, v223\n"
"v_exp_f32 v224, v224\n"
"v_exp_f32 v225, v225\n"
"v_exp_f32 v226, v226\n"
"v_exp_f32 v227, v227\n"
"v_pk_add_f32 v[220:221], v[220:221], v[152:153]\n"
"v_pk_add_f32 v[222:223], v[222:223], v[152:153]\n"
"v_pk_add_f32 v[224:225], v[224:225], v[152:153]\n"
"v_pk_add_f32 v[226:227], v[226:227], v[152:153]\n"
"v_rcp_f32 v220, v220\n"
"v_rcp_f32 v221, v221\n"
"v_rcp_f32 v222, v222\n"
"v_rcp_f32 v223, v223\n"
"v_rcp_f32 v224, v224\n"
"v_rcp_f32 v225, v225\n"
"v_rcp_f32 v226, v226\n"
"v_rcp_f32 v227, v227\n"
"v_pk_mul_f32 v[220:221], v[220:221], v[52:53]\n"
"v_pk_mul_f32 v[222:223], v[222:223], v[54:55]\n"
"v_pk_mul_f32 v[224:225], v[224:225], v[60:61]\n"
"v_pk_mul_f32 v[226:227], v[226:227], v[62:63]\n"
"v_cvt_pk_bf16_f32 v220, v220, v221\n"
"v_cvt_pk_bf16_f32 v221, v222, v223\n"
"v_cvt_pk_bf16_f32 v222, v224, v225\n"
"v_cvt_pk_bf16_f32 v223, v226, v227\n"
"buffer_store_dwordx4 v[220:223], v148, s[32:35], s65 offen offset:3072 sc1\n"
"L_kend_%=:\n"
  :: "s"(w1), "s"(ws), "s"(wave), "s"(blk) : "v0", "v1", "v2", "v3", "v4", "v5", "v6", "v7", "v8", "v9", "v10", "v11", "v12", "v13", "v14", "v15", "v16", "v17", "v18", "v19", "v20", "v21", "v22", "v23", "v24", "v25", "v26", "v27", "v28", "v29", "v30", "v31", "v32", "v33", "v34", "v35", "v36", "v37", "v38", "v39", "v40", "v41", "v42", "v43", "v44", "v45", "v46", "v47", "v48", "v49", "v50", "v51", "v52", "v53", "v54", "v55", "v56", "v57", "v58", "v59", "v60", "v61", "v62", "v63", "v64", "v65", "v66", "v67", "v68", "v69", "v70", "v71", "v72", "v73", "v74", "v75", "v76", "v77", "v78", "v79", "v80", "v81", "v82", "v83", "v84", "v85", "v86", "v87", "v88", "v89", "v90", "v91", "v92", "v93", "v94", "v95", "v96", "v97", "v98", "v99", "v100", "v101", "v102", "v103", "v104", "v105", "v106", "v107", "v108", "v109", "v110", "v111", "v112", "v113", "v114", "v115", "v116", "v117", "v118", "v119", "v120", "v121", "v122", "v123", "v124", "v125", "v126", "v127", "v128", "v129", "v130", "v131", "v132", "v133", "v134", "v135", "v136", "v137", "v138", "v139", "v140", "v141", "v142", "v143", "v144", "v145", "v146", "v147", "v148", "v149", "v150", "v151", "v152", "v153", "v154", "v155", "v156", "v157", "v158", "v159", "v160", "v161", "v162", "v163", "v164", "v165", "v166", "v167", "v168", "v169", "v170", "v171", "v172", "v173", "v174", "v175", "v176", "v177", "v178", "v179", "v180", "v181", "v182", "v183", "v184", "v185", "v186", "v187", "v188", "v189", "v190", "v191", "v192", "v193", "v194", "v195", "v196", "v197", "v198", "v199", "v200", "v201", "v202", "v203", "v204", "v205", "v206", "v207", "v208", "v209", "v210", "v211", "v212", "v213", "v214", "v215", "v216", "v217", "v218", "v219", "v220", "v221", "v222", "v223", "v224", "v225", "v226", "v227", "v228", "v229", "v230", "v231", "v232", "v233", "v234", "v235", "v236", "v237", "v238", "v239", "v240", "v241", "v242", "v243", "v244", "v245", "v246", "v247", "v248", "v249", "v250", "v251", "v252", "v253", "v254", "v255", "a0", "a1", "a2", "a3", "a4", "a5", "a6", "a7", "a8", "a9", "a10", "a11", "a12", "a13", "a14", "a15", "a16", "a17", "a18", "a19", "a20", "a21", "a22", "a23", "a24", "a25", "a26", "a27", "a28", "a29", "a30", "a31", "a32", "a33", "a34", "a35", "a36", "a37", "a38", "a39", "a40", "a41", "a42", "a43", "a44", "a45", "a46", "a47", "a48", "a49", "a50", "a51", "a52", "a53", "a54", "a55", "a56", "a57", "a58", "a59", "a60", "a61", "a62", "a63", "a64", "a65", "a66", "a67", "a68", "a69", "a70", "a71", "a72", "a73", "a74", "a75", "a76", "a77", "a78", "a79", "a80", "a81", "a82", "a83", "a84", "a85", "a86", "a87", "a88", "a89", "a90", "a91", "a92", "a93", "a94", "a95", "a96", "a97", "a98", "a99", "a100", "a101", "a102", "a103", "a104", "a105", "a106", "a107", "a108", "a109", "a110", "a111", "a112", "a113", "a114", "a115", "a116", "a117", "a118", "a119", "a120", "a121", "a122", "a123", "a124", "a125", "a126", "a127", "a128", "a129", "a130", "a131", "a132", "a133", "a134", "a135", "a136", "a137", "a138", "a139", "a140", "a141", "a142", "a143", "a144", "a145", "a146", "a147", "a148", "a149", "a150", "a151", "a152", "a153", "a154", "a155", "a156", "a157", "a158", "a159", "a160", "a161", "a162", "a163", "a164", "a165", "a166", "a167", "a168", "a169", "a170", "a171", "a172", "a173", "a174", "a175", "a176", "a177", "a178", "a179", "a180", "a181", "a182", "a183", "a184", "a185", "a186", "a187", "a188", "a189", "a190", "a191", "a192", "a193", "a194", "a195", "a196", "a197", "a198", "a199", "a200", "a201", "a202", "a203", "a204", "a205", "a206", "a207", "a208", "a209", "a210", "a211", "a212", "a213", "a214", "a215", "a216", "a217", "a218", "a219", "a220", "a221", "a222", "a223", "a224", "a225", "a226", "a227", "a228", "a229", "a230", "a231", "a232", "a233", "a234", "a235", "a236", "a237", "a238", "a239", "a240", "a241", "a242", "a243", "a244", "a245", "a246", "a247", "a248", "a249", "a250", "a251", "a252", "a253", "a254", "a255", "s24", "s25", "s26", "s27", "s28", "s29", "s30", "s31", "s32", "s33", "s34", "s35", "s36", "s37", "s38", "s39", "s40", "s41", "s42", "s43", "s44", "s45", "s46", "s47", "s48", "s49", "s50", "s51", "s52", "s53", "s54", "s55", "s56", "s57", "s58", "s59", "s60", "s61", "s62", "s63", "s64", "s65", "s66", "s67", "s68", "s69", "s70", "s71", "s72", "s73", "s74", "s75", "s76", "s77", "s78", "s79", "s80", "s81", "s82", "s83", "s84", "s85", "s86", "s87", "s88", "s89", "s90", "s91", "s92", "s93", "s94", "s95", "s96", "s97", "s98", "s99", "s100", "s101", "vcc", "memory", "scc");
}
__global__ __launch_bounds__(256, 1) void k6_ffn2(const u16* w2, char* ws, u16* out) {
  unsigned wave = __builtin_amdgcn_readfirstlane(threadIdx.x >> 6); unsigned blk = blockIdx.x;
  asm volatile(
"s_getpc_b64 s[64:65]\n"
"L_kpc_%=:\n"
"s_and_b32 s65, s65, 0xffff\n"
"s_mov_b32 s66, L_kend_%=-L_kpc_%=\n"
"s_mov_b32 s67, 0x20000\n"
"s_lshr_b32 s68, %4, 3\n"
"s_lshl_b32 s68, s68, 2\n"
"s_add_u32 s68, s68, %3\n"
"s_lshl_b32 s68, s68, 12\n"
"v_mbcnt_lo_u32_b32 v255, -1, 0\n"
"v_mbcnt_hi_u32_b32 v255, -1, v255\n"
"v_lshlrev_b32 v255, 6, v255\n"
"buffer_load_dword a255, v255, s[64:67], s68 offen\n"
"s_mov_b32 s48, %3\n"
"s_mov_b32 s49, %4\n"
"s_bfe_u32 s50, s49, 0x20001\n"
"s_lshl_b32 s50, s50, 3\n"
"s_bfe_u32 s51, s49, 0x30003\n"
"s_add_u32 s54, s50, s51\n"
"s_and_b32 s50, s49, 1\n"
"s_lshl_b32 s50, s50, 2\n"
"s_lshr_b32 s51, s49, 6\n"
"s_add_u32 s55, s50, s51\n"
"s_mul_i32 s50, s48, 458752\n"
"s_lshl_b32 s51, s55, 7\n"
"s_add_u32 s50, s50, s51\n"
"s_mov_b64 s[24:25], %0\n"
"s_add_u32 s24, s24, s50\n"
"s_addc_u32 s25, s25, 0\n"
"s_and_b32 s25, s25, 0xffff\n"
"s_mov_b32 s26, 0x70000\n"
"s_mov_b32 s27, 0x20000\n"
"v_mbcnt_lo_u32_b32 v140, -1, 0\n"
"v_mbcnt_hi_u32_b32 v140, -1, v140\n"
"v_lshrrev_b32 v141, 4, v140\n"
"v_and_b32 v144, 15, v140\n"
"v_lshrrev_b32 v142, 2, v144\n"
"v_and_b32 v143, 3, v140\n"
"v_lshrrev_b32 v145, 3, v140\n"
"v_lshlrev_b32 v145, 10, v145\n"
"v_lshlrev_b32 v146, 1, v141\n"
"v_and_b32 v147, 7, v140\n"
"v_xor_b32 v146, v146, v147\n"
"v_lshlrev_b32 v146, 4, v146\n"
"v_add_u32 v124, v145, v146\n"
"v_add_u32 v125, 7168, v124\n"
"v_add_u32 v126, 14336, v124\n"
"v_add_u32 v127, 21504, v124\n"
"s_lshl_b32 s62, s48, 15\n"
"s_mul_i32 s50, s54, 229376\n"
"s_mul_i32 s51, s48, 14336\n"
"s_add_u32 s50, s50, s51\n"
"s_mov_b64 s[28:29], %1\n"
"s_add_u32 s28, s28, s50\n"
"s_addc_u32 s29, s29, 0\n"
"s_add_u32 s28, s28, 83886080\n"
"s_addc_u32 s29, s29, 0\n"
"s_and_b32 s29, s29, 0xffff\n"
"s_mov_b32 s30, 0x38000\n"
"s_mov_b32 s31, 0x20000\n"
"v_lshlrev_b32 v136, 4, v140\n"
"s_xor_b32 s50, s48, 0\n"
"s_mul_i32 s50, s50, 57344\n"
"v_add_u32 v128, s50, v136\n"
"s_xor_b32 s50, s48, 1\n"
"s_mul_i32 s50, s50, 57344\n"
"v_add_u32 v129, s50, v136\n"
"s_xor_b32 s50, s48, 2\n"
"s_mul_i32 s50, s50, 57344\n"
"v_add_u32 v130, s50, v136\n"
"s_xor_b32 s50, s48, 3\n"
"s_mul_i32 s50, s50, 57344\n"
"v_add_u32 v131, s50, v136\n"
"s_lshl_b32 s50, s54, 2\n"
"s_add_u32 s50, s50, s48\n"
"s_lshl_b32 s50, s50, 14\n"
"s_lshl_b32 s51, s55, 11\n"
"s_add_u32 s50, s50, s51\n"
"s_mov_b64 s[36:37], %1\n"
"s_add_u32 s36, s36, s50\n"
"s_addc_u32 s37, s37, 0\n"
"s_add_u32 s36, s36, 75497472\n"
"s_addc_u32 s37, s37, 0\n"
"s_and_b32 s37, s37, 0xffff\n"
"s_mov_b32 s38, 0x800\n"
"s_mov_b32 s39, 0x20000\n"
"s_mov_b64 s[40:41], %1\n"
"s_and_b32 s41, s41, 0xffff\n"
"s_mov_b32 s42, 0x8000000\n"
"s_mov_b32 s43, 0x20000\n"
"s_lshl_b32 s53, s55, 8\n"
"s_add_u32 s53, s53, 14336\n"
"v_lshlrev_b32 v137, 4, v141\n"
"s_add_u32 m0, s62, 0\n"
"s_mov_b32 s61, 0\n"
"s_nop 0\n"
"buffer_load_dwordx4 v124, s[24:27], s61 offen offset:0 lds\n"
"buffer_load_dwordx4 v125, s[24:27], s61 offen offset:1024 lds\n"
"buffer_load_dwordx4 v126, s[24:27], s61 offen offset:2048 lds\n"
"buffer_load_dwordx4 v127, s[24:27], s61 offen offset:3072 lds\n"
"s_movk_i32 s60, 0\n"
"buffer_load_dwordx4 a[0:3], v128, s[28:31], s60 offen offset:0\n"
"buffer_load_dwordx4 a[4:7], v129, s[28:31], s60 offen offset:0\n"
"buffer_load_dwordx4 a[8:11], v130, s[28:31], s60 offen offset:0\n"
"buffer_load_dwordx4 a[12:15], v131, s[28:31], s60 offen offset:0\n"
"s_add_u32 m0, s62, 4096\n"
"s_mov_b32 s61, 32768\n"
"s_nop 0\n"
"buffer_load_dwordx4 v124, s[24:27], s61 offen offset:0 lds\n"
"buffer_load_dwordx4 v125, s[24:27], s61 offen offset:1024 lds\n"
"buffer_load_dwordx4 v126, s[24:27], s61 offen offset:2048 lds\n"
"buffer_load_dwordx4 v127, s[24:27], s61 offen offset:3072 lds\n"
"s_add_u32 m0, s62, 8192\n"
"s_mov_b32 s61, 65536\n"
"s_nop 0\n"
"buffer_load_dwordx4 v124, s[24:27], s61 offen offset:0 lds\n"
"buffer_load_dwordx4 v125, s[24:27], s61 offen offset:1024 lds\n"
"buffer_load_dwordx4 v126, s[24:27], s61 offen offset:2048 lds\n"
"buffer_load_dwordx4 v127, s[24:27], s61 offen offset:3072 lds\n"
"buffer_load_dwordx4 a[16:19], v128, s[28:31], s60 offen offset:1024\n"
"buffer_load_dwordx4 a[20:23], v129, s[28:31], s60 offen offset:1024\n"
"buffer_load_dwordx4 a[24:27], v130, s[28:31], s60 offen offset:1024\n"
"buffer_load_dwordx4 a[28:31], v131, s[28:31], s60 offen offset:1024\n"
"v_lshlrev_b32 v145, 2, v141\n"
"v_add_u32 v145, v145, v142\n"
"v_lshrrev_b32 v146, 1, v145\n"
"v_and_b32 v146, 3, v146\n"
"v_lshlrev_b32 v145, 7, v145\n"
"v_lshlrev_b32 v146, 5, v146\n"
"v_or_b32 v145, v145, v146\n"
"v_lshlrev_b32 v146, 3, v143\n"
"v_or_b32 v145, v145, v146\n"
"v_add_u32 v120, s62, v145\n"
"v_xor_b32 v121, 32, v120\n"
"v_xor_b32 v122, 64, v120\n"
"v_xor_b32 v123, 96, v120\n"
"v_add_u32 v132, s62, v136\n"
"v_xor_b32 v133, 32768, v132\n"
"v_xor_b32 v134, 65536, v132\n"
"v_xor_b32 v135, 98304, v132\n"
"s_lshl_b32 s50, s54, 16\n"
"s_lshl_b32 s51, s48, 14\n"
"s_add_u32 s50, s50, s51\n"
"s_lshl_b32 s51, s55, 7\n"
"s_add_u32 s50, s50, s51\n"
"s_mov_b64 s[32:33], %2\n"
"s_add_u32 s32, s32, s50\n"
"s_addc_u32 s33, s33, 0\n"
"s_and_b32 s33, s33, 0xffff\n"
"s_mov_b32 s34, 0x4000\n"
"s_mov_b32 s35, 0x20000\n"
"v_lshlrev_b32 v145, 10, v144\n"
"v_and_b32 v146, 1, v141\n"
"v_lshlrev_b32 v146, 5, v146\n"
"v_lshrrev_b32 v147, 1, v141\n"
"v_lshlrev_b32 v147, 4, v147\n"
"v_add_u32 v145, v145, v146\n"
"v_add_u32 v138, v145, v147\n"
"v_and_b32 v145, 7, v144\n"
"v_lshlrev_b32 v145, 10, v145\n"
"v_lshrrev_b32 v138, 3, v144\n"
"v_lshlrev_b32 v138, 6, v138\n"
"v_add_u32 v145, v145, v146\n"
"v_add_u32 v145, v145, v147\n"
"v_add_u32 v138, v138, v145\n"
"s_waitcnt vmcnt(16)\n"
"ds_read_b64_tr_b16 v[64:65], v120 offset:0\n"
"ds_read_b64_tr_b16 v[66:67], v120 offset:2048\n"
"ds_read_b64_tr_b16 v[68:69], v121 offset:0\n"
"ds_read_b64_tr_b16 v[70:71], v121 offset:2048\n"
"ds_read_b64_tr_b16 v[72:73], v122 offset:0\n"
"ds_read_b64_tr_b16 v[74:75], v122 offset:2048\n"
"ds_read_b64_tr_b16 v[76:77], v123 offset:0\n"
"ds_read_b64_tr_b16 v[78:79], v123 offset:2048\n"
"s_waitcnt vmcnt(8) lgkmcnt(0)\n"
"v_mfma_f32_16x16x32_bf16 v[0:3], v[64:67], a[0:3], 0\n"
"ds_read_b64_tr_b16 v[80:81], v120 offset:4096\n"
"s_add_u32 m0, s62, 12288\n"
"s_mov_b32 s61, 98304\n"
"s_nop 0\n"
"buffer_load_dwordx4 v124, s[24:27], s61 offen offset:0 lds\n"
"v_mfma_f32_16x16x32_bf16 v[16:19], v[64:67], a[4:7], 0\n"
"buffer_load_dwordx4 v125, s[24:27], s61 offen offset:1024 lds\n"
"v_mfma_f32_16x16x32_bf16 v[32:35], v[64:67], a[8:11], 0\n"
"ds_read_b64_tr_b16 v[82:83], v120 offset:6144\n"
"buffer_load_dwordx4 v126, s[24:27], s61 offen offset:2048 lds\n"
"v_mfma_f32_16x16x32_bf16 v[48:51], v[64:67], a[12:15], 0\n"
"buffer_load_dwordx4 v127, s[24:27], s61 offen offset:3072 lds\n"
"v_mfma_f32_16x16x32_bf16 v[4:7], v[68:71], a[0:3], 0\n"
"ds_read_b64_tr_b16 v[84:85], v121 offset:4096\n"
"buffer_load_dwordx4 a[32:35], v128, s[28:31], s60 offen offset:2048\n"
"v_mfma_f32_16x16x32_bf16 v[20:23], v[68:71], a[4:7], 0\n"
"buffer_load_dwordx4 a[36:39], v129, s[28:31], s60 offen offset:2048\n"
"v_mfma_f32_16x16x32_bf16 v[36:39], v[68:71], a[8:11], 0\n"
"ds_read_b64_tr_b16 v[86:87], v121 offset:6144\n"
"buffer_load_dwordx4 a[40:43], v130, s[28:31], s60 offen offset:2048\n"
"v_mfma_f32_16x16x32_bf16 v[52:55], v[68:71], a[12:15], 0\n"
"buffer_load_dwordx4 a[44:47], v131, s[28:31], s60 offen offset:2048\n"
"s_add_u32 m0, s62, 16384\n"
"s_mov_b32 s61, 131072\n"
"v_mfma_f32_16x16x32_bf16 v[8:11], v[72:75], a[0:3], 0\n"
"ds_read_b64_tr_b16 v[88:89], v122 offset:4096\n"
"buffer_load_dwordx4 v124, s[24:27], s61 offen offset:0 lds\n"
"v_mfma_f32_16x16x32_bf16 v[24:27], v[72:75], a[4:7], 0\n"
"buffer_load_dwordx4 v125, s[24:27], s61 offen offset:1024 lds\n"
"v_mfma_f32_16x16x32_bf16 v[40:43], v[72:75], a[8:11], 0\n"
"ds_read_b64_tr_b16 v[90:91], v122 offset:6144\n"
"buffer_load_dwordx4 v126, s[24:27], s61 offen offset:2048 lds\n"
"v_mfma_f32_16x16x32_bf16 v[56:59], v[72:75], a[12:15], 0\n"
"buffer_load_dwordx4 v127, s[24:27], s61 offen offset:3072 lds\n"
"v_mfma_f32_16x16x32_bf16 v[12:15], v[76:79], a[0:3], 0\n"
"ds_read_b64_tr_b16 v[92:93], v123 offset:4096\n"
"buffer_load_dwordx4 a[48:51], v128, s[28:31], s60 offen offset:3072\n"
"v_mfma_f32_16x16x32_bf16 v[28:31], v[76:79], a[4:7], 0\n"
"buffer_load_dwordx4 a[52:55], v129, s[28:31], s60 offen offset:3072\n"
"v_mfma_f32_16x16x32_bf16 v[44:47], v[76:79], a[8:11], 0\n"
"ds_read_b64_tr_b16 v[94:95], v123 offset:6144\n"
"buffer_load_dwordx4 a[56:59], v130, s[28:31], s60 offen offset:3072\n"
"v_mfma_f32_16x16x32_bf16 v[60:63], v[76:79], a[12:15], 0\n"
"s_waitcnt lgkmcnt(4)\n"
"buffer_load_dwordx4 a[60:63], v131, s[28:31], s60 offen offset:3072\n"
"s_waitcnt vmcnt(16)\n"
"v_mfma_f32_16x16x32_bf16 v[0:3], v[80:83], a[16:19], v[0:3]\n"
"ds_read_b64_tr_b16 v[64:65], v120 offset:8192\n"
"s_add_u32 m0, s62, 20480\n"
"s_mov_b32 s61, 163840\n"
"s_nop 0\n"
"buffer_load_dwordx4 v124, s[24:27], s61 offen offset:0 lds\n"
"v_mfma_f32_16x16x32_bf16 v[16:19], v[80:83], a[20:23], v[16:19]\n"
"buffer_load_dwordx4 v125, s[24:27], s61 offen offset:1024 lds\n"
"v_mfma_f32_16x16x32_bf16 v[32:35], v[80:83], a[24:27], v[32:35]\n"
"ds_read_b64_tr_b16 v[66:67], v120 offset:10240\n"
"buffer_load_dwordx4 v126, s[24:27], s61 offen offset:2048 lds\n"
"v_mfma_f32_16x16x32_bf16 v[48:51], v[80:83], a[28:31], v[48:51]\n"
"buffer_load_dwordx4 v127, s[24:27], s61 offen offset:3072 lds\n"
"s_movk_i32 s60, 4096\n"
"v_mfma_f32_16x16x32_bf16 v[4:7], v[84:87], a[16:19], v[4:7]\n"
"ds_read_b64_tr_b16 v[68:69], v121 offset:8192\n"
"buffer_load_dwordx4 a[64:67], v128, s[28:31], s60 offen offset:0\n"
"v_mfma_f32_16x16x32_bf16 v[20:23], v[84:87], a[20:23], v[20:23]\n"
"buffer_load_dwordx4 a[68:71], v129, s[28:31], s60 offen offset:0\n"
"v_mfma_f32_16x16x32_bf16 v[36:39], v[84:87], a[24:27], v[36:39]\n"
"ds_read_b64_tr_b16 v[70:71], v121 offset:10240\n"
"buffer_load_dwordx4 a[72:75], v130, s[28:31], s60 offen offset:0\n"
"v_mfma_f32_16x16x32_bf16 v[52:55], v[84:87], a[28:31], v[52:55]\n"
"s_waitcnt lgkmcnt(4)\n"
"v_mfma_f32_16x16x32_bf16 v[8:11], v[88:91], a[16:19], v[8:11]\n"
"ds_read_b64_tr_b16 v[72:73], v122 offset:8192\n"
"buffer_load_dwordx4 a[76:79], v131, s[28:31], s60 offen offset:0\n"
"v_mfma_f32_16x16x32_bf16 v[24:27], v[88:91], a[20:23], v[24:27]\n"
"buffer_load_dwordx4 v[96:99], v136, s[36:39], 0 offen offset:0\n"
"v_mfma_f32_16x16x32_bf16 v[40:43], v[88:91], a[24:27], v[40:43]\n"
"ds_read_b64_tr_b16 v[74:75], v122 offset:10240\n"
"buffer_load_dwordx4 v[100:103], v136, s[36:39], 0 offen offset:1024\n"
"v_mfma_f32_16x16x32_bf16 v[56:59], v[88:91], a[28:31], v[56:59]\n"
"buffer_load_dwordx4 v[104:107], v137, s[40:43], s53 offen offset:0\n"
"v_mfma_f32_16x16x32_bf16 v[12:15], v[92:95], a[16:19], v[12:15]\n"
"ds_read_b64_tr_b16 v[76:77], v123 offset:8192\n"
"buffer_load_dwordx4 v[108:111], v137, s[40:43], s53 offen offset:64\n"
"v_mfma_f32_16x16x32_bf16 v[28:31], v[92:95], a[20:23], v[28:31]\n"
"buffer_load_dwordx4 v[112:115], v137, s[40:43], s53 offen offset:128\n"
"v_mfma_f32_16x16x32_bf16 v[44:47], v[92:95], a[24:27], v[44:47]\n"
"ds_read_b64_tr_b16 v[78:79], v123 offset:10240\n"
"buffer_load_dwordx4 v[116:119], v137, s[40:43], s53 offen offset:192\n"
"v_mfma_f32_16x16x32_bf16 v[60:63], v[92:95], a[28:31], v[60:63]\n"
"s_waitcnt lgkmcnt(4)\n"
"s_waitcnt vmcnt(22)\n"
"v_mfma_f32_16x16x32_bf16 v[0:3], v[64:67], a[32:35], v[0:3]\n"
"ds_read_b64_tr_b16 v[80:81], v120 offset:12288\n"
"s_add_u32 m0, s62, 24576\n"
"s_mov_b32 s61, 196608\n"
"v_mfma_f32_16x16x32_bf16 v[16:19], v[64:67], a[36:39], v[16:19]\n"
"buffer_load_dwordx4 v124, s[24:27], s61 offen offset:0 lds\n"
"v_mfma_f32_16x16x32_bf16 v[32:35], v[64:67], a[40:43], v[32:35]\n"
"ds_read_b64_tr_b16 v[82:83], v120 offset:14336\n"
"v_mfma_f32_16x16x32_bf16 v[48:51], v[64:67], a[44:47], v[48:51]\n"
"buffer_load_dwordx4 v125, s[24:27], s61 offen offset:1024 lds\n"
"v_mfma_f32_16x16x32_bf16 v[4:7], v[68:71], a[32:35], v[4:7]\n"
"ds_read_b64_tr_b16 v[84:85], v121 offset:12288\n"
"v_mfma_f32_16x16x32_bf16 v[20:23], v[68:71], a[36:39], v[20:23]\n"
"buffer_load_dwordx4 v126, s[24:27], s61 offen offset:2048 lds\n"
"v_mfma_f32_16x16x32_bf16 v[36:39], v[68:71], a[40:43], v[36:39]\n"
"ds_read_b64_tr_b16 v[86:87], v121 offset:14336\n"
"v_mfma_f32_16x16x32_bf16 v[52:55], v[68:71], a[44:47], v[52:55]\n"
"s_waitcnt lgkmcnt(4)\n"
"buffer_load_dwordx4 v127, s[24:27], s61 offen offset:3072 lds\n"
"v_mfma_f32_16x16x32_bf16 v[8:11], v[72:75], a[32:35], v[8:11]\n"
"ds_read_b64_tr_b16 v[88:89], v122 offset:12288\n"
"v_mfma_f32_16x16x32_bf16 v[24:27], v[72:75], a[36:39], v[24:27]\n"
"buffer_load_dwordx4 a[80:83], v128, s[28:31], s60 offen offset:1024\n"
"v_mfma_f32_16x16x32_bf16 v[40:43], v[72:75], a[40:43], v[40:43]\n"
"ds_read_b64_tr_b16 v[90:91], v122 offset:14336\n"
"v_mfma_f32_16x16x32_bf16 v[56:59], v[72:75], a[44:47], v[56:59]\n"
"buffer_load_dwordx4 a[84:87], v129, s[28:31], s60 offen offset:1024\n"
"v_mfma_f32_16x16x32_bf16 v[12:15], v[76:79], a[32:35], v[12:15]\n"
"ds_read_b64_tr_b16 v[92:93], v123 offset:12288\n"
"v_mfma_f32_16x16x32_bf16 v[28:31], v[76:79], a[36:39], v[28:31]\n"
"buffer_load_dwordx4 a[88:91], v130, s[28:31], s60 offen offset:1024\n"
"v_mfma_f32_16x16x32_bf16 v[44:47], v[76:79], a[40:43], v[44:47]\n"
"ds_read_b64_tr_b16 v[94:95], v123 offset:14336\n"
"v_mfma_f32_16x16x32_bf16 v[60:63], v[76:79], a[44:47], v[60:63]\n"
"s_waitcnt lgkmcnt(4)\n"
"buffer_load_dwordx4 a[92:95], v131, s[28:31], s60 offen offset:1024\n"
"s_waitcnt vmcnt(22)\n"
"v_mfma_f32_16x16x32_bf16 v[0:3], v[80:83], a[48:51], v[0:3]\n"
"ds_read_b64_tr_b16 v[64:65], v120 offset:16384\n"
"s_add_u32 m0, s62, 28672\n"
"s_mov_b32 s61, 229376\n"
"v_mfma_f32_16x16x32_bf16 v[16:19], v[80:83], a[52:55], v[16:19]\n"
"buffer_load_dwordx4 v124, s[24:27], s61 offen offset:0 lds\n"
"v_mfma_f32_16x16x32_bf16 v[32:35], v[80:83], a[56:59], v[32:35]\n"
"ds_read_b64_tr_b16 v[66:67], v120 offset:18432\n"
"v_mfma_f32_16x16x32_bf16 v[48:51], v[80:83], a[60:63], v[48:51]\n"
"buffer_load_dwordx4 v125, s[24:27], s61 offen offset:1024 lds\n"
"v_mfma_f32_16x16x32_bf16 v[4:7], v[84:87], a[48:51], v[4:7]\n"
"ds_read_b64_tr_b16 v[68:69], v121 offset:16384\n"
"v_mfma_f32_16x16x32_bf16 v[20:23], v[84:87], a[52:55], v[20:23]\n"
"buffer_load_dwordx4 v126, s[24:27], s61 offen offset:2048 lds\n"
"v_mfma_f32_16x16x32_bf16 v[36:39], v[84:87], a[56:59], v[36:39]\n"
"ds_read_b64_tr_b16 v[70:71], v121 offset:18432\n"
"v_mfma_f32_16x16x32_bf16 v[52:55], v[84:87], a[60:63], v[52:55]\n"
"s_waitcnt lgkmcnt(4)\n"
"buffer_load_dwordx4 v127, s[24:27], s61 offen offset:3072 lds\n"
"v_mfma_f32_16x16x32_bf16 v[8:11], v[88:91], a[48:51], v[8:11]\n"
"ds_read_b64_tr_b16 v[72:73], v122 offset:16384\n"
"v_mfma_f32_16x16x32_bf16 v[24:27], v[88:91], a[52:55], v[24:27]\n"
"buffer_load_dwordx4 a[96:99], v128, s[28:31], s60 offen offset:2048\n"
"v_mfma_f32_16x16x32_bf16 v[40:43], v[88:91], a[56:59], v[40:43]\n"
"ds_read_b64_tr_b16 v[74:75], v122 offset:18432\n"
"v_mfma_f32_16x16x32_bf16 v[56:59], v[88:91], a[60:63], v[56:59]\n"
"buffer_load_dwordx4 a[100:103], v129, s[28:31], s60 offen offset:2048\n"
"v_mfma_f32_16x16x32_bf16 v[12:15], v[92:95], a[48:51], v[12:15]\n"
"ds_read_b64_tr_b16 v[76:77], v123 offset:16384\n"
"v_mfma_f32_16x16x32_bf16 v[28:31], v[92:95], a[52:55], v[28:31]\n"
"buffer_load_dwordx4 a[104:107], v130, s[28:31], s60 offen offset:2048\n"
"v_mfma_f32_16x16x32_bf16 v[44:47], v[92:95], a[56:59], v[44:47]\n"
"ds_read_b64_tr_b16 v[78:79], v123 offset:18432\n"
"v_mfma_f32_16x16x32_bf16 v[60:63], v[92:95], a[60:63], v[60:63]\n"
"s_waitcnt lgkmcnt(4)\n"
"buffer_load_dwordx4 a[108:111], v131, s[28:31], s60 offen offset:2048\n"
"s_waitcnt vmcnt(22)\n"
"v_mfma_f32_16x16x32_bf16 v[0:3], v[64:67], a[64:67], v[0:3]\n"
"ds_read_b64_tr_b16 v[80:81], v120 offset:20480\n"
"s_add_u32 m0, s62, 0\n"
"s_mov_b32 s61, 262144\n"
"v_mfma_f32_16x16x32_bf16 v[16:19], v[64:67], a[68:71], v[16:19]\n"
"buffer_load_dwordx4 v124, s[24:27], s61 offen offset:0 lds\n"
"v_mfma_f32_16x16x32_bf16 v[32:35], v[64:67], a[72:75], v[32:35]\n"
"ds_read_b64_tr_b16 v[82:83], v120 offset:22528\n"
"v_mfma_f32_16x16x32_bf16 v[48:51], v[64:67], a[76:79], v[48:51]\n"
"buffer_load_dwordx4 v125, s[24:27], s61 offen offset:1024 lds\n"
"v_mfma_f32_16x16x32_bf16 v[4:7], v[68:71], a[64:67], v[4:7]\n"
"ds_read_b64_tr_b16 v[84:85], v121 offset:20480\n"
"v_mfma_f32_16x16x32_bf16 v[20:23], v[68:71], a[68:71], v[20:23]\n"
"buffer_load_dwordx4 v126, s[24:27], s61 offen offset:2048 lds\n"
"v_mfma_f32_16x16x32_bf16 v[36:39], v[68:71], a[72:75], v[36:39]\n"
"ds_read_b64_tr_b16 v[86:87], v121 offset:22528\n"
"v_mfma_f32_16x16x32_bf16 v[52:55], v[68:71], a[76:79], v[52:55]\n"
"s_waitcnt lgkmcnt(4)\n"
"buffer_load_dwordx4 v127, s[24:27], s61 offen offset:3072 lds\n"
"v_mfma_f32_16x16x32_bf16 v[8:11], v[72:75], a[64:67], v[8:11]\n"
"ds_read_b64_tr_b16 v[88:89], v122 offset:20480\n"
"v_mfma_f32_16x16x32_bf16 v[24:27], v[72:75], a[68:71], v[24:27]\n"
"buffer_load_dwordx4 a[112:115], v128, s[28:31], s60 offen offset:3072\n"
"v_mfma_f32_16x16x32_bf16 v[40:43], v[72:75], a[72:75], v[40:43]\n"
"ds_read_b64_tr_b16 v[90:91], v122 offset:22528\n"
"v_mfma_f32_16x16x32_bf16 v[56:59], v[72:75], a[76:79], v[56:59]\n"
"buffer_load_dwordx4 a[116:119], v129, s[28:31], s60 offen offset:3072\n"
"v_mfma_f32_16x16x32_bf16 v[12:15], v[76:79], a[64:67], v[12:15]\n"
"ds_read_b64_tr_b16 v[92:93], v123 offset:20480\n"
"v_mfma_f32_16x16x32_bf16 v[28:31], v[76:79], a[68:71], v[28:31]\n"
"buffer_load_dwordx4 a[120:123], v130, s[28:31], s60 offen offset:3072\n"
"v_mfma_f32_16x16x32_bf16 v[44:47], v[76:79], a[72:75], v[44:47]\n"
"ds_read_b64_tr_b16 v[94:95], v123 offset:22528\n"
"v_mfma_f32_16x16x32_bf16 v[60:63], v[76:79], a[76:79], v[60:63]\n"
"s_waitcnt lgkmcnt(4)\n"
"buffer_load_dwordx4 a[124:127], v131, s[28:31], s60 offen offset:3072\n"
"s_waitcnt vmcnt(16)\n"
"v_mfma_f32_16x16x32_bf16 v[0:3], v[80:83], a[80:83], v[0:3]\n"
"ds_read_b64_tr_b16 v[64:65], v120 offset:24576\n"
"s_add_u32 m0, s62, 4096\n"
"s_mov_b32 s61, 294912\n"
"v_mfma_f32_16x16x32_bf16 v[16:19], v[80:83], a[84:87], v[16:19]\n"
"buffer_load_dwordx4 v124, s[24:27], s61 offen offset:0 lds\n"
"v_mfma_f32_16x16x32_bf16 v[32:35], v[80:83], a[88:91], v[32:35]\n"
"ds_read_b64_tr_b16 v[66:67], v120 offset:26624\n"
"v_mfma_f32_16x16x32_bf16 v[48:51], v[80:83], a[92:95], v[48:51]\n"
"buffer_load_dwordx4 v125, s[24:27], s61 offen offset:1024 lds\n"
"v_mfma_f32_16x16x32_bf16 v[4:7], v[84:87], a[80:83], v[4:7]\n"
"ds_read_b64_tr_b16 v[68:69], v121 offset:24576\n"
"v_mfma_f32_16x16x32_bf16 v[20:23], v[84:87], a[84:87], v[20:23]\n"
"buffer_load_dwordx4 v126, s[24:27], s61 offen offset:2048 lds\n"
"v_mfma_f32_16x16x32_bf16 v[36:39], v[84:87], a[88:91], v[36:39]\n"
"ds_read_b64_tr_b16 v[70:71], v121 offset:26624\n"
"v_mfma_f32_16x16x32_bf16 v[52:55], v[84:87], a[92:95], v[52:55]\n"
"s_waitcnt lgkmcnt(4)\n"
"buffer_load_dwordx4 v127, s[24:27], s61 offen offset:3072 lds\n"
"v_mfma_f32_16x16x32_bf16 v[8:11], v[88:91], a[80:83], v[8:11]\n"
"ds_read_b64_tr_b16 v[72:73], v122 offset:24576\n"
"s_movk_i32 s60, 8192\n"
"v_mfma_f32_16x16x32_bf16 v[24:27], v[88:91], a[84:87], v[24:27]\n"
"buffer_load_dwordx4 a[128:131], v128, s[28:31], s60 offen offset:0\n"
"v_mfma_f32_16x16x32_bf16 v[40:43], v[88:91], a[88:91], v[40:43]\n"
"ds_read_b64_tr_b16 v[74:75], v122 offset:26624\n"
"v_mfma_f32_16x16x32_bf16 v[56:59], v[88:91], a[92:95], v[56:59]\n"
"buffer_load_dwordx4 a[132:135], v129, s[28:31], s60 offen offset:0\n"
"v_mfma_f32_16x16x32_bf16 v[12:15], v[92:95], a[80:83], v[12:15]\n"
"ds_read_b64_tr_b16 v[76:77], v123 offset:24576\n"
"v_mfma_f32_16x16x32_bf16 v[28:31], v[92:95], a[84:87], v[28:31]\n"
"buffer_load_dwordx4 a[136:139], v130, s[28:31], s60 offen offset:0\n"
"v_mfma_f32_16x16x32_bf16 v[44:47], v[92:95], a[88:91], v[44:47]\n"
"ds_read_b64_tr_b16 v[78:79], v123 offset:26624\n"
"v_mfma_f32_16x16x32_bf16 v[60:63], v[92:95], a[92:95], v[60:63]\n"
"s_waitcnt lgkmcnt(4)\n"
"buffer_load_dwordx4 a[140:143], v131, s[28:31], s60 offen offset:0\n"
"s_waitcnt vmcnt(16)\n"
"v_mfma_f32_16x16x32_bf16 v[0:3], v[64:67], a[96:99], v[0:3]\n"
"ds_read_b64_tr_b16 v[80:81], v120 offset:28672\n"
"s_add_u32 m0, s62, 8192\n"
"s_mov_b32 s61, 327680\n"
"v_mfma_f32_16x16x32_bf16 v[16:19], v[64:67], a[100:103], v[16:19]\n"
"buffer_load_dwordx4 v124, s[24:27], s61 offen offset:0 lds\n"
"v_mfma_f32_16x16x32_bf16 v[32:35], v[64:67], a[104:107], v[32:35]\n"
"ds_read_b64_tr_b16 v[82:83], v120 offset:30720\n"
"v_mfma_f32_16x16x32_bf16 v[48:51], v[64:67], a[108:111], v[48:51]\n"
"buffer_load_dwordx4 v125, s[24:27], s61 offen offset:1024 lds\n"
"v_mfma_f32_16x16x32_bf16 v[4:7], v[68:71], a[96:99], v[4:7]\n"
"ds_read_b64_tr_b16 v[84:85], v121 offset:28672\n"
"v_mfma_f32_16x16x32_bf16 v[20:23], v[68:71], a[100:103], v[20:23]\n"
"buffer_load_dwordx4 v126, s[24:27], s61 offen offset:2048 lds\n"
"v_mfma_f32_16x16x32_bf16 v[36:39], v[68:71], a[104:107], v[36:39]\n"
"ds_read_b64_tr_b16 v[86:87], v121 offset:30720\n"
"v_mfma_f32_16x16x32_bf16 v[52:55], v[68:71], a[108:111], v[52:55]\n"
"s_waitcnt lgkmcnt(4)\n"
"buffer_load_dwordx4 v127, s[24:27], s61 offen offset:3072 lds\n"
"v_mfma_f32_16x16x32_bf16 v[8:11], v[72:75], a[96:99], v[8:11]\n"
"ds_read_b64_tr_b16 v[88:89], v122 offset:28672\n"
"v_mfma_f32_16x16x32_bf16 v[24:27], v[72:75], a[100:103], v[24:27]\n"
"buffer_load_dwordx4 a[144:147], v128, s[28:31], s60 offen offset:1024\n"
"v_mfma_f32_16x16x32_bf16 v[40:43], v[72:75], a[104:107], v[40:43]\n"
"ds_read_b64_tr_b16 v[90:91], v122 offset:30720\n"
"v_mfma_f32_16x16x32_bf16 v[56:59], v[72:75], a[108:111], v[56:59]\n"
"buffer_load_dwordx4 a[148:151], v129, s[28:31], s60 offen offset:1024\n"
"v_mfma_f32_16x16x32_bf16 v[12:15], v[76:79], a[96:99], v[12:15]\n"
"ds_read_b64_tr_b16 v[92:93], v123 offset:28672\n"
"v_mfma_f32_16x16x32_bf16 v[28:31], v[76:79], a[100:103], v[28:31]\n"
"buffer_load_dwordx4 a[152:155], v130, s[28:31], s60 offen offset:1024\n"
"v_mfma_f32_16x16x32_bf16 v[44:47], v[76:79], a[104:107], v[44:47]\n"
"ds_read_b64_tr_b16 v[94:95], v123 offset:30720\n"
"v_mfma_f32_16x16x32_bf16 v[60:63], v[76:79], a[108:111], v[60:63]\n"
"s_waitcnt lgkmcnt(4)\n"
"buffer_load_dwordx4 a[156:159], v131, s[28:31], s60 offen offset:1024\n"
"s_waitcnt vmcnt(16)\n"
"v_mfma_f32_16x16x32_bf16 v[0:3], v[80:83], a[112:115], v[0:3]\n"
"ds_read_b64_tr_b16 v[64:65], v120 offset:0\n"
"s_add_u32 m0, s62, 12288\n"
"s_mov_b32 s61, 360448\n"
"v_mfma_f32_16x16x32_bf16 v[16:19], v[80:83], a[116:119], v[16:19]\n"
"buffer_load_dwordx4 v124, s[24:27], s61 offen offset:0 lds\n"
"v_mfma_f32_16x16x32_bf16 v[32:35], v[80:83], a[120:123], v[32:35]\n"
"ds_read_b64_tr_b16 v[66:67], v120 offset:2048\n"
"v_mfma_f32_16x16x32_bf16 v[48:51], v[80:83], a[124:127], v[48:51]\n"
"buffer_load_dwordx4 v125, s[24:27], s61 offen offset:1024 lds\n"
"v_mfma_f32_16x16x32_bf16 v[4:7], v[84:87], a[112:115], v[4:7]\n"
"ds_read_b64_tr_b16 v[68:69], v121 offset:0\n"
"v_mfma_f32_16x16x32_bf16 v[20:23], v[84:87], a[116:119], v[20:23]\n"
"buffer_load_dwordx4 v126, s[24:27], s61 offen offset:2048 lds\n"
"v_mfma_f32_16x16x32_bf16 v[36:39], v[84:87], a[120:123], v[36:39]\n"
"ds_read_b64_tr_b16 v[70:71], v121 offset:2048\n"
"v_mfma_f32_16x16x32_bf16 v[52:55], v[84:87], a[124:127], v[52:55]\n"
"s_waitcnt lgkmcnt(4)\n"
"buffer_load_dwordx4 v127, s[24:27], s61 offen offset:3072 lds\n"
"v_mfma_f32_16x16x32_bf16 v[8:11], v[88:91], a[112:115], v[8:11]\n"
"ds_read_b64_tr_b16 v[72:73], v122 offset:0\n"
"v_mfma_f32_16x16x32_bf16 v[24:27], v[88:91], a[116:119], v[24:27]\n"
"buffer_load_dwordx4 a[160:163], v128, s[28:31], s60 offen offset:2048\n"
"v_mfma_f32_16x16x32_bf16 v[40:43], v[88:91], a[120:123], v[40:43]\n"
"ds_read_b64_tr_b16 v[74:75], v122 offset:2048\n"
"v_mfma_f32_16x16x32_bf16 v[56:59], v[88:91], a[124:127], v[56:59]\n"
"buffer_load_dwordx4 a[164:167], v129, s[28:31], s60 offen offset:2048\n"
"v_mfma_f32_16x16x32_bf16 v[12:15], v[92:95], a[112:115], v[12:15]\n"
"ds_read_b64_tr_b16 v[76:77], v123 offset:0\n"
"v_mfma_f32_16x16x32_bf16 v[28:31], v[92:95], a[116:119], v[28:31]\n"
"buffer_load_dwordx4 a[168:171], v130, s[28:31], s60 offen offset:2048\n"
"v_mfma_f32_16x16x32_bf16 v[44:47], v[92:95], a[120:123], v[44:47]\n"
"ds_read_b64_tr_b16 v[78:79], v123 offset:2048\n"
"v_mfma_f32_16x16x32_bf16 v[60:63], v[92:95], a[124:127], v[60:63]\n"
"s_waitcnt lgkmcnt(4)\n"
"buffer_load_dwordx4 a[172:175], v131, s[28:31], s60 offen offset:2048\n"
"s_waitcnt vmcnt(16)\n"
"v_mfma_f32_16x16x32_bf16 v[0:3], v[64:67], a[128:131], v[0:3]\n"
"ds_read_b64_tr_b16 v[80:81], v120 offset:4096\n"
"s_add_u32 m0, s62, 16384\n"
"s_mov_b32 s61, 393216\n"
"v_mfma_f32_16x16x32_bf16 v[16:19], v[64:67], a[132:135], v[16:19]\n"
"buffer_load_dwordx4 v124, s[24:27], s61 offen offset:0 lds\n"
"v_mfma_f32_16x16x32_bf16 v[32:35], v[64:67], a[136:139], v[32:35]\n"
"ds_read_b64_tr_b16 v[82:83], v120 offset:6144\n"
"v_mfma_f32_16x16x32_bf16 v[48:51], v[64:67], a[140:143], v[48:51]\n"
"buffer_load_dwordx4 v125, s[24:27], s61 offen offset:1024 lds\n"
"v_mfma_f32_16x16x32_bf16 v[4:7], v[68:71], a[128:131], v[4:7]\n"
"ds_read_b64_tr_b16 v[84:85], v121 offset:4096\n"
"v_mfma_f32_16x16x32_bf16 v[20:23], v[68:71], a[132:135], v[20:23]\n"
"buffer_load_dwordx4 v126, s[24:27], s61 offen offset:2048 lds\n"
"v_mfma_f32_16x16x32_bf16 v[36:39], v[68:71], a[136:139], v[36:39]\n"
"ds_read_b64_tr_b16 v[86:87], v121 offset:6144\n"
"v_mfma_f32_16x16x32_bf16 v[52:55], v[68:71], a[140:143], v[52:55]\n"
"s_waitcnt lgkmcnt(4)\n"
"buffer_load_dwordx4 v127, s[24:27], s61 offen offset:3072 lds\n"
"v_mfma_f32_16x16x32_bf16 v[8:11], v[72:75], a[128:131], v[8:11]\n"
"ds_read_b64_tr_b16 v[88:89], v122 offset:4096\n"
"v_mfma_f32_16x16x32_bf16 v[24:27], v[72:75], a[132:135], v[24:27]\n"
"buffer_load_dwordx4 a[176:179], v128, s[28:31], s60 offen offset:3072\n"
"v_mfma_f32_16x16x32_bf16 v[40:43], v[72:75], a[136:139], v[40:43]\n"
"ds_read_b64_tr_b16 v[90:91], v122 offset:6144\n"
"v_mfma_f32_16x16x32_bf16 v[56:59], v[72:75], a[140:143], v[56:59]\n"
"buffer_load_dwordx4 a[180:183], v129, s[28:31], s60 offen offset:3072\n"
"v_mfma_f32_16x16x32_bf16 v[12:15], v[76:79], a[128:131], v[12:15]\n"
"ds_read_b64_tr_b16 v[92:93], v123 offset:4096\n"
"v_mfma_f32_16x16x32_bf16 v[28:31], v[76:79], a[132:135], v[28:31]\n"
"buffer_load_dwordx4 a[184:187], v130, s[28:31], s60 offen offset:3072\n"
"v_mfma_f32_16x16x32_bf16 v[44:47], v[76:79], a[136:139], v[44:47]\n"
"ds_read_b64_tr_b16 v[94:95], v123 offset:6144\n"
"v_mfma_f32_16x16x32_bf16 v[60:63], v[76:79], a[140:143], v[60:63]\n"
"s_waitcnt lgkmcnt(4)\n"
"buffer_load_dwordx4 a[188:191], v131, s[28:31], s60 offen offset:3072\n"
"s_waitcnt vmcnt(16)\n"
"v_mfma_f32_16x16x32_bf16 v[0:3], v[80:83], a[144:147], v[0:3]\n"
"ds_read_b64_tr_b16 v[64:65], v120 offset:8192\n"
"s_add_u32 m0, s62, 20480\n"
"s_mov_b32 s61, 425984\n"
"v_mfma_f32_16x16x32_bf16 v[16:19], v[80:83], a[148:151], v[16:19]\n"
"buffer_load_dwordx4 v124, s[24:27], s61 offen offset:0 lds\n"
"v_mfma_f32_16x16x32_bf16 v[32:35], v[80:83], a[152:155], v[32:35]\n"
"ds_read_b64_tr_b16 v[66:67], v120 offset:10240\n"
"v_mfma_f32_16x16x32_bf16 v[48:51], v[80:83], a[156:159], v[48:51]\n"
"buffer_load_dwordx4 v125, s[24:27], s61 offen offset:1024 lds\n"
"v_mfma_f32_16x16x32_bf16 v[4:7], v[84:87], a[144:147], v[4:7]\n"
"ds_read_b64_tr_b16 v[68:69], v121 offset:8192\n"
"v_mfma_f32_16x16x32_bf16 v[20:23], v[84:87], a[148:151], v[20:23]\n"
"buffer_load_dwordx4 v126, s[24:27], s61 offen offset:2048 lds\n"
"v_mfma_f32_16x16x32_bf16 v[36:39], v[84:87], a[152:155], v[36:39]\n"
"ds_read_b64_tr_b16 v[70:71], v121 offset:10240\n"
"v_mfma_f32_16x16x32_bf16 v[52:55], v[84:87], a[156:159], v[52:55]\n"
"s_waitcnt lgkmcnt(4)\n"
"buffer_load_dwordx4 v127, s[24:27], s61 offen offset:3072 lds\n"
"v_mfma_f32_16x16x32_bf16 v[8:11], v[88:91], a[144:147], v[8:11]\n"
"ds_read_b64_tr_b16 v[72:73], v122 offset:8192\n"
"s_movk_i32 s60, 12288\n"
"v_mfma_f32_16x16x32_bf16 v[24:27], v[88:91], a[148:151], v[24:27]\n"
"buffer_load_dwordx4 a[192:195], v128, s[28:31], s60 offen offset:0\n"
"v_mfma_f32_16x16x32_bf16 v[40:43], v[88:91], a[152:155], v[40:43]\n"
"ds_read_b64_tr_b16 v[74:75], v122 offset:10240\n"
"v_mfma_f32_16x16x32_bf16 v[56:59], v[88:91], a[156:159], v[56:59]\n"
"buffer_load_dwordx4 a[196:199], v129, s[28:31], s60 offen offset:0\n"
"v_mfma_f32_16x16x32_bf16 v[12:15], v[92:95], a[144:147], v[12:15]\n"
"ds_read_b64_tr_b16 v[76:77], v123 offset:8192\n"
"v_mfma_f32_16x16x32_bf16 v[28:31], v[92:95], a[148:151], v[28:31]\n"
"buffer_load_dwordx4 a[200:203], v130, s[28:31], s60 offen offset:0\n"
"v_mfma_f32_16x16x32_bf16 v[44:47], v[92:95], a[152:155], v[44:47]\n"
"ds_read_b64_tr_b16 v[78:79], v123 offset:10240\n"
"v_mfma_f32_16x16x32_bf16 v[60:63], v[92:95], a[156:159], v[60:63]\n"
"s_waitcnt lgkmcnt(4)\n"
"buffer_load_dwordx4 a[204:207], v131, s[28:31], s60 offen offset:0\n"
"s_waitcnt vmcnt(16)\n"
"v_mfma_f32_16x16x32_bf16 v[0:3], v[64:67], a[160:163], v[0:3]\n"
"ds_read_b64_tr_b16 v[80:81], v120 offset:12288\n"
"v_mfma_f32_16x16x32_bf16 v[16:19], v[64:67], a[164:167], v[16:19]\n"
"buffer_load_dwordx4 a[208:211], v128, s[28:31], s60 offen offset:1024\n"
"v_mfma_f32_16x16x32_bf16 v[32:35], v[64:67], a[168:171], v[32:35]\n"
"ds_read_b64_tr_b16 v[82:83], v120 offset:14336\n"
"v_mfma_f32_16x16x32_bf16 v[48:51], v[64:67], a[172:175], v[48:51]\n"
"v_mfma_f32_16x16x32_bf16 v[4:7], v[68:71], a[160:163], v[4:7]\n"
"ds_read_b64_tr_b16 v[84:85], v121 offset:12288\n"
"v_mfma_f32_16x16x32_bf16 v[20:23], v[68:71], a[164:167], v[20:23]\n"
"buffer_load_dwordx4 a[212:215], v129, s[28:31], s60 offen offset:1024\n"
"v_mfma_f32_16x16x32_bf16 v[36:39], v[68:71], a[168:171], v[36:39]\n"
"ds_read_b64_tr_b16 v[86:87], v121 offset:14336\n"
"v_mfma_f32_16x16x32_bf16 v[52:55], v[68:71], a[172:175], v[52:55]\n"
"s_waitcnt lgkmcnt(4)\n"
"v_mfma_f32_16x16x32_bf16 v[8:11], v[72:75], a[160:163], v[8:11]\n"
"ds_read_b64_tr_b16 v[88:89], v122 offset:12288\n"
"v_mfma_f32_16x16x32_bf16 v[24:27], v[72:75], a[164:167], v[24:27]\n"
"buffer_load_dwordx4 a[216:219], v130, s[28:31], s60 offen offset:1024\n"
"v_mfma_f32_16x16x32_bf16 v[40:43], v[72:75], a[168:171], v[40:43]\n"
"ds_read_b64_tr_b16 v[90:91], v122 offset:14336\n"
"v_mfma_f32_16x16x32_bf16 v[56:59], v[72:75], a[172:175], v[56:59]\n"
"v_mfma_f32_16x16x32_bf16 v[12:15], v[76:79], a[160:163], v[12:15]\n"
"ds_read_b64_tr_b16 v[92:93], v123 offset:12288\n"
"v_mfma_f32_16x16x32_bf16 v[28:31], v[76:79], a[164:167], v[28:31]\n"
"buffer_load_dwordx4 a[220:223], v131, s[28:31], s60 offen offset:1024\n"
"v_mfma_f32_16x16x32_bf16 v[44:47], v[76:79], a[168:171], v[44:47]\n"
"ds_read_b64_tr_b16 v[94:95], v123 offset:14336\n"
"v_mfma_f32_16x16x32_bf16 v[60:63], v[76:79], a[172:175], v[60:63]\n"
"s_waitcnt lgkmcnt(4)\n"
"s_waitcnt vmcnt(12)\n"
"v_mfma_f32_16x16x32_bf16 v[0:3], v[80:83], a[176:179], v[0:3]\n"
"ds_read_b64_tr_b16 v[64:65], v120 offset:16384\n"
"v_mfma_f32_16x16x32_bf16 v[16:19], v[80:83], a[180:183], v[16:19]\n"
"v_mfma_f32_16x16x32_bf16 v[32:35], v[80:83], a[184:187], v[32:35]\n"
"ds_read_b64_tr_b16 v[66:67], v120 offset:18432\n"
"v_mfma_f32_16x16x32_bf16 v[48:51], v[80:83], a[188:191], v[48:51]\n"
"v_mfma_f32_16x16x32_bf16 v[4:7], v[84:87], a[176:179], v[4:7]\n"
"ds_read_b64_tr_b16 v[68:69], v121 offset:16384\n"
"v_mfma_f32_16x16x32_bf16 v[20:23], v[84:87], a[180:183], v[20:23]\n"
"v_mfma_f32_16x16x32_bf16 v[36:39], v[84:87], a[184:187], v[36:39]\n"
"ds_read_b64_tr_b16 v[70:71], v121 offset:18432\n"
"v_mfma_f32_16x16x32_bf16 v[52:55], v[84:87], a[188:191], v[52:55]\n"
"s_waitcnt lgkmcnt(4)\n"
"v_mfma_f32_16x16x32_bf16 v[8:11], v[88:91], a[176:179], v[8:11]\n"
"ds_read_b64_tr_b16 v[72:73], v122 offset:16384\n"
"v_mfma_f32_16x16x32_bf16 v[24:27], v[88:91], a[180:183], v[24:27]\n"
"v_mfma_f32_16x16x32_bf16 v[40:43], v[88:91], a[184:187], v[40:43]\n"
"ds_read_b64_tr_b16 v[74:75], v122 offset:18432\n"
"v_mfma_f32_16x16x32_bf16 v[56:59], v[88:91], a[188:191], v[56:59]\n"
"v_mfma_f32_16x16x32_bf16 v[12:15], v[92:95], a[176:179], v[12:15]\n"
"ds_read_b64_tr_b16 v[76:77], v123 offset:16384\n"
"v_mfma_f32_16x16x32_bf16 v[28:31], v[92:95], a[180:183], v[28:31]\n"
"v_mfma_f32_16x16x32_bf16 v[44:47], v[92:95], a[184:187], v[44:47]\n"
"ds_read_b64_tr_b16 v[78:79], v123 offset:18432\n"
"v_mfma_f32_16x16x32_bf16 v[60:63], v[92:95], a[188:191], v[60:63]\n"
"s_waitcnt lgkmcnt(4)\n"
"s_waitcnt vmcnt(4)\n"
"v_mfma_f32_16x16x32_bf16 v[0:3], v[64:67], a[192:195], v[0:3]\n"
"ds_read_b64_tr_b16 v[80:81], v120 offset:20480\n"
"v_mfma_f32_16x16x32_bf16 v[16:19], v[64:67], a[196:199], v[16:19]\n"
"v_mfma_f32_16x16x32_bf16 v[32:35], v[64:67], a[200:203], v[32:35]\n"
"ds_read_b64_tr_b16 v[82:83], v120 offset:22528\n"
"v_mfma_f32_16x16x32_bf16 v[48:51], v[64:67], a[204:207], v[48:51]\n"
"v_mfma_f32_16x16x32_bf16 v[4:7], v[68:71], a[192:195], v[4:7]\n"
"ds_read_b64_tr_b16 v[84:85], v121 offset:20480\n"
"v_mfma_f32_16x16x32_bf16 v[20:23], v[68:71], a[196:199], v[20:23]\n"
"v_mfma_f32_16x16x32_bf16 v[36:39], v[68:71], a[200:203], v[36:39]\n"
"ds_read_b64_tr_b16 v[86:87], v121 offset:22528\n"
"v_mfma_f32_16x16x32_bf16 v[52:55], v[68:71], a[204:207], v[52:55]\n"
"s_waitcnt lgkmcnt(4)\n"
"v_mfma_f32_16x16x32_bf16 v[8:11], v[72:75], a[192:195], v[8:11]\n"
"ds_read_b64_tr_b16 v[88:89], v122 offset:20480\n"
"v_mfma_f32_16x16x32_bf16 v[24:27], v[72:75], a[196:199], v[24:27]\n"
"v_mfma_f32_16x16x32_bf16 v[40:43], v[72:75], a[200:203], v[40:43]\n"
"ds_read_b64_tr_b16 v[90:91], v122 offset:22528\n"
"v_mfma_f32_16x16x32_bf16 v[56:59], v[72:75], a[204:207], v[56:59]\n"
"v_mfma_f32_16x16x32_bf16 v[12:15], v[76:79], a[192:195], v[12:15]\n"
"ds_read_b64_tr_b16 v[92:93], v123 offset:20480\n"
"v_mfma_f32_16x16x32_bf16 v[28:31], v[76:79], a[196:199], v[28:31]\n"
"v_mfma_f32_16x16x32_bf16 v[44:47], v[76:79], a[200:203], v[44:47]\n"
"ds_read_b64_tr_b16 v[94:95], v123 offset:22528\n"
"v_mfma_f32_16x16x32_bf16 v[60:63], v[76:79], a[204:207], v[60:63]\n"
"s_waitcnt lgkmcnt(0)\n"
"s_waitcnt vmcnt(0)\n"
"v_mfma_f32_16x16x32_bf16 v[16:19], v[80:83], a[212:215], v[16:19]\n"
"v_mfma_f32_16x16x32_bf16 v[20:23], v[84:87], a[212:215], v[20:23]\n"
"v_mfma_f32_16x16x32_bf16 v[24:27], v[88:91], a[212:215], v[24:27]\n"
"v_mfma_f32_16x16x32_bf16 v[28:31], v[92:95], a[212:215], v[28:31]\n"
"v_mfma_f32_16x16x32_bf16 v[32:35], v[80:83], a[216:219], v[32:35]\n"
"s_nop 3\n"
"ds_write_b128 v132, v[16:19] offset:0\n"
"v_mfma_f32_16x16x32_bf16 v[36:39], v[84:87], a[216:219], v[36:39]\n"
"ds_write_b128 v132, v[20:23] offset:1024\n"
"v_mfma_f32_16x16x32_bf16 v[40:43], v[88:91], a[216:219], v[40:43]\n"
"ds_write_b128 v132, v[24:27] offset:2048\n"
"v_mfma_f32_16x16x32_bf16 v[44:47], v[92:95], a[216:219], v[44:47]\n"
"ds_write_b128 v132, v[28:31] offset:3072\n"
"v_mfma_f32_16x16x32_bf16 v[48:51], v[80:83], a[220:223], v[48:51]\n"
"ds_write_b128 v132, v[32:35] offset:4096\n"
"v_mfma_f32_16x16x32_bf16 v[52:55], v[84:87], a[220:223], v[52:55]\n"
"ds_write_b128 v132, v[36:39] offset:5120\n"
"v_mfma_f32_16x16x32_bf16 v[56:59], v[88:91], a[220:223], v[56:59]\n"
"ds_write_b128 v132, v[40:43] offset:6144\n"
"v_mfma_f32_16x16x32_bf16 v[60:63], v[92:95], a[220:223], v[60:63]\n"
"ds_write_b128 v132, v[44:47] offset:7168\n"
"v_mfma_f32_16x16x32_bf16 v[0:3], v[80:83], a[208:211], v[0:3]\n"
"ds_write_b128 v132, v[48:51] offset:8192\n"
"v_mfma_f32_16x16x32_bf16 v[4:7], v[84:87], a[208:211], v[4:7]\n"
"ds_write_b128 v132, v[52:55] offset:9216\n"
"v_mfma_f32_16x16x32_bf16 v[8:11], v[88:91], a[208:211], v[8:11]\n"
"ds_write_b128 v132, v[56:59] offset:10240\n"
"v_mfma_f32_16x16x32_bf16 v[12:15], v[92:95], a[208:211], v[12:15]\n"
"ds_write_b128 v132, v[60:63] offset:11264\n"
"s_waitcnt lgkmcnt(0)\n"
"s_barrier\n"
"ds_read_b128 v[148:151], v133 offset:0\n"
"ds_read_b128 v[152:155], v133 offset:1024\n"
"ds_read_b128 v[156:159], v133 offset:2048\n"
"ds_read_b128 v[160:163], v133 offset:3072\n"
"ds_read_b128 v[164:167], v134 offset:4096\n"
"ds_read_b128 v[168:171], v134 offset:5120\n"
"ds_read_b128 v[172:175], v134 offset:6144\n"
"ds_read_b128 v[176:179], v134 offset:7168\n"
"ds_read_b128 v[180:183], v135 offset:8192\n"
"ds_read_b128 v[184:187], v135 offset:9216\n"
"ds_read_b128 v[188:191], v135 offset:10240\n"
"ds_read_b128 v[192:195], v135 offset:11264\n"
"s_waitcnt vmcnt(0)\n"
"v_lshlrev_b32 v64, 16, v96\n"
"v_and_b32 v65, 0xffff0000, v96\n"
"v_lshlrev_b32 v66, 16, v97\n"
"v_and_b32 v67, 0xffff0000, v97\n"
"v_lshlrev_b32 v68, 16, v98\n"
"v_and_b32 v69, 0xffff0000, v98\n"
"v_lshlrev_b32 v70, 16, v99\n"
"v_and_b32 v71, 0xffff0000, v99\n"
"v_lshlrev_b32 v72, 16, v100\n"
"v_and_b32 v73, 0xffff0000, v100\n"
"v_lshlrev_b32 v74, 16, v101\n"
"v_and_b32 v75, 0xffff0000, v101\n"
"v_lshlrev_b32 v76, 16, v102\n"
"v_and_b32 v77, 0xffff0000, v102\n"
"v_lshlrev_b32 v78, 16, v103\n"
"v_and_b32 v79, 0xffff0000, v103\n"
"s_waitcnt lgkmcnt(8)\n"
"v_pk_add_f32 v[0:1], v[0:1], v[148:149]\n"
"v_pk_add_f32 v[2:3], v[2:3], v[150:151]\n"
"v_pk_add_f32 v[4:5], v[4:5], v[152:153]\n"
"v_pk_add_f32 v[6:7], v[6:7], v[154:155]\n"
"v_pk_add_f32 v[8:9], v[8:9], v[156:157]\n"
"v_pk_add_f32 v[10:11], v[10:11], v[158:159]\n"
"v_pk_add_f32 v[12:13], v[12:13], v[160:161]\n"
"v_pk_add_f32 v[14:15], v[14:15], v[162:163]\n"
"s_waitcnt lgkmcnt(4)\n"
"v_pk_add_f32 v[0:1], v[0:1], v[164:165]\n"
"v_pk_add_f32 v[2:3], v[2:3], v[166:167]\n"
"v_pk_add_f32 v[4:5], v[4:5], v[168:169]\n"
"v_pk_add_f32 v[6:7], v[6:7], v[170:171]\n"
"v_pk_add_f32 v[8:9], v[8:9], v[172:173]\n"
"v_pk_add_f32 v[10:11], v[10:11], v[174:175]\n"
"v_pk_add_f32 v[12:13], v[12:13], v[176:177]\n"
"v_pk_add_f32 v[14:15], v[14:15], v[178:179]\n"
"s_waitcnt lgkmcnt(0)\n"
"v_pk_add_f32 v[0:1], v[0:1], v[180:181]\n"
"v_pk_add_f32 v[2:3], v[2:3], v[182:183]\n"
"v_pk_add_f32 v[4:5], v[4:5], v[184:185]\n"
"v_pk_add_f32 v[6:7], v[6:7], v[186:187]\n"
"v_pk_add_f32 v[8:9], v[8:9], v[188:189]\n"
"v_pk_add_f32 v[10:11], v[10:11], v[190:191]\n"
"v_pk_add_f32 v[12:13], v[12:13], v[192:193]\n"
"v_pk_add_f32 v[14:15], v[14:15], v[194:195]\n"
"v_pk_fma_f32 v[0:1], v[0:1], v[104:105], v[64:65]\n"
"v_pk_fma_f32 v[2:3], v[2:3], v[106:107], v[66:67]\n"
"v_pk_fma_f32 v[4:5], v[4:5], v[108:109], v[68:69]\n"
"v_pk_fma_f32 v[6:7], v[6:7], v[110:111], v[70:71]\n"
"v_pk_fma_f32 v[8:9], v[8:9], v[112:113], v[72:73]\n"
"v_pk_fma_f32 v[10:11], v[10:11], v[114:115], v[74:75]\n"
"v_pk_fma_f32 v[12:13], v[12:13], v[116:117], v[76:77]\n"
"v_pk_fma_f32 v[14:15], v[14:15], v[118:119], v[78:79]\n"
"v_cvt_pk_bf16_f32 v16, v0, v1\n"
"v_cvt_pk_bf16_f32 v17, v2, v3\n"
"v_cvt_pk_bf16_f32 v18, v4, v5\n"
"v_cvt_pk_bf16_f32 v19, v6, v7\n"
"s_nop 0\n"
"v_permlane16_swap_b32 v16, v18\n"
"v_permlane16_swap_b32 v17, v19\n"
"v_cvt_pk_bf16_f32 v20, v8, v9\n"
"v_cvt_pk_bf16_f32 v21, v10, v11\n"
"v_cvt_pk_bf16_f32 v22, v12, v13\n"
"v_cvt_pk_bf16_f32 v23, v14, v15\n"
"s_nop 0\n"
"v_permlane16_swap_b32 v20, v22\n"
"v_permlane16_swap_b32 v21, v23\n"
"s_nop 1\n"
"v_mov_b32 v24, v20\n"
"v_mov_b32 v25, v21\n"
"v_mov_b32 v26, v22\n"
"v_mov_b32 v27, v23\n"
"s_nop 0\n"
"v_mov_b32_dpp v20, v16 row_ror:8 row_mask:0xf bank_mask:0x3\n"
"v_mov_b32_dpp v21, v17 row_ror:8 row_mask:0xf bank_mask:0x3\n"
"v_mov_b32_dpp v22, v18 row_ror:8 row_mask:0xf bank_mask:0x3\n"
"v_mov_b32_dpp v23, v19 row_ror:8 row_mask:0xf bank_mask:0x3\n"
"v_mov_b32_dpp v16, v24 row_ror:8 row_mask:0xf bank_mask:0xc\n"
"v_mov_b32_dpp v17, v25 row_ror:8 row_mask:0xf bank_mask:0xc\n"
"v_mov_b32_dpp v18, v26 row_ror:8 row_mask:0xf bank_mask:0xc\n"
"v_mov_b32_dpp v19, v27 row_ror:8 row_mask:0xf bank_mask:0xc\n"
"s_movk_i32 s50, 0x2000\n"
"buffer_store_dwordx4 v[16:19], v138, s[32:35], 0 offen sc1\n"
"buffer_store_dwordx4 v[20:23], v138, s[32:35], s50 offen sc1\n"
"s_endpgm\n"
"L_kend_%=:\n"
"adaln_text_end:\n"
  :: "s"(w2), "s"(ws), "s"(out), "s"(wave), "s"(blk) : "v0", "v1", "v2", "v3", "v4", "v5", "v6", "v7", "v8", "v9", "v10", "v11", "v12", "v13", "v14", "v15", "v16", "v17", "v18", "v19", "v20", "v21", "v22", "v23", "v24", "v25", "v26", "v27", "v28", "v29", "v30", "v31", "v32", "v33", "v34", "v35", "v36", "v37", "v38", "v39", "v40", "v41", "v42", "v43", "v44", "v45", "v46", "v47", "v48", "v49", "v50", "v51", "v52", "v53", "v54", "v55", "v56", "v57", "v58", "v59", "v60", "v61", "v62", "v63", "v64", "v65", "v66", "v67", "v68", "v69", "v70", "v71", "v72", "v73", "v74", "v75", "v76", "v77", "v78", "v79", "v80", "v81", "v82", "v83", "v84", "v85", "v86", "v87", "v88", "v89", "v90", "v91", "v92", "v93", "v94", "v95", "v96", "v97", "v98", "v99", "v100", "v101", "v102", "v103", "v104", "v105", "v106", "v107", "v108", "v109", "v110", "v111", "v112", "v113", "v114", "v115", "v116", "v117", "v118", "v119", "v120", "v121", "v122", "v123", "v124", "v125", "v126", "v127", "v128", "v129", "v130", "v131", "v132", "v133", "v134", "v135", "v136", "v137", "v138", "v139", "v140", "v141", "v142", "v143", "v144", "v145", "v146", "v147", "v148", "v149", "v150", "v151", "v152", "v153", "v154", "v155", "v156", "v157", "v158", "v159", "v160", "v161", "v162", "v163", "v164", "v165", "v166", "v167", "v168", "v169", "v170", "v171", "v172", "v173", "v174", "v175", "v176", "v177", "v178", "v179", "v180", "v181", "v182", "v183", "v184", "v185", "v186", "v187", "v188", "v189", "v190", "v191", "v192", "v193", "v194", "v195", "v196", "v197", "v198", "v199", "v200", "v201", "v202", "v203", "v204", "v205", "v206", "v207", "v208", "v209", "v210", "v211", "v212", "v213", "v214", "v215", "v216", "v217", "v218", "v219", "v220", "v221", "v222", "v223", "v224", "v225", "v226", "v227", "v228", "v229", "v230", "v231", "v232", "v233", "v234", "v235", "v236", "v237", "v238", "v239", "v240", "v241", "v242", "v243", "v244", "v245", "v246", "v247", "v248", "v249", "v250", "v251", "v252", "v253", "v254", "v255", "a0", "a1", "a2", "a3", "a4", "a5", "a6", "a7", "a8", "a9", "a10", "a11", "a12", "a13", "a14", "a15", "a16", "a17", "a18", "a19", "a20", "a21", "a22", "a23", "a24", "a25", "a26", "a27", "a28", "a29", "a30", "a31", "a32", "a33", "a34", "a35", "a36", "a37", "a38", "a39", "a40", "a41", "a42", "a43", "a44", "a45", "a46", "a47", "a48", "a49", "a50", "a51", "a52", "a53", "a54", "a55", "a56", "a57", "a58", "a59", "a60", "a61", "a62", "a63", "a64", "a65", "a66", "a67", "a68", "a69", "a70", "a71", "a72", "a73", "a74", "a75", "a76", "a77", "a78", "a79", "a80", "a81", "a82", "a83", "a84", "a85", "a86", "a87", "a88", "a89", "a90", "a91", "a92", "a93", "a94", "a95", "a96", "a97", "a98", "a99", "a100", "a101", "a102", "a103", "a104", "a105", "a106", "a107", "a108", "a109", "a110", "a111", "a112", "a113", "a114", "a115", "a116", "a117", "a118", "a119", "a120", "a121", "a122", "a123", "a124", "a125", "a126", "a127", "a128", "a129", "a130", "a131", "a132", "a133", "a134", "a135", "a136", "a137", "a138", "a139", "a140", "a141", "a142", "a143", "a144", "a145", "a146", "a147", "a148", "a149", "a150", "a151", "a152", "a153", "a154", "a155", "a156", "a157", "a158", "a159", "a160", "a161", "a162", "a163", "a164", "a165", "a166", "a167", "a168", "a169", "a170", "a171", "a172", "a173", "a174", "a175", "a176", "a177", "a178", "a179", "a180", "a181", "a182", "a183", "a184", "a185", "a186", "a187", "a188", "a189", "a190", "a191", "a192", "a193", "a194", "a195", "a196", "a197", "a198", "a199", "a200", "a201", "a202", "a203", "a204", "a205", "a206", "a207", "a208", "a209", "a210", "a211", "a212", "a213", "a214", "a215", "a216", "a217", "a218", "a219", "a220", "a221", "a222", "a223", "a224", "a225", "a226", "a227", "a228", "a229", "a230", "a231", "a232", "a233", "a234", "a235", "a236", "a237", "a238", "a239", "a240", "a241", "a242", "a243", "a244", "a245", "a246", "a247", "a248", "a249", "a250", "a251", "a252", "a253", "a254", "a255", "s24", "s25", "s26", "s27", "s28", "s29", "s30", "s31", "s32", "s33", "s34", "s35", "s36", "s37", "s38", "s39", "s40", "s41", "s42", "s43", "s44", "s45", "s46", "s47", "s48", "s49", "s50", "s51", "s52", "s53", "s54", "s55", "s56", "s57", "s58", "s59", "s60", "s61", "s62", "s63", "s64", "s65", "s66", "s67", "s68", "s69", "s70", "s71", "s72", "s73", "s74", "s75", "s76", "s77", "s78", "s79", "s80", "s81", "s82", "s83", "s84", "s85", "s86", "s87", "s88", "s89", "s90", "s91", "s92", "s93", "s94", "s95", "s96", "s97", "s98", "s99", "s100", "s101", "vcc", "memory", "scc");
}

extern "C" void kernel_launch(void* const* d_in, const int* in_sizes, int n_in, void* d_out, int out_size, void* d_ws, size_t ws_size, hipStream_t st) {
  const u16* x = (const u16*)d_in[0]; const u16* ckv = (const u16*)d_in[1]; const u16* wq = (const u16*)d_in[2]; const u16* wuk = (const u16*)d_in[3];
  const u16* wuv = (const u16*)d_in[4]; const u16* wo = (const u16*)d_in[5]; const u16* w1 = (const u16*)d_in[7]; const u16* w2 = (const u16*)d_in[8];
  const u16* c = (const u16*)d_in[9]; const u16* wm = (const u16*)d_in[10];
  char* ws = (char*)d_ws;
  hipFuncSetAttribute((const void*)k12_attn, hipFuncAttributeMaxDynamicSharedMemorySize, 163840);
  k12_attn<<<256, 256, 163840, st>>>(ckv, wuk, wuv, x, c, wm, ws, wq, wo, w1, w2);

  hipFuncSetAttribute((const void*)k3_oproj, hipFuncAttributeMaxDynamicSharedMemorySize, 131072);
  hipFuncSetAttribute((const void*)k5_ffn1, hipFuncAttributeMaxDynamicSharedMemorySize, 131072);
  hipFuncSetAttribute((const void*)k6_ffn2, hipFuncAttributeMaxDynamicSharedMemorySize, 131072);
  k3_oproj<<<256, 256, 131072, st>>>(wo, ws);
  k4_ln2<<<256, 256, 0, st>>>(x, ws);
  k5_ffn1<<<224, 256, 131072, st>>>(w1, ws);
  k6_ffn2<<<256, 256, 131072, st>>>(w2, ws, (u16*)d_out);
}
```
